```python
import torch

def _softmax(x, axis=-1):
    return torch.softmax(x, dim=axis)
def _gelu(x):
    return torch.nn.functional.gelu(x, approximate="tanh")
def _silu(x):
    return torch.nn.functional.silu(x)
def _sigmoid(x):
    return torch.sigmoid(x)
def _layernorm(x, axis=-1, eps=1e-5):
    m = x.mean(dim=axis, keepdim=True)
    v = x.var(dim=axis, keepdim=True, unbiased=False)
    return (x - m) / torch.sqrt(v + eps)
def _scatter_add(tgt, idx, src, axis=0):
    return tgt.clone().index_add_(axis, idx, src)
def _segment_sum(src, seg, n):
    out = torch.zeros((n,) + src.shape[1:], dtype=src.dtype)
    return out.index_add_(0, seg, src)
def _pad(x, pw):
    flat = tuple(v for pair in reversed(pw) for v in pair)
    return torch.nn.functional.pad(x, flat)

kernel_name = 'standalone__mha_block_cached__near_ridge'

def setup_inputs():

    _w = (lambda a: (lambda t: t.to(torch.bfloat16) if t.is_floating_point() else t)(torch.as_tensor(a)))
    B, Sq, D = 16, 192, 512
    H, Sk, Dh = 32, 512, 128
    return {
        "x": _w((lambda *s: torch.randn(*s, dtype=torch.bfloat16) * 0.8)(B, Sq, D)),
        "k_cache": _w((lambda *s: torch.randn(*s, dtype=torch.bfloat16) * 0.8)(B, H, Sk, Dh)),
        "v_cache": _w((lambda *s: torch.randn(*s, dtype=torch.bfloat16) * 0.05)(B, H, Sk, Dh)),
        "wq": _w((lambda *s: torch.randn(*s, dtype=torch.bfloat16) * 0.8)(D, H * Dh) * (D ** -0.5)),
        "wo": _w((lambda *s: torch.randn(*s, dtype=torch.bfloat16) * 0.05)(H * Dh, D) * ((H * Dh) ** -0.5)),
    }

def reference(x, k_cache, v_cache, wq, wo):
    B, Sq, D = x.shape
    H, Dh = 32, 128
    q = (lambda x, *d: x.permute(*d))((x @ wq).reshape(B, Sq, H, Dh), 0, 2, 1, 3)
    scores = torch.einsum("bhqd,bhkd->bhqk", q, k_cache) * (Dh ** -0.5)
    p = _softmax(scores, axis=-1)
    o = torch.einsum("bhqk,bhkd->bhqd", p, v_cache)
    return (lambda x, *d: x.permute(*d))(o, 0, 2, 1, 3).reshape(B, Sq, H * Dh) @ wo
```

```cpp
#include <hip/hip_runtime.h>
#include <hip/hip_bf16.h>
typedef unsigned short u16;
__device__ __forceinline__ float bf2f(u16 x){ return __uint_as_float(((unsigned)x)<<16); }
__device__ __forceinline__ u16 f2bf(float f){ unsigned u=__float_as_uint(f); u += 0x7fffu + ((u>>16)&1u); return (u16)(u>>16); }
constexpr int B=16,SQ=192,D=512,H=32,SK=512,DH=128;
__global__ void gemm_naive(const u16* A,const u16* Bm,u16* C,int M,int N,int K){
  __shared__ float As[16][17], Bs[16][17];
  int tx=threadIdx.x&15, ty=threadIdx.x>>4;
  int row=blockIdx.y*16+ty, col=blockIdx.x*16+tx;
  float acc=0;
  for(int k0=0;k0<K;k0+=16){
    As[ty][tx]=bf2f(A[(long)row*K+k0+tx]);
    Bs[ty][tx]=bf2f(Bm[(long)(k0+ty)*N+col]);
    __syncthreads();
    for(int k=0;k<16;k++) acc+=As[ty][k]*Bs[k][tx];
    __syncthreads();
  }
  C[(long)row*N+col]=f2bf(acc);
}
__global__ void attn_naive(const u16* Q,const u16* K,const u16* V,u16* O){
  int q=blockIdx.x%SQ, h=(blockIdx.x/SQ)%H, b=blockIdx.x/(SQ*H);
  __shared__ float qs[DH], p[SK], red[128];
  int t=threadIdx.x;
  qs[t]=bf2f(Q[((long)(b*SQ+q))*(H*DH)+h*DH+t]);
  __syncthreads();
  const u16* Kb=K+((long)(b*H+h))*SK*DH; const u16* Vb=V+((long)(b*H+h))*SK*DH;
  float mx=-1e30f;
  for(int k=t;k<SK;k+=128){ float s=0; for(int d=0;d<DH;d++) s+=qs[d]*bf2f(Kb[k*DH+d]); s*=0.08838834764831845f; p[k]=s; mx=fmaxf(mx,s);}
  red[t]=mx; __syncthreads();
  for(int o=64;o>0;o>>=1){ if(t<o) red[t]=fmaxf(red[t],red[t+o]); __syncthreads(); }
  mx=red[0]; __syncthreads();
  float sm=0; for(int k=t;k<SK;k+=128){ float e=__expf(p[k]-mx); p[k]=e; sm+=e; }
  red[t]=sm; __syncthreads();
  for(int o=64;o>0;o>>=1){ if(t<o) red[t]+=red[t+o]; __syncthreads(); }
  sm=red[0];
  float acc=0; for(int k=0;k<SK;k++) acc+=p[k]*bf2f(Vb[k*DH+t]);
  O[((long)(b*SQ+q))*(H*DH)+h*DH+t]=f2bf(acc/sm);
}
extern "C" void kernel_launch(void* const* d_in, const int* in_sizes, int n_in, void* d_out, int out_size, void* d_ws, size_t ws_size, hipStream_t stream){
  const u16* x=(const u16*)d_in[0]; const u16* k=(const u16*)d_in[1]; const u16* v=(const u16*)d_in[2];
  const u16* wq=(const u16*)d_in[3]; const u16* wo=(const u16*)d_in[4];
  u16* Q=(u16*)d_ws; u16* O=Q+(long)B*SQ*H*DH;
  gemm_naive<<<dim3(H*DH/16,B*SQ/16),256,0,stream>>>(x,wq,Q,B*SQ,H*DH,D);
  attn_naive<<<B*H*SQ,128,0,stream>>>(Q,k,v,O);
  gemm_naive<<<dim3(D/16,B*SQ/16),256,0,stream>>>(O,wo,(u16*)d_out,B*SQ,D,H*DH);
}
```

```cpp
#include <hip/hip_runtime.h>
#include <hip/hip_bf16.h>
typedef unsigned short u16;

__global__ __launch_bounds__(256, 1) void mha_fused(const u16* x, const u16* k, const u16* v, const u16* wq, const u16* wo, u16* out, char* ws) {
  __shared__ __attribute__((aligned(16))) char lds[163840];
  const unsigned bid = blockIdx.x, wid = __builtin_amdgcn_readfirstlane(threadIdx.x >> 6);
  const unsigned xcd = bid & 7, idx = bid >> 3, g = xcd & 3, b = (xcd >> 2) * 8 + (idx >> 2), j = idx & 3, h0 = 8 * g + 2 * j;
  const unsigned hh = wid >> 1, qh = wid & 1;
  const u16* xb = x + (size_t)b * 192 * 512;
  const u16* kb = k + ((size_t)b * 32 + h0 + hh) * 512 * 128;
  const u16* vb = v + ((size_t)b * 32 + h0 + hh) * 512 * 128;
  const u16* wb = wq + h0 * 128;
  const u16* wob = wo + (size_t)(8 * g + hh) * 128 * 512 + j * 128;
  u16* ob = out + ((size_t)b * 192 + 96 * qh) * 512 + 128 * j + 64 * hh + 16 * g;
  const unsigned meta = wid | (j << 2) | (g << 4) | (b << 6);
  asm volatile("; lds base %0" :: "s"((unsigned)(size_t)lds));
  asm volatile(
"v_mbcnt_lo_u32_b32 v212, -1, 0\n"
"v_mbcnt_hi_u32_b32 v212, -1, v212\n"
"s_mov_b64 s[32:33], %0\n"
"s_mov_b32 s34, 196608\n"
"s_mov_b32 s35, 0x20000\n"
"s_mov_b64 s[36:37], %3\n"
"s_mov_b32 s38, 4186624\n"
"s_mov_b32 s39, 0x20000\n"
"s_bfe_u32 s52, %7, 0x20000\n"
"s_bfe_u32 s47, %7, 0x10001\n"
"s_bfe_u32 s54, %7, 0x10000\n"
"v_lshrrev_b32 v199, 5, v212\n"
"v_and_b32 v198, 3, v212\n"
"v_lshlrev_b32 v198, 4, v198\n"
"v_bfe_u32 v200, v212, 2, 3\n"
"v_lshl_add_u32 v199, v199, 6, v198\n"
"v_lshl_add_u32 v211, v200, 13, v199\n"
"v_lshl_add_u32 v210, v200, 10, v199\n"
"v_xor_b32 v217, 32, v210\n"
"s_mul_i32 s66, s52, 49152\n"
"s_mul_i32 s56, s54, 6144\n"
"s_mul_i32 s67, s52, 4104\n"
"s_lshl_b32 s68, s52, 16\n"
"s_add_u32 m0, s67, 131072\n"
"s_add_u32 s55, s68, 0\n"
"buffer_load_dwordx4 v211, s[36:39], s55 offen lds\n"
"s_add_u32 m0, s67, 132096\n"
"s_add_u32 s55, s68, 128\n"
"buffer_load_dwordx4 v211, s[36:39], s55 offen lds\n"
"s_add_u32 m0, s67, 133120\n"
"s_add_u32 s55, s68, 256\n"
"buffer_load_dwordx4 v211, s[36:39], s55 offen lds\n"
"s_add_u32 m0, s67, 134144\n"
"s_add_u32 s55, s68, 384\n"
"buffer_load_dwordx4 v211, s[36:39], s55 offen lds\n"
"s_mov_b32 s65, 147488\n"
"s_cmp_eq_u32 s47, 0\n"
"s_cselect_b32 s65, s65, 16416\n"
"s_add_u32 s65, s65, s56\n"
"s_add_u32 m0, s65, 0\n"
"s_add_u32 s55, s66, 0\n"
"buffer_load_dwordx4 v210, s[32:35], s55 offen lds\n"
"s_add_u32 m0, s65, 1024\n"
"s_add_u32 s55, s66, 8192\n"
"buffer_load_dwordx4 v217, s[32:35], s55 offen lds\n"
"s_add_u32 m0, s65, 2048\n"
"s_add_u32 s55, s66, 16384\n"
"buffer_load_dwordx4 v210, s[32:35], s55 offen lds\n"
"s_add_u32 m0, s65, 3072\n"
"s_add_u32 s55, s66, 24576\n"
"buffer_load_dwordx4 v217, s[32:35], s55 offen lds\n"
"s_add_u32 m0, s65, 4096\n"
"s_add_u32 s55, s66, 32768\n"
"buffer_load_dwordx4 v210, s[32:35], s55 offen lds\n"
"s_add_u32 m0, s65, 5120\n"
"s_add_u32 s55, s66, 40960\n"
"buffer_load_dwordx4 v217, s[32:35], s55 offen lds\n"
"s_add_u32 m0, s67, 0\n"
"s_add_u32 s55, s68, 262144\n"
"buffer_load_dwordx4 v211, s[36:39], s55 offen lds\n"
"s_add_u32 m0, s67, 1024\n"
"s_add_u32 s55, s68, 262272\n"
"buffer_load_dwordx4 v211, s[36:39], s55 offen lds\n"
"s_add_u32 m0, s67, 2048\n"
"s_add_u32 s55, s68, 262400\n"
"buffer_load_dwordx4 v211, s[36:39], s55 offen lds\n"
"s_add_u32 m0, s67, 3072\n"
"s_add_u32 s55, s68, 262528\n"
"buffer_load_dwordx4 v211, s[36:39], s55 offen lds\n"
"s_add_u32 m0, s67, 32768\n"
"s_add_u32 s55, s68, 524288\n"
"buffer_load_dwordx4 v211, s[36:39], s55 offen lds\n"
"s_add_u32 m0, s67, 33792\n"
"s_add_u32 s55, s68, 524416\n"
"buffer_load_dwordx4 v211, s[36:39], s55 offen lds\n"
"s_add_u32 m0, s67, 34816\n"
"s_add_u32 s55, s68, 524544\n"
"buffer_load_dwordx4 v211, s[36:39], s55 offen lds\n"
"s_add_u32 m0, s67, 35840\n"
"s_add_u32 s55, s68, 524672\n"
"buffer_load_dwordx4 v211, s[36:39], s55 offen lds\n"
"s_mov_b32 s65, 49184\n"
"s_cmp_eq_u32 s47, 0\n"
"s_cselect_b32 s65, s65, 81952\n"
"s_add_u32 s65, s65, s56\n"
"s_add_u32 m0, s65, 0\n"
"s_add_u32 s55, s66, 128\n"
"buffer_load_dwordx4 v210, s[32:35], s55 offen lds\n"
"s_add_u32 m0, s65, 1024\n"
"s_add_u32 s55, s66, 8320\n"
"buffer_load_dwordx4 v217, s[32:35], s55 offen lds\n"
"s_add_u32 m0, s65, 2048\n"
"s_add_u32 s55, s66, 16512\n"
"buffer_load_dwordx4 v210, s[32:35], s55 offen lds\n"
"s_add_u32 m0, s65, 3072\n"
"s_add_u32 s55, s66, 24704\n"
"buffer_load_dwordx4 v217, s[32:35], s55 offen lds\n"
"s_add_u32 m0, s65, 4096\n"
"s_add_u32 s55, s66, 32896\n"
"buffer_load_dwordx4 v210, s[32:35], s55 offen lds\n"
"s_add_u32 m0, s65, 5120\n"
"s_add_u32 s55, s66, 41088\n"
"buffer_load_dwordx4 v217, s[32:35], s55 offen lds\n"
"s_getpc_b64 s[50:51]\n"
"s_bfe_u32 s55, %7, 0x40000\n"
"s_bfe_u32 s57, %7, 0x30006\n"
"s_lshl_b32 s57, s57, 4\n"
"s_add_u32 s55, s55, s57\n"
"s_mul_i32 s55, s55, 640\n"
"v_min_u32 v215, 4, v212\n"
"v_lshl_add_u32 v215, v215, 7, s55\n"
"global_load_dword v215, v215, s[50:51]\n"
"s_mov_b64 s[24:25], %1\n"
"s_mov_b32 s26, 131072\n"
"s_mov_b32 s27, 0x20000\n"
"s_mov_b64 s[28:29], %2\n"
"s_mov_b32 s30, 131072\n"
"s_mov_b32 s31, 0x20000\n"
"s_mov_b64 s[92:93], %4\n"
"s_mov_b32 s94, 41943040\n"
"s_mov_b32 s95, 0x20000\n"
"s_lshr_b32 s53, s52, 1\n"
"s_and_b32 s54, s52, 1\n"
"s_bfe_u32 s46, %7, 0x20002\n"
"s_bfe_u32 s45, %7, 0x20004\n"
"s_bfe_u32 s44, %7, 0x40006\n"
"s_lshl_b32 s55, s44, 2\n"
"s_add_u32 s55, s55, s52\n"
"s_lshl_b32 s55, s55, 7\n"
"s_add_u32 s55, s55, 37879808\n"
"v_mov_b32 v222, 0\n"
"buffer_load_dwordx2 v[222:223], v222, s[92:95], s55 offen sc1\n"
"s_mov_b32 s78, 0x3e0293ee\n"
"s_mov_b32 s79, 32768\n"
"v_bfe_u32 v200, v212, 2, 2\n"
"v_lshrrev_b32 v199, 4, v212\n"
"v_lshlrev_b32 v199, 6, v199\n"
"v_lshl_add_u32 v199, v200, 8, v199\n"
"v_and_b32 v198, 3, v212\n"
"v_lshl_add_u32 v199, v198, 4, v199\n"
"s_lshl_b32 s56, s54, 10\n"
"v_add_u32 v195, s56, v199\n"
"v_xor_b32 v196, 32, v195\n"
"s_lshl_b32 s57, s54, 5\n"
"v_xor_b32 v197, s57, v195\n"
"s_lshl_b32 s55, s53, 13\n"
"s_add_u32 s73, s55, s56\n"
"v_lshrrev_b32 v200, 4, v212\n"
"v_bfe_u32 v199, v212, 3, 1\n"
"v_lshlrev_b32 v199, 1, v199\n"
"v_xor_b32 v199, v199, v200\n"
"v_lshlrev_b32 v199, 4, v199\n"
"v_and_b32 v198, 7, v212\n"
"v_lshl_add_u32 v208, v198, 6, v199\n"
"v_bfe_u32 v198, v212, 3, 1\n"
"v_lshl_add_u32 v208, v198, 10, v208\n"
"s_mov_b32 s56, 147488\n"
"s_cmp_eq_u32 s54, 0\n"
"s_cselect_b32 s56, s56, 16416\n"
"v_add_u32 v208, s56, v208\n"
"v_and_b32 v198, 3, v212\n"
"v_lshl_add_u32 v199, v198, 6, v199\n"
"v_bfe_u32 v198, v212, 2, 2\n"
"v_lshl_add_u32 v192, v198, 10, v199\n"
"v_add_u32 v192, s55, v192\n"
"v_lshlrev_b32 v198, 6, v198\n"
"v_and_b32 v199, 3, v212\n"
"v_lshl_add_u32 v209, v199, 4, v198\n"
"v_lshl_add_u32 v193, v199, 3, v198\n"
"v_lshl_add_u32 v193, v200, 10, v193\n"
"v_and_b32 v199, 1, v200\n"
"v_lshl_add_u32 v193, v199, 5, v193\n"
"s_add_u32 s55, s55, 16384\n"
"v_add_u32 v193, s55, v193\n"
"v_xor_b32 v194, 32, v193\n"
"v_mul_u32_u24 v199, 4104, v200\n"
"v_add_u32 v209, v209, v199\n"
"s_lshl_b32 s56, s53, 11\n"
"s_add_u32 s56, s56, 131072\n"
"v_add_u32 v209, s56, v209\n"
"s_cmp_lg_u32 s53, 0\n"
"s_cbranch_scc1 .Lq_B\n"
"s_mov_b32 s47, s54\n"
"s_mul_i32 s66, s54, 98304\n"
"s_mul_i32 s67, s54, 8208\n"
"s_lshl_b32 s68, s54, 17\n"
"s_waitcnt vmcnt(16)\n"
"s_barrier\n"
"ds_read_b64_tr_b16 a[0:1], v209\n"
"ds_read_b64_tr_b16 a[2:3], v209 offset:256\n"
"ds_read_b128 a[32:35], v208\n"
"ds_read_b128 a[36:39], v208 offset:2048\n"
"ds_read_b128 a[40:43], v208 offset:4096\n"
"ds_read_b128 a[44:47], v208 offset:6144\n"
"ds_read_b128 a[48:51], v208 offset:8192\n"
"ds_read_b128 a[52:55], v208 offset:10240\n"
"ds_read_b64_tr_b16 a[4:5], v209 offset:8\n"
"ds_read_b64_tr_b16 a[6:7], v209 offset:264\n"
"ds_read_b64_tr_b16 a[8:9], v209 offset:512\n"
"s_waitcnt lgkmcnt(0)\n"
"ds_read_b64_tr_b16 a[10:11], v209 offset:768\n"
"ds_read_b64_tr_b16 a[12:13], v209 offset:520\n"
"ds_read_b64_tr_b16 a[14:15], v209 offset:776\n"
"ds_read_b64_tr_b16 a[16:17], v209 offset:1024\n"
"ds_read_b64_tr_b16 a[18:19], v209 offset:1280\n"
"ds_read_b64_tr_b16 a[20:21], v209 offset:1032\n"
"ds_read_b64_tr_b16 a[22:23], v209 offset:1288\n"
"ds_read_b64_tr_b16 a[24:25], v209 offset:1536\n"
"ds_read_b64_tr_b16 a[26:27], v209 offset:1792\n"
"ds_read_b64_tr_b16 a[28:29], v209 offset:1544\n"
"ds_read_b64_tr_b16 a[30:31], v209 offset:1800\n"
"v_add_u32 v209, 0xfffe0000, v209\n"
"v_add_u32 v208, 512, v208\n"
"s_waitcnt vmcnt(12) lgkmcnt(0)\n"
"s_barrier\n"
"v_mfma_f32_16x16x32_bf16 v[0:3], a[0:3], a[32:35], 0\n"
"s_add_u32 m0, s67, 65536\n"
"s_add_u32 s55, s68, 786432\n"
"s_mov_b32 s61, 0xfffe7e00\n"
"v_mfma_f32_16x16x32_bf16 v[8:11], a[0:3], a[36:39], 0\n"
"buffer_load_dwordx4 v211, s[36:39], s55 offen lds\n"
"v_mfma_f32_16x16x32_bf16 v[16:19], a[0:3], a[40:43], 0\n"
"s_add_u32 m0, s67, 66560\n"
"s_add_u32 s55, s68, 786560\n"
"ds_read_b64_tr_b16 a[56:57], v209\n"
"v_mfma_f32_16x16x32_bf16 v[24:27], a[0:3], a[44:47], 0\n"
"buffer_load_dwordx4 v211, s[36:39], s55 offen lds\n"
"v_mfma_f32_16x16x32_bf16 v[32:35], a[0:3], a[48:51], 0\n"
"s_add_u32 m0, s67, 67584\n"
"s_add_u32 s55, s68, 786688\n"
"ds_read_b64_tr_b16 a[58:59], v209 offset:256\n"
"v_mfma_f32_16x16x32_bf16 v[40:43], a[0:3], a[52:55], 0\n"
"buffer_load_dwordx4 v211, s[36:39], s55 offen lds\n"
"v_mfma_f32_16x16x32_bf16 v[4:7], a[4:7], a[32:35], 0\n"
"s_add_u32 m0, s67, 68608\n"
"s_add_u32 s55, s68, 786816\n"
"ds_read_b128 a[88:91], v208\n"
"v_mfma_f32_16x16x32_bf16 v[12:15], a[4:7], a[36:39], 0\n"
"buffer_load_dwordx4 v211, s[36:39], s55 offen lds\n"
"v_mfma_f32_16x16x32_bf16 v[20:23], a[4:7], a[40:43], 0\n"
"s_add_u32 m0, s67, 69640\n"
"s_add_u32 s55, s68, 851968\n"
"ds_read_b128 a[92:95], v208 offset:2048\n"
"v_mfma_f32_16x16x32_bf16 v[28:31], a[4:7], a[44:47], 0\n"
"buffer_load_dwordx4 v211, s[36:39], s55 offen lds\n"
"v_mfma_f32_16x16x32_bf16 v[36:39], a[4:7], a[48:51], 0\n"
"s_add_u32 m0, s67, 70664\n"
"s_add_u32 s55, s68, 852096\n"
"ds_read_b128 a[96:99], v208 offset:4096\n"
"v_mfma_f32_16x16x32_bf16 v[44:47], a[4:7], a[52:55], 0\n"
"buffer_load_dwordx4 v211, s[36:39], s55 offen lds\n"
"v_mfma_f32_16x16x32_bf16 v[48:51], a[8:11], a[32:35], 0\n"
"s_add_u32 m0, s67, 71688\n"
"s_add_u32 s55, s68, 852224\n"
"ds_read_b128 a[100:103], v208 offset:6144\n"
"v_mfma_f32_16x16x32_bf16 v[56:59], a[8:11], a[36:39], 0\n"
"buffer_load_dwordx4 v211, s[36:39], s55 offen lds\n"
"v_mfma_f32_16x16x32_bf16 v[64:67], a[8:11], a[40:43], 0\n"
"s_add_u32 m0, s67, 72712\n"
"s_add_u32 s55, s68, 852352\n"
"ds_read_b64_tr_b16 a[60:61], v209 offset:8\n"
"v_mfma_f32_16x16x32_bf16 v[72:75], a[8:11], a[44:47], 0\n"
"buffer_load_dwordx4 v211, s[36:39], s55 offen lds\n"
"v_mfma_f32_16x16x32_bf16 v[80:83], a[8:11], a[48:51], 0\n"
"s_add_u32 m0, s67, 98304\n"
"s_add_u32 s55, s68, 1048576\n"
"ds_read_b64_tr_b16 a[62:63], v209 offset:264\n"
"v_mfma_f32_16x16x32_bf16 v[88:91], a[8:11], a[52:55], 0\n"
"buffer_load_dwordx4 v211, s[36:39], s55 offen lds\n"
"v_mfma_f32_16x16x32_bf16 v[52:55], a[12:15], a[32:35], 0\n"
"s_add_u32 m0, s67, 99328\n"
"s_add_u32 s55, s68, 1048704\n"
"ds_read_b64_tr_b16 a[64:65], v209 offset:512\n"
"v_mfma_f32_16x16x32_bf16 v[60:63], a[12:15], a[36:39], 0\n"
"buffer_load_dwordx4 v211, s[36:39], s55 offen lds\n"
"v_mfma_f32_16x16x32_bf16 v[68:71], a[12:15], a[40:43], 0\n"
"s_add_u32 m0, s67, 100352\n"
"s_add_u32 s55, s68, 1048832\n"
"ds_read_b64_tr_b16 a[66:67], v209 offset:768\n"
"v_mfma_f32_16x16x32_bf16 v[76:79], a[12:15], a[44:47], 0\n"
"buffer_load_dwordx4 v211, s[36:39], s55 offen lds\n"
"v_mfma_f32_16x16x32_bf16 v[84:87], a[12:15], a[48:51], 0\n"
"s_add_u32 m0, s67, 101376\n"
"s_add_u32 s55, s68, 1048960\n"
"ds_read_b64_tr_b16 a[68:69], v209 offset:520\n"
"v_mfma_f32_16x16x32_bf16 v[92:95], a[12:15], a[52:55], 0\n"
"buffer_load_dwordx4 v211, s[36:39], s55 offen lds\n"
"v_mfma_f32_16x16x32_bf16 v[96:99], a[16:19], a[32:35], 0\n"
"s_add_u32 m0, s67, 102408\n"
"s_add_u32 s55, s68, 1114112\n"
"ds_read_b64_tr_b16 a[70:71], v209 offset:776\n"
"v_mfma_f32_16x16x32_bf16 v[104:107], a[16:19], a[36:39], 0\n"
"buffer_load_dwordx4 v211, s[36:39], s55 offen lds\n"
"v_mfma_f32_16x16x32_bf16 v[112:115], a[16:19], a[40:43], 0\n"
"s_add_u32 m0, s67, 103432\n"
"s_add_u32 s55, s68, 1114240\n"
"ds_read_b64_tr_b16 a[72:73], v209 offset:1024\n"
"v_mfma_f32_16x16x32_bf16 v[120:123], a[16:19], a[44:47], 0\n"
"buffer_load_dwordx4 v211, s[36:39], s55 offen lds\n"
"v_mfma_f32_16x16x32_bf16 v[128:131], a[16:19], a[48:51], 0\n"
"s_add_u32 m0, s67, 104456\n"
"s_add_u32 s55, s68, 1114368\n"
"ds_read_b64_tr_b16 a[74:75], v209 offset:1280\n"
"v_mfma_f32_16x16x32_bf16 v[136:139], a[16:19], a[52:55], 0\n"
"buffer_load_dwordx4 v211, s[36:39], s55 offen lds\n"
"v_mfma_f32_16x16x32_bf16 v[100:103], a[20:23], a[32:35], 0\n"
"s_add_u32 m0, s67, 105480\n"
"s_add_u32 s55, s68, 1114496\n"
"ds_read_b64_tr_b16 a[76:77], v209 offset:1032\n"
"v_mfma_f32_16x16x32_bf16 v[108:111], a[20:23], a[36:39], 0\n"
"buffer_load_dwordx4 v211, s[36:39], s55 offen lds\n"
"v_mfma_f32_16x16x32_bf16 v[116:119], a[20:23], a[40:43], 0\n"
"s_add_u32 m0, s67, 131072\n"
"s_add_u32 s55, s68, 1310720\n"
"ds_read_b64_tr_b16 a[78:79], v209 offset:1288\n"
"v_mfma_f32_16x16x32_bf16 v[124:127], a[20:23], a[44:47], 0\n"
"buffer_load_dwordx4 v211, s[36:39], s55 offen lds\n"
"v_mfma_f32_16x16x32_bf16 v[132:135], a[20:23], a[48:51], 0\n"
"s_add_u32 m0, s67, 132096\n"
"s_add_u32 s55, s68, 1310848\n"
"ds_read_b64_tr_b16 a[80:81], v209 offset:1536\n"
"v_mfma_f32_16x16x32_bf16 v[140:143], a[20:23], a[52:55], 0\n"
"buffer_load_dwordx4 v211, s[36:39], s55 offen lds\n"
"v_mfma_f32_16x16x32_bf16 v[144:147], a[24:27], a[32:35], 0\n"
"s_add_u32 m0, s67, 133120\n"
"s_add_u32 s55, s68, 1310976\n"
"ds_read_b64_tr_b16 a[82:83], v209 offset:1792\n"
"v_mfma_f32_16x16x32_bf16 v[152:155], a[24:27], a[36:39], 0\n"
"buffer_load_dwordx4 v211, s[36:39], s55 offen lds\n"
"v_mfma_f32_16x16x32_bf16 v[160:163], a[24:27], a[40:43], 0\n"
"s_add_u32 m0, s67, 134144\n"
"s_add_u32 s55, s68, 1311104\n"
"ds_read_b64_tr_b16 a[84:85], v209 offset:1544\n"
"v_mfma_f32_16x16x32_bf16 v[168:171], a[24:27], a[44:47], 0\n"
"buffer_load_dwordx4 v211, s[36:39], s55 offen lds\n"
"v_mfma_f32_16x16x32_bf16 v[176:179], a[24:27], a[48:51], 0\n"
"s_add_u32 m0, s67, 135176\n"
"s_add_u32 s55, s68, 1376256\n"
"ds_read_b64_tr_b16 a[86:87], v209 offset:1800\n"
"v_mfma_f32_16x16x32_bf16 v[184:187], a[24:27], a[52:55], 0\n"
"buffer_load_dwordx4 v211, s[36:39], s55 offen lds\n"
"v_mfma_f32_16x16x32_bf16 v[148:151], a[28:31], a[32:35], 0\n"
"s_add_u32 m0, s67, 136200\n"
"s_add_u32 s55, s68, 1376384\n"
"ds_read_b128 a[104:107], v208 offset:8192\n"
"v_mfma_f32_16x16x32_bf16 v[156:159], a[28:31], a[36:39], 0\n"
"buffer_load_dwordx4 v211, s[36:39], s55 offen lds\n"
"v_mfma_f32_16x16x32_bf16 v[164:167], a[28:31], a[40:43], 0\n"
"s_add_u32 m0, s67, 137224\n"
"s_add_u32 s55, s68, 1376512\n"
"ds_read_b128 a[108:111], v208 offset:10240\n"
"v_mfma_f32_16x16x32_bf16 v[172:175], a[28:31], a[44:47], 0\n"
"buffer_load_dwordx4 v211, s[36:39], s55 offen lds\n"
"v_mfma_f32_16x16x32_bf16 v[180:183], a[28:31], a[48:51], 0\n"
"s_add_u32 m0, s67, 138248\n"
"s_add_u32 s55, s68, 1376640\n"
"s_cmp_eq_u32 s54, 0\n"
"v_mfma_f32_16x16x32_bf16 v[188:191], a[28:31], a[52:55], 0\n"
"s_cselect_b32 s61, s61, 65024\n"
"buffer_load_dwordx4 v211, s[36:39], s55 offen lds\n"
"v_add_u32 v209, 32768, v209\n"
"v_add_u32 v208, s61, v208\n"
"s_waitcnt vmcnt(26) lgkmcnt(0)\n"
"s_barrier\n"
"v_mfma_f32_16x16x32_bf16 v[0:3], a[56:59], a[88:91], v[0:3]\n"
"s_mov_b32 s65, 114720\n"
"s_cmp_eq_u32 s47, 0\n"
"s_cselect_b32 s65, s65, 147488\n"
"v_mfma_f32_16x16x32_bf16 v[8:11], a[56:59], a[92:95], v[8:11]\n"
"s_add_u32 m0, s65, 0\n"
"s_add_u32 s55, s66, 256\n"
"ds_read_b64_tr_b16 a[0:1], v209\n"
"v_mfma_f32_16x16x32_bf16 v[16:19], a[56:59], a[96:99], v[16:19]\n"
"buffer_load_dwordx4 v210, s[32:35], s55 offen lds\n"
"v_mfma_f32_16x16x32_bf16 v[24:27], a[56:59], a[100:103], v[24:27]\n"
"s_add_u32 m0, s65, 1024\n"
"s_add_u32 s55, s66, 8448\n"
"ds_read_b64_tr_b16 a[2:3], v209 offset:256\n"
"v_mfma_f32_16x16x32_bf16 v[32:35], a[56:59], a[104:107], v[32:35]\n"
"buffer_load_dwordx4 v217, s[32:35], s55 offen lds\n"
"v_mfma_f32_16x16x32_bf16 v[40:43], a[56:59], a[108:111], v[40:43]\n"
"s_add_u32 m0, s65, 2048\n"
"s_add_u32 s55, s66, 16640\n"
"ds_read_b128 a[32:35], v208\n"
"v_mfma_f32_16x16x32_bf16 v[4:7], a[60:63], a[88:91], v[4:7]\n"
"buffer_load_dwordx4 v210, s[32:35], s55 offen lds\n"
"v_mfma_f32_16x16x32_bf16 v[12:15], a[60:63], a[92:95], v[12:15]\n"
"s_add_u32 m0, s65, 3072\n"
"s_add_u32 s55, s66, 24832\n"
"ds_read_b128 a[36:39], v208 offset:2048\n"
"v_mfma_f32_16x16x32_bf16 v[20:23], a[60:63], a[96:99], v[20:23]\n"
"buffer_load_dwordx4 v217, s[32:35], s55 offen lds\n"
"v_mfma_f32_16x16x32_bf16 v[28:31], a[60:63], a[100:103], v[28:31]\n"
"s_add_u32 m0, s65, 4096\n"
"s_add_u32 s55, s66, 33024\n"
"ds_read_b128 a[40:43], v208 offset:4096\n"
"v_mfma_f32_16x16x32_bf16 v[36:39], a[60:63], a[104:107], v[36:39]\n"
"buffer_load_dwordx4 v210, s[32:35], s55 offen lds\n"
"v_mfma_f32_16x16x32_bf16 v[44:47], a[60:63], a[108:111], v[44:47]\n"
"s_add_u32 m0, s65, 5120\n"
"s_add_u32 s55, s66, 41216\n"
"ds_read_b128 a[44:47], v208 offset:6144\n"
"v_mfma_f32_16x16x32_bf16 v[48:51], a[64:67], a[88:91], v[48:51]\n"
"buffer_load_dwordx4 v217, s[32:35], s55 offen lds\n"
"v_mfma_f32_16x16x32_bf16 v[56:59], a[64:67], a[92:95], v[56:59]\n"
"s_add_u32 m0, s65, 6144\n"
"s_add_u32 s55, s66, 49408\n"
"ds_read_b128 a[48:51], v208 offset:8192\n"
"v_mfma_f32_16x16x32_bf16 v[64:67], a[64:67], a[96:99], v[64:67]\n"
"buffer_load_dwordx4 v210, s[32:35], s55 offen lds\n"
"v_mfma_f32_16x16x32_bf16 v[72:75], a[64:67], a[100:103], v[72:75]\n"
"s_add_u32 m0, s65, 7168\n"
"s_add_u32 s55, s66, 57600\n"
"ds_read_b64_tr_b16 a[4:5], v209 offset:8\n"
"v_mfma_f32_16x16x32_bf16 v[80:83], a[64:67], a[104:107], v[80:83]\n"
"buffer_load_dwordx4 v217, s[32:35], s55 offen lds\n"
"v_mfma_f32_16x16x32_bf16 v[88:91], a[64:67], a[108:111], v[88:91]\n"
"s_add_u32 m0, s65, 8192\n"
"s_add_u32 s55, s66, 65792\n"
"ds_read_b64_tr_b16 a[6:7], v209 offset:264\n"
"v_mfma_f32_16x16x32_bf16 v[52:55], a[68:71], a[88:91], v[52:55]\n"
"buffer_load_dwordx4 v210, s[32:35], s55 offen lds\n"
"v_mfma_f32_16x16x32_bf16 v[60:63], a[68:71], a[92:95], v[60:63]\n"
"s_add_u32 m0, s65, 9216\n"
"s_add_u32 s55, s66, 73984\n"
"ds_read_b64_tr_b16 a[8:9], v209 offset:512\n"
"v_mfma_f32_16x16x32_bf16 v[68:71], a[68:71], a[96:99], v[68:71]\n"
"buffer_load_dwordx4 v217, s[32:35], s55 offen lds\n"
"v_mfma_f32_16x16x32_bf16 v[76:79], a[68:71], a[100:103], v[76:79]\n"
"s_add_u32 m0, s65, 10240\n"
"s_add_u32 s55, s66, 82176\n"
"ds_read_b64_tr_b16 a[10:11], v209 offset:768\n"
"v_mfma_f32_16x16x32_bf16 v[84:87], a[68:71], a[104:107], v[84:87]\n"
"buffer_load_dwordx4 v210, s[32:35], s55 offen lds\n"
"v_mfma_f32_16x16x32_bf16 v[92:95], a[68:71], a[108:111], v[92:95]\n"
"s_add_u32 m0, s65, 11264\n"
"s_add_u32 s55, s66, 90368\n"
"ds_read_b64_tr_b16 a[12:13], v209 offset:520\n"
"v_mfma_f32_16x16x32_bf16 v[96:99], a[72:75], a[88:91], v[96:99]\n"
"buffer_load_dwordx4 v217, s[32:35], s55 offen lds\n"
"v_mfma_f32_16x16x32_bf16 v[104:107], a[72:75], a[92:95], v[104:107]\n"
"ds_read_b64_tr_b16 a[14:15], v209 offset:776\n"
"v_mfma_f32_16x16x32_bf16 v[112:115], a[72:75], a[96:99], v[112:115]\n"
"ds_read_b64_tr_b16 a[16:17], v209 offset:1024\n"
"v_mfma_f32_16x16x32_bf16 v[120:123], a[72:75], a[100:103], v[120:123]\n"
"ds_read_b64_tr_b16 a[18:19], v209 offset:1280\n"
"v_mfma_f32_16x16x32_bf16 v[128:131], a[72:75], a[104:107], v[128:131]\n"
"ds_read_b64_tr_b16 a[20:21], v209 offset:1032\n"
"v_mfma_f32_16x16x32_bf16 v[136:139], a[72:75], a[108:111], v[136:139]\n"
"ds_read_b128 a[52:55], v208 offset:10240\n"
"v_add_u32 v208, 512, v208\n"
"v_mfma_f32_16x16x32_bf16 v[100:103], a[76:79], a[88:91], v[100:103]\n"
"ds_read_b64_tr_b16 a[22:23], v209 offset:1288\n"
"v_mfma_f32_16x16x32_bf16 v[108:111], a[76:79], a[92:95], v[108:111]\n"
"ds_read_b64_tr_b16 a[24:25], v209 offset:1536\n"
"v_mfma_f32_16x16x32_bf16 v[116:119], a[76:79], a[96:99], v[116:119]\n"
"v_mfma_f32_16x16x32_bf16 v[124:127], a[76:79], a[100:103], v[124:127]\n"
"ds_read_b64_tr_b16 a[26:27], v209 offset:1792\n"
"v_mfma_f32_16x16x32_bf16 v[132:135], a[76:79], a[104:107], v[132:135]\n"
"v_mfma_f32_16x16x32_bf16 v[140:143], a[76:79], a[108:111], v[140:143]\n"
"ds_read_b64_tr_b16 a[28:29], v209 offset:1544\n"
"v_mfma_f32_16x16x32_bf16 v[144:147], a[80:83], a[88:91], v[144:147]\n"
"ds_read_b64_tr_b16 a[30:31], v209 offset:1800\n"
"v_mfma_f32_16x16x32_bf16 v[152:155], a[80:83], a[92:95], v[152:155]\n"
"v_add_u32 v209, 32768, v209\n"
"v_mfma_f32_16x16x32_bf16 v[160:163], a[80:83], a[96:99], v[160:163]\n"
"v_mfma_f32_16x16x32_bf16 v[168:171], a[80:83], a[100:103], v[168:171]\n"
"v_mfma_f32_16x16x32_bf16 v[176:179], a[80:83], a[104:107], v[176:179]\n"
"v_mfma_f32_16x16x32_bf16 v[184:187], a[80:83], a[108:111], v[184:187]\n"
"v_mfma_f32_16x16x32_bf16 v[148:151], a[84:87], a[88:91], v[148:151]\n"
"v_mfma_f32_16x16x32_bf16 v[156:159], a[84:87], a[92:95], v[156:159]\n"
"v_mfma_f32_16x16x32_bf16 v[164:167], a[84:87], a[96:99], v[164:167]\n"
"v_mfma_f32_16x16x32_bf16 v[172:175], a[84:87], a[100:103], v[172:175]\n"
"v_mfma_f32_16x16x32_bf16 v[180:183], a[84:87], a[104:107], v[180:183]\n"
"v_mfma_f32_16x16x32_bf16 v[188:191], a[84:87], a[108:111], v[188:191]\n"
"s_waitcnt vmcnt(28) lgkmcnt(0)\n"
"s_barrier\n"
"v_mfma_f32_16x16x32_bf16 v[0:3], a[0:3], a[32:35], v[0:3]\n"
"s_add_u32 m0, s67, 0\n"
"s_add_u32 s55, s68, 1572864\n"
"ds_read_b64_tr_b16 a[56:57], v209\n"
"v_mfma_f32_16x16x32_bf16 v[8:11], a[0:3], a[36:39], v[8:11]\n"
"buffer_load_dwordx4 v211, s[36:39], s55 offen lds\n"
"v_mfma_f32_16x16x32_bf16 v[16:19], a[0:3], a[40:43], v[16:19]\n"
"s_add_u32 m0, s67, 1024\n"
"s_add_u32 s55, s68, 1572992\n"
"ds_read_b64_tr_b16 a[58:59], v209 offset:256\n"
"v_mfma_f32_16x16x32_bf16 v[24:27], a[0:3], a[44:47], v[24:27]\n"
"ds_read_b128 a[88:91], v208\n"
"v_mfma_f32_16x16x32_bf16 v[32:35], a[0:3], a[48:51], v[32:35]\n"
"buffer_load_dwordx4 v211, s[36:39], s55 offen lds\n"
"v_mfma_f32_16x16x32_bf16 v[40:43], a[0:3], a[52:55], v[40:43]\n"
"s_add_u32 m0, s67, 2048\n"
"s_add_u32 s55, s68, 1573120\n"
"ds_read_b128 a[92:95], v208 offset:2048\n"
"v_mfma_f32_16x16x32_bf16 v[4:7], a[4:7], a[32:35], v[4:7]\n"
"v_mfma_f32_16x16x32_bf16 v[12:15], a[4:7], a[36:39], v[12:15]\n"
"buffer_load_dwordx4 v211, s[36:39], s55 offen lds\n"
"v_mfma_f32_16x16x32_bf16 v[20:23], a[4:7], a[40:43], v[20:23]\n"
"s_add_u32 m0, s67, 3072\n"
"s_add_u32 s55, s68, 1573248\n"
"ds_read_b128 a[96:99], v208 offset:4096\n"
"v_mfma_f32_16x16x32_bf16 v[28:31], a[4:7], a[44:47], v[28:31]\n"
"ds_read_b128 a[100:103], v208 offset:6144\n"
"v_mfma_f32_16x16x32_bf16 v[36:39], a[4:7], a[48:51], v[36:39]\n"
"buffer_load_dwordx4 v211, s[36:39], s55 offen lds\n"
"v_mfma_f32_16x16x32_bf16 v[44:47], a[4:7], a[52:55], v[44:47]\n"
"s_add_u32 m0, s67, 4104\n"
"s_add_u32 s55, s68, 1638400\n"
"ds_read_b128 a[104:107], v208 offset:8192\n"
"v_mfma_f32_16x16x32_bf16 v[48:51], a[8:11], a[32:35], v[48:51]\n"
"ds_read_b128 a[108:111], v208 offset:10240\n"
"v_add_u32 v208, 65024, v208\n"
"v_mfma_f32_16x16x32_bf16 v[56:59], a[8:11], a[36:39], v[56:59]\n"
"buffer_load_dwordx4 v211, s[36:39], s55 offen lds\n"
"v_mfma_f32_16x16x32_bf16 v[64:67], a[8:11], a[40:43], v[64:67]\n"
"s_add_u32 m0, s67, 5128\n"
"s_add_u32 s55, s68, 1638528\n"
"ds_read_b64_tr_b16 a[60:61], v209 offset:8\n"
"v_mfma_f32_16x16x32_bf16 v[72:75], a[8:11], a[44:47], v[72:75]\n"
"ds_read_b64_tr_b16 a[62:63], v209 offset:264\n"
"v_mfma_f32_16x16x32_bf16 v[80:83], a[8:11], a[48:51], v[80:83]\n"
"buffer_load_dwordx4 v211, s[36:39], s55 offen lds\n"
"v_mfma_f32_16x16x32_bf16 v[88:91], a[8:11], a[52:55], v[88:91]\n"
"s_add_u32 m0, s67, 6152\n"
"s_add_u32 s55, s68, 1638656\n"
"ds_read_b64_tr_b16 a[64:65], v209 offset:512\n"
"v_mfma_f32_16x16x32_bf16 v[52:55], a[12:15], a[32:35], v[52:55]\n"
"buffer_load_dwordx4 v211, s[36:39], s55 offen lds\n"
"v_mfma_f32_16x16x32_bf16 v[60:63], a[12:15], a[36:39], v[60:63]\n"
"s_add_u32 m0, s67, 7176\n"
"s_add_u32 s55, s68, 1638784\n"
"ds_read_b64_tr_b16 a[66:67], v209 offset:768\n"
"v_mfma_f32_16x16x32_bf16 v[68:71], a[12:15], a[40:43], v[68:71]\n"
"v_mfma_f32_16x16x32_bf16 v[76:79], a[12:15], a[44:47], v[76:79]\n"
"buffer_load_dwordx4 v211, s[36:39], s55 offen lds\n"
"v_mfma_f32_16x16x32_bf16 v[84:87], a[12:15], a[48:51], v[84:87]\n"
"s_add_u32 m0, s67, 32768\n"
"s_add_u32 s55, s68, 1835008\n"
"ds_read_b64_tr_b16 a[68:69], v209 offset:520\n"
"v_mfma_f32_16x16x32_bf16 v[92:95], a[12:15], a[52:55], v[92:95]\n"
"ds_read_b64_tr_b16 a[70:71], v209 offset:776\n"
"v_mfma_f32_16x16x32_bf16 v[96:99], a[16:19], a[32:35], v[96:99]\n"
"buffer_load_dwordx4 v211, s[36:39], s55 offen lds\n"
"v_mfma_f32_16x16x32_bf16 v[104:107], a[16:19], a[36:39], v[104:107]\n"
"s_add_u32 m0, s67, 33792\n"
"s_add_u32 s55, s68, 1835136\n"
"ds_read_b64_tr_b16 a[72:73], v209 offset:1024\n"
"v_mfma_f32_16x16x32_bf16 v[112:115], a[16:19], a[40:43], v[112:115]\n"
"ds_read_b64_tr_b16 a[74:75], v209 offset:1280\n"
"v_mfma_f32_16x16x32_bf16 v[120:123], a[16:19], a[44:47], v[120:123]\n"
"buffer_load_dwordx4 v211, s[36:39], s55 offen lds\n"
"v_mfma_f32_16x16x32_bf16 v[128:131], a[16:19], a[48:51], v[128:131]\n"
"s_add_u32 m0, s67, 34816\n"
"s_add_u32 s55, s68, 1835264\n"
"ds_read_b64_tr_b16 a[76:77], v209 offset:1032\n"
"v_mfma_f32_16x16x32_bf16 v[136:139], a[16:19], a[52:55], v[136:139]\n"
"ds_read_b64_tr_b16 a[78:79], v209 offset:1288\n"
"v_mfma_f32_16x16x32_bf16 v[100:103], a[20:23], a[32:35], v[100:103]\n"
"buffer_load_dwordx4 v211, s[36:39], s55 offen lds\n"
"v_mfma_f32_16x16x32_bf16 v[108:111], a[20:23], a[36:39], v[108:111]\n"
"s_add_u32 m0, s67, 35840\n"
"s_add_u32 s55, s68, 1835392\n"
"ds_read_b64_tr_b16 a[80:81], v209 offset:1536\n"
"v_mfma_f32_16x16x32_bf16 v[116:119], a[20:23], a[40:43], v[116:119]\n"
"buffer_load_dwordx4 v211, s[36:39], s55 offen lds\n"
"v_mfma_f32_16x16x32_bf16 v[124:127], a[20:23], a[44:47], v[124:127]\n"
"s_add_u32 m0, s67, 36872\n"
"s_add_u32 s55, s68, 1900544\n"
"ds_read_b64_tr_b16 a[82:83], v209 offset:1792\n"
"v_mfma_f32_16x16x32_bf16 v[132:135], a[20:23], a[48:51], v[132:135]\n"
"v_mfma_f32_16x16x32_bf16 v[140:143], a[20:23], a[52:55], v[140:143]\n"
"buffer_load_dwordx4 v211, s[36:39], s55 offen lds\n"
"v_mfma_f32_16x16x32_bf16 v[144:147], a[24:27], a[32:35], v[144:147]\n"
"s_add_u32 m0, s67, 37896\n"
"s_add_u32 s55, s68, 1900672\n"
"ds_read_b64_tr_b16 a[84:85], v209 offset:1544\n"
"v_mfma_f32_16x16x32_bf16 v[152:155], a[24:27], a[36:39], v[152:155]\n"
"ds_read_b64_tr_b16 a[86:87], v209 offset:1800\n"
"v_add_u32 v209, 32768, v209\n"
"v_mfma_f32_16x16x32_bf16 v[160:163], a[24:27], a[40:43], v[160:163]\n"
"buffer_load_dwordx4 v211, s[36:39], s55 offen lds\n"
"v_mfma_f32_16x16x32_bf16 v[168:171], a[24:27], a[44:47], v[168:171]\n"
"s_add_u32 m0, s67, 38920\n"
"s_add_u32 s55, s68, 1900800\n"
"v_mfma_f32_16x16x32_bf16 v[176:179], a[24:27], a[48:51], v[176:179]\n"
"v_mfma_f32_16x16x32_bf16 v[184:187], a[24:27], a[52:55], v[184:187]\n"
"buffer_load_dwordx4 v211, s[36:39], s55 offen lds\n"
"v_mfma_f32_16x16x32_bf16 v[148:151], a[28:31], a[32:35], v[148:151]\n"
"s_add_u32 m0, s67, 39944\n"
"s_add_u32 s55, s68, 1900928\n"
"v_mfma_f32_16x16x32_bf16 v[156:159], a[28:31], a[36:39], v[156:159]\n"
"v_mfma_f32_16x16x32_bf16 v[164:167], a[28:31], a[40:43], v[164:167]\n"
"buffer_load_dwordx4 v211, s[36:39], s55 offen lds\n"
"v_mfma_f32_16x16x32_bf16 v[172:175], a[28:31], a[44:47], v[172:175]\n"
"v_mfma_f32_16x16x32_bf16 v[180:183], a[28:31], a[48:51], v[180:183]\n"
"v_mfma_f32_16x16x32_bf16 v[188:191], a[28:31], a[52:55], v[188:191]\n"
"s_waitcnt vmcnt(16) lgkmcnt(0)\n"
"s_barrier\n"
"v_mfma_f32_16x16x32_bf16 v[0:3], a[56:59], a[88:91], v[0:3]\n"
"s_mov_b32 s65, 16416\n"
"s_cmp_eq_u32 s47, 0\n"
"s_cselect_b32 s65, s65, 49184\n"
"v_mfma_f32_16x16x32_bf16 v[8:11], a[56:59], a[92:95], v[8:11]\n"
"s_add_u32 m0, s65, 0\n"
"s_add_u32 s55, s66, 384\n"
"ds_read_b64_tr_b16 a[0:1], v209\n"
"v_mfma_f32_16x16x32_bf16 v[16:19], a[56:59], a[96:99], v[16:19]\n"
"buffer_load_dwordx4 v210, s[32:35], s55 offen lds\n"
"v_mfma_f32_16x16x32_bf16 v[24:27], a[56:59], a[100:103], v[24:27]\n"
"s_add_u32 m0, s65, 1024\n"
"s_add_u32 s55, s66, 8576\n"
"ds_read_b64_tr_b16 a[2:3], v209 offset:256\n"
"v_mfma_f32_16x16x32_bf16 v[32:35], a[56:59], a[104:107], v[32:35]\n"
"buffer_load_dwordx4 v217, s[32:35], s55 offen lds\n"
"v_mfma_f32_16x16x32_bf16 v[40:43], a[56:59], a[108:111], v[40:43]\n"
"s_add_u32 m0, s65, 2048\n"
"s_add_u32 s55, s66, 16768\n"
"ds_read_b128 a[32:35], v208\n"
"v_mfma_f32_16x16x32_bf16 v[4:7], a[60:63], a[88:91], v[4:7]\n"
"buffer_load_dwordx4 v210, s[32:35], s55 offen lds\n"
"v_mfma_f32_16x16x32_bf16 v[12:15], a[60:63], a[92:95], v[12:15]\n"
"s_add_u32 m0, s65, 3072\n"
"s_add_u32 s55, s66, 24960\n"
"ds_read_b128 a[36:39], v208 offset:2048\n"
"v_mfma_f32_16x16x32_bf16 v[20:23], a[60:63], a[96:99], v[20:23]\n"
"buffer_load_dwordx4 v217, s[32:35], s55 offen lds\n"
"v_mfma_f32_16x16x32_bf16 v[28:31], a[60:63], a[100:103], v[28:31]\n"
"s_add_u32 m0, s65, 4096\n"
"s_add_u32 s55, s66, 33152\n"
"ds_read_b128 a[40:43], v208 offset:4096\n"
"v_mfma_f32_16x16x32_bf16 v[36:39], a[60:63], a[104:107], v[36:39]\n"
"buffer_load_dwordx4 v210, s[32:35], s55 offen lds\n"
"v_mfma_f32_16x16x32_bf16 v[44:47], a[60:63], a[108:111], v[44:47]\n"
"s_add_u32 m0, s65, 5120\n"
"s_add_u32 s55, s66, 41344\n"
"ds_read_b128 a[44:47], v208 offset:6144\n"
"v_mfma_f32_16x16x32_bf16 v[48:51], a[64:67], a[88:91], v[48:51]\n"
"buffer_load_dwordx4 v217, s[32:35], s55 offen lds\n"
"v_mfma_f32_16x16x32_bf16 v[56:59], a[64:67], a[92:95], v[56:59]\n"
"s_add_u32 m0, s65, 6144\n"
"s_add_u32 s55, s66, 49536\n"
"ds_read_b128 a[48:51], v208 offset:8192\n"
"v_mfma_f32_16x16x32_bf16 v[64:67], a[64:67], a[96:99], v[64:67]\n"
"buffer_load_dwordx4 v210, s[32:35], s55 offen lds\n"
"v_mfma_f32_16x16x32_bf16 v[72:75], a[64:67], a[100:103], v[72:75]\n"
"s_add_u32 m0, s65, 7168\n"
"s_add_u32 s55, s66, 57728\n"
"ds_read_b64_tr_b16 a[4:5], v209 offset:8\n"
"v_mfma_f32_16x16x32_bf16 v[80:83], a[64:67], a[104:107], v[80:83]\n"
"buffer_load_dwordx4 v217, s[32:35], s55 offen lds\n"
"v_mfma_f32_16x16x32_bf16 v[88:91], a[64:67], a[108:111], v[88:91]\n"
"s_add_u32 m0, s65, 8192\n"
"s_add_u32 s55, s66, 65920\n"
"ds_read_b64_tr_b16 a[6:7], v209 offset:264\n"
"v_mfma_f32_16x16x32_bf16 v[52:55], a[68:71], a[88:91], v[52:55]\n"
"buffer_load_dwordx4 v210, s[32:35], s55 offen lds\n"
"v_mfma_f32_16x16x32_bf16 v[60:63], a[68:71], a[92:95], v[60:63]\n"
"s_add_u32 m0, s65, 9216\n"
"s_add_u32 s55, s66, 74112\n"
"ds_read_b64_tr_b16 a[8:9], v209 offset:512\n"
"v_mfma_f32_16x16x32_bf16 v[68:71], a[68:71], a[96:99], v[68:71]\n"
"buffer_load_dwordx4 v217, s[32:35], s55 offen lds\n"
"v_mfma_f32_16x16x32_bf16 v[76:79], a[68:71], a[100:103], v[76:79]\n"
"s_add_u32 m0, s65, 10240\n"
"s_add_u32 s55, s66, 82304\n"
"ds_read_b64_tr_b16 a[10:11], v209 offset:768\n"
"v_mfma_f32_16x16x32_bf16 v[84:87], a[68:71], a[104:107], v[84:87]\n"
"buffer_load_dwordx4 v210, s[32:35], s55 offen lds\n"
"v_mfma_f32_16x16x32_bf16 v[92:95], a[68:71], a[108:111], v[92:95]\n"
"s_add_u32 m0, s65, 11264\n"
"s_add_u32 s55, s66, 90496\n"
"ds_read_b64_tr_b16 a[12:13], v209 offset:520\n"
"v_mfma_f32_16x16x32_bf16 v[96:99], a[72:75], a[88:91], v[96:99]\n"
"buffer_load_dwordx4 v217, s[32:35], s55 offen lds\n"
"v_mfma_f32_16x16x32_bf16 v[104:107], a[72:75], a[92:95], v[104:107]\n"
"ds_read_b64_tr_b16 a[14:15], v209 offset:776\n"
"v_mfma_f32_16x16x32_bf16 v[112:115], a[72:75], a[96:99], v[112:115]\n"
"ds_read_b64_tr_b16 a[16:17], v209 offset:1024\n"
"v_mfma_f32_16x16x32_bf16 v[120:123], a[72:75], a[100:103], v[120:123]\n"
"ds_read_b64_tr_b16 a[18:19], v209 offset:1280\n"
"v_mfma_f32_16x16x32_bf16 v[128:131], a[72:75], a[104:107], v[128:131]\n"
"ds_read_b64_tr_b16 a[20:21], v209 offset:1032\n"
"v_mfma_f32_16x16x32_bf16 v[136:139], a[72:75], a[108:111], v[136:139]\n"
"ds_read_b128 a[52:55], v208 offset:10240\n"
"v_add_u32 v208, 512, v208\n"
"v_mfma_f32_16x16x32_bf16 v[100:103], a[76:79], a[88:91], v[100:103]\n"
"ds_read_b64_tr_b16 a[22:23], v209 offset:1288\n"
"v_mfma_f32_16x16x32_bf16 v[108:111], a[76:79], a[92:95], v[108:111]\n"
"ds_read_b64_tr_b16 a[24:25], v209 offset:1536\n"
"v_mfma_f32_16x16x32_bf16 v[116:119], a[76:79], a[96:99], v[116:119]\n"
"v_mfma_f32_16x16x32_bf16 v[124:127], a[76:79], a[100:103], v[124:127]\n"
"ds_read_b64_tr_b16 a[26:27], v209 offset:1792\n"
"v_mfma_f32_16x16x32_bf16 v[132:135], a[76:79], a[104:107], v[132:135]\n"
"v_mfma_f32_16x16x32_bf16 v[140:143], a[76:79], a[108:111], v[140:143]\n"
"ds_read_b64_tr_b16 a[28:29], v209 offset:1544\n"
"v_mfma_f32_16x16x32_bf16 v[144:147], a[80:83], a[88:91], v[144:147]\n"
"ds_read_b64_tr_b16 a[30:31], v209 offset:1800\n"
"v_mfma_f32_16x16x32_bf16 v[152:155], a[80:83], a[92:95], v[152:155]\n"
"v_add_u32 v209, 32768, v209\n"
"v_mfma_f32_16x16x32_bf16 v[160:163], a[80:83], a[96:99], v[160:163]\n"
"v_mfma_f32_16x16x32_bf16 v[168:171], a[80:83], a[100:103], v[168:171]\n"
"v_mfma_f32_16x16x32_bf16 v[176:179], a[80:83], a[104:107], v[176:179]\n"
"v_mfma_f32_16x16x32_bf16 v[184:187], a[80:83], a[108:111], v[184:187]\n"
"v_mfma_f32_16x16x32_bf16 v[148:151], a[84:87], a[88:91], v[148:151]\n"
"v_mfma_f32_16x16x32_bf16 v[156:159], a[84:87], a[92:95], v[156:159]\n"
"v_mfma_f32_16x16x32_bf16 v[164:167], a[84:87], a[96:99], v[164:167]\n"
"v_mfma_f32_16x16x32_bf16 v[172:175], a[84:87], a[100:103], v[172:175]\n"
"v_mfma_f32_16x16x32_bf16 v[180:183], a[84:87], a[104:107], v[180:183]\n"
"v_mfma_f32_16x16x32_bf16 v[188:191], a[84:87], a[108:111], v[188:191]\n"
"s_waitcnt lgkmcnt(0)\n"
"s_barrier\n"
"v_mfma_f32_16x16x32_bf16 v[0:3], a[0:3], a[32:35], v[0:3]\n"
"s_add_u32 m0, s67, 65536\n"
"s_add_u32 s55, s68, 2097152\n"
"ds_read_b64_tr_b16 a[56:57], v209\n"
"v_mfma_f32_16x16x32_bf16 v[8:11], a[0:3], a[36:39], v[8:11]\n"
"buffer_load_dwordx4 v211, s[36:39], s55 offen lds\n"
"v_mfma_f32_16x16x32_bf16 v[16:19], a[0:3], a[40:43], v[16:19]\n"
"s_add_u32 m0, s67, 66560\n"
"s_add_u32 s55, s68, 2097280\n"
"ds_read_b64_tr_b16 a[58:59], v209 offset:256\n"
"v_mfma_f32_16x16x32_bf16 v[24:27], a[0:3], a[44:47], v[24:27]\n"
"ds_read_b128 a[88:91], v208\n"
"v_mfma_f32_16x16x32_bf16 v[32:35], a[0:3], a[48:51], v[32:35]\n"
"buffer_load_dwordx4 v211, s[36:39], s55 offen lds\n"
"v_mfma_f32_16x16x32_bf16 v[40:43], a[0:3], a[52:55], v[40:43]\n"
"s_add_u32 m0, s67, 67584\n"
"s_add_u32 s55, s68, 2097408\n"
"ds_read_b128 a[92:95], v208 offset:2048\n"
"v_mfma_f32_16x16x32_bf16 v[4:7], a[4:7], a[32:35], v[4:7]\n"
"v_mfma_f32_16x16x32_bf16 v[12:15], a[4:7], a[36:39], v[12:15]\n"
"buffer_load_dwordx4 v211, s[36:39], s55 offen lds\n"
"v_mfma_f32_16x16x32_bf16 v[20:23], a[4:7], a[40:43], v[20:23]\n"
"s_add_u32 m0, s67, 68608\n"
"s_add_u32 s55, s68, 2097536\n"
"ds_read_b128 a[96:99], v208 offset:4096\n"
"v_mfma_f32_16x16x32_bf16 v[28:31], a[4:7], a[44:47], v[28:31]\n"
"ds_read_b128 a[100:103], v208 offset:6144\n"
"v_mfma_f32_16x16x32_bf16 v[36:39], a[4:7], a[48:51], v[36:39]\n"
"buffer_load_dwordx4 v211, s[36:39], s55 offen lds\n"
"v_mfma_f32_16x16x32_bf16 v[44:47], a[4:7], a[52:55], v[44:47]\n"
"s_add_u32 m0, s67, 69640\n"
"s_add_u32 s55, s68, 2162688\n"
"ds_read_b128 a[104:107], v208 offset:8192\n"
"v_mfma_f32_16x16x32_bf16 v[48:51], a[8:11], a[32:35], v[48:51]\n"
"ds_read_b128 a[108:111], v208 offset:10240\n"
"v_add_u32 v208, 0xfffe7e00, v208\n"
"v_mfma_f32_16x16x32_bf16 v[56:59], a[8:11], a[36:39], v[56:59]\n"
"buffer_load_dwordx4 v211, s[36:39], s55 offen lds\n"
"v_mfma_f32_16x16x32_bf16 v[64:67], a[8:11], a[40:43], v[64:67]\n"
"s_add_u32 m0, s67, 70664\n"
"s_add_u32 s55, s68, 2162816\n"
"ds_read_b64_tr_b16 a[60:61], v209 offset:8\n"
"v_mfma_f32_16x16x32_bf16 v[72:75], a[8:11], a[44:47], v[72:75]\n"
"ds_read_b64_tr_b16 a[62:63], v209 offset:264\n"
"v_mfma_f32_16x16x32_bf16 v[80:83], a[8:11], a[48:51], v[80:83]\n"
"buffer_load_dwordx4 v211, s[36:39], s55 offen lds\n"
"v_mfma_f32_16x16x32_bf16 v[88:91], a[8:11], a[52:55], v[88:91]\n"
"s_add_u32 m0, s67, 71688\n"
"s_add_u32 s55, s68, 2162944\n"
"ds_read_b64_tr_b16 a[64:65], v209 offset:512\n"
"v_mfma_f32_16x16x32_bf16 v[52:55], a[12:15], a[32:35], v[52:55]\n"
"buffer_load_dwordx4 v211, s[36:39], s55 offen lds\n"
"v_mfma_f32_16x16x32_bf16 v[60:63], a[12:15], a[36:39], v[60:63]\n"
"s_add_u32 m0, s67, 72712\n"
"s_add_u32 s55, s68, 2163072\n"
"ds_read_b64_tr_b16 a[66:67], v209 offset:768\n"
"v_mfma_f32_16x16x32_bf16 v[68:71], a[12:15], a[40:43], v[68:71]\n"
"v_mfma_f32_16x16x32_bf16 v[76:79], a[12:15], a[44:47], v[76:79]\n"
"buffer_load_dwordx4 v211, s[36:39], s55 offen lds\n"
"v_mfma_f32_16x16x32_bf16 v[84:87], a[12:15], a[48:51], v[84:87]\n"
"s_add_u32 m0, s67, 98304\n"
"s_add_u32 s55, s68, 2359296\n"
"ds_read_b64_tr_b16 a[68:69], v209 offset:520\n"
"v_mfma_f32_16x16x32_bf16 v[92:95], a[12:15], a[52:55], v[92:95]\n"
"ds_read_b64_tr_b16 a[70:71], v209 offset:776\n"
"v_mfma_f32_16x16x32_bf16 v[96:99], a[16:19], a[32:35], v[96:99]\n"
"buffer_load_dwordx4 v211, s[36:39], s55 offen lds\n"
"v_mfma_f32_16x16x32_bf16 v[104:107], a[16:19], a[36:39], v[104:107]\n"
"s_add_u32 m0, s67, 99328\n"
"s_add_u32 s55, s68, 2359424\n"
"ds_read_b64_tr_b16 a[72:73], v209 offset:1024\n"
"v_mfma_f32_16x16x32_bf16 v[112:115], a[16:19], a[40:43], v[112:115]\n"
"ds_read_b64_tr_b16 a[74:75], v209 offset:1280\n"
"v_mfma_f32_16x16x32_bf16 v[120:123], a[16:19], a[44:47], v[120:123]\n"
"buffer_load_dwordx4 v211, s[36:39], s55 offen lds\n"
"v_mfma_f32_16x16x32_bf16 v[128:131], a[16:19], a[48:51], v[128:131]\n"
"s_add_u32 m0, s67, 100352\n"
"s_add_u32 s55, s68, 2359552\n"
"ds_read_b64_tr_b16 a[76:77], v209 offset:1032\n"
"v_mfma_f32_16x16x32_bf16 v[136:139], a[16:19], a[52:55], v[136:139]\n"
"ds_read_b64_tr_b16 a[78:79], v209 offset:1288\n"
"v_mfma_f32_16x16x32_bf16 v[100:103], a[20:23], a[32:35], v[100:103]\n"
"buffer_load_dwordx4 v211, s[36:39], s55 offen lds\n"
"v_mfma_f32_16x16x32_bf16 v[108:111], a[20:23], a[36:39], v[108:111]\n"
"s_add_u32 m0, s67, 101376\n"
"s_add_u32 s55, s68, 2359680\n"
"ds_read_b64_tr_b16 a[80:81], v209 offset:1536\n"
"v_mfma_f32_16x16x32_bf16 v[116:119], a[20:23], a[40:43], v[116:119]\n"
"buffer_load_dwordx4 v211, s[36:39], s55 offen lds\n"
"v_mfma_f32_16x16x32_bf16 v[124:127], a[20:23], a[44:47], v[124:127]\n"
"s_add_u32 m0, s67, 102408\n"
"s_add_u32 s55, s68, 2424832\n"
"ds_read_b64_tr_b16 a[82:83], v209 offset:1792\n"
"v_mfma_f32_16x16x32_bf16 v[132:135], a[20:23], a[48:51], v[132:135]\n"
"v_mfma_f32_16x16x32_bf16 v[140:143], a[20:23], a[52:55], v[140:143]\n"
"buffer_load_dwordx4 v211, s[36:39], s55 offen lds\n"
"v_mfma_f32_16x16x32_bf16 v[144:147], a[24:27], a[32:35], v[144:147]\n"
"s_add_u32 m0, s67, 103432\n"
"s_add_u32 s55, s68, 2424960\n"
"ds_read_b64_tr_b16 a[84:85], v209 offset:1544\n"
"v_mfma_f32_16x16x32_bf16 v[152:155], a[24:27], a[36:39], v[152:155]\n"
"ds_read_b64_tr_b16 a[86:87], v209 offset:1800\n"
"v_add_u32 v209, 0xfffe0000, v209\n"
"v_mfma_f32_16x16x32_bf16 v[160:163], a[24:27], a[40:43], v[160:163]\n"
"buffer_load_dwordx4 v211, s[36:39], s55 offen lds\n"
"v_mfma_f32_16x16x32_bf16 v[168:171], a[24:27], a[44:47], v[168:171]\n"
"s_add_u32 m0, s67, 104456\n"
"s_add_u32 s55, s68, 2425088\n"
"v_mfma_f32_16x16x32_bf16 v[176:179], a[24:27], a[48:51], v[176:179]\n"
"v_mfma_f32_16x16x32_bf16 v[184:187], a[24:27], a[52:55], v[184:187]\n"
"buffer_load_dwordx4 v211, s[36:39], s55 offen lds\n"
"v_mfma_f32_16x16x32_bf16 v[148:151], a[28:31], a[32:35], v[148:151]\n"
"s_add_u32 m0, s67, 105480\n"
"s_add_u32 s55, s68, 2425216\n"
"v_mfma_f32_16x16x32_bf16 v[156:159], a[28:31], a[36:39], v[156:159]\n"
"v_mfma_f32_16x16x32_bf16 v[164:167], a[28:31], a[40:43], v[164:167]\n"
"buffer_load_dwordx4 v211, s[36:39], s55 offen lds\n"
"v_mfma_f32_16x16x32_bf16 v[172:175], a[28:31], a[44:47], v[172:175]\n"
"v_mfma_f32_16x16x32_bf16 v[180:183], a[28:31], a[48:51], v[180:183]\n"
"v_mfma_f32_16x16x32_bf16 v[188:191], a[28:31], a[52:55], v[188:191]\n"
"s_waitcnt vmcnt(16) lgkmcnt(0)\n"
"s_barrier\n"
"v_mfma_f32_16x16x32_bf16 v[0:3], a[56:59], a[88:91], v[0:3]\n"
"s_mov_b32 s65, 81952\n"
"s_cmp_eq_u32 s47, 0\n"
"s_cselect_b32 s65, s65, 114720\n"
"v_mfma_f32_16x16x32_bf16 v[8:11], a[56:59], a[92:95], v[8:11]\n"
"s_add_u32 m0, s65, 0\n"
"s_add_u32 s55, s66, 512\n"
"ds_read_b64_tr_b16 a[0:1], v209\n"
"v_mfma_f32_16x16x32_bf16 v[16:19], a[56:59], a[96:99], v[16:19]\n"
"buffer_load_dwordx4 v210, s[32:35], s55 offen lds\n"
"v_mfma_f32_16x16x32_bf16 v[24:27], a[56:59], a[100:103], v[24:27]\n"
"s_add_u32 m0, s65, 1024\n"
"s_add_u32 s55, s66, 8704\n"
"ds_read_b64_tr_b16 a[2:3], v209 offset:256\n"
"v_mfma_f32_16x16x32_bf16 v[32:35], a[56:59], a[104:107], v[32:35]\n"
"buffer_load_dwordx4 v217, s[32:35], s55 offen lds\n"
"v_mfma_f32_16x16x32_bf16 v[40:43], a[56:59], a[108:111], v[40:43]\n"
"s_add_u32 m0, s65, 2048\n"
"s_add_u32 s55, s66, 16896\n"
"ds_read_b128 a[32:35], v208\n"
"v_mfma_f32_16x16x32_bf16 v[4:7], a[60:63], a[88:91], v[4:7]\n"
"buffer_load_dwordx4 v210, s[32:35], s55 offen lds\n"
"v_mfma_f32_16x16x32_bf16 v[12:15], a[60:63], a[92:95], v[12:15]\n"
"s_add_u32 m0, s65, 3072\n"
"s_add_u32 s55, s66, 25088\n"
"ds_read_b128 a[36:39], v208 offset:2048\n"
"v_mfma_f32_16x16x32_bf16 v[20:23], a[60:63], a[96:99], v[20:23]\n"
"buffer_load_dwordx4 v217, s[32:35], s55 offen lds\n"
"v_mfma_f32_16x16x32_bf16 v[28:31], a[60:63], a[100:103], v[28:31]\n"
"s_add_u32 m0, s65, 4096\n"
"s_add_u32 s55, s66, 33280\n"
"ds_read_b128 a[40:43], v208 offset:4096\n"
"v_mfma_f32_16x16x32_bf16 v[36:39], a[60:63], a[104:107], v[36:39]\n"
"buffer_load_dwordx4 v210, s[32:35], s55 offen lds\n"
"v_mfma_f32_16x16x32_bf16 v[44:47], a[60:63], a[108:111], v[44:47]\n"
"s_add_u32 m0, s65, 5120\n"
"s_add_u32 s55, s66, 41472\n"
"ds_read_b128 a[44:47], v208 offset:6144\n"
"v_mfma_f32_16x16x32_bf16 v[48:51], a[64:67], a[88:91], v[48:51]\n"
"buffer_load_dwordx4 v217, s[32:35], s55 offen lds\n"
"v_mfma_f32_16x16x32_bf16 v[56:59], a[64:67], a[92:95], v[56:59]\n"
"s_add_u32 m0, s65, 6144\n"
"s_add_u32 s55, s66, 49664\n"
"ds_read_b128 a[48:51], v208 offset:8192\n"
"v_mfma_f32_16x16x32_bf16 v[64:67], a[64:67], a[96:99], v[64:67]\n"
"buffer_load_dwordx4 v210, s[32:35], s55 offen lds\n"
"v_mfma_f32_16x16x32_bf16 v[72:75], a[64:67], a[100:103], v[72:75]\n"
"s_add_u32 m0, s65, 7168\n"
"s_add_u32 s55, s66, 57856\n"
"ds_read_b64_tr_b16 a[4:5], v209 offset:8\n"
"v_mfma_f32_16x16x32_bf16 v[80:83], a[64:67], a[104:107], v[80:83]\n"
"buffer_load_dwordx4 v217, s[32:35], s55 offen lds\n"
"v_mfma_f32_16x16x32_bf16 v[88:91], a[64:67], a[108:111], v[88:91]\n"
"s_add_u32 m0, s65, 8192\n"
"s_add_u32 s55, s66, 66048\n"
"ds_read_b64_tr_b16 a[6:7], v209 offset:264\n"
"v_mfma_f32_16x16x32_bf16 v[52:55], a[68:71], a[88:91], v[52:55]\n"
"buffer_load_dwordx4 v210, s[32:35], s55 offen lds\n"
"v_mfma_f32_16x16x32_bf16 v[60:63], a[68:71], a[92:95], v[60:63]\n"
"s_add_u32 m0, s65, 9216\n"
"s_add_u32 s55, s66, 74240\n"
"ds_read_b64_tr_b16 a[8:9], v209 offset:512\n"
"v_mfma_f32_16x16x32_bf16 v[68:71], a[68:71], a[96:99], v[68:71]\n"
"buffer_load_dwordx4 v217, s[32:35], s55 offen lds\n"
"v_mfma_f32_16x16x32_bf16 v[76:79], a[68:71], a[100:103], v[76:79]\n"
"s_add_u32 m0, s65, 10240\n"
"s_add_u32 s55, s66, 82432\n"
"ds_read_b64_tr_b16 a[10:11], v209 offset:768\n"
"v_mfma_f32_16x16x32_bf16 v[84:87], a[68:71], a[104:107], v[84:87]\n"
"buffer_load_dwordx4 v210, s[32:35], s55 offen lds\n"
"v_mfma_f32_16x16x32_bf16 v[92:95], a[68:71], a[108:111], v[92:95]\n"
"s_add_u32 m0, s65, 11264\n"
"s_add_u32 s55, s66, 90624\n"
"ds_read_b64_tr_b16 a[12:13], v209 offset:520\n"
"v_mfma_f32_16x16x32_bf16 v[96:99], a[72:75], a[88:91], v[96:99]\n"
"buffer_load_dwordx4 v217, s[32:35], s55 offen lds\n"
"v_mfma_f32_16x16x32_bf16 v[104:107], a[72:75], a[92:95], v[104:107]\n"
"ds_read_b64_tr_b16 a[14:15], v209 offset:776\n"
"v_mfma_f32_16x16x32_bf16 v[112:115], a[72:75], a[96:99], v[112:115]\n"
"ds_read_b64_tr_b16 a[16:17], v209 offset:1024\n"
"v_mfma_f32_16x16x32_bf16 v[120:123], a[72:75], a[100:103], v[120:123]\n"
"ds_read_b64_tr_b16 a[18:19], v209 offset:1280\n"
"v_mfma_f32_16x16x32_bf16 v[128:131], a[72:75], a[104:107], v[128:131]\n"
"ds_read_b64_tr_b16 a[20:21], v209 offset:1032\n"
"v_mfma_f32_16x16x32_bf16 v[136:139], a[72:75], a[108:111], v[136:139]\n"
"ds_read_b128 a[52:55], v208 offset:10240\n"
"v_add_u32 v208, 512, v208\n"
"v_mfma_f32_16x16x32_bf16 v[100:103], a[76:79], a[88:91], v[100:103]\n"
"ds_read_b64_tr_b16 a[22:23], v209 offset:1288\n"
"v_mfma_f32_16x16x32_bf16 v[108:111], a[76:79], a[92:95], v[108:111]\n"
"ds_read_b64_tr_b16 a[24:25], v209 offset:1536\n"
"v_mfma_f32_16x16x32_bf16 v[116:119], a[76:79], a[96:99], v[116:119]\n"
"v_mfma_f32_16x16x32_bf16 v[124:127], a[76:79], a[100:103], v[124:127]\n"
"ds_read_b64_tr_b16 a[26:27], v209 offset:1792\n"
"v_mfma_f32_16x16x32_bf16 v[132:135], a[76:79], a[104:107], v[132:135]\n"
"v_mfma_f32_16x16x32_bf16 v[140:143], a[76:79], a[108:111], v[140:143]\n"
"ds_read_b64_tr_b16 a[28:29], v209 offset:1544\n"
"v_mfma_f32_16x16x32_bf16 v[144:147], a[80:83], a[88:91], v[144:147]\n"
"ds_read_b64_tr_b16 a[30:31], v209 offset:1800\n"
"v_mfma_f32_16x16x32_bf16 v[152:155], a[80:83], a[92:95], v[152:155]\n"
"v_add_u32 v209, 32768, v209\n"
"v_mfma_f32_16x16x32_bf16 v[160:163], a[80:83], a[96:99], v[160:163]\n"
"v_mfma_f32_16x16x32_bf16 v[168:171], a[80:83], a[100:103], v[168:171]\n"
"v_mfma_f32_16x16x32_bf16 v[176:179], a[80:83], a[104:107], v[176:179]\n"
"v_mfma_f32_16x16x32_bf16 v[184:187], a[80:83], a[108:111], v[184:187]\n"
"v_mfma_f32_16x16x32_bf16 v[148:151], a[84:87], a[88:91], v[148:151]\n"
"v_mfma_f32_16x16x32_bf16 v[156:159], a[84:87], a[92:95], v[156:159]\n"
"v_mfma_f32_16x16x32_bf16 v[164:167], a[84:87], a[96:99], v[164:167]\n"
"v_mfma_f32_16x16x32_bf16 v[172:175], a[84:87], a[100:103], v[172:175]\n"
"v_mfma_f32_16x16x32_bf16 v[180:183], a[84:87], a[104:107], v[180:183]\n"
"v_mfma_f32_16x16x32_bf16 v[188:191], a[84:87], a[108:111], v[188:191]\n"
"s_waitcnt lgkmcnt(0)\n"
"s_barrier\n"
"v_mfma_f32_16x16x32_bf16 v[0:3], a[0:3], a[32:35], v[0:3]\n"
"s_add_u32 m0, s67, 131072\n"
"s_add_u32 s55, s68, 2621440\n"
"ds_read_b64_tr_b16 a[56:57], v209\n"
"v_mfma_f32_16x16x32_bf16 v[8:11], a[0:3], a[36:39], v[8:11]\n"
"buffer_load_dwordx4 v211, s[36:39], s55 offen lds\n"
"v_mfma_f32_16x16x32_bf16 v[16:19], a[0:3], a[40:43], v[16:19]\n"
"s_add_u32 m0, s67, 132096\n"
"s_add_u32 s55, s68, 2621568\n"
"ds_read_b64_tr_b16 a[58:59], v209 offset:256\n"
"v_mfma_f32_16x16x32_bf16 v[24:27], a[0:3], a[44:47], v[24:27]\n"
"ds_read_b128 a[88:91], v208\n"
"v_mfma_f32_16x16x32_bf16 v[32:35], a[0:3], a[48:51], v[32:35]\n"
"buffer_load_dwordx4 v211, s[36:39], s55 offen lds\n"
"v_mfma_f32_16x16x32_bf16 v[40:43], a[0:3], a[52:55], v[40:43]\n"
"s_add_u32 m0, s67, 133120\n"
"s_add_u32 s55, s68, 2621696\n"
"ds_read_b128 a[92:95], v208 offset:2048\n"
"v_mfma_f32_16x16x32_bf16 v[4:7], a[4:7], a[32:35], v[4:7]\n"
"v_mfma_f32_16x16x32_bf16 v[12:15], a[4:7], a[36:39], v[12:15]\n"
"buffer_load_dwordx4 v211, s[36:39], s55 offen lds\n"
"v_mfma_f32_16x16x32_bf16 v[20:23], a[4:7], a[40:43], v[20:23]\n"
"s_add_u32 m0, s67, 134144\n"
"s_add_u32 s55, s68, 2621824\n"
"ds_read_b128 a[96:99], v208 offset:4096\n"
"v_mfma_f32_16x16x32_bf16 v[28:31], a[4:7], a[44:47], v[28:31]\n"
"ds_read_b128 a[100:103], v208 offset:6144\n"
"v_mfma_f32_16x16x32_bf16 v[36:39], a[4:7], a[48:51], v[36:39]\n"
"buffer_load_dwordx4 v211, s[36:39], s55 offen lds\n"
"v_mfma_f32_16x16x32_bf16 v[44:47], a[4:7], a[52:55], v[44:47]\n"
"s_add_u32 m0, s67, 135176\n"
"s_add_u32 s55, s68, 2686976\n"
"ds_read_b128 a[104:107], v208 offset:8192\n"
"v_mfma_f32_16x16x32_bf16 v[48:51], a[8:11], a[32:35], v[48:51]\n"
"ds_read_b128 a[108:111], v208 offset:10240\n"
"v_add_u32 v208, 65024, v208\n"
"v_mfma_f32_16x16x32_bf16 v[56:59], a[8:11], a[36:39], v[56:59]\n"
"buffer_load_dwordx4 v211, s[36:39], s55 offen lds\n"
"v_mfma_f32_16x16x32_bf16 v[64:67], a[8:11], a[40:43], v[64:67]\n"
"s_add_u32 m0, s67, 136200\n"
"s_add_u32 s55, s68, 2687104\n"
"ds_read_b64_tr_b16 a[60:61], v209 offset:8\n"
"v_mfma_f32_16x16x32_bf16 v[72:75], a[8:11], a[44:47], v[72:75]\n"
"ds_read_b64_tr_b16 a[62:63], v209 offset:264\n"
"v_mfma_f32_16x16x32_bf16 v[80:83], a[8:11], a[48:51], v[80:83]\n"
"buffer_load_dwordx4 v211, s[36:39], s55 offen lds\n"
"v_mfma_f32_16x16x32_bf16 v[88:91], a[8:11], a[52:55], v[88:91]\n"
"s_add_u32 m0, s67, 137224\n"
"s_add_u32 s55, s68, 2687232\n"
"ds_read_b64_tr_b16 a[64:65], v209 offset:512\n"
"v_mfma_f32_16x16x32_bf16 v[52:55], a[12:15], a[32:35], v[52:55]\n"
"buffer_load_dwordx4 v211, s[36:39], s55 offen lds\n"
"v_mfma_f32_16x16x32_bf16 v[60:63], a[12:15], a[36:39], v[60:63]\n"
"s_add_u32 m0, s67, 138248\n"
"s_add_u32 s55, s68, 2687360\n"
"ds_read_b64_tr_b16 a[66:67], v209 offset:768\n"
"v_mfma_f32_16x16x32_bf16 v[68:71], a[12:15], a[40:43], v[68:71]\n"
"v_mfma_f32_16x16x32_bf16 v[76:79], a[12:15], a[44:47], v[76:79]\n"
"buffer_load_dwordx4 v211, s[36:39], s55 offen lds\n"
"v_mfma_f32_16x16x32_bf16 v[84:87], a[12:15], a[48:51], v[84:87]\n"
"s_add_u32 m0, s67, 0\n"
"s_add_u32 s55, s68, 2883584\n"
"ds_read_b64_tr_b16 a[68:69], v209 offset:520\n"
"v_mfma_f32_16x16x32_bf16 v[92:95], a[12:15], a[52:55], v[92:95]\n"
"ds_read_b64_tr_b16 a[70:71], v209 offset:776\n"
"v_mfma_f32_16x16x32_bf16 v[96:99], a[16:19], a[32:35], v[96:99]\n"
"buffer_load_dwordx4 v211, s[36:39], s55 offen lds\n"
"v_mfma_f32_16x16x32_bf16 v[104:107], a[16:19], a[36:39], v[104:107]\n"
"s_add_u32 m0, s67, 1024\n"
"s_add_u32 s55, s68, 2883712\n"
"ds_read_b64_tr_b16 a[72:73], v209 offset:1024\n"
"v_mfma_f32_16x16x32_bf16 v[112:115], a[16:19], a[40:43], v[112:115]\n"
"ds_read_b64_tr_b16 a[74:75], v209 offset:1280\n"
"v_mfma_f32_16x16x32_bf16 v[120:123], a[16:19], a[44:47], v[120:123]\n"
"buffer_load_dwordx4 v211, s[36:39], s55 offen lds\n"
"v_mfma_f32_16x16x32_bf16 v[128:131], a[16:19], a[48:51], v[128:131]\n"
"s_add_u32 m0, s67, 2048\n"
"s_add_u32 s55, s68, 2883840\n"
"ds_read_b64_tr_b16 a[76:77], v209 offset:1032\n"
"v_mfma_f32_16x16x32_bf16 v[136:139], a[16:19], a[52:55], v[136:139]\n"
"ds_read_b64_tr_b16 a[78:79], v209 offset:1288\n"
"v_mfma_f32_16x16x32_bf16 v[100:103], a[20:23], a[32:35], v[100:103]\n"
"buffer_load_dwordx4 v211, s[36:39], s55 offen lds\n"
"v_mfma_f32_16x16x32_bf16 v[108:111], a[20:23], a[36:39], v[108:111]\n"
"s_add_u32 m0, s67, 3072\n"
"s_add_u32 s55, s68, 2883968\n"
"ds_read_b64_tr_b16 a[80:81], v209 offset:1536\n"
"v_mfma_f32_16x16x32_bf16 v[116:119], a[20:23], a[40:43], v[116:119]\n"
"buffer_load_dwordx4 v211, s[36:39], s55 offen lds\n"
"v_mfma_f32_16x16x32_bf16 v[124:127], a[20:23], a[44:47], v[124:127]\n"
"s_add_u32 m0, s67, 4104\n"
"s_add_u32 s55, s68, 2949120\n"
"ds_read_b64_tr_b16 a[82:83], v209 offset:1792\n"
"v_mfma_f32_16x16x32_bf16 v[132:135], a[20:23], a[48:51], v[132:135]\n"
"v_mfma_f32_16x16x32_bf16 v[140:143], a[20:23], a[52:55], v[140:143]\n"
"buffer_load_dwordx4 v211, s[36:39], s55 offen lds\n"
"v_mfma_f32_16x16x32_bf16 v[144:147], a[24:27], a[32:35], v[144:147]\n"
"s_add_u32 m0, s67, 5128\n"
"s_add_u32 s55, s68, 2949248\n"
"ds_read_b64_tr_b16 a[84:85], v209 offset:1544\n"
"v_mfma_f32_16x16x32_bf16 v[152:155], a[24:27], a[36:39], v[152:155]\n"
"ds_read_b64_tr_b16 a[86:87], v209 offset:1800\n"
"v_add_u32 v209, 32768, v209\n"
"v_mfma_f32_16x16x32_bf16 v[160:163], a[24:27], a[40:43], v[160:163]\n"
"buffer_load_dwordx4 v211, s[36:39], s55 offen lds\n"
"v_mfma_f32_16x16x32_bf16 v[168:171], a[24:27], a[44:47], v[168:171]\n"
"s_add_u32 m0, s67, 6152\n"
"s_add_u32 s55, s68, 2949376\n"
"v_mfma_f32_16x16x32_bf16 v[176:179], a[24:27], a[48:51], v[176:179]\n"
"v_mfma_f32_16x16x32_bf16 v[184:187], a[24:27], a[52:55], v[184:187]\n"
"buffer_load_dwordx4 v211, s[36:39], s55 offen lds\n"
"v_mfma_f32_16x16x32_bf16 v[148:151], a[28:31], a[32:35], v[148:151]\n"
"s_add_u32 m0, s67, 7176\n"
"s_add_u32 s55, s68, 2949504\n"
"v_mfma_f32_16x16x32_bf16 v[156:159], a[28:31], a[36:39], v[156:159]\n"
"v_mfma_f32_16x16x32_bf16 v[164:167], a[28:31], a[40:43], v[164:167]\n"
"buffer_load_dwordx4 v211, s[36:39], s55 offen lds\n"
"v_mfma_f32_16x16x32_bf16 v[172:175], a[28:31], a[44:47], v[172:175]\n"
"v_mfma_f32_16x16x32_bf16 v[180:183], a[28:31], a[48:51], v[180:183]\n"
"v_mfma_f32_16x16x32_bf16 v[188:191], a[28:31], a[52:55], v[188:191]\n"
"s_waitcnt vmcnt(16) lgkmcnt(0)\n"
"s_barrier\n"
"v_mfma_f32_16x16x32_bf16 v[0:3], a[56:59], a[88:91], v[0:3]\n"
"s_mov_b32 s65, 147488\n"
"s_cmp_eq_u32 s47, 0\n"
"s_cselect_b32 s65, s65, 16416\n"
"v_mfma_f32_16x16x32_bf16 v[8:11], a[56:59], a[92:95], v[8:11]\n"
"s_add_u32 m0, s65, 0\n"
"s_add_u32 s55, s66, 640\n"
"ds_read_b64_tr_b16 a[0:1], v209\n"
"v_mfma_f32_16x16x32_bf16 v[16:19], a[56:59], a[96:99], v[16:19]\n"
"buffer_load_dwordx4 v210, s[32:35], s55 offen lds\n"
"v_mfma_f32_16x16x32_bf16 v[24:27], a[56:59], a[100:103], v[24:27]\n"
"s_add_u32 m0, s65, 1024\n"
"s_add_u32 s55, s66, 8832\n"
"ds_read_b64_tr_b16 a[2:3], v209 offset:256\n"
"v_mfma_f32_16x16x32_bf16 v[32:35], a[56:59], a[104:107], v[32:35]\n"
"buffer_load_dwordx4 v217, s[32:35], s55 offen lds\n"
"v_mfma_f32_16x16x32_bf16 v[40:43], a[56:59], a[108:111], v[40:43]\n"
"s_add_u32 m0, s65, 2048\n"
"s_add_u32 s55, s66, 17024\n"
"ds_read_b128 a[32:35], v208\n"
"v_mfma_f32_16x16x32_bf16 v[4:7], a[60:63], a[88:91], v[4:7]\n"
"buffer_load_dwordx4 v210, s[32:35], s55 offen lds\n"
"v_mfma_f32_16x16x32_bf16 v[12:15], a[60:63], a[92:95], v[12:15]\n"
"s_add_u32 m0, s65, 3072\n"
"s_add_u32 s55, s66, 25216\n"
"ds_read_b128 a[36:39], v208 offset:2048\n"
"v_mfma_f32_16x16x32_bf16 v[20:23], a[60:63], a[96:99], v[20:23]\n"
"buffer_load_dwordx4 v217, s[32:35], s55 offen lds\n"
"v_mfma_f32_16x16x32_bf16 v[28:31], a[60:63], a[100:103], v[28:31]\n"
"s_add_u32 m0, s65, 4096\n"
"s_add_u32 s55, s66, 33408\n"
"ds_read_b128 a[40:43], v208 offset:4096\n"
"v_mfma_f32_16x16x32_bf16 v[36:39], a[60:63], a[104:107], v[36:39]\n"
"buffer_load_dwordx4 v210, s[32:35], s55 offen lds\n"
"v_mfma_f32_16x16x32_bf16 v[44:47], a[60:63], a[108:111], v[44:47]\n"
"s_add_u32 m0, s65, 5120\n"
"s_add_u32 s55, s66, 41600\n"
"ds_read_b128 a[44:47], v208 offset:6144\n"
"v_mfma_f32_16x16x32_bf16 v[48:51], a[64:67], a[88:91], v[48:51]\n"
"buffer_load_dwordx4 v217, s[32:35], s55 offen lds\n"
"v_mfma_f32_16x16x32_bf16 v[56:59], a[64:67], a[92:95], v[56:59]\n"
"s_add_u32 m0, s65, 6144\n"
"s_add_u32 s55, s66, 49792\n"
"ds_read_b128 a[48:51], v208 offset:8192\n"
"v_mfma_f32_16x16x32_bf16 v[64:67], a[64:67], a[96:99], v[64:67]\n"
"buffer_load_dwordx4 v210, s[32:35], s55 offen lds\n"
"v_mfma_f32_16x16x32_bf16 v[72:75], a[64:67], a[100:103], v[72:75]\n"
"s_add_u32 m0, s65, 7168\n"
"s_add_u32 s55, s66, 57984\n"
"ds_read_b64_tr_b16 a[4:5], v209 offset:8\n"
"v_mfma_f32_16x16x32_bf16 v[80:83], a[64:67], a[104:107], v[80:83]\n"
"buffer_load_dwordx4 v217, s[32:35], s55 offen lds\n"
"v_mfma_f32_16x16x32_bf16 v[88:91], a[64:67], a[108:111], v[88:91]\n"
"s_add_u32 m0, s65, 8192\n"
"s_add_u32 s55, s66, 66176\n"
"ds_read_b64_tr_b16 a[6:7], v209 offset:264\n"
"v_mfma_f32_16x16x32_bf16 v[52:55], a[68:71], a[88:91], v[52:55]\n"
"buffer_load_dwordx4 v210, s[32:35], s55 offen lds\n"
"v_mfma_f32_16x16x32_bf16 v[60:63], a[68:71], a[92:95], v[60:63]\n"
"s_add_u32 m0, s65, 9216\n"
"s_add_u32 s55, s66, 74368\n"
"ds_read_b64_tr_b16 a[8:9], v209 offset:512\n"
"v_mfma_f32_16x16x32_bf16 v[68:71], a[68:71], a[96:99], v[68:71]\n"
"buffer_load_dwordx4 v217, s[32:35], s55 offen lds\n"
"v_mfma_f32_16x16x32_bf16 v[76:79], a[68:71], a[100:103], v[76:79]\n"
"s_add_u32 m0, s65, 10240\n"
"s_add_u32 s55, s66, 82560\n"
"ds_read_b64_tr_b16 a[10:11], v209 offset:768\n"
"v_mfma_f32_16x16x32_bf16 v[84:87], a[68:71], a[104:107], v[84:87]\n"
"buffer_load_dwordx4 v210, s[32:35], s55 offen lds\n"
"v_mfma_f32_16x16x32_bf16 v[92:95], a[68:71], a[108:111], v[92:95]\n"
"s_add_u32 m0, s65, 11264\n"
"s_add_u32 s55, s66, 90752\n"
"ds_read_b64_tr_b16 a[12:13], v209 offset:520\n"
"v_mfma_f32_16x16x32_bf16 v[96:99], a[72:75], a[88:91], v[96:99]\n"
"buffer_load_dwordx4 v217, s[32:35], s55 offen lds\n"
"v_mfma_f32_16x16x32_bf16 v[104:107], a[72:75], a[92:95], v[104:107]\n"
"ds_read_b64_tr_b16 a[14:15], v209 offset:776\n"
"v_mfma_f32_16x16x32_bf16 v[112:115], a[72:75], a[96:99], v[112:115]\n"
"ds_read_b64_tr_b16 a[16:17], v209 offset:1024\n"
"v_mfma_f32_16x16x32_bf16 v[120:123], a[72:75], a[100:103], v[120:123]\n"
"ds_read_b64_tr_b16 a[18:19], v209 offset:1280\n"
"v_mfma_f32_16x16x32_bf16 v[128:131], a[72:75], a[104:107], v[128:131]\n"
"ds_read_b64_tr_b16 a[20:21], v209 offset:1032\n"
"v_mfma_f32_16x16x32_bf16 v[136:139], a[72:75], a[108:111], v[136:139]\n"
"ds_read_b128 a[52:55], v208 offset:10240\n"
"v_add_u32 v208, 512, v208\n"
"v_mfma_f32_16x16x32_bf16 v[100:103], a[76:79], a[88:91], v[100:103]\n"
"ds_read_b64_tr_b16 a[22:23], v209 offset:1288\n"
"v_mfma_f32_16x16x32_bf16 v[108:111], a[76:79], a[92:95], v[108:111]\n"
"ds_read_b64_tr_b16 a[24:25], v209 offset:1536\n"
"v_mfma_f32_16x16x32_bf16 v[116:119], a[76:79], a[96:99], v[116:119]\n"
"v_mfma_f32_16x16x32_bf16 v[124:127], a[76:79], a[100:103], v[124:127]\n"
"ds_read_b64_tr_b16 a[26:27], v209 offset:1792\n"
"v_mfma_f32_16x16x32_bf16 v[132:135], a[76:79], a[104:107], v[132:135]\n"
"v_mfma_f32_16x16x32_bf16 v[140:143], a[76:79], a[108:111], v[140:143]\n"
"ds_read_b64_tr_b16 a[28:29], v209 offset:1544\n"
"v_mfma_f32_16x16x32_bf16 v[144:147], a[80:83], a[88:91], v[144:147]\n"
"ds_read_b64_tr_b16 a[30:31], v209 offset:1800\n"
"v_mfma_f32_16x16x32_bf16 v[152:155], a[80:83], a[92:95], v[152:155]\n"
"v_add_u32 v209, 32768, v209\n"
"v_mfma_f32_16x16x32_bf16 v[160:163], a[80:83], a[96:99], v[160:163]\n"
"v_mfma_f32_16x16x32_bf16 v[168:171], a[80:83], a[100:103], v[168:171]\n"
"v_mfma_f32_16x16x32_bf16 v[176:179], a[80:83], a[104:107], v[176:179]\n"
"v_mfma_f32_16x16x32_bf16 v[184:187], a[80:83], a[108:111], v[184:187]\n"
"v_mfma_f32_16x16x32_bf16 v[148:151], a[84:87], a[88:91], v[148:151]\n"
"v_mfma_f32_16x16x32_bf16 v[156:159], a[84:87], a[92:95], v[156:159]\n"
"v_mfma_f32_16x16x32_bf16 v[164:167], a[84:87], a[96:99], v[164:167]\n"
"v_mfma_f32_16x16x32_bf16 v[172:175], a[84:87], a[100:103], v[172:175]\n"
"v_mfma_f32_16x16x32_bf16 v[180:183], a[84:87], a[104:107], v[180:183]\n"
"v_mfma_f32_16x16x32_bf16 v[188:191], a[84:87], a[108:111], v[188:191]\n"
"s_waitcnt lgkmcnt(0)\n"
"s_barrier\n"
"v_mfma_f32_16x16x32_bf16 v[0:3], a[0:3], a[32:35], v[0:3]\n"
"s_add_u32 m0, s67, 32768\n"
"s_add_u32 s55, s68, 3145728\n"
"s_mov_b32 s61, 65024\n"
"v_mfma_f32_16x16x32_bf16 v[8:11], a[0:3], a[36:39], v[8:11]\n"
"buffer_load_dwordx4 v211, s[36:39], s55 offen lds\n"
"v_mfma_f32_16x16x32_bf16 v[16:19], a[0:3], a[40:43], v[16:19]\n"
"s_add_u32 m0, s67, 33792\n"
"s_add_u32 s55, s68, 3145856\n"
"ds_read_b64_tr_b16 a[56:57], v209\n"
"v_mfma_f32_16x16x32_bf16 v[24:27], a[0:3], a[44:47], v[24:27]\n"
"ds_read_b64_tr_b16 a[58:59], v209 offset:256\n"
"v_mfma_f32_16x16x32_bf16 v[32:35], a[0:3], a[48:51], v[32:35]\n"
"buffer_load_dwordx4 v211, s[36:39], s55 offen lds\n"
"v_mfma_f32_16x16x32_bf16 v[40:43], a[0:3], a[52:55], v[40:43]\n"
"s_add_u32 m0, s67, 34816\n"
"s_add_u32 s55, s68, 3145984\n"
"ds_read_b128 a[88:91], v208\n"
"v_mfma_f32_16x16x32_bf16 v[4:7], a[4:7], a[32:35], v[4:7]\n"
"ds_read_b128 a[92:95], v208 offset:2048\n"
"v_mfma_f32_16x16x32_bf16 v[12:15], a[4:7], a[36:39], v[12:15]\n"
"buffer_load_dwordx4 v211, s[36:39], s55 offen lds\n"
"v_mfma_f32_16x16x32_bf16 v[20:23], a[4:7], a[40:43], v[20:23]\n"
"s_add_u32 m0, s67, 35840\n"
"s_add_u32 s55, s68, 3146112\n"
"ds_read_b128 a[96:99], v208 offset:4096\n"
"v_mfma_f32_16x16x32_bf16 v[28:31], a[4:7], a[44:47], v[28:31]\n"
"ds_read_b128 a[100:103], v208 offset:6144\n"
"v_mfma_f32_16x16x32_bf16 v[36:39], a[4:7], a[48:51], v[36:39]\n"
"buffer_load_dwordx4 v211, s[36:39], s55 offen lds\n"
"v_mfma_f32_16x16x32_bf16 v[44:47], a[4:7], a[52:55], v[44:47]\n"
"s_add_u32 m0, s67, 36872\n"
"s_add_u32 s55, s68, 3211264\n"
"ds_read_b128 a[104:107], v208 offset:8192\n"
"v_mfma_f32_16x16x32_bf16 v[48:51], a[8:11], a[32:35], v[48:51]\n"
"ds_read_b128 a[108:111], v208 offset:10240\n"
"v_mfma_f32_16x16x32_bf16 v[56:59], a[8:11], a[36:39], v[56:59]\n"
"buffer_load_dwordx4 v211, s[36:39], s55 offen lds\n"
"v_mfma_f32_16x16x32_bf16 v[64:67], a[8:11], a[40:43], v[64:67]\n"
"s_add_u32 m0, s67, 37896\n"
"s_add_u32 s55, s68, 3211392\n"
"ds_read_b64_tr_b16 a[60:61], v209 offset:8\n"
"v_mfma_f32_16x16x32_bf16 v[72:75], a[8:11], a[44:47], v[72:75]\n"
"ds_read_b64_tr_b16 a[62:63], v209 offset:264\n"
"v_mfma_f32_16x16x32_bf16 v[80:83], a[8:11], a[48:51], v[80:83]\n"
"buffer_load_dwordx4 v211, s[36:39], s55 offen lds\n"
"v_mfma_f32_16x16x32_bf16 v[88:91], a[8:11], a[52:55], v[88:91]\n"
"s_add_u32 m0, s67, 38920\n"
"s_add_u32 s55, s68, 3211520\n"
"ds_read_b64_tr_b16 a[64:65], v209 offset:512\n"
"v_mfma_f32_16x16x32_bf16 v[52:55], a[12:15], a[32:35], v[52:55]\n"
"buffer_load_dwordx4 v211, s[36:39], s55 offen lds\n"
"v_mfma_f32_16x16x32_bf16 v[60:63], a[12:15], a[36:39], v[60:63]\n"
"s_add_u32 m0, s67, 39944\n"
"s_add_u32 s55, s68, 3211648\n"
"ds_read_b64_tr_b16 a[66:67], v209 offset:768\n"
"v_mfma_f32_16x16x32_bf16 v[68:71], a[12:15], a[40:43], v[68:71]\n"
"v_mfma_f32_16x16x32_bf16 v[76:79], a[12:15], a[44:47], v[76:79]\n"
"buffer_load_dwordx4 v211, s[36:39], s55 offen lds\n"
"v_mfma_f32_16x16x32_bf16 v[84:87], a[12:15], a[48:51], v[84:87]\n"
"s_add_u32 m0, s67, 65536\n"
"s_add_u32 s55, s68, 3407872\n"
"ds_read_b64_tr_b16 a[68:69], v209 offset:520\n"
"v_mfma_f32_16x16x32_bf16 v[92:95], a[12:15], a[52:55], v[92:95]\n"
"ds_read_b64_tr_b16 a[70:71], v209 offset:776\n"
"v_mfma_f32_16x16x32_bf16 v[96:99], a[16:19], a[32:35], v[96:99]\n"
"buffer_load_dwordx4 v211, s[36:39], s55 offen lds\n"
"v_mfma_f32_16x16x32_bf16 v[104:107], a[16:19], a[36:39], v[104:107]\n"
"s_add_u32 m0, s67, 66560\n"
"s_add_u32 s55, s68, 3408000\n"
"ds_read_b64_tr_b16 a[72:73], v209 offset:1024\n"
"v_mfma_f32_16x16x32_bf16 v[112:115], a[16:19], a[40:43], v[112:115]\n"
"ds_read_b64_tr_b16 a[74:75], v209 offset:1280\n"
"v_mfma_f32_16x16x32_bf16 v[120:123], a[16:19], a[44:47], v[120:123]\n"
"buffer_load_dwordx4 v211, s[36:39], s55 offen lds\n"
"v_mfma_f32_16x16x32_bf16 v[128:131], a[16:19], a[48:51], v[128:131]\n"
"s_add_u32 m0, s67, 67584\n"
"s_add_u32 s55, s68, 3408128\n"
"ds_read_b64_tr_b16 a[76:77], v209 offset:1032\n"
"v_mfma_f32_16x16x32_bf16 v[136:139], a[16:19], a[52:55], v[136:139]\n"
"ds_read_b64_tr_b16 a[78:79], v209 offset:1288\n"
"v_mfma_f32_16x16x32_bf16 v[100:103], a[20:23], a[32:35], v[100:103]\n"
"buffer_load_dwordx4 v211, s[36:39], s55 offen lds\n"
"v_mfma_f32_16x16x32_bf16 v[108:111], a[20:23], a[36:39], v[108:111]\n"
"s_add_u32 m0, s67, 68608\n"
"s_add_u32 s55, s68, 3408256\n"
"ds_read_b64_tr_b16 a[80:81], v209 offset:1536\n"
"v_mfma_f32_16x16x32_bf16 v[116:119], a[20:23], a[40:43], v[116:119]\n"
"buffer_load_dwordx4 v211, s[36:39], s55 offen lds\n"
"v_mfma_f32_16x16x32_bf16 v[124:127], a[20:23], a[44:47], v[124:127]\n"
"s_add_u32 m0, s67, 69640\n"
"s_add_u32 s55, s68, 3473408\n"
"ds_read_b64_tr_b16 a[82:83], v209 offset:1792\n"
"v_mfma_f32_16x16x32_bf16 v[132:135], a[20:23], a[48:51], v[132:135]\n"
"v_mfma_f32_16x16x32_bf16 v[140:143], a[20:23], a[52:55], v[140:143]\n"
"buffer_load_dwordx4 v211, s[36:39], s55 offen lds\n"
"v_mfma_f32_16x16x32_bf16 v[144:147], a[24:27], a[32:35], v[144:147]\n"
"s_add_u32 m0, s67, 70664\n"
"s_add_u32 s55, s68, 3473536\n"
"ds_read_b64_tr_b16 a[84:85], v209 offset:1544\n"
"v_mfma_f32_16x16x32_bf16 v[152:155], a[24:27], a[36:39], v[152:155]\n"
"ds_read_b64_tr_b16 a[86:87], v209 offset:1800\n"
"v_add_u32 v209, 32768, v209\n"
"v_mfma_f32_16x16x32_bf16 v[160:163], a[24:27], a[40:43], v[160:163]\n"
"buffer_load_dwordx4 v211, s[36:39], s55 offen lds\n"
"v_mfma_f32_16x16x32_bf16 v[168:171], a[24:27], a[44:47], v[168:171]\n"
"s_add_u32 m0, s67, 71688\n"
"s_add_u32 s55, s68, 3473664\n"
"v_mfma_f32_16x16x32_bf16 v[176:179], a[24:27], a[48:51], v[176:179]\n"
"v_mfma_f32_16x16x32_bf16 v[184:187], a[24:27], a[52:55], v[184:187]\n"
"buffer_load_dwordx4 v211, s[36:39], s55 offen lds\n"
"v_mfma_f32_16x16x32_bf16 v[148:151], a[28:31], a[32:35], v[148:151]\n"
"s_add_u32 m0, s67, 72712\n"
"s_add_u32 s55, s68, 3473792\n"
"s_cmp_eq_u32 s54, 0\n"
"v_mfma_f32_16x16x32_bf16 v[156:159], a[28:31], a[36:39], v[156:159]\n"
"s_cselect_b32 s61, s61, 0xfffe7e00\n"
"v_add_u32 v208, s61, v208\n"
"v_mfma_f32_16x16x32_bf16 v[164:167], a[28:31], a[40:43], v[164:167]\n"
"buffer_load_dwordx4 v211, s[36:39], s55 offen lds\n"
"v_mfma_f32_16x16x32_bf16 v[172:175], a[28:31], a[44:47], v[172:175]\n"
"v_mfma_f32_16x16x32_bf16 v[180:183], a[28:31], a[48:51], v[180:183]\n"
"v_mfma_f32_16x16x32_bf16 v[188:191], a[28:31], a[52:55], v[188:191]\n"
"s_waitcnt vmcnt(16) lgkmcnt(0)\n"
"s_barrier\n"
"v_mfma_f32_16x16x32_bf16 v[0:3], a[56:59], a[88:91], v[0:3]\n"
"s_mov_b32 s65, 49184\n"
"s_cmp_eq_u32 s47, 0\n"
"s_cselect_b32 s65, s65, 81952\n"
"v_mfma_f32_16x16x32_bf16 v[8:11], a[56:59], a[92:95], v[8:11]\n"
"s_add_u32 m0, s65, 0\n"
"s_add_u32 s55, s66, 768\n"
"ds_read_b64_tr_b16 a[0:1], v209\n"
"v_mfma_f32_16x16x32_bf16 v[16:19], a[56:59], a[96:99], v[16:19]\n"
"buffer_load_dwordx4 v210, s[32:35], s55 offen lds\n"
"v_mfma_f32_16x16x32_bf16 v[24:27], a[56:59], a[100:103], v[24:27]\n"
"s_add_u32 m0, s65, 1024\n"
"s_add_u32 s55, s66, 8960\n"
"ds_read_b64_tr_b16 a[2:3], v209 offset:256\n"
"v_mfma_f32_16x16x32_bf16 v[32:35], a[56:59], a[104:107], v[32:35]\n"
"buffer_load_dwordx4 v217, s[32:35], s55 offen lds\n"
"v_mfma_f32_16x16x32_bf16 v[40:43], a[56:59], a[108:111], v[40:43]\n"
"s_add_u32 m0, s65, 2048\n"
"s_add_u32 s55, s66, 17152\n"
"ds_read_b128 a[32:35], v208\n"
"v_mfma_f32_16x16x32_bf16 v[4:7], a[60:63], a[88:91], v[4:7]\n"
"buffer_load_dwordx4 v210, s[32:35], s55 offen lds\n"
"v_mfma_f32_16x16x32_bf16 v[12:15], a[60:63], a[92:95], v[12:15]\n"
"s_add_u32 m0, s65, 3072\n"
"s_add_u32 s55, s66, 25344\n"
"ds_read_b128 a[36:39], v208 offset:2048\n"
"v_mfma_f32_16x16x32_bf16 v[20:23], a[60:63], a[96:99], v[20:23]\n"
"buffer_load_dwordx4 v217, s[32:35], s55 offen lds\n"
"v_mfma_f32_16x16x32_bf16 v[28:31], a[60:63], a[100:103], v[28:31]\n"
"s_add_u32 m0, s65, 4096\n"
"s_add_u32 s55, s66, 33536\n"
"ds_read_b128 a[40:43], v208 offset:4096\n"
"v_mfma_f32_16x16x32_bf16 v[36:39], a[60:63], a[104:107], v[36:39]\n"
"buffer_load_dwordx4 v210, s[32:35], s55 offen lds\n"
"v_mfma_f32_16x16x32_bf16 v[44:47], a[60:63], a[108:111], v[44:47]\n"
"s_add_u32 m0, s65, 5120\n"
"s_add_u32 s55, s66, 41728\n"
"ds_read_b128 a[44:47], v208 offset:6144\n"
"v_mfma_f32_16x16x32_bf16 v[48:51], a[64:67], a[88:91], v[48:51]\n"
"buffer_load_dwordx4 v217, s[32:35], s55 offen lds\n"
"v_mfma_f32_16x16x32_bf16 v[56:59], a[64:67], a[92:95], v[56:59]\n"
"s_add_u32 m0, s65, 6144\n"
"s_add_u32 s55, s66, 49920\n"
"ds_read_b128 a[48:51], v208 offset:8192\n"
"v_mfma_f32_16x16x32_bf16 v[64:67], a[64:67], a[96:99], v[64:67]\n"
"buffer_load_dwordx4 v210, s[32:35], s55 offen lds\n"
"v_mfma_f32_16x16x32_bf16 v[72:75], a[64:67], a[100:103], v[72:75]\n"
"s_add_u32 m0, s65, 7168\n"
"s_add_u32 s55, s66, 58112\n"
"ds_read_b64_tr_b16 a[4:5], v209 offset:8\n"
"v_mfma_f32_16x16x32_bf16 v[80:83], a[64:67], a[104:107], v[80:83]\n"
"buffer_load_dwordx4 v217, s[32:35], s55 offen lds\n"
"v_mfma_f32_16x16x32_bf16 v[88:91], a[64:67], a[108:111], v[88:91]\n"
"s_add_u32 m0, s65, 8192\n"
"s_add_u32 s55, s66, 66304\n"
"ds_read_b64_tr_b16 a[6:7], v209 offset:264\n"
"v_mfma_f32_16x16x32_bf16 v[52:55], a[68:71], a[88:91], v[52:55]\n"
"buffer_load_dwordx4 v210, s[32:35], s55 offen lds\n"
"v_mfma_f32_16x16x32_bf16 v[60:63], a[68:71], a[92:95], v[60:63]\n"
"s_add_u32 m0, s65, 9216\n"
"s_add_u32 s55, s66, 74496\n"
"ds_read_b64_tr_b16 a[8:9], v209 offset:512\n"
"v_mfma_f32_16x16x32_bf16 v[68:71], a[68:71], a[96:99], v[68:71]\n"
"buffer_load_dwordx4 v217, s[32:35], s55 offen lds\n"
"v_mfma_f32_16x16x32_bf16 v[76:79], a[68:71], a[100:103], v[76:79]\n"
"s_add_u32 m0, s65, 10240\n"
"s_add_u32 s55, s66, 82688\n"
"ds_read_b64_tr_b16 a[10:11], v209 offset:768\n"
"v_mfma_f32_16x16x32_bf16 v[84:87], a[68:71], a[104:107], v[84:87]\n"
"buffer_load_dwordx4 v210, s[32:35], s55 offen lds\n"
"v_mfma_f32_16x16x32_bf16 v[92:95], a[68:71], a[108:111], v[92:95]\n"
"s_add_u32 m0, s65, 11264\n"
"s_add_u32 s55, s66, 90880\n"
"ds_read_b64_tr_b16 a[12:13], v209 offset:520\n"
"v_mfma_f32_16x16x32_bf16 v[96:99], a[72:75], a[88:91], v[96:99]\n"
"buffer_load_dwordx4 v217, s[32:35], s55 offen lds\n"
"v_mfma_f32_16x16x32_bf16 v[104:107], a[72:75], a[92:95], v[104:107]\n"
"ds_read_b64_tr_b16 a[14:15], v209 offset:776\n"
"v_mfma_f32_16x16x32_bf16 v[112:115], a[72:75], a[96:99], v[112:115]\n"
"ds_read_b64_tr_b16 a[16:17], v209 offset:1024\n"
"v_mfma_f32_16x16x32_bf16 v[120:123], a[72:75], a[100:103], v[120:123]\n"
"ds_read_b64_tr_b16 a[18:19], v209 offset:1280\n"
"v_mfma_f32_16x16x32_bf16 v[128:131], a[72:75], a[104:107], v[128:131]\n"
"ds_read_b64_tr_b16 a[20:21], v209 offset:1032\n"
"v_mfma_f32_16x16x32_bf16 v[136:139], a[72:75], a[108:111], v[136:139]\n"
"ds_read_b128 a[52:55], v208 offset:10240\n"
"v_add_u32 v208, 512, v208\n"
"v_mfma_f32_16x16x32_bf16 v[100:103], a[76:79], a[88:91], v[100:103]\n"
"ds_read_b64_tr_b16 a[22:23], v209 offset:1288\n"
"v_mfma_f32_16x16x32_bf16 v[108:111], a[76:79], a[92:95], v[108:111]\n"
"ds_read_b64_tr_b16 a[24:25], v209 offset:1536\n"
"v_mfma_f32_16x16x32_bf16 v[116:119], a[76:79], a[96:99], v[116:119]\n"
"v_mfma_f32_16x16x32_bf16 v[124:127], a[76:79], a[100:103], v[124:127]\n"
"ds_read_b64_tr_b16 a[26:27], v209 offset:1792\n"
"v_mfma_f32_16x16x32_bf16 v[132:135], a[76:79], a[104:107], v[132:135]\n"
"v_mfma_f32_16x16x32_bf16 v[140:143], a[76:79], a[108:111], v[140:143]\n"
"ds_read_b64_tr_b16 a[28:29], v209 offset:1544\n"
"v_mfma_f32_16x16x32_bf16 v[144:147], a[80:83], a[88:91], v[144:147]\n"
"ds_read_b64_tr_b16 a[30:31], v209 offset:1800\n"
"v_mfma_f32_16x16x32_bf16 v[152:155], a[80:83], a[92:95], v[152:155]\n"
"v_add_u32 v209, 0xfffe0000, v209\n"
"v_mfma_f32_16x16x32_bf16 v[160:163], a[80:83], a[96:99], v[160:163]\n"
"v_mfma_f32_16x16x32_bf16 v[168:171], a[80:83], a[100:103], v[168:171]\n"
"v_mfma_f32_16x16x32_bf16 v[176:179], a[80:83], a[104:107], v[176:179]\n"
"v_mfma_f32_16x16x32_bf16 v[184:187], a[80:83], a[108:111], v[184:187]\n"
"v_mfma_f32_16x16x32_bf16 v[148:151], a[84:87], a[88:91], v[148:151]\n"
"v_mfma_f32_16x16x32_bf16 v[156:159], a[84:87], a[92:95], v[156:159]\n"
"v_mfma_f32_16x16x32_bf16 v[164:167], a[84:87], a[96:99], v[164:167]\n"
"v_mfma_f32_16x16x32_bf16 v[172:175], a[84:87], a[100:103], v[172:175]\n"
"v_mfma_f32_16x16x32_bf16 v[180:183], a[84:87], a[104:107], v[180:183]\n"
"v_mfma_f32_16x16x32_bf16 v[188:191], a[84:87], a[108:111], v[188:191]\n"
"s_waitcnt lgkmcnt(0)\n"
"s_barrier\n"
"v_mfma_f32_16x16x32_bf16 v[0:3], a[0:3], a[32:35], v[0:3]\n"
"s_add_u32 m0, s67, 98304\n"
"s_add_u32 s55, s68, 3670016\n"
"s_mov_b32 s61, 0xfffe7e00\n"
"v_mfma_f32_16x16x32_bf16 v[8:11], a[0:3], a[36:39], v[8:11]\n"
"buffer_load_dwordx4 v211, s[36:39], s55 offen lds\n"
"v_mfma_f32_16x16x32_bf16 v[16:19], a[0:3], a[40:43], v[16:19]\n"
"s_add_u32 m0, s67, 99328\n"
"s_add_u32 s55, s68, 3670144\n"
"ds_read_b64_tr_b16 a[56:57], v209\n"
"v_mfma_f32_16x16x32_bf16 v[24:27], a[0:3], a[44:47], v[24:27]\n"
"ds_read_b64_tr_b16 a[58:59], v209 offset:256\n"
"v_mfma_f32_16x16x32_bf16 v[32:35], a[0:3], a[48:51], v[32:35]\n"
"buffer_load_dwordx4 v211, s[36:39], s55 offen lds\n"
"v_mfma_f32_16x16x32_bf16 v[40:43], a[0:3], a[52:55], v[40:43]\n"
"s_add_u32 m0, s67, 100352\n"
"s_add_u32 s55, s68, 3670272\n"
"ds_read_b128 a[88:91], v208\n"
"v_mfma_f32_16x16x32_bf16 v[4:7], a[4:7], a[32:35], v[4:7]\n"
"ds_read_b128 a[92:95], v208 offset:2048\n"
"v_mfma_f32_16x16x32_bf16 v[12:15], a[4:7], a[36:39], v[12:15]\n"
"buffer_load_dwordx4 v211, s[36:39], s55 offen lds\n"
"v_mfma_f32_16x16x32_bf16 v[20:23], a[4:7], a[40:43], v[20:23]\n"
"s_add_u32 m0, s67, 101376\n"
"s_add_u32 s55, s68, 3670400\n"
"ds_read_b128 a[96:99], v208 offset:4096\n"
"v_mfma_f32_16x16x32_bf16 v[28:31], a[4:7], a[44:47], v[28:31]\n"
"ds_read_b128 a[100:103], v208 offset:6144\n"
"v_mfma_f32_16x16x32_bf16 v[36:39], a[4:7], a[48:51], v[36:39]\n"
"buffer_load_dwordx4 v211, s[36:39], s55 offen lds\n"
"v_mfma_f32_16x16x32_bf16 v[44:47], a[4:7], a[52:55], v[44:47]\n"
"s_add_u32 m0, s67, 102408\n"
"s_add_u32 s55, s68, 3735552\n"
"ds_read_b128 a[104:107], v208 offset:8192\n"
"v_mfma_f32_16x16x32_bf16 v[48:51], a[8:11], a[32:35], v[48:51]\n"
"ds_read_b128 a[108:111], v208 offset:10240\n"
"v_mfma_f32_16x16x32_bf16 v[56:59], a[8:11], a[36:39], v[56:59]\n"
"buffer_load_dwordx4 v211, s[36:39], s55 offen lds\n"
"v_mfma_f32_16x16x32_bf16 v[64:67], a[8:11], a[40:43], v[64:67]\n"
"s_add_u32 m0, s67, 103432\n"
"s_add_u32 s55, s68, 3735680\n"
"ds_read_b64_tr_b16 a[60:61], v209 offset:8\n"
"v_mfma_f32_16x16x32_bf16 v[72:75], a[8:11], a[44:47], v[72:75]\n"
"ds_read_b64_tr_b16 a[62:63], v209 offset:264\n"
"v_mfma_f32_16x16x32_bf16 v[80:83], a[8:11], a[48:51], v[80:83]\n"
"buffer_load_dwordx4 v211, s[36:39], s55 offen lds\n"
"v_mfma_f32_16x16x32_bf16 v[88:91], a[8:11], a[52:55], v[88:91]\n"
"s_add_u32 m0, s67, 104456\n"
"s_add_u32 s55, s68, 3735808\n"
"ds_read_b64_tr_b16 a[64:65], v209 offset:512\n"
"v_mfma_f32_16x16x32_bf16 v[52:55], a[12:15], a[32:35], v[52:55]\n"
"buffer_load_dwordx4 v211, s[36:39], s55 offen lds\n"
"v_mfma_f32_16x16x32_bf16 v[60:63], a[12:15], a[36:39], v[60:63]\n"
"s_add_u32 m0, s67, 105480\n"
"s_add_u32 s55, s68, 3735936\n"
"ds_read_b64_tr_b16 a[66:67], v209 offset:768\n"
"v_mfma_f32_16x16x32_bf16 v[68:71], a[12:15], a[40:43], v[68:71]\n"
"v_mfma_f32_16x16x32_bf16 v[76:79], a[12:15], a[44:47], v[76:79]\n"
"buffer_load_dwordx4 v211, s[36:39], s55 offen lds\n"
"v_mfma_f32_16x16x32_bf16 v[84:87], a[12:15], a[48:51], v[84:87]\n"
"s_add_u32 m0, s67, 131072\n"
"s_add_u32 s55, s68, 3932160\n"
"ds_read_b64_tr_b16 a[68:69], v209 offset:520\n"
"v_mfma_f32_16x16x32_bf16 v[92:95], a[12:15], a[52:55], v[92:95]\n"
"ds_read_b64_tr_b16 a[70:71], v209 offset:776\n"
"v_mfma_f32_16x16x32_bf16 v[96:99], a[16:19], a[32:35], v[96:99]\n"
"buffer_load_dwordx4 v211, s[36:39], s55 offen lds\n"
"v_mfma_f32_16x16x32_bf16 v[104:107], a[16:19], a[36:39], v[104:107]\n"
"s_add_u32 m0, s67, 132096\n"
"s_add_u32 s55, s68, 3932288\n"
"ds_read_b64_tr_b16 a[72:73], v209 offset:1024\n"
"v_mfma_f32_16x16x32_bf16 v[112:115], a[16:19], a[40:43], v[112:115]\n"
"ds_read_b64_tr_b16 a[74:75], v209 offset:1280\n"
"v_mfma_f32_16x16x32_bf16 v[120:123], a[16:19], a[44:47], v[120:123]\n"
"buffer_load_dwordx4 v211, s[36:39], s55 offen lds\n"
"v_mfma_f32_16x16x32_bf16 v[128:131], a[16:19], a[48:51], v[128:131]\n"
"s_add_u32 m0, s67, 133120\n"
"s_add_u32 s55, s68, 3932416\n"
"ds_read_b64_tr_b16 a[76:77], v209 offset:1032\n"
"v_mfma_f32_16x16x32_bf16 v[136:139], a[16:19], a[52:55], v[136:139]\n"
"ds_read_b64_tr_b16 a[78:79], v209 offset:1288\n"
"v_mfma_f32_16x16x32_bf16 v[100:103], a[20:23], a[32:35], v[100:103]\n"
"buffer_load_dwordx4 v211, s[36:39], s55 offen lds\n"
"v_mfma_f32_16x16x32_bf16 v[108:111], a[20:23], a[36:39], v[108:111]\n"
"s_add_u32 m0, s67, 134144\n"
"s_add_u32 s55, s68, 3932544\n"
"ds_read_b64_tr_b16 a[80:81], v209 offset:1536\n"
"v_mfma_f32_16x16x32_bf16 v[116:119], a[20:23], a[40:43], v[116:119]\n"
"buffer_load_dwordx4 v211, s[36:39], s55 offen lds\n"
"v_mfma_f32_16x16x32_bf16 v[124:127], a[20:23], a[44:47], v[124:127]\n"
"s_add_u32 m0, s67, 135176\n"
"s_add_u32 s55, s68, 3997696\n"
"ds_read_b64_tr_b16 a[82:83], v209 offset:1792\n"
"v_mfma_f32_16x16x32_bf16 v[132:135], a[20:23], a[48:51], v[132:135]\n"
"v_mfma_f32_16x16x32_bf16 v[140:143], a[20:23], a[52:55], v[140:143]\n"
"buffer_load_dwordx4 v211, s[36:39], s55 offen lds\n"
"v_mfma_f32_16x16x32_bf16 v[144:147], a[24:27], a[32:35], v[144:147]\n"
"s_add_u32 m0, s67, 136200\n"
"s_add_u32 s55, s68, 3997824\n"
"ds_read_b64_tr_b16 a[84:85], v209 offset:1544\n"
"v_mfma_f32_16x16x32_bf16 v[152:155], a[24:27], a[36:39], v[152:155]\n"
"ds_read_b64_tr_b16 a[86:87], v209 offset:1800\n"
"v_add_u32 v209, 32768, v209\n"
"v_mfma_f32_16x16x32_bf16 v[160:163], a[24:27], a[40:43], v[160:163]\n"
"buffer_load_dwordx4 v211, s[36:39], s55 offen lds\n"
"v_mfma_f32_16x16x32_bf16 v[168:171], a[24:27], a[44:47], v[168:171]\n"
"s_add_u32 m0, s67, 137224\n"
"s_add_u32 s55, s68, 3997952\n"
"v_mfma_f32_16x16x32_bf16 v[176:179], a[24:27], a[48:51], v[176:179]\n"
"v_mfma_f32_16x16x32_bf16 v[184:187], a[24:27], a[52:55], v[184:187]\n"
"buffer_load_dwordx4 v211, s[36:39], s55 offen lds\n"
"v_mfma_f32_16x16x32_bf16 v[148:151], a[28:31], a[32:35], v[148:151]\n"
"s_add_u32 m0, s67, 138248\n"
"s_add_u32 s55, s68, 3998080\n"
"s_cmp_eq_u32 s54, 0\n"
"v_mfma_f32_16x16x32_bf16 v[156:159], a[28:31], a[36:39], v[156:159]\n"
"s_cselect_b32 s61, s61, 65024\n"
"v_add_u32 v208, s61, v208\n"
"v_mfma_f32_16x16x32_bf16 v[164:167], a[28:31], a[40:43], v[164:167]\n"
"buffer_load_dwordx4 v211, s[36:39], s55 offen lds\n"
"v_mfma_f32_16x16x32_bf16 v[172:175], a[28:31], a[44:47], v[172:175]\n"
"v_mfma_f32_16x16x32_bf16 v[180:183], a[28:31], a[48:51], v[180:183]\n"
"v_mfma_f32_16x16x32_bf16 v[188:191], a[28:31], a[52:55], v[188:191]\n"
"s_waitcnt vmcnt(16) lgkmcnt(0)\n"
"s_barrier\n"
"v_mfma_f32_16x16x32_bf16 v[0:3], a[56:59], a[88:91], v[0:3]\n"
"s_mov_b32 s65, 114720\n"
"s_cmp_eq_u32 s47, 0\n"
"s_cselect_b32 s65, s65, 147488\n"
"v_mfma_f32_16x16x32_bf16 v[8:11], a[56:59], a[92:95], v[8:11]\n"
"s_add_u32 m0, s65, 0\n"
"s_add_u32 s55, s66, 896\n"
"ds_read_b64_tr_b16 a[0:1], v209\n"
"v_mfma_f32_16x16x32_bf16 v[16:19], a[56:59], a[96:99], v[16:19]\n"
"buffer_load_dwordx4 v210, s[32:35], s55 offen lds\n"
"v_mfma_f32_16x16x32_bf16 v[24:27], a[56:59], a[100:103], v[24:27]\n"
"s_add_u32 m0, s65, 1024\n"
"s_add_u32 s55, s66, 9088\n"
"ds_read_b64_tr_b16 a[2:3], v209 offset:256\n"
"v_mfma_f32_16x16x32_bf16 v[32:35], a[56:59], a[104:107], v[32:35]\n"
"buffer_load_dwordx4 v217, s[32:35], s55 offen lds\n"
"v_mfma_f32_16x16x32_bf16 v[40:43], a[56:59], a[108:111], v[40:43]\n"
"s_add_u32 m0, s65, 2048\n"
"s_add_u32 s55, s66, 17280\n"
"ds_read_b128 a[32:35], v208\n"
"v_mfma_f32_16x16x32_bf16 v[4:7], a[60:63], a[88:91], v[4:7]\n"
"buffer_load_dwordx4 v210, s[32:35], s55 offen lds\n"
"v_mfma_f32_16x16x32_bf16 v[12:15], a[60:63], a[92:95], v[12:15]\n"
"s_add_u32 m0, s65, 3072\n"
"s_add_u32 s55, s66, 25472\n"
"ds_read_b128 a[36:39], v208 offset:2048\n"
"v_mfma_f32_16x16x32_bf16 v[20:23], a[60:63], a[96:99], v[20:23]\n"
"buffer_load_dwordx4 v217, s[32:35], s55 offen lds\n"
"v_mfma_f32_16x16x32_bf16 v[28:31], a[60:63], a[100:103], v[28:31]\n"
"s_add_u32 m0, s65, 4096\n"
"s_add_u32 s55, s66, 33664\n"
"ds_read_b128 a[40:43], v208 offset:4096\n"
"v_mfma_f32_16x16x32_bf16 v[36:39], a[60:63], a[104:107], v[36:39]\n"
"buffer_load_dwordx4 v210, s[32:35], s55 offen lds\n"
"v_mfma_f32_16x16x32_bf16 v[44:47], a[60:63], a[108:111], v[44:47]\n"
"s_add_u32 m0, s65, 5120\n"
"s_add_u32 s55, s66, 41856\n"
"ds_read_b128 a[44:47], v208 offset:6144\n"
"v_mfma_f32_16x16x32_bf16 v[48:51], a[64:67], a[88:91], v[48:51]\n"
"buffer_load_dwordx4 v217, s[32:35], s55 offen lds\n"
"v_mfma_f32_16x16x32_bf16 v[56:59], a[64:67], a[92:95], v[56:59]\n"
"s_add_u32 m0, s65, 6144\n"
"s_add_u32 s55, s66, 50048\n"
"ds_read_b128 a[48:51], v208 offset:8192\n"
"v_mfma_f32_16x16x32_bf16 v[64:67], a[64:67], a[96:99], v[64:67]\n"
"buffer_load_dwordx4 v210, s[32:35], s55 offen lds\n"
"v_mfma_f32_16x16x32_bf16 v[72:75], a[64:67], a[100:103], v[72:75]\n"
"s_add_u32 m0, s65, 7168\n"
"s_add_u32 s55, s66, 58240\n"
"ds_read_b64_tr_b16 a[4:5], v209 offset:8\n"
"v_mfma_f32_16x16x32_bf16 v[80:83], a[64:67], a[104:107], v[80:83]\n"
"buffer_load_dwordx4 v217, s[32:35], s55 offen lds\n"
"v_mfma_f32_16x16x32_bf16 v[88:91], a[64:67], a[108:111], v[88:91]\n"
"s_add_u32 m0, s65, 8192\n"
"s_add_u32 s55, s66, 66432\n"
"ds_read_b64_tr_b16 a[6:7], v209 offset:264\n"
"v_mfma_f32_16x16x32_bf16 v[52:55], a[68:71], a[88:91], v[52:55]\n"
"buffer_load_dwordx4 v210, s[32:35], s55 offen lds\n"
"v_mfma_f32_16x16x32_bf16 v[60:63], a[68:71], a[92:95], v[60:63]\n"
"s_add_u32 m0, s65, 9216\n"
"s_add_u32 s55, s66, 74624\n"
"ds_read_b64_tr_b16 a[8:9], v209 offset:512\n"
"v_mfma_f32_16x16x32_bf16 v[68:71], a[68:71], a[96:99], v[68:71]\n"
"buffer_load_dwordx4 v217, s[32:35], s55 offen lds\n"
"v_mfma_f32_16x16x32_bf16 v[76:79], a[68:71], a[100:103], v[76:79]\n"
"s_add_u32 m0, s65, 10240\n"
"s_add_u32 s55, s66, 82816\n"
"ds_read_b64_tr_b16 a[10:11], v209 offset:768\n"
"v_mfma_f32_16x16x32_bf16 v[84:87], a[68:71], a[104:107], v[84:87]\n"
"buffer_load_dwordx4 v210, s[32:35], s55 offen lds\n"
"v_mfma_f32_16x16x32_bf16 v[92:95], a[68:71], a[108:111], v[92:95]\n"
"s_add_u32 m0, s65, 11264\n"
"s_add_u32 s55, s66, 91008\n"
"ds_read_b64_tr_b16 a[12:13], v209 offset:520\n"
"v_mfma_f32_16x16x32_bf16 v[96:99], a[72:75], a[88:91], v[96:99]\n"
"buffer_load_dwordx4 v217, s[32:35], s55 offen lds\n"
"v_mfma_f32_16x16x32_bf16 v[104:107], a[72:75], a[92:95], v[104:107]\n"
"ds_read_b64_tr_b16 a[14:15], v209 offset:776\n"
"s_add_u32 m0, s73, 0\n"
"v_mfma_f32_16x16x32_bf16 v[112:115], a[72:75], a[96:99], v[112:115]\n"
"s_mov_b32 s55, 0\n"
"ds_read_b64_tr_b16 a[16:17], v209 offset:1024\n"
"v_mfma_f32_16x16x32_bf16 v[120:123], a[72:75], a[100:103], v[120:123]\n"
"ds_read_b64_tr_b16 a[18:19], v209 offset:1280\n"
"v_mfma_f32_16x16x32_bf16 v[128:131], a[72:75], a[104:107], v[128:131]\n"
"buffer_load_dwordx4 v195, s[24:27], s55 offen nt lds\n"
"v_mfma_f32_16x16x32_bf16 v[136:139], a[72:75], a[108:111], v[136:139]\n"
"ds_read_b64_tr_b16 a[20:21], v209 offset:1032\n"
"v_mfma_f32_16x16x32_bf16 v[100:103], a[76:79], a[88:91], v[100:103]\n"
"s_add_u32 m0, s73, 0\n"
"s_mov_b32 s55, 0\n"
"v_mfma_f32_16x16x32_bf16 v[108:111], a[76:79], a[92:95], v[108:111]\n"
"ds_read_b64_tr_b16 a[22:23], v209 offset:1288\n"
"v_mfma_f32_16x16x32_bf16 v[116:119], a[76:79], a[96:99], v[116:119]\n"
"ds_read_b64_tr_b16 a[24:25], v209 offset:1536\n"
"v_mfma_f32_16x16x32_bf16 v[124:127], a[76:79], a[100:103], v[124:127]\n"
"buffer_load_dwordx4 v196, s[24:27], s55 offen offset:2048 nt lds\n"
"v_mfma_f32_16x16x32_bf16 v[132:135], a[76:79], a[104:107], v[132:135]\n"
"ds_read_b64_tr_b16 a[26:27], v209 offset:1792\n"
"v_mfma_f32_16x16x32_bf16 v[140:143], a[76:79], a[108:111], v[140:143]\n"
"s_add_u32 m0, s73, 4096\n"
"s_mov_b32 s55, 4096\n"
"ds_read_b64_tr_b16 a[28:29], v209 offset:1544\n"
"v_mfma_f32_16x16x32_bf16 v[144:147], a[80:83], a[88:91], v[144:147]\n"
"ds_read_b128 a[52:55], v208 offset:10240\n"
"v_add_u32 v208, 512, v208\n"
"v_mfma_f32_16x16x32_bf16 v[152:155], a[80:83], a[92:95], v[152:155]\n"
"ds_read_b64_tr_b16 a[30:31], v209 offset:1800\n"
"v_add_u32 v209, 32768, v209\n"
"v_mfma_f32_16x16x32_bf16 v[160:163], a[80:83], a[96:99], v[160:163]\n"
"buffer_load_dwordx4 v195, s[24:27], s55 offen nt lds\n"
"v_mfma_f32_16x16x32_bf16 v[168:171], a[80:83], a[100:103], v[168:171]\n"
"s_add_u32 m0, s73, 4096\n"
"v_mfma_f32_16x16x32_bf16 v[176:179], a[80:83], a[104:107], v[176:179]\n"
"s_mov_b32 s55, 4096\n"
"v_mfma_f32_16x16x32_bf16 v[184:187], a[80:83], a[108:111], v[184:187]\n"
"v_mfma_f32_16x16x32_bf16 v[148:151], a[84:87], a[88:91], v[148:151]\n"
"buffer_load_dwordx4 v196, s[24:27], s55 offen offset:2048 nt lds\n"
"v_mfma_f32_16x16x32_bf16 v[156:159], a[84:87], a[92:95], v[156:159]\n"
"v_mfma_f32_16x16x32_bf16 v[164:167], a[84:87], a[96:99], v[164:167]\n"
"v_mfma_f32_16x16x32_bf16 v[172:175], a[84:87], a[100:103], v[172:175]\n"
"v_mfma_f32_16x16x32_bf16 v[180:183], a[84:87], a[104:107], v[180:183]\n"
"v_mfma_f32_16x16x32_bf16 v[188:191], a[84:87], a[108:111], v[188:191]\n"
"s_waitcnt lgkmcnt(0)\n"
"s_barrier\n"
"v_mfma_f32_16x16x32_bf16 v[0:3], a[0:3], a[32:35], v[0:3]\n"
"s_mov_b64 s[32:33], %5\n"
"ds_read_b64_tr_b16 a[56:57], v209\n"
"s_mov_b32 s34, 1048576\n"
"v_mfma_f32_16x16x32_bf16 v[8:11], a[0:3], a[36:39], v[8:11]\n"
"s_add_u32 m0, s73, 32768\n"
"s_mov_b32 s55, 8192\n"
"ds_read_b64_tr_b16 a[58:59], v209 offset:256\n"
"v_mfma_f32_16x16x32_bf16 v[16:19], a[0:3], a[40:43], v[16:19]\n"
"s_mov_b32 s35, 0x20000\n"
"s_mov_b64 s[36:37], %6\n"
"s_mov_b32 s38, 98304\n"
"v_mfma_f32_16x16x32_bf16 v[24:27], a[0:3], a[44:47], v[24:27]\n"
"s_mov_b32 s39, 0x20000\n"
"ds_read_b128 a[88:91], v208\n"
"s_mov_b32 s59, s73\n"
"v_mfma_f32_16x16x32_bf16 v[32:35], a[0:3], a[48:51], v[32:35]\n"
"buffer_load_dwordx4 v195, s[24:27], s55 offen nt lds\n"
"v_mfma_f32_16x16x32_bf16 v[40:43], a[0:3], a[52:55], v[40:43]\n"
"ds_read_b128 a[92:95], v208 offset:2048\n"
"v_mfma_f32_16x16x32_bf16 v[4:7], a[4:7], a[32:35], v[4:7]\n"
"v_mfma_f32_16x16x32_bf16 v[12:15], a[4:7], a[36:39], v[12:15]\n"
"ds_read_b128 a[96:99], v208 offset:4096\n"
"v_mfma_f32_16x16x32_bf16 v[20:23], a[4:7], a[40:43], v[20:23]\n"
"ds_read_b128 a[100:103], v208 offset:6144\n"
"v_mfma_f32_16x16x32_bf16 v[28:31], a[4:7], a[44:47], v[28:31]\n"
"v_mfma_f32_16x16x32_bf16 v[36:39], a[4:7], a[48:51], v[36:39]\n"
"ds_read_b128 a[104:107], v208 offset:8192\n"
"v_mfma_f32_16x16x32_bf16 v[44:47], a[4:7], a[52:55], v[44:47]\n"
"v_mfma_f32_16x16x32_bf16 v[48:51], a[8:11], a[32:35], v[48:51]\n"
"s_add_u32 m0, s73, 32768\n"
"s_mov_b32 s55, 8192\n"
"ds_read_b128 a[108:111], v208 offset:10240\n"
"v_mfma_f32_16x16x32_bf16 v[56:59], a[8:11], a[36:39], v[56:59]\n"
"v_add_u32 v208, 65024, v208\n"
"v_mfma_f32_16x16x32_bf16 v[64:67], a[8:11], a[40:43], v[64:67]\n"
"ds_read_b64_tr_b16 a[60:61], v209 offset:8\n"
"v_mfma_f32_16x16x32_bf16 v[72:75], a[8:11], a[44:47], v[72:75]\n"
"buffer_load_dwordx4 v196, s[24:27], s55 offen offset:2048 nt lds\n"
"v_mfma_f32_16x16x32_bf16 v[80:83], a[8:11], a[48:51], v[80:83]\n"
"ds_read_b64_tr_b16 a[62:63], v209 offset:264\n"
"v_mfma_f32_16x16x32_bf16 v[88:91], a[8:11], a[52:55], v[88:91]\n"
"ds_read_b64_tr_b16 a[64:65], v209 offset:512\n"
"v_mfma_f32_16x16x32_bf16 v[52:55], a[12:15], a[32:35], v[52:55]\n"
"v_mfma_f32_16x16x32_bf16 v[60:63], a[12:15], a[36:39], v[60:63]\n"
"ds_read_b64_tr_b16 a[66:67], v209 offset:768\n"
"v_mfma_f32_16x16x32_bf16 v[68:71], a[12:15], a[40:43], v[68:71]\n"
"v_mfma_f32_16x16x32_bf16 v[76:79], a[12:15], a[44:47], v[76:79]\n"
"ds_read_b64_tr_b16 a[68:69], v209 offset:520\n"
"v_mfma_f32_16x16x32_bf16 v[84:87], a[12:15], a[48:51], v[84:87]\n"
"ds_read_b64_tr_b16 a[70:71], v209 offset:776\n"
"v_mfma_f32_16x16x32_bf16 v[92:95], a[12:15], a[52:55], v[92:95]\n"
"s_add_u32 m0, s73, 36864\n"
"s_mov_b32 s55, 12288\n"
"v_mfma_f32_16x16x32_bf16 v[96:99], a[16:19], a[32:35], v[96:99]\n"
"ds_read_b64_tr_b16 a[72:73], v209 offset:1024\n"
"v_mfma_f32_16x16x32_bf16 v[104:107], a[16:19], a[36:39], v[104:107]\n"
"v_mfma_f32_16x16x32_bf16 v[112:115], a[16:19], a[40:43], v[112:115]\n"
"buffer_load_dwordx4 v195, s[24:27], s55 offen nt lds\n"
"v_mfma_f32_16x16x32_bf16 v[120:123], a[16:19], a[44:47], v[120:123]\n"
"ds_read_b64_tr_b16 a[74:75], v209 offset:1280\n"
"v_mfma_f32_16x16x32_bf16 v[128:131], a[16:19], a[48:51], v[128:131]\n"
"ds_read_b64_tr_b16 a[76:77], v209 offset:1032\n"
"v_mfma_f32_16x16x32_bf16 v[136:139], a[16:19], a[52:55], v[136:139]\n"
"ds_read_b64_tr_b16 a[78:79], v209 offset:1288\n"
"v_mfma_f32_16x16x32_bf16 v[100:103], a[20:23], a[32:35], v[100:103]\n"
"v_mfma_f32_16x16x32_bf16 v[108:111], a[20:23], a[36:39], v[108:111]\n"
"ds_read_b64_tr_b16 a[80:81], v209 offset:1536\n"
"v_mfma_f32_16x16x32_bf16 v[116:119], a[20:23], a[40:43], v[116:119]\n"
"v_mfma_f32_16x16x32_bf16 v[124:127], a[20:23], a[44:47], v[124:127]\n"
"ds_read_b64_tr_b16 a[82:83], v209 offset:1792\n"
"v_mfma_f32_16x16x32_bf16 v[132:135], a[20:23], a[48:51], v[132:135]\n"
"s_add_u32 m0, s73, 36864\n"
"s_lshl_b32 s47, s44, 5\n"
"s_lshl_b32 s48, s45, 3\n"
"v_mfma_f32_16x16x32_bf16 v[140:143], a[20:23], a[52:55], v[140:143]\n"
"s_add_u32 s47, s47, s48\n"
"s_add_u32 s47, s47, s53\n"
"s_lshl_b32 s47, s47, 1\n"
"v_mfma_f32_16x16x32_bf16 v[144:147], a[24:27], a[32:35], v[144:147]\n"
"s_add_u32 s47, s47, s54\n"
"s_lshl_b32 s49, s54, 12\n"
"s_add_u32 s48, s46, 0\n"
"v_mfma_f32_16x16x32_bf16 v[152:155], a[24:27], a[36:39], v[152:155]\n"
"s_and_b32 s48, s48, 3\n"
"s_mul_i32 s47, s47, 24576\n"
"s_mul_i32 s88, s48, 98304\n"
"v_mfma_f32_16x16x32_bf16 v[160:163], a[24:27], a[40:43], v[160:163]\n"
"s_add_u32 s88, s88, s47\n"
"s_lshl_b32 s65, s48, 18\n"
"s_add_u32 s65, s65, s49\n"
"v_mfma_f32_16x16x32_bf16 v[168:171], a[24:27], a[44:47], v[168:171]\n"
"s_add_u32 s48, s46, 1\n"
"s_and_b32 s48, s48, 3\n"
"s_mul_i32 s89, s48, 98304\n"
"v_mfma_f32_16x16x32_bf16 v[176:179], a[24:27], a[48:51], v[176:179]\n"
"s_add_u32 s89, s89, s47\n"
"s_lshl_b32 s66, s48, 18\n"
"s_add_u32 s66, s66, s49\n"
"v_mfma_f32_16x16x32_bf16 v[184:187], a[24:27], a[52:55], v[184:187]\n"
"s_add_u32 s48, s46, 2\n"
"s_and_b32 s48, s48, 3\n"
"s_mul_i32 s90, s48, 98304\n"
"v_mfma_f32_16x16x32_bf16 v[148:151], a[28:31], a[32:35], v[148:151]\n"
"s_add_u32 s90, s90, s47\n"
"s_lshl_b32 s67, s48, 18\n"
"s_add_u32 s67, s67, s49\n"
"v_mfma_f32_16x16x32_bf16 v[156:159], a[28:31], a[36:39], v[156:159]\n"
"s_add_u32 s48, s46, 3\n"
"s_and_b32 s48, s48, 3\n"
"s_mul_i32 s91, s48, 98304\n"
"v_mfma_f32_16x16x32_bf16 v[164:167], a[28:31], a[40:43], v[164:167]\n"
"s_add_u32 s91, s91, s47\n"
"s_lshl_b32 s68, s48, 18\n"
"s_add_u32 s68, s68, s49\n"
"v_mfma_f32_16x16x32_bf16 v[172:175], a[28:31], a[44:47], v[172:175]\n"
"s_lshl_b32 s47, s44, 6\n"
"s_add_u32 s47, s47, s52\n"
"s_lshl_b32 s47, s47, 7\n"
"v_mfma_f32_16x16x32_bf16 v[180:183], a[28:31], a[48:51], v[180:183]\n"
"s_add_u32 s99, s47, 37748736\n"
"s_lshl_b32 s48, s45, 2\n"
"s_add_u32 s48, s48, s46\n"
"v_mfma_f32_16x16x32_bf16 v[188:191], a[28:31], a[52:55], v[188:191]\n"
"s_lshl_b32 s48, s48, 9\n"
"s_add_u32 s98, s99, s48\n"
"s_lshl_b32 s47, s44, 2\n"
"s_add_u32 s47, s47, s46\n"
"s_lshl_b32 s47, s47, 2\n"
"s_add_u32 s47, s47, s52\n"
"s_mul_i32 s47, s47, 49152\n"
"s_add_u32 s47, s47, 25165824\n"
"s_mul_i32 s48, s45, 3072\n"
"s_add_u32 s40, s47, s48\n"
"s_mul_i32 s48, s45, 12288\n"
"s_add_u32 s41, s47, s48\n"
"s_lshl_b32 s47, s44, 2\n"
"s_add_u32 s47, s47, s52\n"
"s_mov_b32 s55, 12288\n"
"s_lshl_b32 s47, s47, 7\n"
"ds_read_b64_tr_b16 a[84:85], v209 offset:1544\n"
"ds_read_b64_tr_b16 a[86:87], v209 offset:1800\n"
"buffer_load_dwordx4 v196, s[24:27], s55 offen offset:2048 nt lds\n"
"s_add_u32 s42, s47, 37879808\n"
"v_add_u32 v209, 32768, v209\n"
"s_waitcnt vmcnt(8) lgkmcnt(0)\n"
"s_barrier\n"
"v_mfma_f32_16x16x32_bf16 v[0:3], a[56:59], a[88:91], v[0:3]\n"
"ds_read_b64_tr_b16 a[0:1], v209\n"
"v_mfma_f32_16x16x32_bf16 v[8:11], a[56:59], a[92:95], v[8:11]\n"
"s_add_u32 m0, s73, 16384\n"
"s_mov_b32 s55, 0\n"
"ds_read_b64_tr_b16 a[2:3], v209 offset:256\n"
"v_mfma_f32_16x16x32_bf16 v[16:19], a[56:59], a[96:99], v[16:19]\n"
"v_mfma_f32_16x16x32_bf16 v[24:27], a[56:59], a[100:103], v[24:27]\n"
"ds_read_b128 a[32:35], v208\n"
"v_mfma_f32_16x16x32_bf16 v[32:35], a[56:59], a[104:107], v[32:35]\n"
"buffer_load_dwordx4 v197, s[28:31], s55 offen nt lds\n"
"v_mfma_f32_16x16x32_bf16 v[40:43], a[56:59], a[108:111], v[40:43]\n"
"ds_read_b128 a[36:39], v208 offset:2048\n"
"v_mfma_f32_16x16x32_bf16 v[4:7], a[60:63], a[88:91], v[4:7]\n"
"v_mfma_f32_16x16x32_bf16 v[12:15], a[60:63], a[92:95], v[12:15]\n"
"ds_read_b128 a[40:43], v208 offset:4096\n"
"v_mfma_f32_16x16x32_bf16 v[20:23], a[60:63], a[96:99], v[20:23]\n"
"ds_read_b128 a[44:47], v208 offset:6144\n"
"v_mfma_f32_16x16x32_bf16 v[28:31], a[60:63], a[100:103], v[28:31]\n"
"v_mfma_f32_16x16x32_bf16 v[36:39], a[60:63], a[104:107], v[36:39]\n"
"ds_read_b128 a[48:51], v208 offset:8192\n"
"v_mfma_f32_16x16x32_bf16 v[44:47], a[60:63], a[108:111], v[44:47]\n"
"v_mfma_f32_16x16x32_bf16 v[48:51], a[64:67], a[88:91], v[48:51]\n"
"ds_read_b128 a[52:55], v208 offset:10240\n"
"v_add_u32 v208, 512, v208\n"
"v_mfma_f32_16x16x32_bf16 v[56:59], a[64:67], a[92:95], v[56:59]\n"
"v_mfma_f32_16x16x32_bf16 v[64:67], a[64:67], a[96:99], v[64:67]\n"
"ds_read_b64_tr_b16 a[4:5], v209 offset:8\n"
"v_mfma_f32_16x16x32_bf16 v[72:75], a[64:67], a[100:103], v[72:75]\n"
"ds_read_b64_tr_b16 a[6:7], v209 offset:264\n"
"v_mfma_f32_16x16x32_bf16 v[80:83], a[64:67], a[104:107], v[80:83]\n"
"v_mfma_f32_16x16x32_bf16 v[88:91], a[64:67], a[108:111], v[88:91]\n"
"ds_read_b64_tr_b16 a[8:9], v209 offset:512\n"
"v_mfma_f32_16x16x32_bf16 v[52:55], a[68:71], a[88:91], v[52:55]\n"
"v_mfma_f32_16x16x32_bf16 v[60:63], a[68:71], a[92:95], v[60:63]\n"
"ds_read_b64_tr_b16 a[10:11], v209 offset:768\n"
"v_mfma_f32_16x16x32_bf16 v[68:71], a[68:71], a[96:99], v[68:71]\n"
"v_mfma_f32_16x16x32_bf16 v[76:79], a[68:71], a[100:103], v[76:79]\n"
"ds_read_b64_tr_b16 a[12:13], v209 offset:520\n"
"v_mfma_f32_16x16x32_bf16 v[84:87], a[68:71], a[104:107], v[84:87]\n"
"ds_read_b64_tr_b16 a[14:15], v209 offset:776\n"
"v_mfma_f32_16x16x32_bf16 v[92:95], a[68:71], a[108:111], v[92:95]\n"
"s_add_u32 m0, s73, 16384\n"
"s_mov_b32 s55, 0\n"
"v_mfma_f32_16x16x32_bf16 v[96:99], a[72:75], a[88:91], v[96:99]\n"
"ds_read_b64_tr_b16 a[16:17], v209 offset:1024\n"
"v_mfma_f32_16x16x32_bf16 v[104:107], a[72:75], a[92:95], v[104:107]\n"
"v_mfma_f32_16x16x32_bf16 v[112:115], a[72:75], a[96:99], v[112:115]\n"
"buffer_load_dwordx4 v197, s[28:31], s55 offen offset:2048 nt lds\n"
"v_mfma_f32_16x16x32_bf16 v[120:123], a[72:75], a[100:103], v[120:123]\n"
"ds_read_b64_tr_b16 a[18:19], v209 offset:1280\n"
"v_mfma_f32_16x16x32_bf16 v[128:131], a[72:75], a[104:107], v[128:131]\n"
"ds_read_b64_tr_b16 a[20:21], v209 offset:1032\n"
"v_mfma_f32_16x16x32_bf16 v[136:139], a[72:75], a[108:111], v[136:139]\n"
"ds_read_b64_tr_b16 a[22:23], v209 offset:1288\n"
"v_mfma_f32_16x16x32_bf16 v[100:103], a[76:79], a[88:91], v[100:103]\n"
"v_mfma_f32_16x16x32_bf16 v[108:111], a[76:79], a[92:95], v[108:111]\n"
"ds_read_b64_tr_b16 a[24:25], v209 offset:1536\n"
"v_mfma_f32_16x16x32_bf16 v[116:119], a[76:79], a[96:99], v[116:119]\n"
"v_mfma_f32_16x16x32_bf16 v[124:127], a[76:79], a[100:103], v[124:127]\n"
"ds_read_b64_tr_b16 a[26:27], v209 offset:1792\n"
"v_mfma_f32_16x16x32_bf16 v[132:135], a[76:79], a[104:107], v[132:135]\n"
"v_mfma_f32_16x16x32_bf16 v[140:143], a[76:79], a[108:111], v[140:143]\n"
"ds_read_b64_tr_b16 a[28:29], v209 offset:1544\n"
"v_mfma_f32_16x16x32_bf16 v[144:147], a[80:83], a[88:91], v[144:147]\n"
"ds_read_b64_tr_b16 a[30:31], v209 offset:1800\n"
"v_mfma_f32_16x16x32_bf16 v[152:155], a[80:83], a[92:95], v[152:155]\n"
"v_add_u32 v209, 32768, v209\n"
"v_mfma_f32_16x16x32_bf16 v[160:163], a[80:83], a[96:99], v[160:163]\n"
"v_mfma_f32_16x16x32_bf16 v[168:171], a[80:83], a[100:103], v[168:171]\n"
"v_mfma_f32_16x16x32_bf16 v[176:179], a[80:83], a[104:107], v[176:179]\n"
"v_mfma_f32_16x16x32_bf16 v[184:187], a[80:83], a[108:111], v[184:187]\n"
"v_mfma_f32_16x16x32_bf16 v[148:151], a[84:87], a[88:91], v[148:151]\n"
"v_mfma_f32_16x16x32_bf16 v[156:159], a[84:87], a[92:95], v[156:159]\n"
"v_mfma_f32_16x16x32_bf16 v[164:167], a[84:87], a[96:99], v[164:167]\n"
"v_mfma_f32_16x16x32_bf16 v[172:175], a[84:87], a[100:103], v[172:175]\n"
"v_mfma_f32_16x16x32_bf16 v[180:183], a[84:87], a[104:107], v[180:183]\n"
"v_mfma_f32_16x16x32_bf16 v[188:191], a[84:87], a[108:111], v[188:191]\n"
"s_waitcnt lgkmcnt(0)\n"
"s_barrier\n"
"v_mfma_f32_16x16x32_bf16 v[0:3], a[0:3], a[32:35], v[0:3]\n"
"ds_read_b64_tr_b16 a[56:57], v209\n"
"v_mov_b32 v204, 0x3f803f80\n"
"v_mfma_f32_16x16x32_bf16 v[8:11], a[0:3], a[36:39], v[8:11]\n"
"s_add_u32 m0, s73, 20480\n"
"s_mov_b32 s55, 4096\n"
"ds_read_b64_tr_b16 a[58:59], v209 offset:256\n"
"v_mfma_f32_16x16x32_bf16 v[16:19], a[0:3], a[40:43], v[16:19]\n"
"v_mov_b32 v205, 0x3f803f80\n"
"v_mov_b32 v206, 0x3f803f80\n"
"v_mfma_f32_16x16x32_bf16 v[24:27], a[0:3], a[44:47], v[24:27]\n"
"ds_read_b128 a[88:91], v208\n"
"v_mov_b32 v207, 0x3f803f80\n"
"v_mfma_f32_16x16x32_bf16 v[32:35], a[0:3], a[48:51], v[32:35]\n"
"buffer_load_dwordx4 v197, s[28:31], s55 offen nt lds\n"
"v_mfma_f32_16x16x32_bf16 v[40:43], a[0:3], a[52:55], v[40:43]\n"
"ds_read_b128 a[92:95], v208 offset:2048\n"
"v_mfma_f32_16x16x32_bf16 v[4:7], a[4:7], a[32:35], v[4:7]\n"
"v_mfma_f32_16x16x32_bf16 v[12:15], a[4:7], a[36:39], v[12:15]\n"
"ds_read_b128 a[96:99], v208 offset:4096\n"
"v_mfma_f32_16x16x32_bf16 v[20:23], a[4:7], a[40:43], v[20:23]\n"
"ds_read_b128 a[100:103], v208 offset:6144\n"
"v_mfma_f32_16x16x32_bf16 v[28:31], a[4:7], a[44:47], v[28:31]\n"
"v_mfma_f32_16x16x32_bf16 v[36:39], a[4:7], a[48:51], v[36:39]\n"
"ds_read_b128 a[104:107], v208 offset:8192\n"
"v_mfma_f32_16x16x32_bf16 v[44:47], a[4:7], a[52:55], v[44:47]\n"
"v_mfma_f32_16x16x32_bf16 v[48:51], a[8:11], a[32:35], v[48:51]\n"
"ds_read_b128 a[108:111], v208 offset:10240\n"
"v_add_u32 v208, 0xfffe7e00, v208\n"
"v_mfma_f32_16x16x32_bf16 v[56:59], a[8:11], a[36:39], v[56:59]\n"
"v_mfma_f32_16x16x32_bf16 v[64:67], a[8:11], a[40:43], v[64:67]\n"
"ds_read_b64_tr_b16 a[60:61], v209 offset:8\n"
"v_mfma_f32_16x16x32_bf16 v[72:75], a[8:11], a[44:47], v[72:75]\n"
"ds_read_b64_tr_b16 a[62:63], v209 offset:264\n"
"v_mfma_f32_16x16x32_bf16 v[80:83], a[8:11], a[48:51], v[80:83]\n"
"v_mfma_f32_16x16x32_bf16 v[88:91], a[8:11], a[52:55], v[88:91]\n"
"ds_read_b64_tr_b16 a[64:65], v209 offset:512\n"
"v_mfma_f32_16x16x32_bf16 v[52:55], a[12:15], a[32:35], v[52:55]\n"
"v_mfma_f32_16x16x32_bf16 v[60:63], a[12:15], a[36:39], v[60:63]\n"
"ds_read_b64_tr_b16 a[66:67], v209 offset:768\n"
"v_mfma_f32_16x16x32_bf16 v[68:71], a[12:15], a[40:43], v[68:71]\n"
"v_mfma_f32_16x16x32_bf16 v[76:79], a[12:15], a[44:47], v[76:79]\n"
"ds_read_b64_tr_b16 a[68:69], v209 offset:520\n"
"v_mfma_f32_16x16x32_bf16 v[84:87], a[12:15], a[48:51], v[84:87]\n"
"ds_read_b64_tr_b16 a[70:71], v209 offset:776\n"
"v_mfma_f32_16x16x32_bf16 v[92:95], a[12:15], a[52:55], v[92:95]\n"
"s_add_u32 m0, s73, 20480\n"
"s_mov_b32 s55, 4096\n"
"v_mfma_f32_16x16x32_bf16 v[96:99], a[16:19], a[32:35], v[96:99]\n"
"ds_read_b64_tr_b16 a[72:73], v209 offset:1024\n"
"v_mfma_f32_16x16x32_bf16 v[104:107], a[16:19], a[36:39], v[104:107]\n"
"v_mfma_f32_16x16x32_bf16 v[112:115], a[16:19], a[40:43], v[112:115]\n"
"buffer_load_dwordx4 v197, s[28:31], s55 offen offset:2048 nt lds\n"
"v_mfma_f32_16x16x32_bf16 v[120:123], a[16:19], a[44:47], v[120:123]\n"
"ds_read_b64_tr_b16 a[74:75], v209 offset:1280\n"
"v_mfma_f32_16x16x32_bf16 v[128:131], a[16:19], a[48:51], v[128:131]\n"
"ds_read_b64_tr_b16 a[76:77], v209 offset:1032\n"
"v_mfma_f32_16x16x32_bf16 v[136:139], a[16:19], a[52:55], v[136:139]\n"
"ds_read_b64_tr_b16 a[78:79], v209 offset:1288\n"
"v_mfma_f32_16x16x32_bf16 v[100:103], a[20:23], a[32:35], v[100:103]\n"
"v_mfma_f32_16x16x32_bf16 v[108:111], a[20:23], a[36:39], v[108:111]\n"
"ds_read_b64_tr_b16 a[80:81], v209 offset:1536\n"
"v_mfma_f32_16x16x32_bf16 v[116:119], a[20:23], a[40:43], v[116:119]\n"
"v_mfma_f32_16x16x32_bf16 v[124:127], a[20:23], a[44:47], v[124:127]\n"
"ds_read_b64_tr_b16 a[82:83], v209 offset:1792\n"
"v_mfma_f32_16x16x32_bf16 v[132:135], a[20:23], a[48:51], v[132:135]\n"
"v_mfma_f32_16x16x32_bf16 v[140:143], a[20:23], a[52:55], v[140:143]\n"
"ds_read_b64_tr_b16 a[84:85], v209 offset:1544\n"
"v_mfma_f32_16x16x32_bf16 v[144:147], a[24:27], a[32:35], v[144:147]\n"
"ds_read_b64_tr_b16 a[86:87], v209 offset:1800\n"
"v_mfma_f32_16x16x32_bf16 v[152:155], a[24:27], a[36:39], v[152:155]\n"
"v_add_u32 v209, 0xfffe0000, v209\n"
"v_mfma_f32_16x16x32_bf16 v[160:163], a[24:27], a[40:43], v[160:163]\n"
"v_mfma_f32_16x16x32_bf16 v[168:171], a[24:27], a[44:47], v[168:171]\n"
"v_mfma_f32_16x16x32_bf16 v[176:179], a[24:27], a[48:51], v[176:179]\n"
"v_mfma_f32_16x16x32_bf16 v[184:187], a[24:27], a[52:55], v[184:187]\n"
"v_mfma_f32_16x16x32_bf16 v[148:151], a[28:31], a[32:35], v[148:151]\n"
"v_mfma_f32_16x16x32_bf16 v[156:159], a[28:31], a[36:39], v[156:159]\n"
"v_mfma_f32_16x16x32_bf16 v[164:167], a[28:31], a[40:43], v[164:167]\n"
"v_mfma_f32_16x16x32_bf16 v[172:175], a[28:31], a[44:47], v[172:175]\n"
"v_mfma_f32_16x16x32_bf16 v[180:183], a[28:31], a[48:51], v[180:183]\n"
"v_mfma_f32_16x16x32_bf16 v[188:191], a[28:31], a[52:55], v[188:191]\n"
"s_waitcnt vmcnt(4) lgkmcnt(0)\n"
"s_barrier\n"
"s_branch .Lq_join\n"
".Lq_B:\n"
"s_mul_i32 s55, s46, 49152\n"
"s_and_b32 s57, s44, 7\n"
"s_lshl_b32 s57, s57, 16\n"
"s_add_u32 s55, s55, 256\n"
"s_add_u32 s57, s57, 524288\n"
"s_cmp_eq_u32 s54, 0\n"
"s_cselect_b32 s40, s32, s36\n"
"s_cselect_b32 s41, s33, s37\n"
"s_cselect_b32 s42, s34, s38\n"
"s_cselect_b32 s43, s35, s39\n"
"s_mov_b32 s48, 128\n"
"s_cselect_b32 s48, s48, 524288\n"
"s_cselect_b32 s49, s55, s57\n"
"v_min_u32 v199, 47, v212\n"
"v_lshlrev_b32 v199, 10, v199\n"
"v_bfe_u32 v200, v212, 2, 3\n"
"v_lshlrev_b32 v200, 13, v200\n"
"v_and_b32 v198, 3, v212\n"
"v_lshl_add_u32 v200, v198, 7, v200\n"
"s_cselect_b64 vcc, -1, 0\n"
"v_cndmask_b32 v213, v200, v199, vcc\n"
"s_sub_u32 s80, s24, 131072\n"
"s_subb_u32 s81, s25, 0\n"
"s_mov_b32 s82, 262144\n"
"s_mov_b32 s83, 0x20000\n"
"s_sub_u32 s88, s28, 131072\n"
"s_subb_u32 s89, s29, 0\n"
"s_mov_b32 s90, 262144\n"
"s_mov_b32 s91, 0x20000\n"
"s_lshl_b32 s55, s54, 10\n"
"s_add_u32 s55, s55, 49152\n"
"v_lshl_add_u32 v219, v212, 4, s55\n"
"buffer_load_dword v214, v213, s[40:43], s49 offen\n"
"s_add_u32 s49, s49, s48\n"
"buffer_load_dword v214, v213, s[40:43], s49 offen\n"
"s_add_u32 s49, s49, s48\n"
"buffer_load_dword v214, v213, s[40:43], s49 offen\n"
"s_add_u32 s49, s49, s48\n"
"s_waitcnt vmcnt(19)\n"
"s_barrier\n"
"ds_read_b64_tr_b16 a[0:1], v209\n"
"ds_read_b64_tr_b16 a[2:3], v209 offset:256\n"
"ds_read_b128 a[32:35], v208\n"
"ds_read_b128 a[36:39], v208 offset:2048\n"
"ds_read_b128 a[40:43], v208 offset:4096\n"
"ds_read_b128 a[44:47], v208 offset:6144\n"
"ds_read_b128 a[48:51], v208 offset:8192\n"
"ds_read_b128 a[52:55], v208 offset:10240\n"
"ds_read_b64_tr_b16 a[4:5], v209 offset:8\n"
"ds_read_b64_tr_b16 a[6:7], v209 offset:264\n"
"ds_read_b64_tr_b16 a[8:9], v209 offset:512\n"
"s_waitcnt lgkmcnt(0)\n"
"ds_read_b64_tr_b16 a[10:11], v209 offset:768\n"
"ds_read_b64_tr_b16 a[12:13], v209 offset:520\n"
"ds_read_b64_tr_b16 a[14:15], v209 offset:776\n"
"ds_read_b64_tr_b16 a[16:17], v209 offset:1024\n"
"ds_read_b64_tr_b16 a[18:19], v209 offset:1280\n"
"ds_read_b64_tr_b16 a[20:21], v209 offset:1032\n"
"ds_read_b64_tr_b16 a[22:23], v209 offset:1288\n"
"ds_read_b64_tr_b16 a[24:25], v209 offset:1536\n"
"ds_read_b64_tr_b16 a[26:27], v209 offset:1792\n"
"ds_read_b64_tr_b16 a[28:29], v209 offset:1544\n"
"ds_read_b64_tr_b16 a[30:31], v209 offset:1800\n"
"v_add_u32 v209, 0xfffe0000, v209\n"
"v_add_u32 v208, 512, v208\n"
"s_waitcnt vmcnt(15) lgkmcnt(0)\n"
"s_barrier\n"
"v_mfma_f32_16x16x32_bf16 v[0:3], a[0:3], a[32:35], 0\n"
"s_mov_b32 s61, 0xfffe7e00\n"
"s_cmp_eq_u32 s54, 0\n"
"ds_read_b64_tr_b16 a[56:57], v209\n"
"v_mfma_f32_16x16x32_bf16 v[8:11], a[0:3], a[36:39], 0\n"
"s_cselect_b32 s61, s61, 65024\n"
"ds_read_b64_tr_b16 a[58:59], v209 offset:256\n"
"v_mfma_f32_16x16x32_bf16 v[16:19], a[0:3], a[40:43], 0\n"
"v_mfma_f32_16x16x32_bf16 v[24:27], a[0:3], a[44:47], 0\n"
"ds_read_b128 a[88:91], v208\n"
"v_mfma_f32_16x16x32_bf16 v[32:35], a[0:3], a[48:51], 0\n"
"v_mfma_f32_16x16x32_bf16 v[40:43], a[0:3], a[52:55], 0\n"
"ds_read_b128 a[92:95], v208 offset:2048\n"
"v_mfma_f32_16x16x32_bf16 v[4:7], a[4:7], a[32:35], 0\n"
"v_mfma_f32_16x16x32_bf16 v[12:15], a[4:7], a[36:39], 0\n"
"ds_read_b128 a[96:99], v208 offset:4096\n"
"v_mfma_f32_16x16x32_bf16 v[20:23], a[4:7], a[40:43], 0\n"
"ds_read_b128 a[100:103], v208 offset:6144\n"
"v_mfma_f32_16x16x32_bf16 v[28:31], a[4:7], a[44:47], 0\n"
"v_mfma_f32_16x16x32_bf16 v[36:39], a[4:7], a[48:51], 0\n"
"ds_read_b128 a[104:107], v208 offset:8192\n"
"v_mfma_f32_16x16x32_bf16 v[44:47], a[4:7], a[52:55], 0\n"
"v_mfma_f32_16x16x32_bf16 v[48:51], a[8:11], a[32:35], 0\n"
"ds_read_b128 a[108:111], v208 offset:10240\n"
"v_add_u32 v208, s61, v208\n"
"v_mfma_f32_16x16x32_bf16 v[56:59], a[8:11], a[36:39], 0\n"
"v_mfma_f32_16x16x32_bf16 v[64:67], a[8:11], a[40:43], 0\n"
"ds_read_b64_tr_b16 a[60:61], v209 offset:8\n"
"v_mfma_f32_16x16x32_bf16 v[72:75], a[8:11], a[44:47], 0\n"
"ds_read_b64_tr_b16 a[62:63], v209 offset:264\n"
"v_mfma_f32_16x16x32_bf16 v[80:83], a[8:11], a[48:51], 0\n"
"v_mfma_f32_16x16x32_bf16 v[88:91], a[8:11], a[52:55], 0\n"
"ds_read_b64_tr_b16 a[64:65], v209 offset:512\n"
"v_mfma_f32_16x16x32_bf16 v[52:55], a[12:15], a[32:35], 0\n"
"v_mfma_f32_16x16x32_bf16 v[60:63], a[12:15], a[36:39], 0\n"
"ds_read_b64_tr_b16 a[66:67], v209 offset:768\n"
"v_mfma_f32_16x16x32_bf16 v[68:71], a[12:15], a[40:43], 0\n"
"v_mfma_f32_16x16x32_bf16 v[76:79], a[12:15], a[44:47], 0\n"
"ds_read_b64_tr_b16 a[68:69], v209 offset:520\n"
"v_mfma_f32_16x16x32_bf16 v[84:87], a[12:15], a[48:51], 0\n"
"ds_read_b64_tr_b16 a[70:71], v209 offset:776\n"
"v_mfma_f32_16x16x32_bf16 v[92:95], a[12:15], a[52:55], 0\n"
"v_mfma_f32_16x16x32_bf16 v[96:99], a[16:19], a[32:35], 0\n"
"ds_read_b64_tr_b16 a[72:73], v209 offset:1024\n"
"v_mfma_f32_16x16x32_bf16 v[104:107], a[16:19], a[36:39], 0\n"
"v_mfma_f32_16x16x32_bf16 v[112:115], a[16:19], a[40:43], 0\n"
"ds_read_b64_tr_b16 a[74:75], v209 offset:1280\n"
"v_mfma_f32_16x16x32_bf16 v[120:123], a[16:19], a[44:47], 0\n"
"v_mfma_f32_16x16x32_bf16 v[128:131], a[16:19], a[48:51], 0\n"
"ds_read_b64_tr_b16 a[76:77], v209 offset:1032\n"
"v_mfma_f32_16x16x32_bf16 v[136:139], a[16:19], a[52:55], 0\n"
"ds_read_b64_tr_b16 a[78:79], v209 offset:1288\n"
"v_mfma_f32_16x16x32_bf16 v[100:103], a[20:23], a[32:35], 0\n"
"v_mfma_f32_16x16x32_bf16 v[108:111], a[20:23], a[36:39], 0\n"
"ds_read_b64_tr_b16 a[80:81], v209 offset:1536\n"
"v_mfma_f32_16x16x32_bf16 v[116:119], a[20:23], a[40:43], 0\n"
"v_mfma_f32_16x16x32_bf16 v[124:127], a[20:23], a[44:47], 0\n"
"ds_read_b64_tr_b16 a[82:83], v209 offset:1792\n"
"v_mfma_f32_16x16x32_bf16 v[132:135], a[20:23], a[48:51], 0\n"
"v_mfma_f32_16x16x32_bf16 v[140:143], a[20:23], a[52:55], 0\n"
"ds_read_b64_tr_b16 a[84:85], v209 offset:1544\n"
"v_mfma_f32_16x16x32_bf16 v[144:147], a[24:27], a[32:35], 0\n"
"ds_read_b64_tr_b16 a[86:87], v209 offset:1800\n"
"v_mfma_f32_16x16x32_bf16 v[152:155], a[24:27], a[36:39], 0\n"
"v_add_u32 v209, 32768, v209\n"
"v_mfma_f32_16x16x32_bf16 v[160:163], a[24:27], a[40:43], 0\n"
"v_mfma_f32_16x16x32_bf16 v[168:171], a[24:27], a[44:47], 0\n"
"v_mfma_f32_16x16x32_bf16 v[176:179], a[24:27], a[48:51], 0\n"
"v_mfma_f32_16x16x32_bf16 v[184:187], a[24:27], a[52:55], 0\n"
"v_mfma_f32_16x16x32_bf16 v[148:151], a[28:31], a[32:35], 0\n"
"v_mfma_f32_16x16x32_bf16 v[156:159], a[28:31], a[36:39], 0\n"
"v_mfma_f32_16x16x32_bf16 v[164:167], a[28:31], a[40:43], 0\n"
"v_mfma_f32_16x16x32_bf16 v[172:175], a[28:31], a[44:47], 0\n"
"v_mfma_f32_16x16x32_bf16 v[180:183], a[28:31], a[48:51], 0\n"
"v_mfma_f32_16x16x32_bf16 v[188:191], a[28:31], a[52:55], 0\n"
"s_waitcnt vmcnt(5) lgkmcnt(0)\n"
"s_barrier\n"
"v_mfma_f32_16x16x32_bf16 v[0:3], a[56:59], a[88:91], v[0:3]\n"
"ds_read_b64_tr_b16 a[0:1], v209\n"
"v_mfma_f32_16x16x32_bf16 v[8:11], a[56:59], a[92:95], v[8:11]\n"
"buffer_load_dword v214, v213, s[40:43], s49 offen\n"
"v_mfma_f32_16x16x32_bf16 v[16:19], a[56:59], a[96:99], v[16:19]\n"
"s_add_u32 s49, s49, s48\n"
"ds_read_b64_tr_b16 a[2:3], v209 offset:256\n"
"v_mfma_f32_16x16x32_bf16 v[24:27], a[56:59], a[100:103], v[24:27]\n"
"buffer_load_dword v214, v213, s[40:43], s49 offen\n"
"v_mfma_f32_16x16x32_bf16 v[32:35], a[56:59], a[104:107], v[32:35]\n"
"s_add_u32 s49, s49, s48\n"
"ds_read_b128 a[32:35], v208\n"
"v_mfma_f32_16x16x32_bf16 v[40:43], a[56:59], a[108:111], v[40:43]\n"
"ds_read_b128 a[36:39], v208 offset:2048\n"
"v_mfma_f32_16x16x32_bf16 v[4:7], a[60:63], a[88:91], v[4:7]\n"
"v_mfma_f32_16x16x32_bf16 v[12:15], a[60:63], a[92:95], v[12:15]\n"
"ds_read_b128 a[40:43], v208 offset:4096\n"
"v_mfma_f32_16x16x32_bf16 v[20:23], a[60:63], a[96:99], v[20:23]\n"
"ds_read_b128 a[44:47], v208 offset:6144\n"
"v_mfma_f32_16x16x32_bf16 v[28:31], a[60:63], a[100:103], v[28:31]\n"
"v_mfma_f32_16x16x32_bf16 v[36:39], a[60:63], a[104:107], v[36:39]\n"
"ds_read_b128 a[48:51], v208 offset:8192\n"
"v_mfma_f32_16x16x32_bf16 v[44:47], a[60:63], a[108:111], v[44:47]\n"
"v_mfma_f32_16x16x32_bf16 v[48:51], a[64:67], a[88:91], v[48:51]\n"
"ds_read_b128 a[52:55], v208 offset:10240\n"
"v_add_u32 v208, 512, v208\n"
"v_mfma_f32_16x16x32_bf16 v[56:59], a[64:67], a[92:95], v[56:59]\n"
"v_mfma_f32_16x16x32_bf16 v[64:67], a[64:67], a[96:99], v[64:67]\n"
"ds_read_b64_tr_b16 a[4:5], v209 offset:8\n"
"v_mfma_f32_16x16x32_bf16 v[72:75], a[64:67], a[100:103], v[72:75]\n"
"ds_read_b64_tr_b16 a[6:7], v209 offset:264\n"
"v_mfma_f32_16x16x32_bf16 v[80:83], a[64:67], a[104:107], v[80:83]\n"
"v_mfma_f32_16x16x32_bf16 v[88:91], a[64:67], a[108:111], v[88:91]\n"
"ds_read_b64_tr_b16 a[8:9], v209 offset:512\n"
"v_mfma_f32_16x16x32_bf16 v[52:55], a[68:71], a[88:91], v[52:55]\n"
"v_mfma_f32_16x16x32_bf16 v[60:63], a[68:71], a[92:95], v[60:63]\n"
"ds_read_b64_tr_b16 a[10:11], v209 offset:768\n"
"v_mfma_f32_16x16x32_bf16 v[68:71], a[68:71], a[96:99], v[68:71]\n"
"v_mfma_f32_16x16x32_bf16 v[76:79], a[68:71], a[100:103], v[76:79]\n"
"ds_read_b64_tr_b16 a[12:13], v209 offset:520\n"
"v_mfma_f32_16x16x32_bf16 v[84:87], a[68:71], a[104:107], v[84:87]\n"
"ds_read_b64_tr_b16 a[14:15], v209 offset:776\n"
"v_mfma_f32_16x16x32_bf16 v[92:95], a[68:71], a[108:111], v[92:95]\n"
"v_mfma_f32_16x16x32_bf16 v[96:99], a[72:75], a[88:91], v[96:99]\n"
"ds_read_b64_tr_b16 a[16:17], v209 offset:1024\n"
"v_mfma_f32_16x16x32_bf16 v[104:107], a[72:75], a[92:95], v[104:107]\n"
"v_mfma_f32_16x16x32_bf16 v[112:115], a[72:75], a[96:99], v[112:115]\n"
"ds_read_b64_tr_b16 a[18:19], v209 offset:1280\n"
"v_mfma_f32_16x16x32_bf16 v[120:123], a[72:75], a[100:103], v[120:123]\n"
"v_mfma_f32_16x16x32_bf16 v[128:131], a[72:75], a[104:107], v[128:131]\n"
"ds_read_b64_tr_b16 a[20:21], v209 offset:1032\n"
"v_mfma_f32_16x16x32_bf16 v[136:139], a[72:75], a[108:111], v[136:139]\n"
"ds_read_b64_tr_b16 a[22:23], v209 offset:1288\n"
"v_mfma_f32_16x16x32_bf16 v[100:103], a[76:79], a[88:91], v[100:103]\n"
"v_mfma_f32_16x16x32_bf16 v[108:111], a[76:79], a[92:95], v[108:111]\n"
"ds_read_b64_tr_b16 a[24:25], v209 offset:1536\n"
"v_mfma_f32_16x16x32_bf16 v[116:119], a[76:79], a[96:99], v[116:119]\n"
"v_mfma_f32_16x16x32_bf16 v[124:127], a[76:79], a[100:103], v[124:127]\n"
"ds_read_b64_tr_b16 a[26:27], v209 offset:1792\n"
"v_mfma_f32_16x16x32_bf16 v[132:135], a[76:79], a[104:107], v[132:135]\n"
"v_mfma_f32_16x16x32_bf16 v[140:143], a[76:79], a[108:111], v[140:143]\n"
"ds_read_b64_tr_b16 a[28:29], v209 offset:1544\n"
"v_mfma_f32_16x16x32_bf16 v[144:147], a[80:83], a[88:91], v[144:147]\n"
"ds_read_b64_tr_b16 a[30:31], v209 offset:1800\n"
"v_mfma_f32_16x16x32_bf16 v[152:155], a[80:83], a[92:95], v[152:155]\n"
"v_add_u32 v209, 32768, v209\n"
"v_mfma_f32_16x16x32_bf16 v[160:163], a[80:83], a[96:99], v[160:163]\n"
"v_mfma_f32_16x16x32_bf16 v[168:171], a[80:83], a[100:103], v[168:171]\n"
"v_mfma_f32_16x16x32_bf16 v[176:179], a[80:83], a[104:107], v[176:179]\n"
"v_mfma_f32_16x16x32_bf16 v[184:187], a[80:83], a[108:111], v[184:187]\n"
"v_mfma_f32_16x16x32_bf16 v[148:151], a[84:87], a[88:91], v[148:151]\n"
"v_mfma_f32_16x16x32_bf16 v[156:159], a[84:87], a[92:95], v[156:159]\n"
"v_mfma_f32_16x16x32_bf16 v[164:167], a[84:87], a[96:99], v[164:167]\n"
"v_mfma_f32_16x16x32_bf16 v[172:175], a[84:87], a[100:103], v[172:175]\n"
"v_mfma_f32_16x16x32_bf16 v[180:183], a[84:87], a[104:107], v[180:183]\n"
"v_mfma_f32_16x16x32_bf16 v[188:191], a[84:87], a[108:111], v[188:191]\n"
"s_waitcnt lgkmcnt(0)\n"
"s_barrier\n"
"v_mfma_f32_16x16x32_bf16 v[0:3], a[0:3], a[32:35], v[0:3]\n"
"ds_read_b64_tr_b16 a[56:57], v209\n"
"v_mfma_f32_16x16x32_bf16 v[8:11], a[0:3], a[36:39], v[8:11]\n"
"ds_read_b64_tr_b16 a[58:59], v209 offset:256\n"
"v_mfma_f32_16x16x32_bf16 v[16:19], a[0:3], a[40:43], v[16:19]\n"
"v_mfma_f32_16x16x32_bf16 v[24:27], a[0:3], a[44:47], v[24:27]\n"
"ds_read_b128 a[88:91], v208\n"
"v_mfma_f32_16x16x32_bf16 v[32:35], a[0:3], a[48:51], v[32:35]\n"
"v_mfma_f32_16x16x32_bf16 v[40:43], a[0:3], a[52:55], v[40:43]\n"
"ds_read_b128 a[92:95], v208 offset:2048\n"
"v_mfma_f32_16x16x32_bf16 v[4:7], a[4:7], a[32:35], v[4:7]\n"
"v_mfma_f32_16x16x32_bf16 v[12:15], a[4:7], a[36:39], v[12:15]\n"
"ds_read_b128 a[96:99], v208 offset:4096\n"
"v_mfma_f32_16x16x32_bf16 v[20:23], a[4:7], a[40:43], v[20:23]\n"
"ds_read_b128 a[100:103], v208 offset:6144\n"
"v_mfma_f32_16x16x32_bf16 v[28:31], a[4:7], a[44:47], v[28:31]\n"
"v_mfma_f32_16x16x32_bf16 v[36:39], a[4:7], a[48:51], v[36:39]\n"
"ds_read_b128 a[104:107], v208 offset:8192\n"
"v_mfma_f32_16x16x32_bf16 v[44:47], a[4:7], a[52:55], v[44:47]\n"
"v_mfma_f32_16x16x32_bf16 v[48:51], a[8:11], a[32:35], v[48:51]\n"
"ds_read_b128 a[108:111], v208 offset:10240\n"
"v_add_u32 v208, 65024, v208\n"
"v_mfma_f32_16x16x32_bf16 v[56:59], a[8:11], a[36:39], v[56:59]\n"
"v_mfma_f32_16x16x32_bf16 v[64:67], a[8:11], a[40:43], v[64:67]\n"
"ds_read_b64_tr_b16 a[60:61], v209 offset:8\n"
"v_mfma_f32_16x16x32_bf16 v[72:75], a[8:11], a[44:47], v[72:75]\n"
"ds_read_b64_tr_b16 a[62:63], v209 offset:264\n"
"v_mfma_f32_16x16x32_bf16 v[80:83], a[8:11], a[48:51], v[80:83]\n"
"v_mfma_f32_16x16x32_bf16 v[88:91], a[8:11], a[52:55], v[88:91]\n"
"ds_read_b64_tr_b16 a[64:65], v209 offset:512\n"
"v_mfma_f32_16x16x32_bf16 v[52:55], a[12:15], a[32:35], v[52:55]\n"
"v_mfma_f32_16x16x32_bf16 v[60:63], a[12:15], a[36:39], v[60:63]\n"
"ds_read_b64_tr_b16 a[66:67], v209 offset:768\n"
"v_mfma_f32_16x16x32_bf16 v[68:71], a[12:15], a[40:43], v[68:71]\n"
"v_mfma_f32_16x16x32_bf16 v[76:79], a[12:15], a[44:47], v[76:79]\n"
"ds_read_b64_tr_b16 a[68:69], v209 offset:520\n"
"v_mfma_f32_16x16x32_bf16 v[84:87], a[12:15], a[48:51], v[84:87]\n"
"ds_read_b64_tr_b16 a[70:71], v209 offset:776\n"
"v_mfma_f32_16x16x32_bf16 v[92:95], a[12:15], a[52:55], v[92:95]\n"
"v_mfma_f32_16x16x32_bf16 v[96:99], a[16:19], a[32:35], v[96:99]\n"
"ds_read_b64_tr_b16 a[72:73], v209 offset:1024\n"
"v_mfma_f32_16x16x32_bf16 v[104:107], a[16:19], a[36:39], v[104:107]\n"
"v_mfma_f32_16x16x32_bf16 v[112:115], a[16:19], a[40:43], v[112:115]\n"
"ds_read_b64_tr_b16 a[74:75], v209 offset:1280\n"
"v_mfma_f32_16x16x32_bf16 v[120:123], a[16:19], a[44:47], v[120:123]\n"
"v_mfma_f32_16x16x32_bf16 v[128:131], a[16:19], a[48:51], v[128:131]\n"
"ds_read_b64_tr_b16 a[76:77], v209 offset:1032\n"
"v_mfma_f32_16x16x32_bf16 v[136:139], a[16:19], a[52:55], v[136:139]\n"
"ds_read_b64_tr_b16 a[78:79], v209 offset:1288\n"
"v_mfma_f32_16x16x32_bf16 v[100:103], a[20:23], a[32:35], v[100:103]\n"
"v_mfma_f32_16x16x32_bf16 v[108:111], a[20:23], a[36:39], v[108:111]\n"
"ds_read_b64_tr_b16 a[80:81], v209 offset:1536\n"
"v_mfma_f32_16x16x32_bf16 v[116:119], a[20:23], a[40:43], v[116:119]\n"
"v_mfma_f32_16x16x32_bf16 v[124:127], a[20:23], a[44:47], v[124:127]\n"
"ds_read_b64_tr_b16 a[82:83], v209 offset:1792\n"
"v_mfma_f32_16x16x32_bf16 v[132:135], a[20:23], a[48:51], v[132:135]\n"
"v_mfma_f32_16x16x32_bf16 v[140:143], a[20:23], a[52:55], v[140:143]\n"
"ds_read_b64_tr_b16 a[84:85], v209 offset:1544\n"
"v_mfma_f32_16x16x32_bf16 v[144:147], a[24:27], a[32:35], v[144:147]\n"
"ds_read_b64_tr_b16 a[86:87], v209 offset:1800\n"
"v_mfma_f32_16x16x32_bf16 v[152:155], a[24:27], a[36:39], v[152:155]\n"
"v_add_u32 v209, 32768, v209\n"
"v_mfma_f32_16x16x32_bf16 v[160:163], a[24:27], a[40:43], v[160:163]\n"
"v_mfma_f32_16x16x32_bf16 v[168:171], a[24:27], a[44:47], v[168:171]\n"
"v_mfma_f32_16x16x32_bf16 v[176:179], a[24:27], a[48:51], v[176:179]\n"
"v_mfma_f32_16x16x32_bf16 v[184:187], a[24:27], a[52:55], v[184:187]\n"
"v_mfma_f32_16x16x32_bf16 v[148:151], a[28:31], a[32:35], v[148:151]\n"
"v_mfma_f32_16x16x32_bf16 v[156:159], a[28:31], a[36:39], v[156:159]\n"
"v_mfma_f32_16x16x32_bf16 v[164:167], a[28:31], a[40:43], v[164:167]\n"
"v_mfma_f32_16x16x32_bf16 v[172:175], a[28:31], a[44:47], v[172:175]\n"
"v_mfma_f32_16x16x32_bf16 v[180:183], a[28:31], a[48:51], v[180:183]\n"
"v_mfma_f32_16x16x32_bf16 v[188:191], a[28:31], a[52:55], v[188:191]\n"
"s_waitcnt lgkmcnt(0)\n"
"s_barrier\n"
"v_mfma_f32_16x16x32_bf16 v[0:3], a[56:59], a[88:91], v[0:3]\n"
"ds_read_b64_tr_b16 a[0:1], v209\n"
"v_mfma_f32_16x16x32_bf16 v[8:11], a[56:59], a[92:95], v[8:11]\n"
"buffer_load_dword v214, v213, s[40:43], s49 offen\n"
"v_mfma_f32_16x16x32_bf16 v[16:19], a[56:59], a[96:99], v[16:19]\n"
"s_add_u32 s49, s49, s48\n"
"ds_read_b64_tr_b16 a[2:3], v209 offset:256\n"
"v_mfma_f32_16x16x32_bf16 v[24:27], a[56:59], a[100:103], v[24:27]\n"
"buffer_load_dword v214, v213, s[40:43], s49 offen\n"
"v_mfma_f32_16x16x32_bf16 v[32:35], a[56:59], a[104:107], v[32:35]\n"
"s_add_u32 s49, s49, s48\n"
"ds_read_b128 a[32:35], v208\n"
"v_mfma_f32_16x16x32_bf16 v[40:43], a[56:59], a[108:111], v[40:43]\n"
"ds_read_b128 a[36:39], v208 offset:2048\n"
"v_mfma_f32_16x16x32_bf16 v[4:7], a[60:63], a[88:91], v[4:7]\n"
"v_mfma_f32_16x16x32_bf16 v[12:15], a[60:63], a[92:95], v[12:15]\n"
"ds_read_b128 a[40:43], v208 offset:4096\n"
"v_mfma_f32_16x16x32_bf16 v[20:23], a[60:63], a[96:99], v[20:23]\n"
"ds_read_b128 a[44:47], v208 offset:6144\n"
"v_mfma_f32_16x16x32_bf16 v[28:31], a[60:63], a[100:103], v[28:31]\n"
"v_mfma_f32_16x16x32_bf16 v[36:39], a[60:63], a[104:107], v[36:39]\n"
"ds_read_b128 a[48:51], v208 offset:8192\n"
"v_mfma_f32_16x16x32_bf16 v[44:47], a[60:63], a[108:111], v[44:47]\n"
"v_mfma_f32_16x16x32_bf16 v[48:51], a[64:67], a[88:91], v[48:51]\n"
"ds_read_b128 a[52:55], v208 offset:10240\n"
"v_add_u32 v208, 512, v208\n"
"v_mfma_f32_16x16x32_bf16 v[56:59], a[64:67], a[92:95], v[56:59]\n"
"v_mfma_f32_16x16x32_bf16 v[64:67], a[64:67], a[96:99], v[64:67]\n"
"ds_read_b64_tr_b16 a[4:5], v209 offset:8\n"
"v_mfma_f32_16x16x32_bf16 v[72:75], a[64:67], a[100:103], v[72:75]\n"
"ds_read_b64_tr_b16 a[6:7], v209 offset:264\n"
"v_mfma_f32_16x16x32_bf16 v[80:83], a[64:67], a[104:107], v[80:83]\n"
"v_mfma_f32_16x16x32_bf16 v[88:91], a[64:67], a[108:111], v[88:91]\n"
"ds_read_b64_tr_b16 a[8:9], v209 offset:512\n"
"v_mfma_f32_16x16x32_bf16 v[52:55], a[68:71], a[88:91], v[52:55]\n"
"v_mfma_f32_16x16x32_bf16 v[60:63], a[68:71], a[92:95], v[60:63]\n"
"ds_read_b64_tr_b16 a[10:11], v209 offset:768\n"
"v_mfma_f32_16x16x32_bf16 v[68:71], a[68:71], a[96:99], v[68:71]\n"
"v_mfma_f32_16x16x32_bf16 v[76:79], a[68:71], a[100:103], v[76:79]\n"
"ds_read_b64_tr_b16 a[12:13], v209 offset:520\n"
"v_mfma_f32_16x16x32_bf16 v[84:87], a[68:71], a[104:107], v[84:87]\n"
"ds_read_b64_tr_b16 a[14:15], v209 offset:776\n"
"v_mfma_f32_16x16x32_bf16 v[92:95], a[68:71], a[108:111], v[92:95]\n"
"s_mov_b32 s56, 8192\n"
"v_mfma_f32_16x16x32_bf16 v[96:99], a[72:75], a[88:91], v[96:99]\n"
"ds_read_b64_tr_b16 a[16:17], v209 offset:1024\n"
"v_mfma_f32_16x16x32_bf16 v[104:107], a[72:75], a[92:95], v[104:107]\n"
"v_mfma_f32_16x16x32_bf16 v[112:115], a[72:75], a[96:99], v[112:115]\n"
"buffer_load_dwordx4 a[112:115], v197, s[88:91], s56 offen nt\n"
"v_mfma_f32_16x16x32_bf16 v[120:123], a[72:75], a[100:103], v[120:123]\n"
"s_mov_b32 s56, 8192\n"
"ds_read_b64_tr_b16 a[18:19], v209 offset:1280\n"
"v_mfma_f32_16x16x32_bf16 v[128:131], a[72:75], a[104:107], v[128:131]\n"
"buffer_load_dwordx4 a[116:119], v197, s[88:91], s56 offen offset:2048 nt\n"
"v_mfma_f32_16x16x32_bf16 v[136:139], a[72:75], a[108:111], v[136:139]\n"
"s_mov_b32 s56, 12288\n"
"ds_read_b64_tr_b16 a[20:21], v209 offset:1032\n"
"v_mfma_f32_16x16x32_bf16 v[100:103], a[76:79], a[88:91], v[100:103]\n"
"buffer_load_dwordx4 a[120:123], v197, s[88:91], s56 offen nt\n"
"v_mfma_f32_16x16x32_bf16 v[108:111], a[76:79], a[92:95], v[108:111]\n"
"s_mov_b32 s56, 12288\n"
"ds_read_b64_tr_b16 a[22:23], v209 offset:1288\n"
"v_mfma_f32_16x16x32_bf16 v[116:119], a[76:79], a[96:99], v[116:119]\n"
"buffer_load_dwordx4 a[124:127], v197, s[88:91], s56 offen offset:2048 nt\n"
"v_mfma_f32_16x16x32_bf16 v[124:127], a[76:79], a[100:103], v[124:127]\n"
"s_mov_b32 s56, 139264\n"
"ds_read_b64_tr_b16 a[24:25], v209 offset:1536\n"
"v_mfma_f32_16x16x32_bf16 v[132:135], a[76:79], a[104:107], v[132:135]\n"
"buffer_load_dwordx4 a[128:131], v197, s[88:91], s56 offen nt\n"
"v_mfma_f32_16x16x32_bf16 v[140:143], a[76:79], a[108:111], v[140:143]\n"
"s_mov_b32 s56, 139264\n"
"ds_read_b64_tr_b16 a[26:27], v209 offset:1792\n"
"v_mfma_f32_16x16x32_bf16 v[144:147], a[80:83], a[88:91], v[144:147]\n"
"buffer_load_dwordx4 a[132:135], v197, s[88:91], s56 offen offset:2048 nt\n"
"v_mfma_f32_16x16x32_bf16 v[152:155], a[80:83], a[92:95], v[152:155]\n"
"s_mov_b32 s56, 143360\n"
"ds_read_b64_tr_b16 a[28:29], v209 offset:1544\n"
"v_mfma_f32_16x16x32_bf16 v[160:163], a[80:83], a[96:99], v[160:163]\n"
"buffer_load_dwordx4 a[136:139], v197, s[88:91], s56 offen nt\n"
"v_mfma_f32_16x16x32_bf16 v[168:171], a[80:83], a[100:103], v[168:171]\n"
"s_mov_b32 s56, 143360\n"
"ds_read_b64_tr_b16 a[30:31], v209 offset:1800\n"
"v_add_u32 v209, 32768, v209\n"
"v_mfma_f32_16x16x32_bf16 v[176:179], a[80:83], a[104:107], v[176:179]\n"
"buffer_load_dwordx4 a[140:143], v197, s[88:91], s56 offen offset:2048 nt\n"
"v_mfma_f32_16x16x32_bf16 v[184:187], a[80:83], a[108:111], v[184:187]\n"
"v_mfma_f32_16x16x32_bf16 v[148:151], a[84:87], a[88:91], v[148:151]\n"
"v_mfma_f32_16x16x32_bf16 v[156:159], a[84:87], a[92:95], v[156:159]\n"
"v_mfma_f32_16x16x32_bf16 v[164:167], a[84:87], a[96:99], v[164:167]\n"
"v_mfma_f32_16x16x32_bf16 v[172:175], a[84:87], a[100:103], v[172:175]\n"
"v_mfma_f32_16x16x32_bf16 v[180:183], a[84:87], a[104:107], v[180:183]\n"
"v_mfma_f32_16x16x32_bf16 v[188:191], a[84:87], a[108:111], v[188:191]\n"
"s_waitcnt lgkmcnt(0)\n"
"s_barrier\n"
"v_mfma_f32_16x16x32_bf16 v[0:3], a[0:3], a[32:35], v[0:3]\n"
"ds_read_b64_tr_b16 a[56:57], v209\n"
"v_mfma_f32_16x16x32_bf16 v[8:11], a[0:3], a[36:39], v[8:11]\n"
"ds_read_b64_tr_b16 a[58:59], v209 offset:256\n"
"v_mfma_f32_16x16x32_bf16 v[16:19], a[0:3], a[40:43], v[16:19]\n"
"v_mfma_f32_16x16x32_bf16 v[24:27], a[0:3], a[44:47], v[24:27]\n"
"ds_read_b128 a[88:91], v208\n"
"v_mfma_f32_16x16x32_bf16 v[32:35], a[0:3], a[48:51], v[32:35]\n"
"v_mfma_f32_16x16x32_bf16 v[40:43], a[0:3], a[52:55], v[40:43]\n"
"ds_read_b128 a[92:95], v208 offset:2048\n"
"v_mfma_f32_16x16x32_bf16 v[4:7], a[4:7], a[32:35], v[4:7]\n"
"v_mfma_f32_16x16x32_bf16 v[12:15], a[4:7], a[36:39], v[12:15]\n"
"ds_read_b128 a[96:99], v208 offset:4096\n"
"v_mfma_f32_16x16x32_bf16 v[20:23], a[4:7], a[40:43], v[20:23]\n"
"ds_read_b128 a[100:103], v208 offset:6144\n"
"v_mfma_f32_16x16x32_bf16 v[28:31], a[4:7], a[44:47], v[28:31]\n"
"v_mfma_f32_16x16x32_bf16 v[36:39], a[4:7], a[48:51], v[36:39]\n"
"ds_read_b128 a[104:107], v208 offset:8192\n"
"v_mfma_f32_16x16x32_bf16 v[44:47], a[4:7], a[52:55], v[44:47]\n"
"v_mfma_f32_16x16x32_bf16 v[48:51], a[8:11], a[32:35], v[48:51]\n"
"ds_read_b128 a[108:111], v208 offset:10240\n"
"v_add_u32 v208, 0xfffe7e00, v208\n"
"v_mfma_f32_16x16x32_bf16 v[56:59], a[8:11], a[36:39], v[56:59]\n"
"v_mfma_f32_16x16x32_bf16 v[64:67], a[8:11], a[40:43], v[64:67]\n"
"ds_read_b64_tr_b16 a[60:61], v209 offset:8\n"
"v_mfma_f32_16x16x32_bf16 v[72:75], a[8:11], a[44:47], v[72:75]\n"
"ds_read_b64_tr_b16 a[62:63], v209 offset:264\n"
"v_mfma_f32_16x16x32_bf16 v[80:83], a[8:11], a[48:51], v[80:83]\n"
"v_mfma_f32_16x16x32_bf16 v[88:91], a[8:11], a[52:55], v[88:91]\n"
"ds_read_b64_tr_b16 a[64:65], v209 offset:512\n"
"v_mfma_f32_16x16x32_bf16 v[52:55], a[12:15], a[32:35], v[52:55]\n"
"v_mfma_f32_16x16x32_bf16 v[60:63], a[12:15], a[36:39], v[60:63]\n"
"ds_read_b64_tr_b16 a[66:67], v209 offset:768\n"
"v_mfma_f32_16x16x32_bf16 v[68:71], a[12:15], a[40:43], v[68:71]\n"
"v_mfma_f32_16x16x32_bf16 v[76:79], a[12:15], a[44:47], v[76:79]\n"
"ds_read_b64_tr_b16 a[68:69], v209 offset:520\n"
"v_mfma_f32_16x16x32_bf16 v[84:87], a[12:15], a[48:51], v[84:87]\n"
"ds_read_b64_tr_b16 a[70:71], v209 offset:776\n"
"v_mfma_f32_16x16x32_bf16 v[92:95], a[12:15], a[52:55], v[92:95]\n"
"v_mfma_f32_16x16x32_bf16 v[96:99], a[16:19], a[32:35], v[96:99]\n"
"ds_read_b64_tr_b16 a[72:73], v209 offset:1024\n"
"v_mfma_f32_16x16x32_bf16 v[104:107], a[16:19], a[36:39], v[104:107]\n"
"v_mfma_f32_16x16x32_bf16 v[112:115], a[16:19], a[40:43], v[112:115]\n"
"ds_read_b64_tr_b16 a[74:75], v209 offset:1280\n"
"v_mfma_f32_16x16x32_bf16 v[120:123], a[16:19], a[44:47], v[120:123]\n"
"v_mfma_f32_16x16x32_bf16 v[128:131], a[16:19], a[48:51], v[128:131]\n"
"ds_read_b64_tr_b16 a[76:77], v209 offset:1032\n"
"v_mfma_f32_16x16x32_bf16 v[136:139], a[16:19], a[52:55], v[136:139]\n"
"ds_read_b64_tr_b16 a[78:79], v209 offset:1288\n"
"v_mfma_f32_16x16x32_bf16 v[100:103], a[20:23], a[32:35], v[100:103]\n"
"v_mfma_f32_16x16x32_bf16 v[108:111], a[20:23], a[36:39], v[108:111]\n"
"ds_read_b64_tr_b16 a[80:81], v209 offset:1536\n"
"v_mfma_f32_16x16x32_bf16 v[116:119], a[20:23], a[40:43], v[116:119]\n"
"v_mfma_f32_16x16x32_bf16 v[124:127], a[20:23], a[44:47], v[124:127]\n"
"ds_read_b64_tr_b16 a[82:83], v209 offset:1792\n"
"v_mfma_f32_16x16x32_bf16 v[132:135], a[20:23], a[48:51], v[132:135]\n"
"v_mfma_f32_16x16x32_bf16 v[140:143], a[20:23], a[52:55], v[140:143]\n"
"ds_read_b64_tr_b16 a[84:85], v209 offset:1544\n"
"v_mfma_f32_16x16x32_bf16 v[144:147], a[24:27], a[32:35], v[144:147]\n"
"ds_read_b64_tr_b16 a[86:87], v209 offset:1800\n"
"v_mfma_f32_16x16x32_bf16 v[152:155], a[24:27], a[36:39], v[152:155]\n"
"v_add_u32 v209, 0xfffe0000, v209\n"
"v_mfma_f32_16x16x32_bf16 v[160:163], a[24:27], a[40:43], v[160:163]\n"
"v_mfma_f32_16x16x32_bf16 v[168:171], a[24:27], a[44:47], v[168:171]\n"
"v_mfma_f32_16x16x32_bf16 v[176:179], a[24:27], a[48:51], v[176:179]\n"
"v_mfma_f32_16x16x32_bf16 v[184:187], a[24:27], a[52:55], v[184:187]\n"
"v_mfma_f32_16x16x32_bf16 v[148:151], a[28:31], a[32:35], v[148:151]\n"
"v_mfma_f32_16x16x32_bf16 v[156:159], a[28:31], a[36:39], v[156:159]\n"
"v_mfma_f32_16x16x32_bf16 v[164:167], a[28:31], a[40:43], v[164:167]\n"
"v_mfma_f32_16x16x32_bf16 v[172:175], a[28:31], a[44:47], v[172:175]\n"
"v_mfma_f32_16x16x32_bf16 v[180:183], a[28:31], a[48:51], v[180:183]\n"
"v_mfma_f32_16x16x32_bf16 v[188:191], a[28:31], a[52:55], v[188:191]\n"
"s_waitcnt lgkmcnt(0)\n"
"s_barrier\n"
"v_mfma_f32_16x16x32_bf16 v[0:3], a[56:59], a[88:91], v[0:3]\n"
"ds_read_b64_tr_b16 a[0:1], v209\n"
"v_mfma_f32_16x16x32_bf16 v[8:11], a[56:59], a[92:95], v[8:11]\n"
"ds_read_b64_tr_b16 a[2:3], v209 offset:256\n"
"v_mfma_f32_16x16x32_bf16 v[16:19], a[56:59], a[96:99], v[16:19]\n"
"v_mfma_f32_16x16x32_bf16 v[24:27], a[56:59], a[100:103], v[24:27]\n"
"ds_read_b128 a[32:35], v208\n"
"v_mfma_f32_16x16x32_bf16 v[32:35], a[56:59], a[104:107], v[32:35]\n"
"v_mfma_f32_16x16x32_bf16 v[40:43], a[56:59], a[108:111], v[40:43]\n"
"ds_read_b128 a[36:39], v208 offset:2048\n"
"v_mfma_f32_16x16x32_bf16 v[4:7], a[60:63], a[88:91], v[4:7]\n"
"v_mfma_f32_16x16x32_bf16 v[12:15], a[60:63], a[92:95], v[12:15]\n"
"ds_read_b128 a[40:43], v208 offset:4096\n"
"v_mfma_f32_16x16x32_bf16 v[20:23], a[60:63], a[96:99], v[20:23]\n"
"ds_read_b128 a[44:47], v208 offset:6144\n"
"v_mfma_f32_16x16x32_bf16 v[28:31], a[60:63], a[100:103], v[28:31]\n"
"v_mfma_f32_16x16x32_bf16 v[36:39], a[60:63], a[104:107], v[36:39]\n"
"ds_read_b128 a[48:51], v208 offset:8192\n"
"v_mfma_f32_16x16x32_bf16 v[44:47], a[60:63], a[108:111], v[44:47]\n"
"v_mfma_f32_16x16x32_bf16 v[48:51], a[64:67], a[88:91], v[48:51]\n"
"ds_read_b128 a[52:55], v208 offset:10240\n"
"v_add_u32 v208, 512, v208\n"
"v_mfma_f32_16x16x32_bf16 v[56:59], a[64:67], a[92:95], v[56:59]\n"
"v_mfma_f32_16x16x32_bf16 v[64:67], a[64:67], a[96:99], v[64:67]\n"
"ds_read_b64_tr_b16 a[4:5], v209 offset:8\n"
"v_mfma_f32_16x16x32_bf16 v[72:75], a[64:67], a[100:103], v[72:75]\n"
"ds_read_b64_tr_b16 a[6:7], v209 offset:264\n"
"v_mfma_f32_16x16x32_bf16 v[80:83], a[64:67], a[104:107], v[80:83]\n"
"v_mfma_f32_16x16x32_bf16 v[88:91], a[64:67], a[108:111], v[88:91]\n"
"ds_read_b64_tr_b16 a[8:9], v209 offset:512\n"
"v_mfma_f32_16x16x32_bf16 v[52:55], a[68:71], a[88:91], v[52:55]\n"
"v_mfma_f32_16x16x32_bf16 v[60:63], a[68:71], a[92:95], v[60:63]\n"
"ds_read_b64_tr_b16 a[10:11], v209 offset:768\n"
"v_mfma_f32_16x16x32_bf16 v[68:71], a[68:71], a[96:99], v[68:71]\n"
"v_mfma_f32_16x16x32_bf16 v[76:79], a[68:71], a[100:103], v[76:79]\n"
"ds_read_b64_tr_b16 a[12:13], v209 offset:520\n"
"v_mfma_f32_16x16x32_bf16 v[84:87], a[68:71], a[104:107], v[84:87]\n"
"ds_read_b64_tr_b16 a[14:15], v209 offset:776\n"
"v_mfma_f32_16x16x32_bf16 v[92:95], a[68:71], a[108:111], v[92:95]\n"
"s_mov_b32 s56, 16384\n"
"v_mfma_f32_16x16x32_bf16 v[96:99], a[72:75], a[88:91], v[96:99]\n"
"ds_read_b64_tr_b16 a[16:17], v209 offset:1024\n"
"v_mfma_f32_16x16x32_bf16 v[104:107], a[72:75], a[92:95], v[104:107]\n"
"v_mfma_f32_16x16x32_bf16 v[112:115], a[72:75], a[96:99], v[112:115]\n"
"buffer_load_dwordx4 a[144:147], v195, s[80:83], s56 offen nt\n"
"v_mfma_f32_16x16x32_bf16 v[120:123], a[72:75], a[100:103], v[120:123]\n"
"s_mov_b32 s56, 16384\n"
"ds_read_b64_tr_b16 a[18:19], v209 offset:1280\n"
"v_mfma_f32_16x16x32_bf16 v[128:131], a[72:75], a[104:107], v[128:131]\n"
"buffer_load_dwordx4 a[148:151], v196, s[80:83], s56 offen offset:2048 nt\n"
"v_mfma_f32_16x16x32_bf16 v[136:139], a[72:75], a[108:111], v[136:139]\n"
"s_mov_b32 s56, 20480\n"
"ds_read_b64_tr_b16 a[20:21], v209 offset:1032\n"
"v_mfma_f32_16x16x32_bf16 v[100:103], a[76:79], a[88:91], v[100:103]\n"
"buffer_load_dwordx4 a[152:155], v195, s[80:83], s56 offen nt\n"
"v_mfma_f32_16x16x32_bf16 v[108:111], a[76:79], a[92:95], v[108:111]\n"
"s_mov_b32 s56, 20480\n"
"ds_read_b64_tr_b16 a[22:23], v209 offset:1288\n"
"v_mfma_f32_16x16x32_bf16 v[116:119], a[76:79], a[96:99], v[116:119]\n"
"buffer_load_dwordx4 a[156:159], v196, s[80:83], s56 offen offset:2048 nt\n"
"v_mfma_f32_16x16x32_bf16 v[124:127], a[76:79], a[100:103], v[124:127]\n"
"s_mov_b32 s56, 147456\n"
"ds_read_b64_tr_b16 a[24:25], v209 offset:1536\n"
"v_mfma_f32_16x16x32_bf16 v[132:135], a[76:79], a[104:107], v[132:135]\n"
"buffer_load_dwordx4 a[160:163], v195, s[80:83], s56 offen nt\n"
"v_mfma_f32_16x16x32_bf16 v[140:143], a[76:79], a[108:111], v[140:143]\n"
"s_mov_b32 s56, 147456\n"
"ds_read_b64_tr_b16 a[26:27], v209 offset:1792\n"
"v_mfma_f32_16x16x32_bf16 v[144:147], a[80:83], a[88:91], v[144:147]\n"
"buffer_load_dwordx4 a[164:167], v196, s[80:83], s56 offen offset:2048 nt\n"
"v_mfma_f32_16x16x32_bf16 v[152:155], a[80:83], a[92:95], v[152:155]\n"
"s_mov_b32 s56, 151552\n"
"ds_read_b64_tr_b16 a[28:29], v209 offset:1544\n"
"v_mfma_f32_16x16x32_bf16 v[160:163], a[80:83], a[96:99], v[160:163]\n"
"buffer_load_dwordx4 a[168:171], v195, s[80:83], s56 offen nt\n"
"v_mfma_f32_16x16x32_bf16 v[168:171], a[80:83], a[100:103], v[168:171]\n"
"s_mov_b32 s56, 151552\n"
"ds_read_b64_tr_b16 a[30:31], v209 offset:1800\n"
"v_add_u32 v209, 32768, v209\n"
"v_mfma_f32_16x16x32_bf16 v[176:179], a[80:83], a[104:107], v[176:179]\n"
"buffer_load_dwordx4 a[172:175], v196, s[80:83], s56 offen offset:2048 nt\n"
"v_mfma_f32_16x16x32_bf16 v[184:187], a[80:83], a[108:111], v[184:187]\n"
"v_mfma_f32_16x16x32_bf16 v[148:151], a[84:87], a[88:91], v[148:151]\n"
"v_mfma_f32_16x16x32_bf16 v[156:159], a[84:87], a[92:95], v[156:159]\n"
"v_mfma_f32_16x16x32_bf16 v[164:167], a[84:87], a[96:99], v[164:167]\n"
"v_mfma_f32_16x16x32_bf16 v[172:175], a[84:87], a[100:103], v[172:175]\n"
"v_mfma_f32_16x16x32_bf16 v[180:183], a[84:87], a[104:107], v[180:183]\n"
"v_mfma_f32_16x16x32_bf16 v[188:191], a[84:87], a[108:111], v[188:191]\n"
"s_waitcnt lgkmcnt(0)\n"
"s_barrier\n"
"v_mfma_f32_16x16x32_bf16 v[0:3], a[0:3], a[32:35], v[0:3]\n"
"ds_read_b64_tr_b16 a[56:57], v209\n"
"v_mfma_f32_16x16x32_bf16 v[8:11], a[0:3], a[36:39], v[8:11]\n"
"ds_read_b64_tr_b16 a[58:59], v209 offset:256\n"
"v_mfma_f32_16x16x32_bf16 v[16:19], a[0:3], a[40:43], v[16:19]\n"
"v_mfma_f32_16x16x32_bf16 v[24:27], a[0:3], a[44:47], v[24:27]\n"
"ds_read_b128 a[88:91], v208\n"
"v_mfma_f32_16x16x32_bf16 v[32:35], a[0:3], a[48:51], v[32:35]\n"
"v_mfma_f32_16x16x32_bf16 v[40:43], a[0:3], a[52:55], v[40:43]\n"
"ds_read_b128 a[92:95], v208 offset:2048\n"
"v_mfma_f32_16x16x32_bf16 v[4:7], a[4:7], a[32:35], v[4:7]\n"
"v_mfma_f32_16x16x32_bf16 v[12:15], a[4:7], a[36:39], v[12:15]\n"
"ds_read_b128 a[96:99], v208 offset:4096\n"
"v_mfma_f32_16x16x32_bf16 v[20:23], a[4:7], a[40:43], v[20:23]\n"
"ds_read_b128 a[100:103], v208 offset:6144\n"
"v_mfma_f32_16x16x32_bf16 v[28:31], a[4:7], a[44:47], v[28:31]\n"
"v_mfma_f32_16x16x32_bf16 v[36:39], a[4:7], a[48:51], v[36:39]\n"
"ds_read_b128 a[104:107], v208 offset:8192\n"
"v_mfma_f32_16x16x32_bf16 v[44:47], a[4:7], a[52:55], v[44:47]\n"
"v_mfma_f32_16x16x32_bf16 v[48:51], a[8:11], a[32:35], v[48:51]\n"
"ds_read_b128 a[108:111], v208 offset:10240\n"
"v_add_u32 v208, 65024, v208\n"
"v_mfma_f32_16x16x32_bf16 v[56:59], a[8:11], a[36:39], v[56:59]\n"
"v_mfma_f32_16x16x32_bf16 v[64:67], a[8:11], a[40:43], v[64:67]\n"
"ds_read_b64_tr_b16 a[60:61], v209 offset:8\n"
"v_mfma_f32_16x16x32_bf16 v[72:75], a[8:11], a[44:47], v[72:75]\n"
"ds_read_b64_tr_b16 a[62:63], v209 offset:264\n"
"v_mfma_f32_16x16x32_bf16 v[80:83], a[8:11], a[48:51], v[80:83]\n"
"v_mfma_f32_16x16x32_bf16 v[88:91], a[8:11], a[52:55], v[88:91]\n"
"ds_read_b64_tr_b16 a[64:65], v209 offset:512\n"
"v_mfma_f32_16x16x32_bf16 v[52:55], a[12:15], a[32:35], v[52:55]\n"
"v_mfma_f32_16x16x32_bf16 v[60:63], a[12:15], a[36:39], v[60:63]\n"
"ds_read_b64_tr_b16 a[66:67], v209 offset:768\n"
"v_mfma_f32_16x16x32_bf16 v[68:71], a[12:15], a[40:43], v[68:71]\n"
"v_mfma_f32_16x16x32_bf16 v[76:79], a[12:15], a[44:47], v[76:79]\n"
"ds_read_b64_tr_b16 a[68:69], v209 offset:520\n"
"v_mfma_f32_16x16x32_bf16 v[84:87], a[12:15], a[48:51], v[84:87]\n"
"ds_read_b64_tr_b16 a[70:71], v209 offset:776\n"
"v_mfma_f32_16x16x32_bf16 v[92:95], a[12:15], a[52:55], v[92:95]\n"
"v_mfma_f32_16x16x32_bf16 v[96:99], a[16:19], a[32:35], v[96:99]\n"
"ds_read_b64_tr_b16 a[72:73], v209 offset:1024\n"
"v_mfma_f32_16x16x32_bf16 v[104:107], a[16:19], a[36:39], v[104:107]\n"
"v_mfma_f32_16x16x32_bf16 v[112:115], a[16:19], a[40:43], v[112:115]\n"
"ds_read_b64_tr_b16 a[74:75], v209 offset:1280\n"
"v_mfma_f32_16x16x32_bf16 v[120:123], a[16:19], a[44:47], v[120:123]\n"
"v_mfma_f32_16x16x32_bf16 v[128:131], a[16:19], a[48:51], v[128:131]\n"
"ds_read_b64_tr_b16 a[76:77], v209 offset:1032\n"
"v_mfma_f32_16x16x32_bf16 v[136:139], a[16:19], a[52:55], v[136:139]\n"
"ds_read_b64_tr_b16 a[78:79], v209 offset:1288\n"
"v_mfma_f32_16x16x32_bf16 v[100:103], a[20:23], a[32:35], v[100:103]\n"
"v_mfma_f32_16x16x32_bf16 v[108:111], a[20:23], a[36:39], v[108:111]\n"
"ds_read_b64_tr_b16 a[80:81], v209 offset:1536\n"
"v_mfma_f32_16x16x32_bf16 v[116:119], a[20:23], a[40:43], v[116:119]\n"
"v_mfma_f32_16x16x32_bf16 v[124:127], a[20:23], a[44:47], v[124:127]\n"
"ds_read_b64_tr_b16 a[82:83], v209 offset:1792\n"
"v_mfma_f32_16x16x32_bf16 v[132:135], a[20:23], a[48:51], v[132:135]\n"
"v_mfma_f32_16x16x32_bf16 v[140:143], a[20:23], a[52:55], v[140:143]\n"
"ds_read_b64_tr_b16 a[84:85], v209 offset:1544\n"
"v_mfma_f32_16x16x32_bf16 v[144:147], a[24:27], a[32:35], v[144:147]\n"
"ds_read_b64_tr_b16 a[86:87], v209 offset:1800\n"
"v_mfma_f32_16x16x32_bf16 v[152:155], a[24:27], a[36:39], v[152:155]\n"
"v_add_u32 v209, 32768, v209\n"
"v_mfma_f32_16x16x32_bf16 v[160:163], a[24:27], a[40:43], v[160:163]\n"
"v_mfma_f32_16x16x32_bf16 v[168:171], a[24:27], a[44:47], v[168:171]\n"
"v_mfma_f32_16x16x32_bf16 v[176:179], a[24:27], a[48:51], v[176:179]\n"
"v_mfma_f32_16x16x32_bf16 v[184:187], a[24:27], a[52:55], v[184:187]\n"
"v_mfma_f32_16x16x32_bf16 v[148:151], a[28:31], a[32:35], v[148:151]\n"
"v_mfma_f32_16x16x32_bf16 v[156:159], a[28:31], a[36:39], v[156:159]\n"
"v_mfma_f32_16x16x32_bf16 v[164:167], a[28:31], a[40:43], v[164:167]\n"
"v_mfma_f32_16x16x32_bf16 v[172:175], a[28:31], a[44:47], v[172:175]\n"
"v_mfma_f32_16x16x32_bf16 v[180:183], a[28:31], a[48:51], v[180:183]\n"
"v_mfma_f32_16x16x32_bf16 v[188:191], a[28:31], a[52:55], v[188:191]\n"
"s_waitcnt lgkmcnt(0)\n"
"s_barrier\n"
"v_mfma_f32_16x16x32_bf16 v[0:3], a[56:59], a[88:91], v[0:3]\n"
"ds_read_b64_tr_b16 a[0:1], v209\n"
"v_mfma_f32_16x16x32_bf16 v[8:11], a[56:59], a[92:95], v[8:11]\n"
"ds_read_b64_tr_b16 a[2:3], v209 offset:256\n"
"v_mfma_f32_16x16x32_bf16 v[16:19], a[56:59], a[96:99], v[16:19]\n"
"v_mfma_f32_16x16x32_bf16 v[24:27], a[56:59], a[100:103], v[24:27]\n"
"ds_read_b128 a[32:35], v208\n"
"v_mfma_f32_16x16x32_bf16 v[32:35], a[56:59], a[104:107], v[32:35]\n"
"v_mfma_f32_16x16x32_bf16 v[40:43], a[56:59], a[108:111], v[40:43]\n"
"ds_read_b128 a[36:39], v208 offset:2048\n"
"v_mfma_f32_16x16x32_bf16 v[4:7], a[60:63], a[88:91], v[4:7]\n"
"v_mfma_f32_16x16x32_bf16 v[12:15], a[60:63], a[92:95], v[12:15]\n"
"ds_read_b128 a[40:43], v208 offset:4096\n"
"v_mfma_f32_16x16x32_bf16 v[20:23], a[60:63], a[96:99], v[20:23]\n"
"ds_read_b128 a[44:47], v208 offset:6144\n"
"v_mfma_f32_16x16x32_bf16 v[28:31], a[60:63], a[100:103], v[28:31]\n"
"v_mfma_f32_16x16x32_bf16 v[36:39], a[60:63], a[104:107], v[36:39]\n"
"ds_read_b128 a[48:51], v208 offset:8192\n"
"v_mfma_f32_16x16x32_bf16 v[44:47], a[60:63], a[108:111], v[44:47]\n"
"v_mfma_f32_16x16x32_bf16 v[48:51], a[64:67], a[88:91], v[48:51]\n"
"ds_read_b128 a[52:55], v208 offset:10240\n"
"v_add_u32 v208, 512, v208\n"
"v_mfma_f32_16x16x32_bf16 v[56:59], a[64:67], a[92:95], v[56:59]\n"
"v_mfma_f32_16x16x32_bf16 v[64:67], a[64:67], a[96:99], v[64:67]\n"
"ds_read_b64_tr_b16 a[4:5], v209 offset:8\n"
"v_mfma_f32_16x16x32_bf16 v[72:75], a[64:67], a[100:103], v[72:75]\n"
"ds_read_b64_tr_b16 a[6:7], v209 offset:264\n"
"v_mfma_f32_16x16x32_bf16 v[80:83], a[64:67], a[104:107], v[80:83]\n"
"v_mfma_f32_16x16x32_bf16 v[88:91], a[64:67], a[108:111], v[88:91]\n"
"ds_read_b64_tr_b16 a[8:9], v209 offset:512\n"
"v_mfma_f32_16x16x32_bf16 v[52:55], a[68:71], a[88:91], v[52:55]\n"
"v_mfma_f32_16x16x32_bf16 v[60:63], a[68:71], a[92:95], v[60:63]\n"
"ds_read_b64_tr_b16 a[10:11], v209 offset:768\n"
"v_mfma_f32_16x16x32_bf16 v[68:71], a[68:71], a[96:99], v[68:71]\n"
"v_mfma_f32_16x16x32_bf16 v[76:79], a[68:71], a[100:103], v[76:79]\n"
"ds_read_b64_tr_b16 a[12:13], v209 offset:520\n"
"v_mfma_f32_16x16x32_bf16 v[84:87], a[68:71], a[104:107], v[84:87]\n"
"ds_read_b64_tr_b16 a[14:15], v209 offset:776\n"
"v_mfma_f32_16x16x32_bf16 v[92:95], a[68:71], a[108:111], v[92:95]\n"
"s_mov_b32 s56, 16384\n"
"v_mfma_f32_16x16x32_bf16 v[96:99], a[72:75], a[88:91], v[96:99]\n"
"ds_read_b64_tr_b16 a[16:17], v209 offset:1024\n"
"v_mfma_f32_16x16x32_bf16 v[104:107], a[72:75], a[92:95], v[104:107]\n"
"v_mfma_f32_16x16x32_bf16 v[112:115], a[72:75], a[96:99], v[112:115]\n"
"buffer_load_dwordx4 a[176:179], v197, s[88:91], s56 offen nt\n"
"v_mfma_f32_16x16x32_bf16 v[120:123], a[72:75], a[100:103], v[120:123]\n"
"s_mov_b32 s56, 16384\n"
"ds_read_b64_tr_b16 a[18:19], v209 offset:1280\n"
"v_mfma_f32_16x16x32_bf16 v[128:131], a[72:75], a[104:107], v[128:131]\n"
"buffer_load_dwordx4 a[180:183], v197, s[88:91], s56 offen offset:2048 nt\n"
"v_mfma_f32_16x16x32_bf16 v[136:139], a[72:75], a[108:111], v[136:139]\n"
"s_mov_b32 s56, 20480\n"
"ds_read_b64_tr_b16 a[20:21], v209 offset:1032\n"
"v_mfma_f32_16x16x32_bf16 v[100:103], a[76:79], a[88:91], v[100:103]\n"
"buffer_load_dwordx4 a[184:187], v197, s[88:91], s56 offen nt\n"
"v_mfma_f32_16x16x32_bf16 v[108:111], a[76:79], a[92:95], v[108:111]\n"
"s_mov_b32 s56, 20480\n"
"ds_read_b64_tr_b16 a[22:23], v209 offset:1288\n"
"v_mfma_f32_16x16x32_bf16 v[116:119], a[76:79], a[96:99], v[116:119]\n"
"buffer_load_dwordx4 a[188:191], v197, s[88:91], s56 offen offset:2048 nt\n"
"v_mfma_f32_16x16x32_bf16 v[124:127], a[76:79], a[100:103], v[124:127]\n"
"s_mov_b32 s56, 147456\n"
"ds_read_b64_tr_b16 a[24:25], v209 offset:1536\n"
"v_mfma_f32_16x16x32_bf16 v[132:135], a[76:79], a[104:107], v[132:135]\n"
"buffer_load_dwordx4 a[192:195], v197, s[88:91], s56 offen nt\n"
"v_mfma_f32_16x16x32_bf16 v[140:143], a[76:79], a[108:111], v[140:143]\n"
"s_mov_b32 s56, 147456\n"
"ds_read_b64_tr_b16 a[26:27], v209 offset:1792\n"
"v_mfma_f32_16x16x32_bf16 v[144:147], a[80:83], a[88:91], v[144:147]\n"
"buffer_load_dwordx4 a[196:199], v197, s[88:91], s56 offen offset:2048 nt\n"
"v_mfma_f32_16x16x32_bf16 v[152:155], a[80:83], a[92:95], v[152:155]\n"
"s_mov_b32 s56, 151552\n"
"ds_read_b64_tr_b16 a[28:29], v209 offset:1544\n"
"v_mfma_f32_16x16x32_bf16 v[160:163], a[80:83], a[96:99], v[160:163]\n"
"buffer_load_dwordx4 a[200:203], v197, s[88:91], s56 offen nt\n"
"v_mfma_f32_16x16x32_bf16 v[168:171], a[80:83], a[100:103], v[168:171]\n"
"s_mov_b32 s56, 151552\n"
"ds_read_b64_tr_b16 a[30:31], v209 offset:1800\n"
"v_add_u32 v209, 32768, v209\n"
"v_mfma_f32_16x16x32_bf16 v[176:179], a[80:83], a[104:107], v[176:179]\n"
"buffer_load_dwordx4 a[204:207], v197, s[88:91], s56 offen offset:2048 nt\n"
"v_mfma_f32_16x16x32_bf16 v[184:187], a[80:83], a[108:111], v[184:187]\n"
"v_mfma_f32_16x16x32_bf16 v[148:151], a[84:87], a[88:91], v[148:151]\n"
"v_mfma_f32_16x16x32_bf16 v[156:159], a[84:87], a[92:95], v[156:159]\n"
"v_mfma_f32_16x16x32_bf16 v[164:167], a[84:87], a[96:99], v[164:167]\n"
"v_mfma_f32_16x16x32_bf16 v[172:175], a[84:87], a[100:103], v[172:175]\n"
"v_mfma_f32_16x16x32_bf16 v[180:183], a[84:87], a[104:107], v[180:183]\n"
"v_mfma_f32_16x16x32_bf16 v[188:191], a[84:87], a[108:111], v[188:191]\n"
"s_waitcnt lgkmcnt(0)\n"
"s_barrier\n"
"v_mfma_f32_16x16x32_bf16 v[0:3], a[0:3], a[32:35], v[0:3]\n"
"s_mov_b32 s61, 65024\n"
"s_cmp_eq_u32 s54, 0\n"
"ds_read_b64_tr_b16 a[56:57], v209\n"
"v_mfma_f32_16x16x32_bf16 v[8:11], a[0:3], a[36:39], v[8:11]\n"
"s_cselect_b32 s61, s61, 0xfffe7e00\n"
"ds_read_b64_tr_b16 a[58:59], v209 offset:256\n"
"v_mfma_f32_16x16x32_bf16 v[16:19], a[0:3], a[40:43], v[16:19]\n"
"v_mfma_f32_16x16x32_bf16 v[24:27], a[0:3], a[44:47], v[24:27]\n"
"ds_read_b128 a[88:91], v208\n"
"v_mfma_f32_16x16x32_bf16 v[32:35], a[0:3], a[48:51], v[32:35]\n"
"v_mfma_f32_16x16x32_bf16 v[40:43], a[0:3], a[52:55], v[40:43]\n"
"ds_read_b128 a[92:95], v208 offset:2048\n"
"v_mfma_f32_16x16x32_bf16 v[4:7], a[4:7], a[32:35], v[4:7]\n"
"v_mfma_f32_16x16x32_bf16 v[12:15], a[4:7], a[36:39], v[12:15]\n"
"ds_read_b128 a[96:99], v208 offset:4096\n"
"v_mfma_f32_16x16x32_bf16 v[20:23], a[4:7], a[40:43], v[20:23]\n"
"ds_read_b128 a[100:103], v208 offset:6144\n"
"v_mfma_f32_16x16x32_bf16 v[28:31], a[4:7], a[44:47], v[28:31]\n"
"v_mfma_f32_16x16x32_bf16 v[36:39], a[4:7], a[48:51], v[36:39]\n"
"ds_read_b128 a[104:107], v208 offset:8192\n"
"v_mfma_f32_16x16x32_bf16 v[44:47], a[4:7], a[52:55], v[44:47]\n"
"v_mfma_f32_16x16x32_bf16 v[48:51], a[8:11], a[32:35], v[48:51]\n"
"ds_read_b128 a[108:111], v208 offset:10240\n"
"v_add_u32 v208, s61, v208\n"
"v_mfma_f32_16x16x32_bf16 v[56:59], a[8:11], a[36:39], v[56:59]\n"
"v_mfma_f32_16x16x32_bf16 v[64:67], a[8:11], a[40:43], v[64:67]\n"
"ds_read_b64_tr_b16 a[60:61], v209 offset:8\n"
"v_mfma_f32_16x16x32_bf16 v[72:75], a[8:11], a[44:47], v[72:75]\n"
"ds_read_b64_tr_b16 a[62:63], v209 offset:264\n"
"v_mfma_f32_16x16x32_bf16 v[80:83], a[8:11], a[48:51], v[80:83]\n"
"v_mfma_f32_16x16x32_bf16 v[88:91], a[8:11], a[52:55], v[88:91]\n"
"ds_read_b64_tr_b16 a[64:65], v209 offset:512\n"
"v_mfma_f32_16x16x32_bf16 v[52:55], a[12:15], a[32:35], v[52:55]\n"
"v_mfma_f32_16x16x32_bf16 v[60:63], a[12:15], a[36:39], v[60:63]\n"
"ds_read_b64_tr_b16 a[66:67], v209 offset:768\n"
"v_mfma_f32_16x16x32_bf16 v[68:71], a[12:15], a[40:43], v[68:71]\n"
"v_mfma_f32_16x16x32_bf16 v[76:79], a[12:15], a[44:47], v[76:79]\n"
"ds_read_b64_tr_b16 a[68:69], v209 offset:520\n"
"v_mfma_f32_16x16x32_bf16 v[84:87], a[12:15], a[48:51], v[84:87]\n"
"ds_read_b64_tr_b16 a[70:71], v209 offset:776\n"
"v_mfma_f32_16x16x32_bf16 v[92:95], a[12:15], a[52:55], v[92:95]\n"
"v_mfma_f32_16x16x32_bf16 v[96:99], a[16:19], a[32:35], v[96:99]\n"
"ds_read_b64_tr_b16 a[72:73], v209 offset:1024\n"
"v_mfma_f32_16x16x32_bf16 v[104:107], a[16:19], a[36:39], v[104:107]\n"
"v_mfma_f32_16x16x32_bf16 v[112:115], a[16:19], a[40:43], v[112:115]\n"
"ds_read_b64_tr_b16 a[74:75], v209 offset:1280\n"
"v_mfma_f32_16x16x32_bf16 v[120:123], a[16:19], a[44:47], v[120:123]\n"
"v_mfma_f32_16x16x32_bf16 v[128:131], a[16:19], a[48:51], v[128:131]\n"
"ds_read_b64_tr_b16 a[76:77], v209 offset:1032\n"
"v_mfma_f32_16x16x32_bf16 v[136:139], a[16:19], a[52:55], v[136:139]\n"
"ds_read_b64_tr_b16 a[78:79], v209 offset:1288\n"
"v_mfma_f32_16x16x32_bf16 v[100:103], a[20:23], a[32:35], v[100:103]\n"
"v_mfma_f32_16x16x32_bf16 v[108:111], a[20:23], a[36:39], v[108:111]\n"
"ds_read_b64_tr_b16 a[80:81], v209 offset:1536\n"
"v_mfma_f32_16x16x32_bf16 v[116:119], a[20:23], a[40:43], v[116:119]\n"
"v_mfma_f32_16x16x32_bf16 v[124:127], a[20:23], a[44:47], v[124:127]\n"
"ds_read_b64_tr_b16 a[82:83], v209 offset:1792\n"
"v_mfma_f32_16x16x32_bf16 v[132:135], a[20:23], a[48:51], v[132:135]\n"
"v_mfma_f32_16x16x32_bf16 v[140:143], a[20:23], a[52:55], v[140:143]\n"
"ds_read_b64_tr_b16 a[84:85], v209 offset:1544\n"
"v_mfma_f32_16x16x32_bf16 v[144:147], a[24:27], a[32:35], v[144:147]\n"
"ds_read_b64_tr_b16 a[86:87], v209 offset:1800\n"
"v_mfma_f32_16x16x32_bf16 v[152:155], a[24:27], a[36:39], v[152:155]\n"
"v_add_u32 v209, 32768, v209\n"
"v_mfma_f32_16x16x32_bf16 v[160:163], a[24:27], a[40:43], v[160:163]\n"
"v_mfma_f32_16x16x32_bf16 v[168:171], a[24:27], a[44:47], v[168:171]\n"
"v_mfma_f32_16x16x32_bf16 v[176:179], a[24:27], a[48:51], v[176:179]\n"
"v_mfma_f32_16x16x32_bf16 v[184:187], a[24:27], a[52:55], v[184:187]\n"
"v_mfma_f32_16x16x32_bf16 v[148:151], a[28:31], a[32:35], v[148:151]\n"
"v_mfma_f32_16x16x32_bf16 v[156:159], a[28:31], a[36:39], v[156:159]\n"
"v_mfma_f32_16x16x32_bf16 v[164:167], a[28:31], a[40:43], v[164:167]\n"
"v_mfma_f32_16x16x32_bf16 v[172:175], a[28:31], a[44:47], v[172:175]\n"
"v_mfma_f32_16x16x32_bf16 v[180:183], a[28:31], a[48:51], v[180:183]\n"
"v_mfma_f32_16x16x32_bf16 v[188:191], a[28:31], a[52:55], v[188:191]\n"
"s_waitcnt lgkmcnt(0)\n"
"s_barrier\n"
"v_mfma_f32_16x16x32_bf16 v[0:3], a[56:59], a[88:91], v[0:3]\n"
"ds_read_b64_tr_b16 a[0:1], v209\n"
"v_mfma_f32_16x16x32_bf16 v[8:11], a[56:59], a[92:95], v[8:11]\n"
"ds_read_b64_tr_b16 a[2:3], v209 offset:256\n"
"v_mfma_f32_16x16x32_bf16 v[16:19], a[56:59], a[96:99], v[16:19]\n"
"v_mfma_f32_16x16x32_bf16 v[24:27], a[56:59], a[100:103], v[24:27]\n"
"ds_read_b128 a[32:35], v208\n"
"v_mfma_f32_16x16x32_bf16 v[32:35], a[56:59], a[104:107], v[32:35]\n"
"v_mfma_f32_16x16x32_bf16 v[40:43], a[56:59], a[108:111], v[40:43]\n"
"ds_read_b128 a[36:39], v208 offset:2048\n"
"v_mfma_f32_16x16x32_bf16 v[4:7], a[60:63], a[88:91], v[4:7]\n"
"v_mfma_f32_16x16x32_bf16 v[12:15], a[60:63], a[92:95], v[12:15]\n"
"ds_read_b128 a[40:43], v208 offset:4096\n"
"v_mfma_f32_16x16x32_bf16 v[20:23], a[60:63], a[96:99], v[20:23]\n"
"ds_read_b128 a[44:47], v208 offset:6144\n"
"v_mfma_f32_16x16x32_bf16 v[28:31], a[60:63], a[100:103], v[28:31]\n"
"v_mfma_f32_16x16x32_bf16 v[36:39], a[60:63], a[104:107], v[36:39]\n"
"ds_read_b128 a[48:51], v208 offset:8192\n"
"v_mfma_f32_16x16x32_bf16 v[44:47], a[60:63], a[108:111], v[44:47]\n"
"v_mfma_f32_16x16x32_bf16 v[48:51], a[64:67], a[88:91], v[48:51]\n"
"ds_read_b128 a[52:55], v208 offset:10240\n"
"v_add_u32 v208, 512, v208\n"
"v_mfma_f32_16x16x32_bf16 v[56:59], a[64:67], a[92:95], v[56:59]\n"
"v_mfma_f32_16x16x32_bf16 v[64:67], a[64:67], a[96:99], v[64:67]\n"
"ds_read_b64_tr_b16 a[4:5], v209 offset:8\n"
"v_mfma_f32_16x16x32_bf16 v[72:75], a[64:67], a[100:103], v[72:75]\n"
"ds_read_b64_tr_b16 a[6:7], v209 offset:264\n"
"v_mfma_f32_16x16x32_bf16 v[80:83], a[64:67], a[104:107], v[80:83]\n"
"v_mfma_f32_16x16x32_bf16 v[88:91], a[64:67], a[108:111], v[88:91]\n"
"ds_read_b64_tr_b16 a[8:9], v209 offset:512\n"
"v_mfma_f32_16x16x32_bf16 v[52:55], a[68:71], a[88:91], v[52:55]\n"
"v_mfma_f32_16x16x32_bf16 v[60:63], a[68:71], a[92:95], v[60:63]\n"
"ds_read_b64_tr_b16 a[10:11], v209 offset:768\n"
"v_mfma_f32_16x16x32_bf16 v[68:71], a[68:71], a[96:99], v[68:71]\n"
"v_mfma_f32_16x16x32_bf16 v[76:79], a[68:71], a[100:103], v[76:79]\n"
"ds_read_b64_tr_b16 a[12:13], v209 offset:520\n"
"v_mfma_f32_16x16x32_bf16 v[84:87], a[68:71], a[104:107], v[84:87]\n"
"ds_read_b64_tr_b16 a[14:15], v209 offset:776\n"
"v_mfma_f32_16x16x32_bf16 v[92:95], a[68:71], a[108:111], v[92:95]\n"
"s_mov_b32 s56, 24576\n"
"v_mfma_f32_16x16x32_bf16 v[96:99], a[72:75], a[88:91], v[96:99]\n"
"ds_read_b64_tr_b16 a[16:17], v209 offset:1024\n"
"v_mfma_f32_16x16x32_bf16 v[104:107], a[72:75], a[92:95], v[104:107]\n"
"v_mfma_f32_16x16x32_bf16 v[112:115], a[72:75], a[96:99], v[112:115]\n"
"buffer_load_dwordx4 a[208:211], v195, s[80:83], s56 offen nt\n"
"v_mfma_f32_16x16x32_bf16 v[120:123], a[72:75], a[100:103], v[120:123]\n"
"s_mov_b32 s56, 24576\n"
"ds_read_b64_tr_b16 a[18:19], v209 offset:1280\n"
"v_mfma_f32_16x16x32_bf16 v[128:131], a[72:75], a[104:107], v[128:131]\n"
"buffer_load_dwordx4 a[212:215], v196, s[80:83], s56 offen offset:2048 nt\n"
"v_mfma_f32_16x16x32_bf16 v[136:139], a[72:75], a[108:111], v[136:139]\n"
"s_mov_b32 s56, 28672\n"
"ds_read_b64_tr_b16 a[20:21], v209 offset:1032\n"
"v_mfma_f32_16x16x32_bf16 v[100:103], a[76:79], a[88:91], v[100:103]\n"
"buffer_load_dwordx4 a[216:219], v195, s[80:83], s56 offen nt\n"
"v_mfma_f32_16x16x32_bf16 v[108:111], a[76:79], a[92:95], v[108:111]\n"
"s_mov_b32 s56, 28672\n"
"ds_read_b64_tr_b16 a[22:23], v209 offset:1288\n"
"v_mfma_f32_16x16x32_bf16 v[116:119], a[76:79], a[96:99], v[116:119]\n"
"buffer_load_dwordx4 a[220:223], v196, s[80:83], s56 offen offset:2048 nt\n"
"v_mfma_f32_16x16x32_bf16 v[124:127], a[76:79], a[100:103], v[124:127]\n"
"s_mov_b32 s56, 155648\n"
"ds_read_b64_tr_b16 a[24:25], v209 offset:1536\n"
"v_mfma_f32_16x16x32_bf16 v[132:135], a[76:79], a[104:107], v[132:135]\n"
"buffer_load_dwordx4 a[224:227], v195, s[80:83], s56 offen nt\n"
"v_mfma_f32_16x16x32_bf16 v[140:143], a[76:79], a[108:111], v[140:143]\n"
"s_mov_b32 s56, 155648\n"
"ds_read_b64_tr_b16 a[26:27], v209 offset:1792\n"
"v_mfma_f32_16x16x32_bf16 v[144:147], a[80:83], a[88:91], v[144:147]\n"
"buffer_load_dwordx4 a[228:231], v196, s[80:83], s56 offen offset:2048 nt\n"
"v_mfma_f32_16x16x32_bf16 v[152:155], a[80:83], a[92:95], v[152:155]\n"
"s_mov_b32 s56, 159744\n"
"ds_read_b64_tr_b16 a[28:29], v209 offset:1544\n"
"v_mfma_f32_16x16x32_bf16 v[160:163], a[80:83], a[96:99], v[160:163]\n"
"buffer_load_dwordx4 a[232:235], v195, s[80:83], s56 offen nt\n"
"v_mfma_f32_16x16x32_bf16 v[168:171], a[80:83], a[100:103], v[168:171]\n"
"s_mov_b32 s56, 159744\n"
"ds_read_b64_tr_b16 a[30:31], v209 offset:1800\n"
"v_add_u32 v209, 0xfffe0000, v209\n"
"v_mfma_f32_16x16x32_bf16 v[176:179], a[80:83], a[104:107], v[176:179]\n"
"buffer_load_dwordx4 a[236:239], v196, s[80:83], s56 offen offset:2048 nt\n"
"v_mfma_f32_16x16x32_bf16 v[184:187], a[80:83], a[108:111], v[184:187]\n"
"v_mfma_f32_16x16x32_bf16 v[148:151], a[84:87], a[88:91], v[148:151]\n"
"v_mfma_f32_16x16x32_bf16 v[156:159], a[84:87], a[92:95], v[156:159]\n"
"v_mfma_f32_16x16x32_bf16 v[164:167], a[84:87], a[96:99], v[164:167]\n"
"v_mfma_f32_16x16x32_bf16 v[172:175], a[84:87], a[100:103], v[172:175]\n"
"v_mfma_f32_16x16x32_bf16 v[180:183], a[84:87], a[104:107], v[180:183]\n"
"v_mfma_f32_16x16x32_bf16 v[188:191], a[84:87], a[108:111], v[188:191]\n"
"s_waitcnt lgkmcnt(0)\n"
"s_barrier\n"
"v_mfma_f32_16x16x32_bf16 v[0:3], a[0:3], a[32:35], v[0:3]\n"
"s_mov_b32 s61, 0xfffe7e00\n"
"s_cmp_eq_u32 s54, 0\n"
"ds_read_b64_tr_b16 a[56:57], v209\n"
"v_mfma_f32_16x16x32_bf16 v[8:11], a[0:3], a[36:39], v[8:11]\n"
"s_cselect_b32 s61, s61, 65024\n"
"ds_read_b64_tr_b16 a[58:59], v209 offset:256\n"
"v_mfma_f32_16x16x32_bf16 v[16:19], a[0:3], a[40:43], v[16:19]\n"
"v_mfma_f32_16x16x32_bf16 v[24:27], a[0:3], a[44:47], v[24:27]\n"
"ds_read_b128 a[88:91], v208\n"
"v_mfma_f32_16x16x32_bf16 v[32:35], a[0:3], a[48:51], v[32:35]\n"
"v_mfma_f32_16x16x32_bf16 v[40:43], a[0:3], a[52:55], v[40:43]\n"
"ds_read_b128 a[92:95], v208 offset:2048\n"
"v_mfma_f32_16x16x32_bf16 v[4:7], a[4:7], a[32:35], v[4:7]\n"
"v_mfma_f32_16x16x32_bf16 v[12:15], a[4:7], a[36:39], v[12:15]\n"
"ds_read_b128 a[96:99], v208 offset:4096\n"
"v_mfma_f32_16x16x32_bf16 v[20:23], a[4:7], a[40:43], v[20:23]\n"
"ds_read_b128 a[100:103], v208 offset:6144\n"
"v_mfma_f32_16x16x32_bf16 v[28:31], a[4:7], a[44:47], v[28:31]\n"
"v_mfma_f32_16x16x32_bf16 v[36:39], a[4:7], a[48:51], v[36:39]\n"
"ds_read_b128 a[104:107], v208 offset:8192\n"
"v_mfma_f32_16x16x32_bf16 v[44:47], a[4:7], a[52:55], v[44:47]\n"
"v_mfma_f32_16x16x32_bf16 v[48:51], a[8:11], a[32:35], v[48:51]\n"
"ds_read_b128 a[108:111], v208 offset:10240\n"
"v_add_u32 v208, s61, v208\n"
"v_mfma_f32_16x16x32_bf16 v[56:59], a[8:11], a[36:39], v[56:59]\n"
"v_mfma_f32_16x16x32_bf16 v[64:67], a[8:11], a[40:43], v[64:67]\n"
"ds_read_b64_tr_b16 a[60:61], v209 offset:8\n"
"v_mfma_f32_16x16x32_bf16 v[72:75], a[8:11], a[44:47], v[72:75]\n"
"ds_read_b64_tr_b16 a[62:63], v209 offset:264\n"
"v_mfma_f32_16x16x32_bf16 v[80:83], a[8:11], a[48:51], v[80:83]\n"
"v_mfma_f32_16x16x32_bf16 v[88:91], a[8:11], a[52:55], v[88:91]\n"
"ds_read_b64_tr_b16 a[64:65], v209 offset:512\n"
"v_mfma_f32_16x16x32_bf16 v[52:55], a[12:15], a[32:35], v[52:55]\n"
"v_mfma_f32_16x16x32_bf16 v[60:63], a[12:15], a[36:39], v[60:63]\n"
"ds_read_b64_tr_b16 a[66:67], v209 offset:768\n"
"v_mfma_f32_16x16x32_bf16 v[68:71], a[12:15], a[40:43], v[68:71]\n"
"v_mfma_f32_16x16x32_bf16 v[76:79], a[12:15], a[44:47], v[76:79]\n"
"ds_read_b64_tr_b16 a[68:69], v209 offset:520\n"
"v_mfma_f32_16x16x32_bf16 v[84:87], a[12:15], a[48:51], v[84:87]\n"
"ds_read_b64_tr_b16 a[70:71], v209 offset:776\n"
"v_mfma_f32_16x16x32_bf16 v[92:95], a[12:15], a[52:55], v[92:95]\n"
"v_mfma_f32_16x16x32_bf16 v[96:99], a[16:19], a[32:35], v[96:99]\n"
"ds_read_b64_tr_b16 a[72:73], v209 offset:1024\n"
"v_mfma_f32_16x16x32_bf16 v[104:107], a[16:19], a[36:39], v[104:107]\n"
"v_mfma_f32_16x16x32_bf16 v[112:115], a[16:19], a[40:43], v[112:115]\n"
"ds_read_b64_tr_b16 a[74:75], v209 offset:1280\n"
"v_mfma_f32_16x16x32_bf16 v[120:123], a[16:19], a[44:47], v[120:123]\n"
"v_mfma_f32_16x16x32_bf16 v[128:131], a[16:19], a[48:51], v[128:131]\n"
"ds_read_b64_tr_b16 a[76:77], v209 offset:1032\n"
"v_mfma_f32_16x16x32_bf16 v[136:139], a[16:19], a[52:55], v[136:139]\n"
"ds_read_b64_tr_b16 a[78:79], v209 offset:1288\n"
"v_mfma_f32_16x16x32_bf16 v[100:103], a[20:23], a[32:35], v[100:103]\n"
"v_mfma_f32_16x16x32_bf16 v[108:111], a[20:23], a[36:39], v[108:111]\n"
"ds_read_b64_tr_b16 a[80:81], v209 offset:1536\n"
"v_mfma_f32_16x16x32_bf16 v[116:119], a[20:23], a[40:43], v[116:119]\n"
"v_mfma_f32_16x16x32_bf16 v[124:127], a[20:23], a[44:47], v[124:127]\n"
"ds_read_b64_tr_b16 a[82:83], v209 offset:1792\n"
"v_mfma_f32_16x16x32_bf16 v[132:135], a[20:23], a[48:51], v[132:135]\n"
"v_mfma_f32_16x16x32_bf16 v[140:143], a[20:23], a[52:55], v[140:143]\n"
"ds_read_b64_tr_b16 a[84:85], v209 offset:1544\n"
"v_mfma_f32_16x16x32_bf16 v[144:147], a[24:27], a[32:35], v[144:147]\n"
"ds_read_b64_tr_b16 a[86:87], v209 offset:1800\n"
"v_mfma_f32_16x16x32_bf16 v[152:155], a[24:27], a[36:39], v[152:155]\n"
"v_add_u32 v209, 32768, v209\n"
"v_mfma_f32_16x16x32_bf16 v[160:163], a[24:27], a[40:43], v[160:163]\n"
"v_mfma_f32_16x16x32_bf16 v[168:171], a[24:27], a[44:47], v[168:171]\n"
"v_mfma_f32_16x16x32_bf16 v[176:179], a[24:27], a[48:51], v[176:179]\n"
"v_mfma_f32_16x16x32_bf16 v[184:187], a[24:27], a[52:55], v[184:187]\n"
"v_mfma_f32_16x16x32_bf16 v[148:151], a[28:31], a[32:35], v[148:151]\n"
"v_mfma_f32_16x16x32_bf16 v[156:159], a[28:31], a[36:39], v[156:159]\n"
"v_mfma_f32_16x16x32_bf16 v[164:167], a[28:31], a[40:43], v[164:167]\n"
"v_mfma_f32_16x16x32_bf16 v[172:175], a[28:31], a[44:47], v[172:175]\n"
"v_mfma_f32_16x16x32_bf16 v[180:183], a[28:31], a[48:51], v[180:183]\n"
"v_mfma_f32_16x16x32_bf16 v[188:191], a[28:31], a[52:55], v[188:191]\n"
"s_waitcnt lgkmcnt(0)\n"
"s_barrier\n"
"v_mfma_f32_16x16x32_bf16 v[0:3], a[56:59], a[88:91], v[0:3]\n"
"ds_read_b64_tr_b16 a[0:1], v209\n"
"v_mfma_f32_16x16x32_bf16 v[8:11], a[56:59], a[92:95], v[8:11]\n"
"ds_read_b64_tr_b16 a[2:3], v209 offset:256\n"
"v_mfma_f32_16x16x32_bf16 v[16:19], a[56:59], a[96:99], v[16:19]\n"
"v_mfma_f32_16x16x32_bf16 v[24:27], a[56:59], a[100:103], v[24:27]\n"
"ds_read_b128 a[32:35], v208\n"
"v_mfma_f32_16x16x32_bf16 v[32:35], a[56:59], a[104:107], v[32:35]\n"
"v_mfma_f32_16x16x32_bf16 v[40:43], a[56:59], a[108:111], v[40:43]\n"
"ds_read_b128 a[36:39], v208 offset:2048\n"
"v_mfma_f32_16x16x32_bf16 v[4:7], a[60:63], a[88:91], v[4:7]\n"
"v_mfma_f32_16x16x32_bf16 v[12:15], a[60:63], a[92:95], v[12:15]\n"
"ds_read_b128 a[40:43], v208 offset:4096\n"
"v_mfma_f32_16x16x32_bf16 v[20:23], a[60:63], a[96:99], v[20:23]\n"
"ds_read_b128 a[44:47], v208 offset:6144\n"
"v_mfma_f32_16x16x32_bf16 v[28:31], a[60:63], a[100:103], v[28:31]\n"
"v_mfma_f32_16x16x32_bf16 v[36:39], a[60:63], a[104:107], v[36:39]\n"
"ds_read_b128 a[48:51], v208 offset:8192\n"
"v_mfma_f32_16x16x32_bf16 v[44:47], a[60:63], a[108:111], v[44:47]\n"
"v_mfma_f32_16x16x32_bf16 v[48:51], a[64:67], a[88:91], v[48:51]\n"
"ds_read_b128 a[52:55], v208 offset:10240\n"
"v_add_u32 v208, 512, v208\n"
"v_mfma_f32_16x16x32_bf16 v[56:59], a[64:67], a[92:95], v[56:59]\n"
"v_mfma_f32_16x16x32_bf16 v[64:67], a[64:67], a[96:99], v[64:67]\n"
"ds_read_b64_tr_b16 a[4:5], v209 offset:8\n"
"v_mfma_f32_16x16x32_bf16 v[72:75], a[64:67], a[100:103], v[72:75]\n"
"ds_read_b64_tr_b16 a[6:7], v209 offset:264\n"
"v_mfma_f32_16x16x32_bf16 v[80:83], a[64:67], a[104:107], v[80:83]\n"
"v_mfma_f32_16x16x32_bf16 v[88:91], a[64:67], a[108:111], v[88:91]\n"
"ds_read_b64_tr_b16 a[8:9], v209 offset:512\n"
"v_mfma_f32_16x16x32_bf16 v[52:55], a[68:71], a[88:91], v[52:55]\n"
"v_mfma_f32_16x16x32_bf16 v[60:63], a[68:71], a[92:95], v[60:63]\n"
"ds_read_b64_tr_b16 a[10:11], v209 offset:768\n"
"v_mfma_f32_16x16x32_bf16 v[68:71], a[68:71], a[96:99], v[68:71]\n"
"v_mfma_f32_16x16x32_bf16 v[76:79], a[68:71], a[100:103], v[76:79]\n"
"ds_read_b64_tr_b16 a[12:13], v209 offset:520\n"
"v_mfma_f32_16x16x32_bf16 v[84:87], a[68:71], a[104:107], v[84:87]\n"
"ds_read_b64_tr_b16 a[14:15], v209 offset:776\n"
"v_mfma_f32_16x16x32_bf16 v[92:95], a[68:71], a[108:111], v[92:95]\n"
"v_mfma_f32_16x16x32_bf16 v[96:99], a[72:75], a[88:91], v[96:99]\n"
"ds_read_b64_tr_b16 a[16:17], v209 offset:1024\n"
"v_mfma_f32_16x16x32_bf16 v[104:107], a[72:75], a[92:95], v[104:107]\n"
"s_add_u32 m0, s73, 0\n"
"v_mfma_f32_16x16x32_bf16 v[112:115], a[72:75], a[96:99], v[112:115]\n"
"s_mov_b32 s55, 0\n"
"ds_read_b64_tr_b16 a[18:19], v209 offset:1280\n"
"v_mfma_f32_16x16x32_bf16 v[120:123], a[72:75], a[100:103], v[120:123]\n"
"v_mfma_f32_16x16x32_bf16 v[128:131], a[72:75], a[104:107], v[128:131]\n"
"buffer_load_dwordx4 v195, s[24:27], s55 offen nt lds\n"
"v_mfma_f32_16x16x32_bf16 v[136:139], a[72:75], a[108:111], v[136:139]\n"
"ds_read_b64_tr_b16 a[20:21], v209 offset:1032\n"
"v_mfma_f32_16x16x32_bf16 v[100:103], a[76:79], a[88:91], v[100:103]\n"
"s_add_u32 m0, s73, 0\n"
"s_mov_b32 s55, 0\n"
"v_mfma_f32_16x16x32_bf16 v[108:111], a[76:79], a[92:95], v[108:111]\n"
"ds_read_b64_tr_b16 a[22:23], v209 offset:1288\n"
"v_mfma_f32_16x16x32_bf16 v[116:119], a[76:79], a[96:99], v[116:119]\n"
"ds_read_b64_tr_b16 a[24:25], v209 offset:1536\n"
"v_mfma_f32_16x16x32_bf16 v[124:127], a[76:79], a[100:103], v[124:127]\n"
"buffer_load_dwordx4 v196, s[24:27], s55 offen offset:2048 nt lds\n"
"v_mfma_f32_16x16x32_bf16 v[132:135], a[76:79], a[104:107], v[132:135]\n"
"ds_read_b64_tr_b16 a[26:27], v209 offset:1792\n"
"v_mfma_f32_16x16x32_bf16 v[140:143], a[76:79], a[108:111], v[140:143]\n"
"s_add_u32 m0, s73, 4096\n"
"s_mov_b32 s55, 4096\n"
"ds_read_b64_tr_b16 a[28:29], v209 offset:1544\n"
"v_mfma_f32_16x16x32_bf16 v[144:147], a[80:83], a[88:91], v[144:147]\n"
"ds_read_b64_tr_b16 a[30:31], v209 offset:1800\n"
"v_mfma_f32_16x16x32_bf16 v[152:155], a[80:83], a[92:95], v[152:155]\n"
"v_add_u32 v209, 32768, v209\n"
"v_mfma_f32_16x16x32_bf16 v[160:163], a[80:83], a[96:99], v[160:163]\n"
"buffer_load_dwordx4 v195, s[24:27], s55 offen nt lds\n"
"v_mfma_f32_16x16x32_bf16 v[168:171], a[80:83], a[100:103], v[168:171]\n"
"s_add_u32 m0, s73, 4096\n"
"v_mfma_f32_16x16x32_bf16 v[176:179], a[80:83], a[104:107], v[176:179]\n"
"s_mov_b32 s55, 4096\n"
"v_mfma_f32_16x16x32_bf16 v[184:187], a[80:83], a[108:111], v[184:187]\n"
"v_mfma_f32_16x16x32_bf16 v[148:151], a[84:87], a[88:91], v[148:151]\n"
"buffer_load_dwordx4 v196, s[24:27], s55 offen offset:2048 nt lds\n"
"v_mfma_f32_16x16x32_bf16 v[156:159], a[84:87], a[92:95], v[156:159]\n"
"v_mfma_f32_16x16x32_bf16 v[164:167], a[84:87], a[96:99], v[164:167]\n"
"v_mfma_f32_16x16x32_bf16 v[172:175], a[84:87], a[100:103], v[172:175]\n"
"v_mfma_f32_16x16x32_bf16 v[180:183], a[84:87], a[104:107], v[180:183]\n"
"v_mfma_f32_16x16x32_bf16 v[188:191], a[84:87], a[108:111], v[188:191]\n"
"s_waitcnt lgkmcnt(0)\n"
"s_barrier\n"
"v_mfma_f32_16x16x32_bf16 v[0:3], a[0:3], a[32:35], v[0:3]\n"
"s_mov_b64 s[32:33], %5\n"
"ds_read_b64_tr_b16 a[56:57], v209\n"
"s_mov_b32 s34, 1048576\n"
"v_mfma_f32_16x16x32_bf16 v[8:11], a[0:3], a[36:39], v[8:11]\n"
"s_add_u32 m0, s73, 32768\n"
"s_mov_b32 s55, 8192\n"
"ds_read_b64_tr_b16 a[58:59], v209 offset:256\n"
"v_mfma_f32_16x16x32_bf16 v[16:19], a[0:3], a[40:43], v[16:19]\n"
"s_mov_b32 s35, 0x20000\n"
"s_mov_b64 s[36:37], %6\n"
"s_mov_b32 s38, 98304\n"
"v_mfma_f32_16x16x32_bf16 v[24:27], a[0:3], a[44:47], v[24:27]\n"
"s_mov_b32 s39, 0x20000\n"
"ds_read_b128 a[88:91], v208\n"
"s_mov_b32 s59, s73\n"
"v_mfma_f32_16x16x32_bf16 v[32:35], a[0:3], a[48:51], v[32:35]\n"
"buffer_load_dwordx4 v195, s[24:27], s55 offen nt lds\n"
"v_mfma_f32_16x16x32_bf16 v[40:43], a[0:3], a[52:55], v[40:43]\n"
"ds_read_b128 a[92:95], v208 offset:2048\n"
"v_mfma_f32_16x16x32_bf16 v[4:7], a[4:7], a[32:35], v[4:7]\n"
"v_mfma_f32_16x16x32_bf16 v[12:15], a[4:7], a[36:39], v[12:15]\n"
"ds_read_b128 a[96:99], v208 offset:4096\n"
"v_mfma_f32_16x16x32_bf16 v[20:23], a[4:7], a[40:43], v[20:23]\n"
"ds_read_b128 a[100:103], v208 offset:6144\n"
"v_mfma_f32_16x16x32_bf16 v[28:31], a[4:7], a[44:47], v[28:31]\n"
"v_mfma_f32_16x16x32_bf16 v[36:39], a[4:7], a[48:51], v[36:39]\n"
"ds_read_b128 a[104:107], v208 offset:8192\n"
"v_mfma_f32_16x16x32_bf16 v[44:47], a[4:7], a[52:55], v[44:47]\n"
"v_mfma_f32_16x16x32_bf16 v[48:51], a[8:11], a[32:35], v[48:51]\n"
"s_add_u32 m0, s73, 32768\n"
"s_mov_b32 s55, 8192\n"
"ds_read_b128 a[108:111], v208 offset:10240\n"
"v_mfma_f32_16x16x32_bf16 v[56:59], a[8:11], a[36:39], v[56:59]\n"
"v_add_u32 v208, 65024, v208\n"
"v_mfma_f32_16x16x32_bf16 v[64:67], a[8:11], a[40:43], v[64:67]\n"
"ds_read_b64_tr_b16 a[60:61], v209 offset:8\n"
"v_mfma_f32_16x16x32_bf16 v[72:75], a[8:11], a[44:47], v[72:75]\n"
"buffer_load_dwordx4 v196, s[24:27], s55 offen offset:2048 nt lds\n"
"v_mfma_f32_16x16x32_bf16 v[80:83], a[8:11], a[48:51], v[80:83]\n"
"ds_read_b64_tr_b16 a[62:63], v209 offset:264\n"
"v_mfma_f32_16x16x32_bf16 v[88:91], a[8:11], a[52:55], v[88:91]\n"
"ds_read_b64_tr_b16 a[64:65], v209 offset:512\n"
"v_mfma_f32_16x16x32_bf16 v[52:55], a[12:15], a[32:35], v[52:55]\n"
"v_mfma_f32_16x16x32_bf16 v[60:63], a[12:15], a[36:39], v[60:63]\n"
"ds_read_b64_tr_b16 a[66:67], v209 offset:768\n"
"v_mfma_f32_16x16x32_bf16 v[68:71], a[12:15], a[40:43], v[68:71]\n"
"v_mfma_f32_16x16x32_bf16 v[76:79], a[12:15], a[44:47], v[76:79]\n"
"ds_read_b64_tr_b16 a[68:69], v209 offset:520\n"
"v_mfma_f32_16x16x32_bf16 v[84:87], a[12:15], a[48:51], v[84:87]\n"
"ds_read_b64_tr_b16 a[70:71], v209 offset:776\n"
"v_mfma_f32_16x16x32_bf16 v[92:95], a[12:15], a[52:55], v[92:95]\n"
"s_add_u32 m0, s73, 36864\n"
"s_mov_b32 s55, 12288\n"
"v_mfma_f32_16x16x32_bf16 v[96:99], a[16:19], a[32:35], v[96:99]\n"
"ds_read_b64_tr_b16 a[72:73], v209 offset:1024\n"
"v_mfma_f32_16x16x32_bf16 v[104:107], a[16:19], a[36:39], v[104:107]\n"
"v_mfma_f32_16x16x32_bf16 v[112:115], a[16:19], a[40:43], v[112:115]\n"
"buffer_load_dwordx4 v195, s[24:27], s55 offen nt lds\n"
"v_mfma_f32_16x16x32_bf16 v[120:123], a[16:19], a[44:47], v[120:123]\n"
"ds_read_b64_tr_b16 a[74:75], v209 offset:1280\n"
"v_mfma_f32_16x16x32_bf16 v[128:131], a[16:19], a[48:51], v[128:131]\n"
"ds_read_b64_tr_b16 a[76:77], v209 offset:1032\n"
"v_mfma_f32_16x16x32_bf16 v[136:139], a[16:19], a[52:55], v[136:139]\n"
"ds_read_b64_tr_b16 a[78:79], v209 offset:1288\n"
"v_mfma_f32_16x16x32_bf16 v[100:103], a[20:23], a[32:35], v[100:103]\n"
"v_mfma_f32_16x16x32_bf16 v[108:111], a[20:23], a[36:39], v[108:111]\n"
"ds_read_b64_tr_b16 a[80:81], v209 offset:1536\n"
"v_mfma_f32_16x16x32_bf16 v[116:119], a[20:23], a[40:43], v[116:119]\n"
"v_mfma_f32_16x16x32_bf16 v[124:127], a[20:23], a[44:47], v[124:127]\n"
"ds_read_b64_tr_b16 a[82:83], v209 offset:1792\n"
"v_mfma_f32_16x16x32_bf16 v[132:135], a[20:23], a[48:51], v[132:135]\n"
"s_add_u32 m0, s73, 36864\n"
"s_lshl_b32 s47, s44, 5\n"
"s_lshl_b32 s48, s45, 3\n"
"v_mfma_f32_16x16x32_bf16 v[140:143], a[20:23], a[52:55], v[140:143]\n"
"s_add_u32 s47, s47, s48\n"
"s_add_u32 s47, s47, s53\n"
"s_lshl_b32 s47, s47, 1\n"
"v_mfma_f32_16x16x32_bf16 v[144:147], a[24:27], a[32:35], v[144:147]\n"
"s_add_u32 s47, s47, s54\n"
"s_lshl_b32 s49, s54, 12\n"
"s_add_u32 s48, s46, 0\n"
"v_mfma_f32_16x16x32_bf16 v[152:155], a[24:27], a[36:39], v[152:155]\n"
"s_and_b32 s48, s48, 3\n"
"s_mul_i32 s47, s47, 24576\n"
"s_mul_i32 s88, s48, 98304\n"
"v_mfma_f32_16x16x32_bf16 v[160:163], a[24:27], a[40:43], v[160:163]\n"
"s_add_u32 s88, s88, s47\n"
"s_lshl_b32 s65, s48, 18\n"
"s_add_u32 s65, s65, s49\n"
"v_mfma_f32_16x16x32_bf16 v[168:171], a[24:27], a[44:47], v[168:171]\n"
"s_add_u32 s48, s46, 1\n"
"s_and_b32 s48, s48, 3\n"
"s_mul_i32 s89, s48, 98304\n"
"v_mfma_f32_16x16x32_bf16 v[176:179], a[24:27], a[48:51], v[176:179]\n"
"s_add_u32 s89, s89, s47\n"
"s_lshl_b32 s66, s48, 18\n"
"s_add_u32 s66, s66, s49\n"
"v_mfma_f32_16x16x32_bf16 v[184:187], a[24:27], a[52:55], v[184:187]\n"
"s_add_u32 s48, s46, 2\n"
"s_and_b32 s48, s48, 3\n"
"s_mul_i32 s90, s48, 98304\n"
"v_mfma_f32_16x16x32_bf16 v[148:151], a[28:31], a[32:35], v[148:151]\n"
"s_add_u32 s90, s90, s47\n"
"s_lshl_b32 s67, s48, 18\n"
"s_add_u32 s67, s67, s49\n"
"v_mfma_f32_16x16x32_bf16 v[156:159], a[28:31], a[36:39], v[156:159]\n"
"s_add_u32 s48, s46, 3\n"
"s_and_b32 s48, s48, 3\n"
"s_mul_i32 s91, s48, 98304\n"
"v_mfma_f32_16x16x32_bf16 v[164:167], a[28:31], a[40:43], v[164:167]\n"
"s_add_u32 s91, s91, s47\n"
"s_lshl_b32 s68, s48, 18\n"
"s_add_u32 s68, s68, s49\n"
"v_mfma_f32_16x16x32_bf16 v[172:175], a[28:31], a[44:47], v[172:175]\n"
"s_lshl_b32 s47, s44, 6\n"
"s_add_u32 s47, s47, s52\n"
"s_lshl_b32 s47, s47, 7\n"
"v_mfma_f32_16x16x32_bf16 v[180:183], a[28:31], a[48:51], v[180:183]\n"
"s_add_u32 s99, s47, 37748736\n"
"s_lshl_b32 s48, s45, 2\n"
"s_add_u32 s48, s48, s46\n"
"v_mfma_f32_16x16x32_bf16 v[188:191], a[28:31], a[52:55], v[188:191]\n"
"s_lshl_b32 s48, s48, 9\n"
"s_add_u32 s98, s99, s48\n"
"s_lshl_b32 s47, s44, 2\n"
"s_add_u32 s47, s47, s46\n"
"s_lshl_b32 s47, s47, 2\n"
"s_add_u32 s47, s47, s52\n"
"s_mul_i32 s47, s47, 49152\n"
"s_add_u32 s47, s47, 25165824\n"
"s_mul_i32 s48, s45, 3072\n"
"s_add_u32 s40, s47, s48\n"
"s_mul_i32 s48, s45, 12288\n"
"s_add_u32 s41, s47, s48\n"
"s_lshl_b32 s47, s44, 2\n"
"s_add_u32 s47, s47, s52\n"
"s_mov_b32 s55, 12288\n"
"s_lshl_b32 s47, s47, 7\n"
"ds_read_b64_tr_b16 a[84:85], v209 offset:1544\n"
"ds_read_b64_tr_b16 a[86:87], v209 offset:1800\n"
"buffer_load_dwordx4 v196, s[24:27], s55 offen offset:2048 nt lds\n"
"s_add_u32 s42, s47, 37879808\n"
"v_add_u32 v209, 32768, v209\n"
"s_waitcnt lgkmcnt(0)\n"
"s_barrier\n"
"v_mfma_f32_16x16x32_bf16 v[0:3], a[56:59], a[88:91], v[0:3]\n"
"s_waitcnt vmcnt(26)\n"
"ds_read_b64_tr_b16 a[0:1], v209\n"
"v_mfma_f32_16x16x32_bf16 v[8:11], a[56:59], a[92:95], v[8:11]\n"
"s_add_u32 m0, s73, 16384\n"
"s_mov_b32 s55, 0\n"
"ds_read_b64_tr_b16 a[2:3], v209 offset:256\n"
"v_mfma_f32_16x16x32_bf16 v[16:19], a[56:59], a[96:99], v[16:19]\n"
"ds_write_b64 v219, a[112:113]\n"
"v_mfma_f32_16x16x32_bf16 v[24:27], a[56:59], a[100:103], v[24:27]\n"
"ds_read_b128 a[32:35], v208\n"
"v_mfma_f32_16x16x32_bf16 v[32:35], a[56:59], a[104:107], v[32:35]\n"
"buffer_load_dwordx4 v197, s[28:31], s55 offen nt lds\n"
"v_mfma_f32_16x16x32_bf16 v[40:43], a[56:59], a[108:111], v[40:43]\n"
"ds_read_b128 a[36:39], v208 offset:2048\n"
"v_mfma_f32_16x16x32_bf16 v[4:7], a[60:63], a[88:91], v[4:7]\n"
"ds_write_b64 v219, a[114:115] offset:8\n"
"v_mfma_f32_16x16x32_bf16 v[12:15], a[60:63], a[92:95], v[12:15]\n"
"ds_read_b128 a[40:43], v208 offset:4096\n"
"v_mfma_f32_16x16x32_bf16 v[20:23], a[60:63], a[96:99], v[20:23]\n"
"ds_write_b64 v219, a[116:117] offset:2048\n"
"v_mfma_f32_16x16x32_bf16 v[28:31], a[60:63], a[100:103], v[28:31]\n"
"ds_read_b128 a[44:47], v208 offset:6144\n"
"v_mfma_f32_16x16x32_bf16 v[36:39], a[60:63], a[104:107], v[36:39]\n"
"ds_read_b128 a[48:51], v208 offset:8192\n"
"v_mfma_f32_16x16x32_bf16 v[44:47], a[60:63], a[108:111], v[44:47]\n"
"ds_write_b64 v219, a[118:119] offset:2056\n"
"v_mfma_f32_16x16x32_bf16 v[48:51], a[64:67], a[88:91], v[48:51]\n"
"ds_read_b128 a[52:55], v208 offset:10240\n"
"v_add_u32 v208, 512, v208\n"
"v_mfma_f32_16x16x32_bf16 v[56:59], a[64:67], a[92:95], v[56:59]\n"
"ds_write_b64 v219, a[120:121] offset:4096\n"
"v_mfma_f32_16x16x32_bf16 v[64:67], a[64:67], a[96:99], v[64:67]\n"
"ds_read_b64_tr_b16 a[4:5], v209 offset:8\n"
"v_mfma_f32_16x16x32_bf16 v[72:75], a[64:67], a[100:103], v[72:75]\n"
"ds_write_b64 v219, a[122:123] offset:4104\n"
"v_mfma_f32_16x16x32_bf16 v[80:83], a[64:67], a[104:107], v[80:83]\n"
"ds_read_b64_tr_b16 a[6:7], v209 offset:264\n"
"v_mfma_f32_16x16x32_bf16 v[88:91], a[64:67], a[108:111], v[88:91]\n"
"ds_read_b64_tr_b16 a[8:9], v209 offset:512\n"
"v_mfma_f32_16x16x32_bf16 v[52:55], a[68:71], a[88:91], v[52:55]\n"
"ds_write_b64 v219, a[124:125] offset:6144\n"
"v_mfma_f32_16x16x32_bf16 v[60:63], a[68:71], a[92:95], v[60:63]\n"
"ds_read_b64_tr_b16 a[10:11], v209 offset:768\n"
"v_mfma_f32_16x16x32_bf16 v[68:71], a[68:71], a[96:99], v[68:71]\n"
"ds_write_b64 v219, a[126:127] offset:6152\n"
"v_mfma_f32_16x16x32_bf16 v[76:79], a[68:71], a[100:103], v[76:79]\n"
"ds_read_b64_tr_b16 a[12:13], v209 offset:520\n"
"v_mfma_f32_16x16x32_bf16 v[84:87], a[68:71], a[104:107], v[84:87]\n"
"ds_write_b64 v219, a[128:129] offset:8192\n"
"v_mfma_f32_16x16x32_bf16 v[92:95], a[68:71], a[108:111], v[92:95]\n"
"s_add_u32 m0, s73, 16384\n"
"s_mov_b32 s55, 0\n"
"ds_read_b64_tr_b16 a[14:15], v209 offset:776\n"
"v_mfma_f32_16x16x32_bf16 v[96:99], a[72:75], a[88:91], v[96:99]\n"
"ds_read_b64_tr_b16 a[16:17], v209 offset:1024\n"
"v_mfma_f32_16x16x32_bf16 v[104:107], a[72:75], a[92:95], v[104:107]\n"
"ds_write_b64 v219, a[130:131] offset:8200\n"
"v_mfma_f32_16x16x32_bf16 v[112:115], a[72:75], a[96:99], v[112:115]\n"
"buffer_load_dwordx4 v197, s[28:31], s55 offen offset:2048 nt lds\n"
"v_mfma_f32_16x16x32_bf16 v[120:123], a[72:75], a[100:103], v[120:123]\n"
"ds_read_b64_tr_b16 a[18:19], v209 offset:1280\n"
"v_mfma_f32_16x16x32_bf16 v[128:131], a[72:75], a[104:107], v[128:131]\n"
"ds_read_b64_tr_b16 a[20:21], v209 offset:1032\n"
"v_mfma_f32_16x16x32_bf16 v[136:139], a[72:75], a[108:111], v[136:139]\n"
"ds_write_b64 v219, a[132:133] offset:10240\n"
"v_mfma_f32_16x16x32_bf16 v[100:103], a[76:79], a[88:91], v[100:103]\n"
"ds_read_b64_tr_b16 a[22:23], v209 offset:1288\n"
"v_mfma_f32_16x16x32_bf16 v[108:111], a[76:79], a[92:95], v[108:111]\n"
"ds_read_b64_tr_b16 a[24:25], v209 offset:1536\n"
"v_mfma_f32_16x16x32_bf16 v[116:119], a[76:79], a[96:99], v[116:119]\n"
"ds_write_b64 v219, a[134:135] offset:10248\n"
"v_mfma_f32_16x16x32_bf16 v[124:127], a[76:79], a[100:103], v[124:127]\n"
"ds_read_b64_tr_b16 a[26:27], v209 offset:1792\n"
"v_mfma_f32_16x16x32_bf16 v[132:135], a[76:79], a[104:107], v[132:135]\n"
"ds_write_b64 v219, a[136:137] offset:12288\n"
"v_mfma_f32_16x16x32_bf16 v[140:143], a[76:79], a[108:111], v[140:143]\n"
"ds_read_b64_tr_b16 a[28:29], v209 offset:1544\n"
"v_mfma_f32_16x16x32_bf16 v[144:147], a[80:83], a[88:91], v[144:147]\n"
"ds_write_b64 v219, a[138:139] offset:12296\n"
"v_mfma_f32_16x16x32_bf16 v[152:155], a[80:83], a[92:95], v[152:155]\n"
"ds_read_b64_tr_b16 a[30:31], v209 offset:1800\n"
"v_add_u32 v209, 32768, v209\n"
"v_mfma_f32_16x16x32_bf16 v[160:163], a[80:83], a[96:99], v[160:163]\n"
"ds_write_b64 v219, a[140:141] offset:14336\n"
"v_mfma_f32_16x16x32_bf16 v[168:171], a[80:83], a[100:103], v[168:171]\n"
"ds_write_b64 v219, a[142:143] offset:14344\n"
"v_mfma_f32_16x16x32_bf16 v[176:179], a[80:83], a[104:107], v[176:179]\n"
"ds_write_b64 v219, a[144:145] offset:16384\n"
"v_mfma_f32_16x16x32_bf16 v[184:187], a[80:83], a[108:111], v[184:187]\n"
"ds_write_b64 v219, a[146:147] offset:16392\n"
"v_mfma_f32_16x16x32_bf16 v[148:151], a[84:87], a[88:91], v[148:151]\n"
"ds_write_b64 v219, a[148:149] offset:18432\n"
"v_mfma_f32_16x16x32_bf16 v[156:159], a[84:87], a[92:95], v[156:159]\n"
"ds_write_b64 v219, a[150:151] offset:18440\n"
"v_mfma_f32_16x16x32_bf16 v[164:167], a[84:87], a[96:99], v[164:167]\n"
"ds_write_b64 v219, a[152:153] offset:20480\n"
"v_mfma_f32_16x16x32_bf16 v[172:175], a[84:87], a[100:103], v[172:175]\n"
"ds_write_b64 v219, a[154:155] offset:20488\n"
"v_mfma_f32_16x16x32_bf16 v[180:183], a[84:87], a[104:107], v[180:183]\n"
"ds_write_b64 v219, a[156:157] offset:22528\n"
"v_mfma_f32_16x16x32_bf16 v[188:191], a[84:87], a[108:111], v[188:191]\n"
"ds_write_b64 v219, a[158:159] offset:22536\n"
"ds_write_b64 v219, a[160:161] offset:24576\n"
"ds_write_b64 v219, a[162:163] offset:24584\n"
"ds_write_b64 v219, a[164:165] offset:26624\n"
"ds_write_b64 v219, a[166:167] offset:26632\n"
"ds_write_b64 v219, a[168:169] offset:28672\n"
"ds_write_b64 v219, a[170:171] offset:28680\n"
"ds_write_b64 v219, a[172:173] offset:30720\n"
"ds_write_b64 v219, a[174:175] offset:30728\n"
"s_waitcnt lgkmcnt(0)\n"
"s_barrier\n"
"v_mfma_f32_16x16x32_bf16 v[0:3], a[0:3], a[32:35], v[0:3]\n"
"s_waitcnt vmcnt(12)\n"
"ds_read_b64_tr_b16 a[56:57], v209\n"
"v_mov_b32 v204, 0x3f803f80\n"
"v_mfma_f32_16x16x32_bf16 v[8:11], a[0:3], a[36:39], v[8:11]\n"
"s_add_u32 m0, s73, 20480\n"
"s_mov_b32 s55, 4096\n"
"ds_read_b64_tr_b16 a[58:59], v209 offset:256\n"
"v_mfma_f32_16x16x32_bf16 v[16:19], a[0:3], a[40:43], v[16:19]\n"
"ds_write_b64 v219, a[176:177] offset:32768\n"
"v_mfma_f32_16x16x32_bf16 v[24:27], a[0:3], a[44:47], v[24:27]\n"
"ds_read_b128 a[88:91], v208\n"
"v_mov_b32 v205, 0x3f803f80\n"
"v_mfma_f32_16x16x32_bf16 v[32:35], a[0:3], a[48:51], v[32:35]\n"
"buffer_load_dwordx4 v197, s[28:31], s55 offen nt lds\n"
"v_mfma_f32_16x16x32_bf16 v[40:43], a[0:3], a[52:55], v[40:43]\n"
"ds_read_b128 a[92:95], v208 offset:2048\n"
"v_mov_b32 v206, 0x3f803f80\n"
"v_mfma_f32_16x16x32_bf16 v[4:7], a[4:7], a[32:35], v[4:7]\n"
"ds_write_b64 v219, a[178:179] offset:32776\n"
"v_mfma_f32_16x16x32_bf16 v[12:15], a[4:7], a[36:39], v[12:15]\n"
"ds_read_b128 a[96:99], v208 offset:4096\n"
"v_mov_b32 v207, 0x3f803f80\n"
"v_mfma_f32_16x16x32_bf16 v[20:23], a[4:7], a[40:43], v[20:23]\n"
"ds_write_b64 v219, a[180:181] offset:34816\n"
"v_mfma_f32_16x16x32_bf16 v[28:31], a[4:7], a[44:47], v[28:31]\n"
"ds_read_b128 a[100:103], v208 offset:6144\n"
"v_mfma_f32_16x16x32_bf16 v[36:39], a[4:7], a[48:51], v[36:39]\n"
"ds_read_b128 a[104:107], v208 offset:8192\n"
"v_mfma_f32_16x16x32_bf16 v[44:47], a[4:7], a[52:55], v[44:47]\n"
"ds_write_b64 v219, a[182:183] offset:34824\n"
"v_mfma_f32_16x16x32_bf16 v[48:51], a[8:11], a[32:35], v[48:51]\n"
"ds_read_b128 a[108:111], v208 offset:10240\n"
"v_add_u32 v208, 0xfffe7e00, v208\n"
"v_mfma_f32_16x16x32_bf16 v[56:59], a[8:11], a[36:39], v[56:59]\n"
"ds_write_b64 v219, a[184:185] offset:36864\n"
"v_mfma_f32_16x16x32_bf16 v[64:67], a[8:11], a[40:43], v[64:67]\n"
"ds_read_b64_tr_b16 a[60:61], v209 offset:8\n"
"v_mfma_f32_16x16x32_bf16 v[72:75], a[8:11], a[44:47], v[72:75]\n"
"ds_write_b64 v219, a[186:187] offset:36872\n"
"v_mfma_f32_16x16x32_bf16 v[80:83], a[8:11], a[48:51], v[80:83]\n"
"ds_read_b64_tr_b16 a[62:63], v209 offset:264\n"
"v_mfma_f32_16x16x32_bf16 v[88:91], a[8:11], a[52:55], v[88:91]\n"
"ds_read_b64_tr_b16 a[64:65], v209 offset:512\n"
"v_mfma_f32_16x16x32_bf16 v[52:55], a[12:15], a[32:35], v[52:55]\n"
"ds_write_b64 v219, a[188:189] offset:38912\n"
"v_mfma_f32_16x16x32_bf16 v[60:63], a[12:15], a[36:39], v[60:63]\n"
"ds_read_b64_tr_b16 a[66:67], v209 offset:768\n"
"v_mfma_f32_16x16x32_bf16 v[68:71], a[12:15], a[40:43], v[68:71]\n"
"ds_write_b64 v219, a[190:191] offset:38920\n"
"v_mfma_f32_16x16x32_bf16 v[76:79], a[12:15], a[44:47], v[76:79]\n"
"ds_read_b64_tr_b16 a[68:69], v209 offset:520\n"
"v_mfma_f32_16x16x32_bf16 v[84:87], a[12:15], a[48:51], v[84:87]\n"
"ds_write_b64 v219, a[192:193] offset:40960\n"
"v_mfma_f32_16x16x32_bf16 v[92:95], a[12:15], a[52:55], v[92:95]\n"
"s_add_u32 m0, s73, 20480\n"
"s_mov_b32 s55, 4096\n"
"ds_read_b64_tr_b16 a[70:71], v209 offset:776\n"
"v_mfma_f32_16x16x32_bf16 v[96:99], a[16:19], a[32:35], v[96:99]\n"
"ds_read_b64_tr_b16 a[72:73], v209 offset:1024\n"
"v_mfma_f32_16x16x32_bf16 v[104:107], a[16:19], a[36:39], v[104:107]\n"
"ds_write_b64 v219, a[194:195] offset:40968\n"
"v_mfma_f32_16x16x32_bf16 v[112:115], a[16:19], a[40:43], v[112:115]\n"
"buffer_load_dwordx4 v197, s[28:31], s55 offen offset:2048 nt lds\n"
"v_mfma_f32_16x16x32_bf16 v[120:123], a[16:19], a[44:47], v[120:123]\n"
"ds_read_b64_tr_b16 a[74:75], v209 offset:1280\n"
"v_mfma_f32_16x16x32_bf16 v[128:131], a[16:19], a[48:51], v[128:131]\n"
"ds_read_b64_tr_b16 a[76:77], v209 offset:1032\n"
"v_mfma_f32_16x16x32_bf16 v[136:139], a[16:19], a[52:55], v[136:139]\n"
"ds_write_b64 v219, a[196:197] offset:43008\n"
"v_mfma_f32_16x16x32_bf16 v[100:103], a[20:23], a[32:35], v[100:103]\n"
"ds_read_b64_tr_b16 a[78:79], v209 offset:1288\n"
"v_mfma_f32_16x16x32_bf16 v[108:111], a[20:23], a[36:39], v[108:111]\n"
"ds_read_b64_tr_b16 a[80:81], v209 offset:1536\n"
"v_mfma_f32_16x16x32_bf16 v[116:119], a[20:23], a[40:43], v[116:119]\n"
"ds_write_b64 v219, a[198:199] offset:43016\n"
"v_mfma_f32_16x16x32_bf16 v[124:127], a[20:23], a[44:47], v[124:127]\n"
"ds_read_b64_tr_b16 a[82:83], v209 offset:1792\n"
"v_mfma_f32_16x16x32_bf16 v[132:135], a[20:23], a[48:51], v[132:135]\n"
"ds_write_b64 v219, a[200:201] offset:45056\n"
"v_mfma_f32_16x16x32_bf16 v[140:143], a[20:23], a[52:55], v[140:143]\n"
"ds_read_b64_tr_b16 a[84:85], v209 offset:1544\n"
"v_mfma_f32_16x16x32_bf16 v[144:147], a[24:27], a[32:35], v[144:147]\n"
"ds_write_b64 v219, a[202:203] offset:45064\n"
"v_mfma_f32_16x16x32_bf16 v[152:155], a[24:27], a[36:39], v[152:155]\n"
"ds_read_b64_tr_b16 a[86:87], v209 offset:1800\n"
"v_add_u32 v209, 0xfffe0000, v209\n"
"v_mfma_f32_16x16x32_bf16 v[160:163], a[24:27], a[40:43], v[160:163]\n"
"ds_write_b64 v219, a[204:205] offset:47104\n"
"v_mfma_f32_16x16x32_bf16 v[168:171], a[24:27], a[44:47], v[168:171]\n"
"ds_write_b64 v219, a[206:207] offset:47112\n"
"v_mfma_f32_16x16x32_bf16 v[176:179], a[24:27], a[48:51], v[176:179]\n"
"ds_write_b64 v219, a[208:209] offset:49152\n"
"v_mfma_f32_16x16x32_bf16 v[184:187], a[24:27], a[52:55], v[184:187]\n"
"ds_write_b64 v219, a[210:211] offset:49160\n"
"v_mfma_f32_16x16x32_bf16 v[148:151], a[28:31], a[32:35], v[148:151]\n"
"ds_write_b64 v219, a[212:213] offset:51200\n"
"v_mfma_f32_16x16x32_bf16 v[156:159], a[28:31], a[36:39], v[156:159]\n"
"ds_write_b64 v219, a[214:215] offset:51208\n"
"v_mfma_f32_16x16x32_bf16 v[164:167], a[28:31], a[40:43], v[164:167]\n"
"ds_write_b64 v219, a[216:217] offset:53248\n"
"v_mfma_f32_16x16x32_bf16 v[172:175], a[28:31], a[44:47], v[172:175]\n"
"ds_write_b64 v219, a[218:219] offset:53256\n"
"v_mfma_f32_16x16x32_bf16 v[180:183], a[28:31], a[48:51], v[180:183]\n"
"ds_write_b64 v219, a[220:221] offset:55296\n"
"v_mfma_f32_16x16x32_bf16 v[188:191], a[28:31], a[52:55], v[188:191]\n"
"ds_write_b64 v219, a[222:223] offset:55304\n"
"ds_write_b64 v219, a[224:225] offset:57344\n"
"ds_write_b64 v219, a[226:227] offset:57352\n"
"ds_write_b64 v219, a[228:229] offset:59392\n"
"ds_write_b64 v219, a[230:231] offset:59400\n"
"ds_write_b64 v219, a[232:233] offset:61440\n"
"ds_write_b64 v219, a[234:235] offset:61448\n"
"ds_write_b64 v219, a[236:237] offset:63488\n"
"ds_write_b64 v219, a[238:239] offset:63496\n"
"s_waitcnt vmcnt(4) lgkmcnt(0)\n"
"s_barrier\n"
".Lq_join:\n"
"v_mfma_f32_16x16x32_bf16 v[0:3], a[56:59], a[88:91], v[0:3]\n"
"s_mov_b32 s64, 8192\n"
"v_readfirstlane_b32 s62, v222\n"
"v_readfirstlane_b32 s63, v223\n"
"v_mfma_f32_16x16x32_bf16 v[4:7], a[60:63], a[88:91], v[4:7]\n"
"s_add_u32 m0, s73, 131072\n"
"s_mov_b32 s55, 32768\n"
"v_mfma_f32_16x16x32_bf16 v[8:11], a[56:59], a[92:95], v[8:11]\n"
"v_mfma_f32_16x16x32_bf16 v[12:15], a[60:63], a[92:95], v[12:15]\n"
"v_mul_f32 v0, s78, v0\n"
"v_mul_f32 v1, s78, v1\n"
"v_mfma_f32_16x16x32_bf16 v[16:19], a[56:59], a[96:99], v[16:19]\n"
"buffer_load_dwordx4 v195, s[24:27], s55 offen nt lds\n"
"v_mfma_f32_16x16x32_bf16 v[20:23], a[60:63], a[96:99], v[20:23]\n"
"v_mul_f32 v2, s78, v2\n"
"v_mul_f32 v3, s78, v3\n"
"v_mfma_f32_16x16x32_bf16 v[24:27], a[56:59], a[100:103], v[24:27]\n"
"ds_read_b128 a[192:195], v192\n"
"v_cvt_pk_bf16_f32 v0, v0, v1\n"
"v_mfma_f32_16x16x32_bf16 v[28:31], a[60:63], a[100:103], v[28:31]\n"
"ds_read_b128 a[196:199], v192 offset:4096\n"
"v_mul_f32 v4, s78, v4\n"
"v_mfma_f32_16x16x32_bf16 v[32:35], a[56:59], a[104:107], v[32:35]\n"
"v_mul_f32 v5, s78, v5\n"
"v_mul_f32 v6, s78, v6\n"
"v_mfma_f32_16x16x32_bf16 v[36:39], a[60:63], a[104:107], v[36:39]\n"
"v_mul_f32 v7, s78, v7\n"
"v_cvt_pk_bf16_f32 v1, v2, v3\n"
"v_mfma_f32_16x16x32_bf16 v[40:43], a[56:59], a[108:111], v[40:43]\n"
"v_cvt_pk_bf16_f32 v2, v4, v5\n"
"v_cvt_pk_bf16_f32 v3, v6, v7\n"
"v_mfma_f32_16x16x32_bf16 v[44:47], a[60:63], a[108:111], v[44:47]\n"
"v_mul_f32 v8, s78, v8\n"
"v_mul_f32 v9, s78, v9\n"
"v_mfma_f32_16x16x32_bf16 v[48:51], a[64:67], a[88:91], v[48:51]\n"
"v_mul_f32 v10, s78, v10\n"
"v_mul_f32 v11, s78, v11\n"
"v_mfma_f32_16x16x32_bf16 v[52:55], a[68:71], a[88:91], v[52:55]\n"
"v_mul_f32 v12, s78, v12\n"
"v_mul_f32 v13, s78, v13\n"
"v_mfma_f32_16x16x32_bf16 v[56:59], a[64:67], a[92:95], v[56:59]\n"
"v_mul_f32 v14, s78, v14\n"
"v_mul_f32 v15, s78, v15\n"
"v_mfma_f32_16x16x32_bf16 v[60:63], a[68:71], a[92:95], v[60:63]\n"
"v_cvt_pk_bf16_f32 v4, v8, v9\n"
"v_cvt_pk_bf16_f32 v5, v10, v11\n"
"v_mfma_f32_16x16x32_bf16 v[64:67], a[64:67], a[96:99], v[64:67]\n"
"v_cvt_pk_bf16_f32 v6, v12, v13\n"
"v_cvt_pk_bf16_f32 v7, v14, v15\n"
"v_mfma_f32_16x16x32_bf16 v[68:71], a[68:71], a[96:99], v[68:71]\n"
"s_add_u32 m0, s73, 131072\n"
"s_mov_b32 s55, 32768\n"
"v_mul_f32 v16, s78, v16\n"
"v_mfma_f32_16x16x32_bf16 v[72:75], a[64:67], a[100:103], v[72:75]\n"
"v_mul_f32 v17, s78, v17\n"
"v_mul_f32 v18, s78, v18\n"
"v_mfma_f32_16x16x32_bf16 v[76:79], a[68:71], a[100:103], v[76:79]\n"
"v_mul_f32 v19, s78, v19\n"
"v_mul_f32 v20, s78, v20\n"
"v_mfma_f32_16x16x32_bf16 v[80:83], a[64:67], a[104:107], v[80:83]\n"
"buffer_load_dwordx4 v196, s[24:27], s55 offen offset:2048 nt lds\n"
"v_mfma_f32_16x16x32_bf16 v[84:87], a[68:71], a[104:107], v[84:87]\n"
"v_mul_f32 v21, s78, v21\n"
"v_mul_f32 v22, s78, v22\n"
"v_mfma_f32_16x16x32_bf16 v[88:91], a[64:67], a[108:111], v[88:91]\n"
"v_mul_f32 v23, s78, v23\n"
"v_cvt_pk_bf16_f32 v8, v16, v17\n"
"v_mfma_f32_16x16x32_bf16 v[92:95], a[68:71], a[108:111], v[92:95]\n"
"v_cvt_pk_bf16_f32 v9, v18, v19\n"
"v_cvt_pk_bf16_f32 v10, v20, v21\n"
"v_mfma_f32_16x16x32_bf16 v[96:99], a[72:75], a[88:91], v[96:99]\n"
"v_cvt_pk_bf16_f32 v11, v22, v23\n"
"ds_read_b128 a[200:203], v192 offset:256\n"
"v_mfma_f32_16x16x32_bf16 v[100:103], a[76:79], a[88:91], v[100:103]\n"
"v_mul_f32 v24, s78, v24\n"
"v_mul_f32 v25, s78, v25\n"
"v_mfma_f32_16x16x32_bf16 v[104:107], a[72:75], a[92:95], v[104:107]\n"
"v_mul_f32 v26, s78, v26\n"
"v_mul_f32 v27, s78, v27\n"
"v_mfma_f32_16x16x32_bf16 v[108:111], a[76:79], a[92:95], v[108:111]\n"
"v_mul_f32 v28, s78, v28\n"
"v_mul_f32 v29, s78, v29\n"
"v_mfma_f32_16x16x32_bf16 v[112:115], a[72:75], a[96:99], v[112:115]\n"
"v_mul_f32 v30, s78, v30\n"
"v_mul_f32 v31, s78, v31\n"
"v_mfma_f32_16x16x32_bf16 v[116:119], a[76:79], a[96:99], v[116:119]\n"
"v_cvt_pk_bf16_f32 v12, v24, v25\n"
"v_cvt_pk_bf16_f32 v13, v26, v27\n"
"v_mfma_f32_16x16x32_bf16 v[120:123], a[72:75], a[100:103], v[120:123]\n"
"v_cvt_pk_bf16_f32 v14, v28, v29\n"
"v_cvt_pk_bf16_f32 v15, v30, v31\n"
"v_mfma_f32_16x16x32_bf16 v[124:127], a[76:79], a[100:103], v[124:127]\n"
"v_mul_f32 v32, s78, v32\n"
"v_mul_f32 v33, s78, v33\n"
"v_mfma_f32_16x16x32_bf16 v[128:131], a[72:75], a[104:107], v[128:131]\n"
"s_add_u32 m0, s73, 135168\n"
"s_mov_b32 s55, 36864\n"
"v_mfma_f32_16x16x32_bf16 v[132:135], a[76:79], a[104:107], v[132:135]\n"
"v_mul_f32 v34, s78, v34\n"
"v_mul_f32 v35, s78, v35\n"
"v_mfma_f32_16x16x32_bf16 v[136:139], a[72:75], a[108:111], v[136:139]\n"
"v_mul_f32 v36, s78, v36\n"
"v_mul_f32 v37, s78, v37\n"
"v_mfma_f32_16x16x32_bf16 v[140:143], a[76:79], a[108:111], v[140:143]\n"
"buffer_load_dwordx4 v195, s[24:27], s55 offen nt lds\n"
"v_mfma_f32_16x16x32_bf16 v[144:147], a[80:83], a[88:91], v[144:147]\n"
"v_mul_f32 v38, s78, v38\n"
"v_mul_f32 v39, s78, v39\n"
"v_mfma_f32_16x16x32_bf16 v[148:151], a[84:87], a[88:91], v[148:151]\n"
"v_cvt_pk_bf16_f32 v16, v32, v33\n"
"v_cvt_pk_bf16_f32 v17, v34, v35\n"
"v_mfma_f32_16x16x32_bf16 v[152:155], a[80:83], a[92:95], v[152:155]\n"
"v_cvt_pk_bf16_f32 v18, v36, v37\n"
"v_cvt_pk_bf16_f32 v19, v38, v39\n"
"v_mfma_f32_16x16x32_bf16 v[156:159], a[84:87], a[92:95], v[156:159]\n"
"v_mul_f32 v40, s78, v40\n"
"v_mul_f32 v41, s78, v41\n"
"v_mfma_f32_16x16x32_bf16 v[160:163], a[80:83], a[96:99], v[160:163]\n"
"v_mul_f32 v42, s78, v42\n"
"v_mul_f32 v43, s78, v43\n"
"v_mfma_f32_16x16x32_bf16 v[164:167], a[84:87], a[96:99], v[164:167]\n"
"v_mul_f32 v44, s78, v44\n"
"v_mul_f32 v45, s78, v45\n"
"v_mfma_f32_16x16x32_bf16 v[168:171], a[80:83], a[100:103], v[168:171]\n"
"v_mul_f32 v46, s78, v46\n"
"v_mul_f32 v47, s78, v47\n"
"v_mfma_f32_16x16x32_bf16 v[172:175], a[84:87], a[100:103], v[172:175]\n"
"v_cvt_pk_bf16_f32 v20, v40, v41\n"
"v_cvt_pk_bf16_f32 v21, v42, v43\n"
"v_mfma_f32_16x16x32_bf16 v[176:179], a[80:83], a[104:107], v[176:179]\n"
"v_cvt_pk_bf16_f32 v22, v44, v45\n"
"v_cvt_pk_bf16_f32 v23, v46, v47\n"
"v_mfma_f32_16x16x32_bf16 v[180:183], a[84:87], a[104:107], v[180:183]\n"
"ds_read_b128 a[204:207], v192 offset:4352\n"
"v_mul_f32 v48, s78, v48\n"
"v_mfma_f32_16x16x32_bf16 v[184:187], a[80:83], a[108:111], v[184:187]\n"
"v_mul_f32 v49, s78, v49\n"
"v_mul_f32 v50, s78, v50\n"
"v_mfma_f32_16x16x32_bf16 v[188:191], a[84:87], a[108:111], v[188:191]\n"
"s_waitcnt lgkmcnt(1)\n"
"v_mul_f32 v51, s78, v51\n"
"v_mul_f32 v52, s78, v52\n"
"v_mfma_f32_16x16x32_bf16 v[208:211], a[192:195], v[0:3], 0\n"
"s_add_u32 m0, s73, 135168\n"
"s_mov_b32 s55, 36864\n"
"v_mul_f32 v53, s78, v53\n"
"v_mfma_f32_16x16x32_bf16 v[216:219], a[192:195], v[4:7], 0\n"
"v_mul_f32 v54, s78, v54\n"
"v_mul_f32 v55, s78, v55\n"
"v_mfma_f32_16x16x32_bf16 v[224:227], a[192:195], v[8:11], 0\n"
"v_cvt_pk_bf16_f32 v24, v48, v49\n"
"v_cvt_pk_bf16_f32 v25, v50, v51\n"
"v_mfma_f32_16x16x32_bf16 v[232:235], a[192:195], v[12:15], 0\n"
"buffer_load_dwordx4 v196, s[24:27], s55 offen offset:2048 nt lds\n"
"v_mfma_f32_16x16x32_bf16 v[240:243], a[192:195], v[16:19], 0\n"
"v_cvt_pk_bf16_f32 v26, v52, v53\n"
"v_cvt_pk_bf16_f32 v27, v54, v55\n"
"v_mfma_f32_16x16x32_bf16 v[248:251], a[192:195], v[20:23], 0\n"
"v_mul_f32 v56, s78, v56\n"
"v_mul_f32 v57, s78, v57\n"
"v_mfma_f32_16x16x32_bf16 v[212:215], a[196:199], v[0:3], 0\n"
"v_mul_f32 v58, s78, v58\n"
"v_mul_f32 v59, s78, v59\n"
"v_mfma_f32_16x16x32_bf16 v[220:223], a[196:199], v[4:7], 0\n"
"v_mul_f32 v60, s78, v60\n"
"v_mul_f32 v61, s78, v61\n"
"v_mfma_f32_16x16x32_bf16 v[228:231], a[196:199], v[8:11], 0\n"
"v_mul_f32 v62, s78, v62\n"
"v_mul_f32 v63, s78, v63\n"
"v_mfma_f32_16x16x32_bf16 v[236:239], a[196:199], v[12:15], 0\n"
"v_cvt_pk_bf16_f32 v28, v56, v57\n"
"v_cvt_pk_bf16_f32 v29, v58, v59\n"
"v_mfma_f32_16x16x32_bf16 v[244:247], a[196:199], v[16:19], 0\n"
"v_cvt_pk_bf16_f32 v30, v60, v61\n"
"v_cvt_pk_bf16_f32 v31, v62, v63\n"
"v_mfma_f32_16x16x32_bf16 v[252:255], a[196:199], v[20:23], 0\n"
"v_mul_f32 v64, s78, v64\n"
"v_mul_f32 v65, s78, v65\n"
"v_mfma_f32_16x16x32_bf16 v[208:211], a[200:203], v[24:27], v[208:211]\n"
"v_mul_f32 v66, s78, v66\n"
"v_mul_f32 v67, s78, v67\n"
"v_mfma_f32_16x16x32_bf16 v[216:219], a[200:203], v[28:31], v[216:219]\n"
"v_mul_f32 v68, s78, v68\n"
"v_mul_f32 v69, s78, v69\n"
"v_mul_f32 v70, s78, v70\n"
"v_mul_f32 v71, s78, v71\n"
"v_cvt_pk_bf16_f32 v32, v64, v65\n"
"v_cvt_pk_bf16_f32 v33, v66, v67\n"
"v_cvt_pk_bf16_f32 v34, v68, v69\n"
"v_cvt_pk_bf16_f32 v35, v70, v71\n"
"ds_read_b128 a[208:211], v192 offset:512\n"
"s_add_u32 m0, s73, 114688\n"
"v_mfma_f32_16x16x32_bf16 v[224:227], a[200:203], v[32:35], v[224:227]\n"
"s_mov_b32 s55, 24576\n"
"v_mul_f32 v72, s78, v72\n"
"v_mul_f32 v73, s78, v73\n"
"v_mul_f32 v74, s78, v74\n"
"v_mul_f32 v75, s78, v75\n"
"v_mul_f32 v76, s78, v76\n"
"v_mul_f32 v77, s78, v77\n"
"v_mul_f32 v78, s78, v78\n"
"buffer_load_dwordx4 v197, s[28:31], s55 offen nt lds\n"
"v_mul_f32 v79, s78, v79\n"
"v_cvt_pk_bf16_f32 v36, v72, v73\n"
"v_cvt_pk_bf16_f32 v37, v74, v75\n"
"v_cvt_pk_bf16_f32 v38, v76, v77\n"
"v_cvt_pk_bf16_f32 v39, v78, v79\n"
"v_mul_f32 v80, s78, v80\n"
"v_mul_f32 v81, s78, v81\n"
"v_mfma_f32_16x16x32_bf16 v[232:235], a[200:203], v[36:39], v[232:235]\n"
"v_mul_f32 v82, s78, v82\n"
"v_mul_f32 v83, s78, v83\n"
"v_mul_f32 v84, s78, v84\n"
"v_mul_f32 v85, s78, v85\n"
"v_mul_f32 v86, s78, v86\n"
"v_mul_f32 v87, s78, v87\n"
"v_cvt_pk_bf16_f32 v40, v80, v81\n"
"v_cvt_pk_bf16_f32 v41, v82, v83\n"
"v_cvt_pk_bf16_f32 v42, v84, v85\n"
"v_cvt_pk_bf16_f32 v43, v86, v87\n"
"v_mul_f32 v88, s78, v88\n"
"v_mul_f32 v89, s78, v89\n"
"v_mfma_f32_16x16x32_bf16 v[240:243], a[200:203], v[40:43], v[240:243]\n"
"v_mul_f32 v90, s78, v90\n"
"v_mul_f32 v91, s78, v91\n"
"v_mul_f32 v92, s78, v92\n"
"v_mul_f32 v93, s78, v93\n"
"v_mul_f32 v94, s78, v94\n"
"v_mul_f32 v95, s78, v95\n"
"v_cvt_pk_bf16_f32 v44, v88, v89\n"
"v_cvt_pk_bf16_f32 v45, v90, v91\n"
"v_cvt_pk_bf16_f32 v46, v92, v93\n"
"v_cvt_pk_bf16_f32 v47, v94, v95\n"
"ds_read_b128 a[212:215], v192 offset:4608\n"
"v_mul_f32 v96, s78, v96\n"
"v_mfma_f32_16x16x32_bf16 v[248:251], a[200:203], v[44:47], v[248:251]\n"
"s_waitcnt lgkmcnt(1)\n"
"v_mul_f32 v97, s78, v97\n"
"v_mul_f32 v98, s78, v98\n"
"v_mfma_f32_16x16x32_bf16 v[212:215], a[204:207], v[24:27], v[212:215]\n"
"v_mul_f32 v99, s78, v99\n"
"v_mul_f32 v100, s78, v100\n"
"v_mfma_f32_16x16x32_bf16 v[220:223], a[204:207], v[28:31], v[220:223]\n"
"v_mul_f32 v101, s78, v101\n"
"v_mul_f32 v102, s78, v102\n"
"v_mfma_f32_16x16x32_bf16 v[228:231], a[204:207], v[32:35], v[228:231]\n"
"v_mul_f32 v103, s78, v103\n"
"s_add_u32 m0, s73, 114688\n"
"v_mfma_f32_16x16x32_bf16 v[236:239], a[204:207], v[36:39], v[236:239]\n"
"s_mov_b32 s55, 24576\n"
"v_cvt_pk_bf16_f32 v48, v96, v97\n"
"v_cvt_pk_bf16_f32 v49, v98, v99\n"
"v_mfma_f32_16x16x32_bf16 v[244:247], a[204:207], v[40:43], v[244:247]\n"
"v_cvt_pk_bf16_f32 v50, v100, v101\n"
"v_cvt_pk_bf16_f32 v51, v102, v103\n"
"v_mfma_f32_16x16x32_bf16 v[252:255], a[204:207], v[44:47], v[252:255]\n"
"buffer_load_dwordx4 v197, s[28:31], s55 offen offset:2048 nt lds\n"
"v_mfma_f32_16x16x32_bf16 v[208:211], a[208:211], v[48:51], v[208:211]\n"
"v_mul_f32 v104, s78, v104\n"
"v_mul_f32 v105, s78, v105\n"
"v_mul_f32 v106, s78, v106\n"
"v_mul_f32 v107, s78, v107\n"
"v_mul_f32 v108, s78, v108\n"
"v_mul_f32 v109, s78, v109\n"
"v_mul_f32 v110, s78, v110\n"
"v_mul_f32 v111, s78, v111\n"
"v_cvt_pk_bf16_f32 v52, v104, v105\n"
"v_cvt_pk_bf16_f32 v53, v106, v107\n"
"v_cvt_pk_bf16_f32 v54, v108, v109\n"
"v_cvt_pk_bf16_f32 v55, v110, v111\n"
"v_mul_f32 v112, s78, v112\n"
"v_mul_f32 v113, s78, v113\n"
"v_mfma_f32_16x16x32_bf16 v[216:219], a[208:211], v[52:55], v[216:219]\n"
"v_mul_f32 v114, s78, v114\n"
"v_mul_f32 v115, s78, v115\n"
"v_mul_f32 v116, s78, v116\n"
"v_mul_f32 v117, s78, v117\n"
"v_mul_f32 v118, s78, v118\n"
"v_mul_f32 v119, s78, v119\n"
"v_cvt_pk_bf16_f32 v56, v112, v113\n"
"v_cvt_pk_bf16_f32 v57, v114, v115\n"
"v_cvt_pk_bf16_f32 v58, v116, v117\n"
"v_cvt_pk_bf16_f32 v59, v118, v119\n"
"ds_read_b128 a[216:219], v192 offset:768\n"
"v_mul_f32 v120, s78, v120\n"
"v_mfma_f32_16x16x32_bf16 v[224:227], a[208:211], v[56:59], v[224:227]\n"
"v_mul_f32 v121, s78, v121\n"
"v_mul_f32 v122, s78, v122\n"
"v_mul_f32 v123, s78, v123\n"
"v_mul_f32 v124, s78, v124\n"
"v_mul_f32 v125, s78, v125\n"
"v_mul_f32 v126, s78, v126\n"
"v_mul_f32 v127, s78, v127\n"
"v_cvt_pk_bf16_f32 v60, v120, v121\n"
"v_cvt_pk_bf16_f32 v61, v122, v123\n"
"v_cvt_pk_bf16_f32 v62, v124, v125\n"
"v_cvt_pk_bf16_f32 v63, v126, v127\n"
"v_mul_f32 v128, s78, v128\n"
"v_mul_f32 v129, s78, v129\n"
"v_mfma_f32_16x16x32_bf16 v[232:235], a[208:211], v[60:63], v[232:235]\n"
"v_mul_f32 v130, s78, v130\n"
"v_mul_f32 v131, s78, v131\n"
"s_add_u32 m0, s73, 118784\n"
"s_mov_b32 s55, 28672\n"
"v_mul_f32 v132, s78, v132\n"
"v_mul_f32 v133, s78, v133\n"
"v_mul_f32 v134, s78, v134\n"
"v_mul_f32 v135, s78, v135\n"
"v_cvt_pk_bf16_f32 v64, v128, v129\n"
"v_cvt_pk_bf16_f32 v65, v130, v131\n"
"v_cvt_pk_bf16_f32 v66, v132, v133\n"
"v_cvt_pk_bf16_f32 v67, v134, v135\n"
"v_mul_f32 v136, s78, v136\n"
"v_mul_f32 v137, s78, v137\n"
"v_mfma_f32_16x16x32_bf16 v[240:243], a[208:211], v[64:67], v[240:243]\n"
"buffer_load_dwordx4 v197, s[28:31], s55 offen nt lds\n"
"v_mul_f32 v138, s78, v138\n"
"v_mul_f32 v139, s78, v139\n"
"v_mul_f32 v140, s78, v140\n"
"v_mul_f32 v141, s78, v141\n"
"v_mul_f32 v142, s78, v142\n"
"v_mul_f32 v143, s78, v143\n"
"v_cvt_pk_bf16_f32 v68, v136, v137\n"
"v_cvt_pk_bf16_f32 v69, v138, v139\n"
"v_cvt_pk_bf16_f32 v70, v140, v141\n"
"v_cvt_pk_bf16_f32 v71, v142, v143\n"
"ds_read_b128 a[220:223], v192 offset:4864\n"
"v_mul_f32 v144, s78, v144\n"
"v_mfma_f32_16x16x32_bf16 v[248:251], a[208:211], v[68:71], v[248:251]\n"
"s_waitcnt lgkmcnt(1)\n"
"v_mul_f32 v145, s78, v145\n"
"v_mul_f32 v146, s78, v146\n"
"v_mfma_f32_16x16x32_bf16 v[212:215], a[212:215], v[48:51], v[212:215]\n"
"v_mul_f32 v147, s78, v147\n"
"v_mul_f32 v148, s78, v148\n"
"v_mfma_f32_16x16x32_bf16 v[220:223], a[212:215], v[52:55], v[220:223]\n"
"v_mul_f32 v149, s78, v149\n"
"v_mul_f32 v150, s78, v150\n"
"v_mfma_f32_16x16x32_bf16 v[228:231], a[212:215], v[56:59], v[228:231]\n"
"v_mul_f32 v151, s78, v151\n"
"v_cvt_pk_bf16_f32 v72, v144, v145\n"
"v_mfma_f32_16x16x32_bf16 v[236:239], a[212:215], v[60:63], v[236:239]\n"
"v_cvt_pk_bf16_f32 v73, v146, v147\n"
"v_cvt_pk_bf16_f32 v74, v148, v149\n"
"v_mfma_f32_16x16x32_bf16 v[244:247], a[212:215], v[64:67], v[244:247]\n"
"v_cvt_pk_bf16_f32 v75, v150, v151\n"
"v_mul_f32 v152, s78, v152\n"
"v_mfma_f32_16x16x32_bf16 v[252:255], a[212:215], v[68:71], v[252:255]\n"
"v_mul_f32 v153, s78, v153\n"
"v_mul_f32 v154, s78, v154\n"
"v_mfma_f32_16x16x32_bf16 v[208:211], a[216:219], v[72:75], v[208:211]\n"
"v_mul_f32 v155, s78, v155\n"
"v_mul_f32 v156, s78, v156\n"
"v_mul_f32 v157, s78, v157\n"
"v_mul_f32 v158, s78, v158\n"
"v_mul_f32 v159, s78, v159\n"
"s_add_u32 m0, s73, 118784\n"
"s_lshl_b32 s96, s62, 3\n"
"s_xor_b32 s97, s63, 0x5bd1e995\n"
"v_cvt_pk_bf16_f32 v76, v152, v153\n"
"v_cvt_pk_bf16_f32 v77, v154, v155\n"
"v_cvt_pk_bf16_f32 v78, v156, v157\n"
"v_cvt_pk_bf16_f32 v79, v158, v159\n"
"v_mul_f32 v160, s78, v160\n"
"v_mul_f32 v161, s78, v161\n"
"v_mfma_f32_16x16x32_bf16 v[216:219], a[216:219], v[76:79], v[216:219]\n"
"s_mov_b32 s55, 28672\n"
"v_mul_f32 v162, s78, v162\n"
"v_mul_f32 v163, s78, v163\n"
"v_mul_f32 v164, s78, v164\n"
"v_mul_f32 v165, s78, v165\n"
"v_mul_f32 v166, s78, v166\n"
"v_mul_f32 v167, s78, v167\n"
"v_cvt_pk_bf16_f32 v80, v160, v161\n"
"v_cvt_pk_bf16_f32 v81, v162, v163\n"
"v_cvt_pk_bf16_f32 v82, v164, v165\n"
"v_cvt_pk_bf16_f32 v83, v166, v167\n"
"v_mul_f32 v168, s78, v168\n"
"v_mul_f32 v169, s78, v169\n"
"v_mfma_f32_16x16x32_bf16 v[224:227], a[216:219], v[80:83], v[224:227]\n"
"buffer_load_dwordx4 v197, s[28:31], s55 offen offset:2048 nt lds\n"
"v_mul_f32 v170, s78, v170\n"
"v_mul_f32 v171, s78, v171\n"
"v_mul_f32 v172, s78, v172\n"
"v_mul_f32 v173, s78, v173\n"
"v_mul_f32 v174, s78, v174\n"
"v_mul_f32 v175, s78, v175\n"
"v_cvt_pk_bf16_f32 v84, v168, v169\n"
"v_cvt_pk_bf16_f32 v85, v170, v171\n"
"v_cvt_pk_bf16_f32 v86, v172, v173\n"
"v_cvt_pk_bf16_f32 v87, v174, v175\n"
"v_mul_f32 v176, s78, v176\n"
"v_mul_f32 v177, s78, v177\n"
"v_mfma_f32_16x16x32_bf16 v[232:235], a[216:219], v[84:87], v[232:235]\n"
"v_mul_f32 v178, s78, v178\n"
"v_mul_f32 v179, s78, v179\n"
"v_mul_f32 v180, s78, v180\n"
"v_mul_f32 v181, s78, v181\n"
"v_mul_f32 v182, s78, v182\n"
"v_mul_f32 v183, s78, v183\n"
"v_cvt_pk_bf16_f32 v88, v176, v177\n"
"v_cvt_pk_bf16_f32 v89, v178, v179\n"
"v_cvt_pk_bf16_f32 v90, v180, v181\n"
"v_cvt_pk_bf16_f32 v91, v182, v183\n"
"v_mul_f32 v184, s78, v184\n"
"v_mul_f32 v185, s78, v185\n"
"v_mfma_f32_16x16x32_bf16 v[240:243], a[216:219], v[88:91], v[240:243]\n"
"v_mul_f32 v186, s78, v186\n"
"v_mul_f32 v187, s78, v187\n"
"v_mul_f32 v188, s78, v188\n"
"v_mul_f32 v189, s78, v189\n"
"v_mul_f32 v190, s78, v190\n"
"v_mul_f32 v191, s78, v191\n"
"v_cvt_pk_bf16_f32 v92, v184, v185\n"
"v_cvt_pk_bf16_f32 v93, v186, v187\n"
"v_cvt_pk_bf16_f32 v94, v188, v189\n"
"v_cvt_pk_bf16_f32 v95, v190, v191\n"
"v_add_u32 v192, s79, v192\n"
"ds_read_b128 a[192:195], v192\n"
"v_mfma_f32_16x16x32_bf16 v[248:251], a[216:219], v[92:95], v[248:251]\n"
"s_waitcnt lgkmcnt(1)\n"
"ds_read_b128 a[196:199], v192 offset:4096\n"
"v_mfma_f32_16x16x32_bf16 v[212:215], a[220:223], v[72:75], v[212:215]\n"
"ds_read_b128 a[200:203], v192 offset:256\n"
"v_mfma_f32_16x16x32_bf16 v[220:223], a[220:223], v[76:79], v[220:223]\n"
"ds_read_b128 a[204:207], v192 offset:4352\n"
"v_mfma_f32_16x16x32_bf16 v[228:231], a[220:223], v[80:83], v[228:231]\n"
"ds_read_b128 a[208:211], v192 offset:512\n"
"v_mfma_f32_16x16x32_bf16 v[236:239], a[220:223], v[84:87], v[236:239]\n"
"ds_read_b128 a[212:215], v192 offset:4608\n"
"v_mfma_f32_16x16x32_bf16 v[244:247], a[220:223], v[88:91], v[244:247]\n"
"ds_read_b128 a[216:219], v192 offset:768\n"
"v_mfma_f32_16x16x32_bf16 v[252:255], a[220:223], v[92:95], v[252:255]\n"
"ds_read_b128 a[220:223], v192 offset:4864\n"
"v_add_u32 v192, s79, v192\n"
"s_waitcnt vmcnt(8) lgkmcnt(0)\n"
"s_barrier\n"
"v_max3_f32 v144, v208, v209, v210\n"
"v_max3_f32 v148, v216, v217, v218\n"
"v_max3_f32 v152, v224, v225, v226\n"
"v_max3_f32 v156, v232, v233, v234\n"
"v_max3_f32 v160, v240, v241, v242\n"
"v_max3_f32 v164, v248, v249, v250\n"
"v_max3_f32 v144, v144, v211, v212\n"
"v_max3_f32 v148, v148, v219, v220\n"
"v_max3_f32 v152, v152, v227, v228\n"
"v_max3_f32 v156, v156, v235, v236\n"
"v_max3_f32 v160, v160, v243, v244\n"
"v_max3_f32 v164, v164, v251, v252\n"
"v_max3_f32 v144, v144, v213, v214\n"
"v_max3_f32 v148, v148, v221, v222\n"
"v_max3_f32 v152, v152, v229, v230\n"
"v_max3_f32 v156, v156, v237, v238\n"
"v_max3_f32 v160, v160, v245, v246\n"
"v_max3_f32 v164, v164, v253, v254\n"
"v_max_f32 v144, v144, v215\n"
"v_max_f32 v148, v148, v223\n"
"v_max_f32 v152, v152, v231\n"
"v_max_f32 v156, v156, v239\n"
"v_max_f32 v160, v160, v247\n"
"v_max_f32 v164, v164, v255\n"
"v_mov_b32 v145, v144\n"
"v_mov_b32 v149, v148\n"
"v_mov_b32 v153, v152\n"
"v_mov_b32 v157, v156\n"
"v_mov_b32 v161, v160\n"
"v_mov_b32 v165, v164\n"
"v_permlane32_swap_b32 v144, v145\n"
"v_permlane32_swap_b32 v148, v149\n"
"v_permlane32_swap_b32 v152, v153\n"
"v_permlane32_swap_b32 v156, v157\n"
"v_permlane32_swap_b32 v160, v161\n"
"v_permlane32_swap_b32 v164, v165\n"
"v_max_f32 v144, v144, v145\n"
"v_max_f32 v148, v148, v149\n"
"v_max_f32 v152, v152, v153\n"
"v_max_f32 v156, v156, v157\n"
"v_max_f32 v160, v160, v161\n"
"v_max_f32 v164, v164, v165\n"
"v_mov_b32 v145, v144\n"
"v_mov_b32 v149, v148\n"
"v_mov_b32 v153, v152\n"
"v_mov_b32 v157, v156\n"
"v_mov_b32 v161, v160\n"
"v_mov_b32 v165, v164\n"
"v_permlane16_swap_b32 v144, v145\n"
"v_permlane16_swap_b32 v148, v149\n"
"v_permlane16_swap_b32 v152, v153\n"
"v_permlane16_swap_b32 v156, v157\n"
"v_permlane16_swap_b32 v160, v161\n"
"v_permlane16_swap_b32 v164, v165\n"
"v_max_f32 v144, v144, v145\n"
"v_max_f32 v148, v148, v149\n"
"v_max_f32 v152, v152, v153\n"
"v_max_f32 v156, v156, v157\n"
"v_max_f32 v160, v160, v161\n"
"v_max_f32 v164, v164, v165\n"
"v_sub_f32 v144, 0xc1800000, v144\n"
"v_sub_f32 v148, 0xc1800000, v148\n"
"v_sub_f32 v152, 0xc1800000, v152\n"
"v_sub_f32 v156, 0xc1800000, v156\n"
"v_sub_f32 v160, 0xc1800000, v160\n"
"v_sub_f32 v164, 0xc1800000, v164\n"
"v_mov_b32 v145, v144\n"
"v_mov_b32 v146, v144\n"
"v_mov_b32 v147, v144\n"
"v_mov_b32 v149, v148\n"
"v_mov_b32 v150, v148\n"
"v_mfma_f32_16x16x32_bf16 v[96:99], a[192:195], v[0:3], v[144:147]\n"
"s_add_u32 m0, s73, 0\n"
"v_mov_b32 v151, v148\n"
"v_mov_b32 v153, v152\n"
"v_mov_b32 v154, v152\n"
"v_mfma_f32_16x16x32_bf16 v[104:107], a[192:195], v[4:7], v[148:151]\n"
"s_mov_b32 s55, 40960\n"
"v_mov_b32 v155, v152\n"
"v_mov_b32 v157, v156\n"
"v_mov_b32 v158, v156\n"
"v_mfma_f32_16x16x32_bf16 v[112:115], a[192:195], v[8:11], v[152:155]\n"
"buffer_load_dwordx4 v195, s[24:27], s55 offen nt lds\n"
"v_mov_b32 v159, v156\n"
"v_mov_b32 v161, v160\n"
"v_mov_b32 v162, v160\n"
"v_mfma_f32_16x16x32_bf16 v[120:123], a[192:195], v[12:15], v[156:159]\n"
"s_add_u32 m0, s73, 0\n"
"v_mov_b32 v163, v160\n"
"v_mov_b32 v165, v164\n"
"v_mov_b32 v166, v164\n"
"v_mfma_f32_16x16x32_bf16 v[128:131], a[192:195], v[16:19], v[160:163]\n"
"s_mov_b32 s55, 40960\n"
"v_mov_b32 v167, v164\n"
"ds_read_b64_tr_b16 a[224:225], v193\n"
"v_add_f32 v208, v208, v144\n"
"v_mfma_f32_16x16x32_bf16 v[136:139], a[192:195], v[20:23], v[164:167]\n"
"buffer_load_dwordx4 v196, s[24:27], s55 offen offset:2048 nt lds\n"
"v_mfma_f32_16x16x32_bf16 v[100:103], a[196:199], v[0:3], v[144:147]\n"
"ds_read_b64_tr_b16 a[226:227], v193 offset:4096\n"
"v_add_f32 v209, v209, v144\n"
"v_mfma_f32_16x16x32_bf16 v[108:111], a[196:199], v[4:7], v[148:151]\n"
"ds_read_b64_tr_b16 a[228:229], v194\n"
"v_add_f32 v210, v210, v144\n"
"v_mfma_f32_16x16x32_bf16 v[116:119], a[196:199], v[8:11], v[152:155]\n"
"ds_read_b64_tr_b16 a[230:231], v194 offset:4096\n"
"v_add_f32 v211, v211, v144\n"
"v_mfma_f32_16x16x32_bf16 v[124:127], a[196:199], v[12:15], v[156:159]\n"
"ds_read_b64_tr_b16 a[232:233], v193 offset:256\n"
"v_add_f32 v212, v212, v144\n"
"v_mfma_f32_16x16x32_bf16 v[132:135], a[196:199], v[16:19], v[160:163]\n"
"ds_read_b64_tr_b16 a[234:235], v193 offset:4352\n"
"v_add_f32 v213, v213, v144\n"
"v_mfma_f32_16x16x32_bf16 v[140:143], a[196:199], v[20:23], v[164:167]\n"
"ds_read_b64_tr_b16 a[236:237], v194 offset:256\n"
"v_add_f32 v214, v214, v144\n"
"v_mfma_f32_16x16x32_bf16 v[96:99], a[200:203], v[24:27], v[96:99]\n"
"ds_read_b64_tr_b16 a[238:239], v194 offset:4352\n"
"v_add_f32 v215, v215, v144\n"
"v_mfma_f32_16x16x32_bf16 v[104:107], a[200:203], v[28:31], v[104:107]\n"
"ds_read_b64_tr_b16 a[240:241], v193 offset:512\n"
"v_add_f32 v216, v216, v148\n"
"v_mfma_f32_16x16x32_bf16 v[112:115], a[200:203], v[32:35], v[112:115]\n"
"ds_read_b64_tr_b16 a[242:243], v193 offset:4608\n"
"v_add_f32 v217, v217, v148\n"
"v_mfma_f32_16x16x32_bf16 v[120:123], a[200:203], v[36:39], v[120:123]\n"
"s_add_u32 m0, s73, 4096\n"
"ds_read_b64_tr_b16 a[244:245], v194 offset:512\n"
"v_add_f32 v218, v218, v148\n"
"v_mfma_f32_16x16x32_bf16 v[128:131], a[200:203], v[40:43], v[128:131]\n"
"s_mov_b32 s55, 45056\n"
"ds_read_b64_tr_b16 a[246:247], v194 offset:4608\n"
"v_add_f32 v219, v219, v148\n"
"v_mfma_f32_16x16x32_bf16 v[136:139], a[200:203], v[44:47], v[136:139]\n"
"v_exp_f32 v208, v208\n"
"v_mfma_f32_16x16x32_bf16 v[100:103], a[204:207], v[24:27], v[100:103]\n"
"buffer_load_dwordx4 v195, s[24:27], s55 offen nt lds\n"
"v_mfma_f32_16x16x32_bf16 v[108:111], a[204:207], v[28:31], v[108:111]\n"
"v_exp_f32 v209, v209\n"
"v_mfma_f32_16x16x32_bf16 v[116:119], a[204:207], v[32:35], v[116:119]\n"
"v_exp_f32 v210, v210\n"
"v_mfma_f32_16x16x32_bf16 v[124:127], a[204:207], v[36:39], v[124:127]\n"
"v_exp_f32 v211, v211\n"
"v_mfma_f32_16x16x32_bf16 v[132:135], a[204:207], v[40:43], v[132:135]\n"
"v_exp_f32 v212, v212\n"
"v_mfma_f32_16x16x32_bf16 v[140:143], a[204:207], v[44:47], v[140:143]\n"
"v_exp_f32 v213, v213\n"
"v_mfma_f32_16x16x32_bf16 v[96:99], a[208:211], v[48:51], v[96:99]\n"
"v_exp_f32 v214, v214\n"
"v_mfma_f32_16x16x32_bf16 v[104:107], a[208:211], v[52:55], v[104:107]\n"
"v_exp_f32 v215, v215\n"
"v_mfma_f32_16x16x32_bf16 v[112:115], a[208:211], v[56:59], v[112:115]\n"
"v_cvt_pk_bf16_f32 v208, v208, v209\n"
"v_cvt_pk_bf16_f32 v209, v210, v211\n"
"v_mfma_f32_16x16x32_bf16 v[120:123], a[208:211], v[60:63], v[120:123]\n"
"v_cvt_pk_bf16_f32 v210, v212, v213\n"
"v_cvt_pk_bf16_f32 v211, v214, v215\n"
"v_mfma_f32_16x16x32_bf16 v[128:131], a[208:211], v[64:67], v[128:131]\n"
"v_exp_f32 v216, v216\n"
"v_mfma_f32_16x16x32_bf16 v[136:139], a[208:211], v[68:71], v[136:139]\n"
"v_exp_f32 v217, v217\n"
"v_mfma_f32_16x16x32_bf16 v[100:103], a[212:215], v[48:51], v[100:103]\n"
"v_add_f32 v220, v220, v148\n"
"v_add_f32 v221, v221, v148\n"
"v_mfma_f32_16x16x32_bf16 v[108:111], a[212:215], v[52:55], v[108:111]\n"
"v_add_f32 v222, v222, v148\n"
"v_add_f32 v223, v223, v148\n"
"v_mfma_f32_16x16x32_bf16 v[116:119], a[212:215], v[56:59], v[116:119]\n"
"s_add_u32 m0, s73, 4096\n"
"v_exp_f32 v218, v218\n"
"v_mfma_f32_16x16x32_bf16 v[124:127], a[212:215], v[60:63], v[124:127]\n"
"s_mov_b32 s55, 45056\n"
"v_exp_f32 v219, v219\n"
"v_mfma_f32_16x16x32_bf16 v[132:135], a[212:215], v[64:67], v[132:135]\n"
"v_exp_f32 v220, v220\n"
"v_mfma_f32_16x16x32_bf16 v[140:143], a[212:215], v[68:71], v[140:143]\n"
"buffer_load_dwordx4 v196, s[24:27], s55 offen offset:2048 nt lds\n"
"v_mfma_f32_16x16x32_bf16 v[96:99], a[216:219], v[72:75], v[96:99]\n"
"v_exp_f32 v221, v221\n"
"v_mfma_f32_16x16x32_bf16 v[104:107], a[216:219], v[76:79], v[104:107]\n"
"v_exp_f32 v222, v222\n"
"v_mfma_f32_16x16x32_bf16 v[112:115], a[216:219], v[80:83], v[112:115]\n"
"v_exp_f32 v223, v223\n"
"v_mfma_f32_16x16x32_bf16 v[120:123], a[216:219], v[84:87], v[120:123]\n"
"v_cvt_pk_bf16_f32 v216, v216, v217\n"
"v_cvt_pk_bf16_f32 v217, v218, v219\n"
"v_mfma_f32_16x16x32_bf16 v[128:131], a[216:219], v[88:91], v[128:131]\n"
"v_cvt_pk_bf16_f32 v218, v220, v221\n"
"v_cvt_pk_bf16_f32 v219, v222, v223\n"
"v_mfma_f32_16x16x32_bf16 v[136:139], a[216:219], v[92:95], v[136:139]\n"
"v_add_f32 v224, v224, v152\n"
"v_add_f32 v225, v225, v152\n"
"v_mfma_f32_16x16x32_bf16 v[100:103], a[220:223], v[72:75], v[100:103]\n"
"v_add_f32 v226, v226, v152\n"
"v_add_f32 v227, v227, v152\n"
"v_mfma_f32_16x16x32_bf16 v[108:111], a[220:223], v[76:79], v[108:111]\n"
"v_exp_f32 v224, v224\n"
"v_mfma_f32_16x16x32_bf16 v[116:119], a[220:223], v[80:83], v[116:119]\n"
"v_exp_f32 v225, v225\n"
"v_mfma_f32_16x16x32_bf16 v[124:127], a[220:223], v[84:87], v[124:127]\n"
"v_add_f32 v228, v228, v152\n"
"v_add_f32 v229, v229, v152\n"
"v_mfma_f32_16x16x32_bf16 v[132:135], a[220:223], v[88:91], v[132:135]\n"
"v_add_f32 v230, v230, v152\n"
"v_add_f32 v231, v231, v152\n"
"v_mfma_f32_16x16x32_bf16 v[140:143], a[220:223], v[92:95], v[140:143]\n"
"v_exp_f32 v226, v226\n"
"v_mfma_f32_16x16x32_bf16 v[168:171], v[204:207], v[208:211], 0\n"
"s_waitcnt lgkmcnt(0)\n"
"v_exp_f32 v227, v227\n"
"v_mfma_f32_16x16x32_bf16 a[0:3], a[224:227], v[208:211], 0\n"
"s_add_u32 m0, s73, 147456\n"
"v_exp_f32 v228, v228\n"
"v_mfma_f32_16x16x32_bf16 a[24:27], a[228:231], v[208:211], 0\n"
"s_mov_b32 s55, 32768\n"
"v_exp_f32 v229, v229\n"
"v_mfma_f32_16x16x32_bf16 a[48:51], a[232:235], v[208:211], 0\n"
"buffer_load_dwordx4 v197, s[28:31], s55 offen nt lds\n"
"v_mfma_f32_16x16x32_bf16 a[72:75], a[236:239], v[208:211], 0\n"
"v_exp_f32 v230, v230\n"
"v_mfma_f32_16x16x32_bf16 a[96:99], a[240:243], v[208:211], 0\n"
"v_exp_f32 v231, v231\n"
"v_mfma_f32_16x16x32_bf16 a[120:123], a[244:247], v[208:211], 0\n"
"v_cvt_pk_bf16_f32 v224, v224, v225\n"
"v_cvt_pk_bf16_f32 v225, v226, v227\n"
"v_mfma_f32_16x16x32_bf16 v[172:175], v[204:207], v[216:219], 0\n"
"v_cvt_pk_bf16_f32 v226, v228, v229\n"
"v_cvt_pk_bf16_f32 v227, v230, v231\n"
"v_mfma_f32_16x16x32_bf16 a[4:7], a[224:227], v[216:219], 0\n"
"v_add_f32 v232, v232, v156\n"
"v_add_f32 v233, v233, v156\n"
"v_mfma_f32_16x16x32_bf16 a[28:31], a[228:231], v[216:219], 0\n"
"v_add_f32 v234, v234, v156\n"
"v_add_f32 v235, v235, v156\n"
"v_mfma_f32_16x16x32_bf16 a[52:55], a[232:235], v[216:219], 0\n"
"v_exp_f32 v232, v232\n"
"v_mfma_f32_16x16x32_bf16 a[76:79], a[236:239], v[216:219], 0\n"
"v_exp_f32 v233, v233\n"
"v_mfma_f32_16x16x32_bf16 a[100:103], a[240:243], v[216:219], 0\n"
"v_add_f32 v236, v236, v156\n"
"v_add_f32 v237, v237, v156\n"
"v_mfma_f32_16x16x32_bf16 a[124:127], a[244:247], v[216:219], 0\n"
"v_add_f32 v238, v238, v156\n"
"v_add_f32 v239, v239, v156\n"
"v_mfma_f32_16x16x32_bf16 v[176:179], v[204:207], v[224:227], 0\n"
"v_exp_f32 v234, v234\n"
"v_mfma_f32_16x16x32_bf16 a[8:11], a[224:227], v[224:227], 0\n"
"v_exp_f32 v235, v235\n"
"v_mfma_f32_16x16x32_bf16 a[32:35], a[228:231], v[224:227], 0\n"
"v_exp_f32 v236, v236\n"
"v_mfma_f32_16x16x32_bf16 a[56:59], a[232:235], v[224:227], 0\n"
"v_exp_f32 v237, v237\n"
"v_mfma_f32_16x16x32_bf16 a[80:83], a[236:239], v[224:227], 0\n"
"s_add_u32 m0, s73, 147456\n"
"v_exp_f32 v238, v238\n"
"v_mfma_f32_16x16x32_bf16 a[104:107], a[240:243], v[224:227], 0\n"
"s_mov_b32 s55, 32768\n"
"v_exp_f32 v239, v239\n"
"v_mfma_f32_16x16x32_bf16 a[128:131], a[244:247], v[224:227], 0\n"
"buffer_load_dwordx4 v197, s[28:31], s55 offen offset:2048 nt lds\n"
"v_cvt_pk_bf16_f32 v232, v232, v233\n"
"v_cvt_pk_bf16_f32 v233, v234, v235\n"
"v_cvt_pk_bf16_f32 v234, v236, v237\n"
"v_cvt_pk_bf16_f32 v235, v238, v239\n"
"v_add_f32 v240, v240, v160\n"
"v_add_f32 v241, v241, v160\n"
"v_mfma_f32_16x16x32_bf16 v[180:183], v[204:207], v[232:235], 0\n"
"v_add_f32 v242, v242, v160\n"
"v_add_f32 v243, v243, v160\n"
"v_mfma_f32_16x16x32_bf16 a[12:15], a[224:227], v[232:235], 0\n"
"v_exp_f32 v240, v240\n"
"v_mfma_f32_16x16x32_bf16 a[36:39], a[228:231], v[232:235], 0\n"
"v_exp_f32 v241, v241\n"
"v_mfma_f32_16x16x32_bf16 a[60:63], a[232:235], v[232:235], 0\n"
"v_add_f32 v244, v244, v160\n"
"v_add_f32 v245, v245, v160\n"
"v_mfma_f32_16x16x32_bf16 a[84:87], a[236:239], v[232:235], 0\n"
"v_add_f32 v246, v246, v160\n"
"v_add_f32 v247, v247, v160\n"
"v_mfma_f32_16x16x32_bf16 a[108:111], a[240:243], v[232:235], 0\n"
"v_exp_f32 v242, v242\n"
"v_mfma_f32_16x16x32_bf16 a[132:135], a[244:247], v[232:235], 0\n"
"v_exp_f32 v243, v243\n"
"v_exp_f32 v244, v244\n"
"v_exp_f32 v245, v245\n"
"v_exp_f32 v246, v246\n"
"v_exp_f32 v247, v247\n"
"v_cvt_pk_bf16_f32 v240, v240, v241\n"
"v_cvt_pk_bf16_f32 v241, v242, v243\n"
"v_cvt_pk_bf16_f32 v242, v244, v245\n"
"v_cvt_pk_bf16_f32 v243, v246, v247\n"
"v_add_f32 v248, v248, v164\n"
"v_add_f32 v249, v249, v164\n"
"v_mfma_f32_16x16x32_bf16 v[184:187], v[204:207], v[240:243], 0\n"
"v_add_f32 v250, v250, v164\n"
"v_add_f32 v251, v251, v164\n"
"v_mfma_f32_16x16x32_bf16 a[16:19], a[224:227], v[240:243], 0\n"
"v_exp_f32 v248, v248\n"
"v_mfma_f32_16x16x32_bf16 a[40:43], a[228:231], v[240:243], 0\n"
"s_add_u32 m0, s73, 151552\n"
"v_exp_f32 v249, v249\n"
"v_mfma_f32_16x16x32_bf16 a[64:67], a[232:235], v[240:243], 0\n"
"s_mov_b32 s55, 36864\n"
"v_add_f32 v252, v252, v164\n"
"v_add_f32 v253, v253, v164\n"
"v_mfma_f32_16x16x32_bf16 a[88:91], a[236:239], v[240:243], 0\n"
"buffer_load_dwordx4 v197, s[28:31], s55 offen nt lds\n"
"v_mfma_f32_16x16x32_bf16 a[112:115], a[240:243], v[240:243], 0\n"
"v_add_f32 v254, v254, v164\n"
"v_add_f32 v255, v255, v164\n"
"v_mfma_f32_16x16x32_bf16 a[136:139], a[244:247], v[240:243], 0\n"
"v_exp_f32 v250, v250\n"
"v_exp_f32 v251, v251\n"
"v_exp_f32 v252, v252\n"
"v_exp_f32 v253, v253\n"
"v_exp_f32 v254, v254\n"
"v_exp_f32 v255, v255\n"
"v_cvt_pk_bf16_f32 v248, v248, v249\n"
"v_cvt_pk_bf16_f32 v249, v250, v251\n"
"v_cvt_pk_bf16_f32 v250, v252, v253\n"
"v_cvt_pk_bf16_f32 v251, v254, v255\n"
"v_or_b32 v198, v208, v209\n"
"v_or3_b32 v198, v198, v210, v211\n"
"v_mfma_f32_16x16x32_bf16 v[188:191], v[204:207], v[248:251], 0\n"
"v_or3_b32 v198, v198, v216, v217\n"
"v_or3_b32 v198, v198, v218, v219\n"
"v_mfma_f32_16x16x32_bf16 a[20:23], a[224:227], v[248:251], 0\n"
"v_or3_b32 v198, v198, v224, v225\n"
"v_or3_b32 v198, v198, v226, v227\n"
"v_mfma_f32_16x16x32_bf16 a[44:47], a[228:231], v[248:251], 0\n"
"v_or3_b32 v198, v198, v232, v233\n"
"v_or3_b32 v198, v198, v234, v235\n"
"v_mfma_f32_16x16x32_bf16 a[68:71], a[232:235], v[248:251], 0\n"
"v_exp_f32 v96, v96\n"
"v_mfma_f32_16x16x32_bf16 a[92:95], a[236:239], v[248:251], 0\n"
"v_exp_f32 v97, v97\n"
"v_mfma_f32_16x16x32_bf16 a[116:119], a[240:243], v[248:251], 0\n"
"v_or3_b32 v198, v198, v240, v241\n"
"v_or3_b32 v198, v198, v242, v243\n"
"v_mfma_f32_16x16x32_bf16 a[140:143], a[244:247], v[248:251], 0\n"
"v_exp_f32 v98, v98\n"
"v_exp_f32 v99, v99\n"
"v_exp_f32 v100, v100\n"
"v_exp_f32 v101, v101\n"
"v_exp_f32 v102, v102\n"
"v_exp_f32 v103, v103\n"
"v_exp_f32 v104, v104\n"
"v_exp_f32 v105, v105\n"
"v_or3_b32 v198, v198, v248, v249\n"
"v_cvt_pk_bf16_f32 v96, v96, v97\n"
"v_exp_f32 v106, v106\n"
"v_exp_f32 v107, v107\n"
"v_or3_b32 v198, v198, v250, v251\n"
"v_cvt_pk_bf16_f32 v97, v98, v99\n"
"v_exp_f32 v108, v108\n"
"v_exp_f32 v109, v109\n"
"v_exp_f32 v110, v110\n"
"v_exp_f32 v111, v111\n"
"v_exp_f32 v112, v112\n"
"v_exp_f32 v113, v113\n"
"v_or_b32 v198, v96, v97\n"
"v_cvt_pk_bf16_f32 v98, v100, v101\n"
"v_cvt_pk_bf16_f32 v99, v102, v103\n"
"v_cvt_pk_bf16_f32 v104, v104, v105\n"
"v_exp_f32 v114, v114\n"
"v_exp_f32 v115, v115\n"
"v_or3_b32 v198, v198, v98, v99\n"
"v_cvt_pk_bf16_f32 v105, v106, v107\n"
"v_exp_f32 v116, v116\n"
"v_exp_f32 v117, v117\n"
"s_add_u32 m0, s73, 151552\n"
"v_or3_b32 v198, v198, v104, v105\n"
"v_cvt_pk_bf16_f32 v106, v108, v109\n"
"v_cvt_pk_bf16_f32 v107, v110, v111\n"
"v_cvt_pk_bf16_f32 v112, v112, v113\n"
"s_mov_b32 s55, 36864\n"
"v_or3_b32 v198, v198, v106, v107\n"
"v_cvt_pk_bf16_f32 v113, v114, v115\n"
"ds_read_b64_tr_b16 a[248:249], v193 offset:768\n"
"ds_read_b64_tr_b16 a[250:251], v193 offset:4864\n"
"ds_read_b64_tr_b16 a[252:253], v194 offset:768\n"
"ds_read_b64_tr_b16 a[254:255], v194 offset:4864\n"
"buffer_load_dwordx4 v197, s[28:31], s55 offen offset:2048 nt lds\n"
"v_or3_b32 v198, v198, v112, v113\n"
"v_exp_f32 v118, v118\n"
"v_exp_f32 v119, v119\n"
"v_cvt_pk_bf16_f32 v114, v116, v117\n"
"v_add_u32 v193, s79, v193\n"
"v_add_u32 v194, s79, v194\n"
"s_memrealtime s[70:71]\n"
"s_waitcnt lgkmcnt(0)\n"
"s_sub_u32 s72, s70, 0\n"
"s_waitcnt lgkmcnt(0)\n"
"s_barrier\n"
"v_mfma_f32_16x16x32_bf16 a[144:147], a[248:251], v[208:211], 0\n"
"s_add_u32 m0, s73, 32768\n"
"ds_read_b128 a[192:195], v192\n"
"v_cvt_pk_bf16_f32 v115, v118, v119\n"
"v_mfma_f32_16x16x32_bf16 a[148:151], a[248:251], v[216:219], 0\n"
"s_mov_b32 s55, 49152\n"
"ds_read_b128 a[196:199], v192 offset:4096\n"
"v_or3_b32 v198, v198, v114, v115\n"
"v_mfma_f32_16x16x32_bf16 a[152:155], a[248:251], v[224:227], 0\n"
"ds_read_b128 a[200:203], v192 offset:256\n"
"v_mfma_f32_16x16x32_bf16 a[156:159], a[248:251], v[232:235], 0\n"
"buffer_load_dwordx4 v195, s[24:27], s55 offen nt lds\n"
"v_mfma_f32_16x16x32_bf16 a[160:163], a[248:251], v[240:243], 0\n"
"ds_read_b128 a[204:207], v192 offset:4352\n"
"v_mfma_f32_16x16x32_bf16 a[164:167], a[248:251], v[248:251], 0\n"
"ds_read_b128 a[208:211], v192 offset:512\n"
"v_mfma_f32_16x16x32_bf16 a[168:171], a[252:255], v[208:211], 0\n"
"ds_read_b128 a[212:215], v192 offset:4608\n"
"v_mfma_f32_16x16x32_bf16 a[172:175], a[252:255], v[216:219], 0\n"
"ds_read_b128 a[216:219], v192 offset:768\n"
"v_mfma_f32_16x16x32_bf16 a[176:179], a[252:255], v[224:227], 0\n"
"ds_read_b128 a[220:223], v192 offset:4864\n"
"v_add_u32 v192, 32768, v192\n"
"v_mfma_f32_16x16x32_bf16 a[180:183], a[252:255], v[232:235], 0\n"
"v_exp_f32 v120, v120\n"
"v_mfma_f32_16x16x32_bf16 a[184:187], a[252:255], v[240:243], 0\n"
"v_exp_f32 v121, v121\n"
"v_mfma_f32_16x16x32_bf16 a[188:191], a[252:255], v[248:251], 0\n"
"s_waitcnt lgkmcnt(5)\n"
"v_exp_f32 v122, v122\n"
"v_mfma_f32_16x16x32_bf16 v[208:211], a[192:195], v[0:3], v[144:147]\n"
"v_exp_f32 v123, v123\n"
"v_mfma_f32_16x16x32_bf16 v[216:219], a[192:195], v[4:7], v[148:151]\n"
"v_exp_f32 v124, v124\n"
"v_mfma_f32_16x16x32_bf16 v[224:227], a[192:195], v[8:11], v[152:155]\n"
"s_add_u32 m0, s73, 32768\n"
"ds_read_b64_tr_b16 a[224:225], v193\n"
"v_cvt_pk_bf16_f32 v120, v120, v121\n"
"v_mfma_f32_16x16x32_bf16 v[232:235], a[192:195], v[12:15], v[156:159]\n"
"s_mov_b32 s55, 49152\n"
"v_exp_f32 v125, v125\n"
"v_mfma_f32_16x16x32_bf16 v[240:243], a[192:195], v[16:19], v[160:163]\n"
"v_exp_f32 v126, v126\n"
"v_mfma_f32_16x16x32_bf16 v[248:251], a[192:195], v[20:23], v[164:167]\n"
"buffer_load_dwordx4 v196, s[24:27], s55 offen offset:2048 nt lds\n"
"v_mfma_f32_16x16x32_bf16 v[212:215], a[196:199], v[0:3], v[144:147]\n"
"ds_read_b64_tr_b16 a[226:227], v193 offset:4096\n"
"v_cvt_pk_bf16_f32 v121, v122, v123\n"
"v_mfma_f32_16x16x32_bf16 v[220:223], a[196:199], v[4:7], v[148:151]\n"
"v_exp_f32 v127, v127\n"
"v_mfma_f32_16x16x32_bf16 v[228:231], a[196:199], v[8:11], v[152:155]\n"
"v_exp_f32 v128, v128\n"
"v_mfma_f32_16x16x32_bf16 v[236:239], a[196:199], v[12:15], v[156:159]\n"
"v_exp_f32 v129, v129\n"
"v_mfma_f32_16x16x32_bf16 v[244:247], a[196:199], v[16:19], v[160:163]\n"
"v_exp_f32 v130, v130\n"
"v_mfma_f32_16x16x32_bf16 v[252:255], a[196:199], v[20:23], v[164:167]\n"
"v_exp_f32 v131, v131\n"
"v_mfma_f32_16x16x32_bf16 v[208:211], a[200:203], v[24:27], v[208:211]\n"
"v_exp_f32 v132, v132\n"
"v_mfma_f32_16x16x32_bf16 v[216:219], a[200:203], v[28:31], v[216:219]\n"
"v_exp_f32 v133, v133\n"
"v_mfma_f32_16x16x32_bf16 v[224:227], a[200:203], v[32:35], v[224:227]\n"
"v_exp_f32 v134, v134\n"
"v_mfma_f32_16x16x32_bf16 v[232:235], a[200:203], v[36:39], v[232:235]\n"
"v_exp_f32 v135, v135\n"
"v_mfma_f32_16x16x32_bf16 v[240:243], a[200:203], v[40:43], v[240:243]\n"
"s_add_u32 m0, s73, 36864\n"
"v_exp_f32 v136, v136\n"
"v_mfma_f32_16x16x32_bf16 v[248:251], a[200:203], v[44:47], v[248:251]\n"
"s_waitcnt lgkmcnt(0)\n"
"v_exp_f32 v137, v137\n"
"v_mfma_f32_16x16x32_bf16 v[212:215], a[204:207], v[24:27], v[212:215]\n"
"s_mov_b32 s55, 53248\n"
"v_or3_b32 v198, v198, v120, v121\n"
"v_cvt_pk_bf16_f32 v122, v124, v125\n"
"v_mfma_f32_16x16x32_bf16 v[220:223], a[204:207], v[28:31], v[220:223]\n"
"buffer_load_dwordx4 v195, s[24:27], s55 offen nt lds\n"
"v_mfma_f32_16x16x32_bf16 v[228:231], a[204:207], v[32:35], v[228:231]\n"
"v_cvt_pk_bf16_f32 v123, v126, v127\n"
"v_cvt_pk_bf16_f32 v128, v128, v129\n"
"v_mfma_f32_16x16x32_bf16 v[236:239], a[204:207], v[36:39], v[236:239]\n"
"v_exp_f32 v138, v138\n"
"v_mfma_f32_16x16x32_bf16 v[244:247], a[204:207], v[40:43], v[244:247]\n"
"v_exp_f32 v139, v139\n"
"v_mfma_f32_16x16x32_bf16 v[252:255], a[204:207], v[44:47], v[252:255]\n"
"v_or3_b32 v198, v198, v122, v123\n"
"v_cvt_pk_bf16_f32 v129, v130, v131\n"
"v_mfma_f32_16x16x32_bf16 v[208:211], a[208:211], v[48:51], v[208:211]\n"
"v_exp_f32 v140, v140\n"
"v_mfma_f32_16x16x32_bf16 v[216:219], a[208:211], v[52:55], v[216:219]\n"
"v_exp_f32 v141, v141\n"
"v_mfma_f32_16x16x32_bf16 v[224:227], a[208:211], v[56:59], v[224:227]\n"
"v_exp_f32 v142, v142\n"
"v_mfma_f32_16x16x32_bf16 v[232:235], a[208:211], v[60:63], v[232:235]\n"
"v_exp_f32 v143, v143\n"
"v_mfma_f32_16x16x32_bf16 v[240:243], a[208:211], v[64:67], v[240:243]\n"
"v_or3_b32 v198, v198, v128, v129\n"
"v_cvt_pk_bf16_f32 v130, v132, v133\n"
"v_mfma_f32_16x16x32_bf16 v[248:251], a[208:211], v[68:71], v[248:251]\n"
"v_cvt_pk_bf16_f32 v131, v134, v135\n"
"v_cvt_pk_bf16_f32 v136, v136, v137\n"
"v_mfma_f32_16x16x32_bf16 v[212:215], a[212:215], v[48:51], v[212:215]\n"
"s_add_u32 m0, s73, 36864\n"
"v_or3_b32 v198, v198, v130, v131\n"
"v_cvt_pk_bf16_f32 v137, v138, v139\n"
"v_mfma_f32_16x16x32_bf16 v[220:223], a[212:215], v[52:55], v[220:223]\n"
"s_mov_b32 s55, 53248\n"
"v_or3_b32 v198, v198, v136, v137\n"
"v_cvt_pk_bf16_f32 v138, v140, v141\n"
"v_mfma_f32_16x16x32_bf16 v[228:231], a[212:215], v[56:59], v[228:231]\n"
"v_cvt_pk_bf16_f32 v139, v142, v143\n"
"v_or3_b32 v198, v198, v138, v139\n"
"v_mfma_f32_16x16x32_bf16 v[236:239], a[212:215], v[60:63], v[236:239]\n"
"buffer_load_dwordx4 v196, s[24:27], s55 offen offset:2048 nt lds\n"
"v_mfma_f32_16x16x32_bf16 v[244:247], a[212:215], v[64:67], v[244:247]\n"
"v_and_b32 v199, 0x40004000, v198\n"
"v_cmp_ne_u32 vcc, 0, v199\n"
"v_mfma_f32_16x16x32_bf16 v[252:255], a[212:215], v[68:71], v[252:255]\n"
"ds_read_b64_tr_b16 a[228:229], v194\n"
"v_mfma_f32_16x16x32_bf16 v[208:211], a[216:219], v[72:75], v[208:211]\n"
"ds_read_b64_tr_b16 a[230:231], v194 offset:4096\n"
"v_mfma_f32_16x16x32_bf16 v[216:219], a[216:219], v[76:79], v[216:219]\n"
"ds_read_b64_tr_b16 a[232:233], v193 offset:256\n"
"v_mfma_f32_16x16x32_bf16 v[224:227], a[216:219], v[80:83], v[224:227]\n"
"ds_read_b64_tr_b16 a[234:235], v193 offset:4352\n"
"v_mfma_f32_16x16x32_bf16 v[232:235], a[216:219], v[84:87], v[232:235]\n"
"ds_read_b64_tr_b16 a[236:237], v194 offset:256\n"
"v_mfma_f32_16x16x32_bf16 v[240:243], a[216:219], v[88:91], v[240:243]\n"
"ds_read_b64_tr_b16 a[238:239], v194 offset:4352\n"
"v_mfma_f32_16x16x32_bf16 v[248:251], a[216:219], v[92:95], v[248:251]\n"
"ds_read_b64_tr_b16 a[240:241], v193 offset:512\n"
"v_mfma_f32_16x16x32_bf16 v[212:215], a[220:223], v[72:75], v[212:215]\n"
"ds_read_b64_tr_b16 a[242:243], v193 offset:4608\n"
"v_mfma_f32_16x16x32_bf16 v[220:223], a[220:223], v[76:79], v[220:223]\n"
"ds_read_b64_tr_b16 a[244:245], v194 offset:512\n"
"v_mfma_f32_16x16x32_bf16 v[228:231], a[220:223], v[80:83], v[228:231]\n"
"s_add_u32 m0, s73, 16384\n"
"ds_read_b64_tr_b16 a[246:247], v194 offset:4608\n"
"s_mov_b32 s55, 40960\n"
"v_mfma_f32_16x16x32_bf16 v[236:239], a[220:223], v[84:87], v[236:239]\n"
"ds_read_b64_tr_b16 a[248:249], v193 offset:768\n"
"v_mfma_f32_16x16x32_bf16 v[244:247], a[220:223], v[88:91], v[244:247]\n"
"ds_read_b64_tr_b16 a[250:251], v193 offset:4864\n"
"v_add_u32 v193, 32768, v193\n"
"v_mfma_f32_16x16x32_bf16 v[252:255], a[220:223], v[92:95], v[252:255]\n"
"s_cbranch_vccnz .Lrare3\n"
".Lrare3_ret:\n"
"ds_read_b64_tr_b16 a[252:253], v194 offset:768\n"
"v_mfma_f32_16x16x32_bf16 v[168:171], v[204:207], v[96:99], v[168:171]\n"
"buffer_load_dwordx4 v197, s[28:31], s55 offen nt lds\n"
"v_mfma_f32_16x16x32_bf16 v[172:175], v[204:207], v[104:107], v[172:175]\n"
"ds_read_b64_tr_b16 a[254:255], v194 offset:4864\n"
"v_add_u32 v194, 32768, v194\n"
"v_mfma_f32_16x16x32_bf16 v[176:179], v[204:207], v[112:115], v[176:179]\n"
"v_exp_f32 v208, v208\n"
"v_mfma_f32_16x16x32_bf16 v[180:183], v[204:207], v[120:123], v[180:183]\n"
"v_exp_f32 v209, v209\n"
"v_mfma_f32_16x16x32_bf16 v[184:187], v[204:207], v[128:131], v[184:187]\n"
"v_exp_f32 v210, v210\n"
"v_mfma_f32_16x16x32_bf16 v[188:191], v[204:207], v[136:139], v[188:191]\n"
"v_exp_f32 v211, v211\n"
"v_mfma_f32_16x16x32_bf16 a[0:3], a[224:227], v[96:99], a[0:3]\n"
"v_exp_f32 v212, v212\n"
"v_mfma_f32_16x16x32_bf16 a[4:7], a[224:227], v[104:107], a[4:7]\n"
"v_exp_f32 v213, v213\n"
"v_mfma_f32_16x16x32_bf16 a[8:11], a[224:227], v[112:115], a[8:11]\n"
"v_exp_f32 v214, v214\n"
"v_mfma_f32_16x16x32_bf16 a[12:15], a[224:227], v[120:123], a[12:15]\n"
"v_exp_f32 v215, v215\n"
"v_mfma_f32_16x16x32_bf16 a[16:19], a[224:227], v[128:131], a[16:19]\n"
"s_add_u32 m0, s73, 16384\n"
"v_cvt_pk_bf16_f32 v208, v208, v209\n"
"v_mfma_f32_16x16x32_bf16 a[20:23], a[224:227], v[136:139], a[20:23]\n"
"s_waitcnt lgkmcnt(0)\n"
"v_exp_f32 v216, v216\n"
"v_mfma_f32_16x16x32_bf16 a[24:27], a[228:231], v[96:99], a[24:27]\n"
"s_mov_b32 s55, 40960\n"
"v_exp_f32 v217, v217\n"
"v_mfma_f32_16x16x32_bf16 a[28:31], a[228:231], v[104:107], a[28:31]\n"
"buffer_load_dwordx4 v197, s[28:31], s55 offen offset:2048 nt lds\n"
"v_mfma_f32_16x16x32_bf16 a[32:35], a[228:231], v[112:115], a[32:35]\n"
"s_add_u32 s64, s64, 8192\n"
"v_exp_f32 v218, v218\n"
"v_mfma_f32_16x16x32_bf16 a[36:39], a[228:231], v[120:123], a[36:39]\n"
"s_add_u32 s72, s72, 112\n"
"v_exp_f32 v219, v219\n"
"v_mfma_f32_16x16x32_bf16 a[40:43], a[228:231], v[128:131], a[40:43]\n"
"v_cvt_pk_bf16_f32 v209, v210, v211\n"
"v_or_b32 v198, v208, v209\n"
"v_mfma_f32_16x16x32_bf16 a[44:47], a[228:231], v[136:139], a[44:47]\n"
"v_exp_f32 v220, v220\n"
"v_mfma_f32_16x16x32_bf16 a[48:51], a[232:235], v[96:99], a[48:51]\n"
"v_exp_f32 v221, v221\n"
"v_mfma_f32_16x16x32_bf16 a[52:55], a[232:235], v[104:107], a[52:55]\n"
"v_exp_f32 v222, v222\n"
"v_mfma_f32_16x16x32_bf16 a[56:59], a[232:235], v[112:115], a[56:59]\n"
"v_exp_f32 v223, v223\n"
"v_mfma_f32_16x16x32_bf16 a[60:63], a[232:235], v[120:123], a[60:63]\n"
"v_exp_f32 v224, v224\n"
"v_mfma_f32_16x16x32_bf16 a[64:67], a[232:235], v[128:131], a[64:67]\n"
"v_exp_f32 v225, v225\n"
"v_mfma_f32_16x16x32_bf16 a[68:71], a[232:235], v[136:139], a[68:71]\n"
"v_cvt_pk_bf16_f32 v210, v212, v213\n"
"v_cvt_pk_bf16_f32 v211, v214, v215\n"
"v_mfma_f32_16x16x32_bf16 a[72:75], a[236:239], v[96:99], a[72:75]\n"
"v_cvt_pk_bf16_f32 v216, v216, v217\n"
"v_or3_b32 v198, v198, v210, v211\n"
"v_mfma_f32_16x16x32_bf16 a[76:79], a[236:239], v[104:107], a[76:79]\n"
"v_exp_f32 v226, v226\n"
"v_mfma_f32_16x16x32_bf16 a[80:83], a[236:239], v[112:115], a[80:83]\n"
"v_exp_f32 v227, v227\n"
"v_mfma_f32_16x16x32_bf16 a[84:87], a[236:239], v[120:123], a[84:87]\n"
"v_cvt_pk_bf16_f32 v217, v218, v219\n"
"v_or3_b32 v198, v198, v216, v217\n"
"v_mfma_f32_16x16x32_bf16 a[88:91], a[236:239], v[128:131], a[88:91]\n"
"v_exp_f32 v228, v228\n"
"v_mfma_f32_16x16x32_bf16 a[92:95], a[236:239], v[136:139], a[92:95]\n"
"v_exp_f32 v229, v229\n"
"v_mfma_f32_16x16x32_bf16 a[96:99], a[240:243], v[96:99], a[96:99]\n"
"s_memrealtime s[70:71]\n"
"v_cvt_pk_bf16_f32 v218, v220, v221\n"
"v_cvt_pk_bf16_f32 v219, v222, v223\n"
"v_mfma_f32_16x16x32_bf16 a[100:103], a[240:243], v[104:107], a[100:103]\n"
"v_cvt_pk_bf16_f32 v224, v224, v225\n"
"v_or3_b32 v198, v198, v218, v219\n"
"v_mfma_f32_16x16x32_bf16 a[104:107], a[240:243], v[112:115], a[104:107]\n"
"v_cvt_pk_bf16_f32 v225, v226, v227\n"
"v_or3_b32 v198, v198, v224, v225\n"
"v_mfma_f32_16x16x32_bf16 a[108:111], a[240:243], v[120:123], a[108:111]\n"
"v_exp_f32 v230, v230\n"
"v_mfma_f32_16x16x32_bf16 a[112:115], a[240:243], v[128:131], a[112:115]\n"
"v_exp_f32 v231, v231\n"
"v_mfma_f32_16x16x32_bf16 a[116:119], a[240:243], v[136:139], a[116:119]\n"
"v_cvt_pk_bf16_f32 v226, v228, v229\n"
"v_mfma_f32_16x16x32_bf16 a[120:123], a[244:247], v[96:99], a[120:123]\n"
"v_mfma_f32_16x16x32_bf16 a[124:127], a[244:247], v[104:107], a[124:127]\n"
"v_mfma_f32_16x16x32_bf16 a[128:131], a[244:247], v[112:115], a[128:131]\n"
"v_mfma_f32_16x16x32_bf16 a[132:135], a[244:247], v[120:123], a[132:135]\n"
"v_mfma_f32_16x16x32_bf16 a[136:139], a[244:247], v[128:131], a[136:139]\n"
"v_mfma_f32_16x16x32_bf16 a[140:143], a[244:247], v[136:139], a[140:143]\n"
"s_waitcnt lgkmcnt(0)\n"
"s_sub_u32 s71, s72, s70\n"
"s_sub_u32 s71, s71, 16\n"
"s_cmp_lt_u32 s71, 400\n"
"s_cbranch_scc1 .Lpg1\n"
".Lpg1x:\n"
"s_barrier\n"
"v_mfma_f32_16x16x32_bf16 a[144:147], a[248:251], v[96:99], a[144:147]\n"
"s_add_u32 m0, s73, 20480\n"
"ds_read_b128 a[192:195], v192\n"
"v_cvt_pk_bf16_f32 v227, v230, v231\n"
"v_mfma_f32_16x16x32_bf16 a[148:151], a[248:251], v[104:107], a[148:151]\n"
"s_mov_b32 s55, 45056\n"
"ds_read_b128 a[196:199], v192 offset:4096\n"
"v_or3_b32 v198, v198, v226, v227\n"
"v_mfma_f32_16x16x32_bf16 a[152:155], a[248:251], v[112:115], a[152:155]\n"
"ds_read_b128 a[200:203], v192 offset:256\n"
"v_mfma_f32_16x16x32_bf16 a[156:159], a[248:251], v[120:123], a[156:159]\n"
"buffer_load_dwordx4 v197, s[28:31], s55 offen nt lds\n"
"v_mfma_f32_16x16x32_bf16 a[160:163], a[248:251], v[128:131], a[160:163]\n"
"ds_read_b128 a[204:207], v192 offset:4352\n"
"v_mfma_f32_16x16x32_bf16 a[164:167], a[248:251], v[136:139], a[164:167]\n"
"ds_read_b128 a[208:211], v192 offset:512\n"
"v_mfma_f32_16x16x32_bf16 a[168:171], a[252:255], v[96:99], a[168:171]\n"
"ds_read_b128 a[212:215], v192 offset:4608\n"
"v_mfma_f32_16x16x32_bf16 a[172:175], a[252:255], v[104:107], a[172:175]\n"
"ds_read_b128 a[216:219], v192 offset:768\n"
"v_mfma_f32_16x16x32_bf16 a[176:179], a[252:255], v[112:115], a[176:179]\n"
"ds_read_b128 a[220:223], v192 offset:4864\n"
"v_add_u32 v192, 32768, v192\n"
"v_mfma_f32_16x16x32_bf16 a[180:183], a[252:255], v[120:123], a[180:183]\n"
"v_exp_f32 v232, v232\n"
"v_mfma_f32_16x16x32_bf16 a[184:187], a[252:255], v[128:131], a[184:187]\n"
"v_exp_f32 v233, v233\n"
"v_mfma_f32_16x16x32_bf16 a[188:191], a[252:255], v[136:139], a[188:191]\n"
"s_waitcnt lgkmcnt(5)\n"
"v_exp_f32 v234, v234\n"
"v_mfma_f32_16x16x32_bf16 v[96:99], a[192:195], v[0:3], v[144:147]\n"
"v_exp_f32 v235, v235\n"
"v_mfma_f32_16x16x32_bf16 v[104:107], a[192:195], v[4:7], v[148:151]\n"
"v_exp_f32 v236, v236\n"
"v_mfma_f32_16x16x32_bf16 v[112:115], a[192:195], v[8:11], v[152:155]\n"
"s_add_u32 m0, s73, 20480\n"
"ds_read_b64_tr_b16 a[224:225], v193\n"
"v_cvt_pk_bf16_f32 v232, v232, v233\n"
"v_mfma_f32_16x16x32_bf16 v[120:123], a[192:195], v[12:15], v[156:159]\n"
"s_mov_b32 s55, 45056\n"
"v_exp_f32 v237, v237\n"
"v_mfma_f32_16x16x32_bf16 v[128:131], a[192:195], v[16:19], v[160:163]\n"
"v_exp_f32 v238, v238\n"
"v_mfma_f32_16x16x32_bf16 v[136:139], a[192:195], v[20:23], v[164:167]\n"
"buffer_load_dwordx4 v197, s[28:31], s55 offen offset:2048 nt lds\n"
"v_mfma_f32_16x16x32_bf16 v[100:103], a[196:199], v[0:3], v[144:147]\n"
"ds_read_b64_tr_b16 a[226:227], v193 offset:4096\n"
"v_cvt_pk_bf16_f32 v233, v234, v235\n"
"v_mfma_f32_16x16x32_bf16 v[108:111], a[196:199], v[4:7], v[148:151]\n"
"v_exp_f32 v239, v239\n"
"v_mfma_f32_16x16x32_bf16 v[116:119], a[196:199], v[8:11], v[152:155]\n"
"v_exp_f32 v240, v240\n"
"v_mfma_f32_16x16x32_bf16 v[124:127], a[196:199], v[12:15], v[156:159]\n"
"v_exp_f32 v241, v241\n"
"v_mfma_f32_16x16x32_bf16 v[132:135], a[196:199], v[16:19], v[160:163]\n"
"v_exp_f32 v242, v242\n"
"v_mfma_f32_16x16x32_bf16 v[140:143], a[196:199], v[20:23], v[164:167]\n"
"v_exp_f32 v243, v243\n"
"v_mfma_f32_16x16x32_bf16 v[96:99], a[200:203], v[24:27], v[96:99]\n"
"v_exp_f32 v244, v244\n"
"v_mfma_f32_16x16x32_bf16 v[104:107], a[200:203], v[28:31], v[104:107]\n"
"v_exp_f32 v245, v245\n"
"v_mfma_f32_16x16x32_bf16 v[112:115], a[200:203], v[32:35], v[112:115]\n"
"v_exp_f32 v246, v246\n"
"v_mfma_f32_16x16x32_bf16 v[120:123], a[200:203], v[36:39], v[120:123]\n"
"v_exp_f32 v247, v247\n"
"v_mfma_f32_16x16x32_bf16 v[128:131], a[200:203], v[40:43], v[128:131]\n"
"s_add_u32 m0, s73, 65536\n"
"v_exp_f32 v248, v248\n"
"v_mfma_f32_16x16x32_bf16 v[136:139], a[200:203], v[44:47], v[136:139]\n"
"s_waitcnt lgkmcnt(0)\n"
"v_exp_f32 v249, v249\n"
"v_mfma_f32_16x16x32_bf16 v[100:103], a[204:207], v[24:27], v[100:103]\n"
"s_mov_b32 s55, 57344\n"
"v_or3_b32 v198, v198, v232, v233\n"
"v_cvt_pk_bf16_f32 v234, v236, v237\n"
"v_mfma_f32_16x16x32_bf16 v[108:111], a[204:207], v[28:31], v[108:111]\n"
"buffer_load_dwordx4 v195, s[24:27], s55 offen nt lds\n"
"v_mfma_f32_16x16x32_bf16 v[116:119], a[204:207], v[32:35], v[116:119]\n"
"v_cvt_pk_bf16_f32 v235, v238, v239\n"
"v_cvt_pk_bf16_f32 v240, v240, v241\n"
"v_mfma_f32_16x16x32_bf16 v[124:127], a[204:207], v[36:39], v[124:127]\n"
"v_exp_f32 v250, v250\n"
"v_mfma_f32_16x16x32_bf16 v[132:135], a[204:207], v[40:43], v[132:135]\n"
"v_exp_f32 v251, v251\n"
"v_mfma_f32_16x16x32_bf16 v[140:143], a[204:207], v[44:47], v[140:143]\n"
"v_or3_b32 v198, v198, v234, v235\n"
"v_cvt_pk_bf16_f32 v241, v242, v243\n"
"v_mfma_f32_16x16x32_bf16 v[96:99], a[208:211], v[48:51], v[96:99]\n"
"v_exp_f32 v252, v252\n"
"v_mfma_f32_16x16x32_bf16 v[104:107], a[208:211], v[52:55], v[104:107]\n"
"v_exp_f32 v253, v253\n"
"v_mfma_f32_16x16x32_bf16 v[112:115], a[208:211], v[56:59], v[112:115]\n"
"v_exp_f32 v254, v254\n"
"v_mfma_f32_16x16x32_bf16 v[120:123], a[208:211], v[60:63], v[120:123]\n"
"v_exp_f32 v255, v255\n"
"v_mfma_f32_16x16x32_bf16 v[128:131], a[208:211], v[64:67], v[128:131]\n"
"v_or3_b32 v198, v198, v240, v241\n"
"v_cvt_pk_bf16_f32 v242, v244, v245\n"
"v_mfma_f32_16x16x32_bf16 v[136:139], a[208:211], v[68:71], v[136:139]\n"
"v_cvt_pk_bf16_f32 v243, v246, v247\n"
"v_cvt_pk_bf16_f32 v248, v248, v249\n"
"v_mfma_f32_16x16x32_bf16 v[100:103], a[212:215], v[48:51], v[100:103]\n"
"s_add_u32 m0, s73, 65536\n"
"v_or3_b32 v198, v198, v242, v243\n"
"v_cvt_pk_bf16_f32 v249, v250, v251\n"
"v_mfma_f32_16x16x32_bf16 v[108:111], a[212:215], v[52:55], v[108:111]\n"
"s_mov_b32 s55, 57344\n"
"v_or3_b32 v198, v198, v248, v249\n"
"v_cvt_pk_bf16_f32 v250, v252, v253\n"
"v_mfma_f32_16x16x32_bf16 v[116:119], a[212:215], v[56:59], v[116:119]\n"
"v_cvt_pk_bf16_f32 v251, v254, v255\n"
"v_or3_b32 v198, v198, v250, v251\n"
"v_mfma_f32_16x16x32_bf16 v[124:127], a[212:215], v[60:63], v[124:127]\n"
"buffer_load_dwordx4 v196, s[24:27], s55 offen offset:2048 nt lds\n"
"v_mfma_f32_16x16x32_bf16 v[132:135], a[212:215], v[64:67], v[132:135]\n"
"v_and_b32 v199, 0x40004000, v198\n"
"v_cmp_ne_u32 vcc, 0, v199\n"
"v_mfma_f32_16x16x32_bf16 v[140:143], a[212:215], v[68:71], v[140:143]\n"
"ds_read_b64_tr_b16 a[228:229], v194\n"
"v_mfma_f32_16x16x32_bf16 v[96:99], a[216:219], v[72:75], v[96:99]\n"
"ds_read_b64_tr_b16 a[230:231], v194 offset:4096\n"
"v_mfma_f32_16x16x32_bf16 v[104:107], a[216:219], v[76:79], v[104:107]\n"
"ds_read_b64_tr_b16 a[232:233], v193 offset:256\n"
"v_mfma_f32_16x16x32_bf16 v[112:115], a[216:219], v[80:83], v[112:115]\n"
"ds_read_b64_tr_b16 a[234:235], v193 offset:4352\n"
"v_mfma_f32_16x16x32_bf16 v[120:123], a[216:219], v[84:87], v[120:123]\n"
"ds_read_b64_tr_b16 a[236:237], v194 offset:256\n"
"v_mfma_f32_16x16x32_bf16 v[128:131], a[216:219], v[88:91], v[128:131]\n"
"ds_read_b64_tr_b16 a[238:239], v194 offset:4352\n"
"v_mfma_f32_16x16x32_bf16 v[136:139], a[216:219], v[92:95], v[136:139]\n"
"ds_read_b64_tr_b16 a[240:241], v193 offset:512\n"
"v_mfma_f32_16x16x32_bf16 v[100:103], a[220:223], v[72:75], v[100:103]\n"
"ds_read_b64_tr_b16 a[242:243], v193 offset:4608\n"
"v_mfma_f32_16x16x32_bf16 v[108:111], a[220:223], v[76:79], v[108:111]\n"
"ds_read_b64_tr_b16 a[244:245], v194 offset:512\n"
"v_mfma_f32_16x16x32_bf16 v[116:119], a[220:223], v[80:83], v[116:119]\n"
"s_add_u32 m0, s73, 69632\n"
"ds_read_b64_tr_b16 a[246:247], v194 offset:4608\n"
"s_mov_b32 s55, 61440\n"
"v_mfma_f32_16x16x32_bf16 v[124:127], a[220:223], v[84:87], v[124:127]\n"
"ds_read_b64_tr_b16 a[248:249], v193 offset:768\n"
"v_mfma_f32_16x16x32_bf16 v[132:135], a[220:223], v[88:91], v[132:135]\n"
"ds_read_b64_tr_b16 a[250:251], v193 offset:4864\n"
"v_add_u32 v193, 32768, v193\n"
"v_mfma_f32_16x16x32_bf16 v[140:143], a[220:223], v[92:95], v[140:143]\n"
"s_cbranch_vccnz .Lrare4\n"
".Lrare4_ret:\n"
"ds_read_b64_tr_b16 a[252:253], v194 offset:768\n"
"v_mfma_f32_16x16x32_bf16 v[168:171], v[204:207], v[208:211], v[168:171]\n"
"buffer_load_dwordx4 v195, s[24:27], s55 offen nt lds\n"
"v_mfma_f32_16x16x32_bf16 v[172:175], v[204:207], v[216:219], v[172:175]\n"
"ds_read_b64_tr_b16 a[254:255], v194 offset:4864\n"
"v_add_u32 v194, 32768, v194\n"
"v_mfma_f32_16x16x32_bf16 v[176:179], v[204:207], v[224:227], v[176:179]\n"
"v_exp_f32 v96, v96\n"
"v_mfma_f32_16x16x32_bf16 v[180:183], v[204:207], v[232:235], v[180:183]\n"
"v_exp_f32 v97, v97\n"
"v_mfma_f32_16x16x32_bf16 v[184:187], v[204:207], v[240:243], v[184:187]\n"
"v_exp_f32 v98, v98\n"
"v_mfma_f32_16x16x32_bf16 v[188:191], v[204:207], v[248:251], v[188:191]\n"
"v_exp_f32 v99, v99\n"
"v_mfma_f32_16x16x32_bf16 a[0:3], a[224:227], v[208:211], a[0:3]\n"
"v_exp_f32 v100, v100\n"
"v_mfma_f32_16x16x32_bf16 a[4:7], a[224:227], v[216:219], a[4:7]\n"
"v_exp_f32 v101, v101\n"
"v_mfma_f32_16x16x32_bf16 a[8:11], a[224:227], v[224:227], a[8:11]\n"
"v_exp_f32 v102, v102\n"
"v_mfma_f32_16x16x32_bf16 a[12:15], a[224:227], v[232:235], a[12:15]\n"
"v_exp_f32 v103, v103\n"
"v_mfma_f32_16x16x32_bf16 a[16:19], a[224:227], v[240:243], a[16:19]\n"
"s_add_u32 m0, s73, 69632\n"
"v_cvt_pk_bf16_f32 v96, v96, v97\n"
"v_mfma_f32_16x16x32_bf16 a[20:23], a[224:227], v[248:251], a[20:23]\n"
"s_waitcnt lgkmcnt(0)\n"
"v_exp_f32 v104, v104\n"
"v_mfma_f32_16x16x32_bf16 a[24:27], a[228:231], v[208:211], a[24:27]\n"
"s_mov_b32 s55, 61440\n"
"v_exp_f32 v105, v105\n"
"v_mfma_f32_16x16x32_bf16 a[28:31], a[228:231], v[216:219], a[28:31]\n"
"buffer_load_dwordx4 v196, s[24:27], s55 offen offset:2048 nt lds\n"
"v_mfma_f32_16x16x32_bf16 a[32:35], a[228:231], v[224:227], a[32:35]\n"
"s_add_u32 s64, s64, 8192\n"
"v_exp_f32 v106, v106\n"
"v_mfma_f32_16x16x32_bf16 a[36:39], a[228:231], v[232:235], a[36:39]\n"
"s_add_u32 s72, s72, 112\n"
"v_exp_f32 v107, v107\n"
"v_mfma_f32_16x16x32_bf16 a[40:43], a[228:231], v[240:243], a[40:43]\n"
"v_cvt_pk_bf16_f32 v97, v98, v99\n"
"v_or_b32 v198, v96, v97\n"
"v_mfma_f32_16x16x32_bf16 a[44:47], a[228:231], v[248:251], a[44:47]\n"
"v_exp_f32 v108, v108\n"
"v_mfma_f32_16x16x32_bf16 a[48:51], a[232:235], v[208:211], a[48:51]\n"
"v_exp_f32 v109, v109\n"
"v_mfma_f32_16x16x32_bf16 a[52:55], a[232:235], v[216:219], a[52:55]\n"
"v_exp_f32 v110, v110\n"
"v_mfma_f32_16x16x32_bf16 a[56:59], a[232:235], v[224:227], a[56:59]\n"
"v_exp_f32 v111, v111\n"
"v_mfma_f32_16x16x32_bf16 a[60:63], a[232:235], v[232:235], a[60:63]\n"
"v_exp_f32 v112, v112\n"
"v_mfma_f32_16x16x32_bf16 a[64:67], a[232:235], v[240:243], a[64:67]\n"
"v_exp_f32 v113, v113\n"
"v_mfma_f32_16x16x32_bf16 a[68:71], a[232:235], v[248:251], a[68:71]\n"
"v_cvt_pk_bf16_f32 v98, v100, v101\n"
"v_cvt_pk_bf16_f32 v99, v102, v103\n"
"v_mfma_f32_16x16x32_bf16 a[72:75], a[236:239], v[208:211], a[72:75]\n"
"v_cvt_pk_bf16_f32 v104, v104, v105\n"
"v_or3_b32 v198, v198, v98, v99\n"
"v_mfma_f32_16x16x32_bf16 a[76:79], a[236:239], v[216:219], a[76:79]\n"
"v_exp_f32 v114, v114\n"
"v_mfma_f32_16x16x32_bf16 a[80:83], a[236:239], v[224:227], a[80:83]\n"
"v_exp_f32 v115, v115\n"
"v_mfma_f32_16x16x32_bf16 a[84:87], a[236:239], v[232:235], a[84:87]\n"
"v_cvt_pk_bf16_f32 v105, v106, v107\n"
"v_or3_b32 v198, v198, v104, v105\n"
"v_mfma_f32_16x16x32_bf16 a[88:91], a[236:239], v[240:243], a[88:91]\n"
"v_exp_f32 v116, v116\n"
"v_mfma_f32_16x16x32_bf16 a[92:95], a[236:239], v[248:251], a[92:95]\n"
"v_exp_f32 v117, v117\n"
"v_mfma_f32_16x16x32_bf16 a[96:99], a[240:243], v[208:211], a[96:99]\n"
"s_memrealtime s[70:71]\n"
"v_cvt_pk_bf16_f32 v106, v108, v109\n"
"v_cvt_pk_bf16_f32 v107, v110, v111\n"
"v_mfma_f32_16x16x32_bf16 a[100:103], a[240:243], v[216:219], a[100:103]\n"
"v_cvt_pk_bf16_f32 v112, v112, v113\n"
"v_or3_b32 v198, v198, v106, v107\n"
"v_mfma_f32_16x16x32_bf16 a[104:107], a[240:243], v[224:227], a[104:107]\n"
"v_cvt_pk_bf16_f32 v113, v114, v115\n"
"v_or3_b32 v198, v198, v112, v113\n"
"v_mfma_f32_16x16x32_bf16 a[108:111], a[240:243], v[232:235], a[108:111]\n"
"v_exp_f32 v118, v118\n"
"v_mfma_f32_16x16x32_bf16 a[112:115], a[240:243], v[240:243], a[112:115]\n"
"v_exp_f32 v119, v119\n"
"v_mfma_f32_16x16x32_bf16 a[116:119], a[240:243], v[248:251], a[116:119]\n"
"v_cvt_pk_bf16_f32 v114, v116, v117\n"
"v_mfma_f32_16x16x32_bf16 a[120:123], a[244:247], v[208:211], a[120:123]\n"
"v_mfma_f32_16x16x32_bf16 a[124:127], a[244:247], v[216:219], a[124:127]\n"
"v_mfma_f32_16x16x32_bf16 a[128:131], a[244:247], v[224:227], a[128:131]\n"
"v_mfma_f32_16x16x32_bf16 a[132:135], a[244:247], v[232:235], a[132:135]\n"
"v_mfma_f32_16x16x32_bf16 a[136:139], a[244:247], v[240:243], a[136:139]\n"
"v_mfma_f32_16x16x32_bf16 a[140:143], a[244:247], v[248:251], a[140:143]\n"
"s_waitcnt vmcnt(20) lgkmcnt(0)\n"
"s_sub_u32 s71, s72, s70\n"
"s_sub_u32 s71, s71, 16\n"
"s_cmp_lt_u32 s71, 400\n"
"s_cbranch_scc1 .Lpg2\n"
".Lpg2x:\n"
"s_barrier\n"
"v_mfma_f32_16x16x32_bf16 a[144:147], a[248:251], v[208:211], a[144:147]\n"
"s_add_u32 m0, s73, 49152\n"
"ds_read_b128 a[192:195], v192\n"
"v_cvt_pk_bf16_f32 v115, v118, v119\n"
"v_mfma_f32_16x16x32_bf16 a[148:151], a[248:251], v[216:219], a[148:151]\n"
"s_mov_b32 s55, 49152\n"
"ds_read_b128 a[196:199], v192 offset:4096\n"
"v_or3_b32 v198, v198, v114, v115\n"
"v_mfma_f32_16x16x32_bf16 a[152:155], a[248:251], v[224:227], a[152:155]\n"
"ds_read_b128 a[200:203], v192 offset:256\n"
"v_mfma_f32_16x16x32_bf16 a[156:159], a[248:251], v[232:235], a[156:159]\n"
"buffer_load_dwordx4 v197, s[28:31], s55 offen nt lds\n"
"v_mfma_f32_16x16x32_bf16 a[160:163], a[248:251], v[240:243], a[160:163]\n"
"ds_read_b128 a[204:207], v192 offset:4352\n"
"v_mfma_f32_16x16x32_bf16 a[164:167], a[248:251], v[248:251], a[164:167]\n"
"ds_read_b128 a[208:211], v192 offset:512\n"
"v_mfma_f32_16x16x32_bf16 a[168:171], a[252:255], v[208:211], a[168:171]\n"
"ds_read_b128 a[212:215], v192 offset:4608\n"
"v_mfma_f32_16x16x32_bf16 a[172:175], a[252:255], v[216:219], a[172:175]\n"
"ds_read_b128 a[216:219], v192 offset:768\n"
"v_mfma_f32_16x16x32_bf16 a[176:179], a[252:255], v[224:227], a[176:179]\n"
"ds_read_b128 a[220:223], v192 offset:4864\n"
"v_add_u32 v192, 0xfffe0000, v192\n"
"v_mfma_f32_16x16x32_bf16 a[180:183], a[252:255], v[232:235], a[180:183]\n"
"v_exp_f32 v120, v120\n"
"v_mfma_f32_16x16x32_bf16 a[184:187], a[252:255], v[240:243], a[184:187]\n"
"v_exp_f32 v121, v121\n"
"v_mfma_f32_16x16x32_bf16 a[188:191], a[252:255], v[248:251], a[188:191]\n"
"s_waitcnt lgkmcnt(5)\n"
"v_exp_f32 v122, v122\n"
"v_mfma_f32_16x16x32_bf16 v[208:211], a[192:195], v[0:3], v[144:147]\n"
"s_add_u32 m0, s73, 49152\n"
"v_exp_f32 v123, v123\n"
"v_mfma_f32_16x16x32_bf16 v[216:219], a[192:195], v[4:7], v[148:151]\n"
"s_mov_b32 s55, 49152\n"
"v_exp_f32 v124, v124\n"
"v_mfma_f32_16x16x32_bf16 v[224:227], a[192:195], v[8:11], v[152:155]\n"
"ds_read_b64_tr_b16 a[224:225], v193\n"
"v_cvt_pk_bf16_f32 v120, v120, v121\n"
"v_mfma_f32_16x16x32_bf16 v[232:235], a[192:195], v[12:15], v[156:159]\n"
"buffer_load_dwordx4 v197, s[28:31], s55 offen offset:2048 nt lds\n"
"v_mfma_f32_16x16x32_bf16 v[240:243], a[192:195], v[16:19], v[160:163]\n"
"v_exp_f32 v125, v125\n"
"v_mfma_f32_16x16x32_bf16 v[248:251], a[192:195], v[20:23], v[164:167]\n"
"ds_read_b64_tr_b16 a[226:227], v193 offset:4096\n"
"v_cvt_pk_bf16_f32 v121, v122, v123\n"
"v_mfma_f32_16x16x32_bf16 v[212:215], a[196:199], v[0:3], v[144:147]\n"
"v_exp_f32 v126, v126\n"
"v_mfma_f32_16x16x32_bf16 v[220:223], a[196:199], v[4:7], v[148:151]\n"
"v_exp_f32 v127, v127\n"
"v_mfma_f32_16x16x32_bf16 v[228:231], a[196:199], v[8:11], v[152:155]\n"
"v_exp_f32 v128, v128\n"
"v_mfma_f32_16x16x32_bf16 v[236:239], a[196:199], v[12:15], v[156:159]\n"
"v_exp_f32 v129, v129\n"
"v_mfma_f32_16x16x32_bf16 v[244:247], a[196:199], v[16:19], v[160:163]\n"
"v_exp_f32 v130, v130\n"
"v_mfma_f32_16x16x32_bf16 v[252:255], a[196:199], v[20:23], v[164:167]\n"
"v_exp_f32 v131, v131\n"
"v_mfma_f32_16x16x32_bf16 v[208:211], a[200:203], v[24:27], v[208:211]\n"
"s_add_u32 m0, s73, 53248\n"
"v_exp_f32 v132, v132\n"
"v_mfma_f32_16x16x32_bf16 v[216:219], a[200:203], v[28:31], v[216:219]\n"
"s_mov_b32 s55, 53248\n"
"v_exp_f32 v133, v133\n"
"v_mfma_f32_16x16x32_bf16 v[224:227], a[200:203], v[32:35], v[224:227]\n"
"v_exp_f32 v134, v134\n"
"v_mfma_f32_16x16x32_bf16 v[232:235], a[200:203], v[36:39], v[232:235]\n"
"buffer_load_dwordx4 v197, s[28:31], s55 offen nt lds\n"
"v_mfma_f32_16x16x32_bf16 v[240:243], a[200:203], v[40:43], v[240:243]\n"
"v_exp_f32 v135, v135\n"
"v_mfma_f32_16x16x32_bf16 v[248:251], a[200:203], v[44:47], v[248:251]\n"
"s_waitcnt lgkmcnt(0)\n"
"v_exp_f32 v136, v136\n"
"v_mfma_f32_16x16x32_bf16 v[212:215], a[204:207], v[24:27], v[212:215]\n"
"v_exp_f32 v137, v137\n"
"v_mfma_f32_16x16x32_bf16 v[220:223], a[204:207], v[28:31], v[220:223]\n"
"v_or3_b32 v198, v198, v120, v121\n"
"v_cvt_pk_bf16_f32 v122, v124, v125\n"
"v_mfma_f32_16x16x32_bf16 v[228:231], a[204:207], v[32:35], v[228:231]\n"
"v_cvt_pk_bf16_f32 v123, v126, v127\n"
"v_cvt_pk_bf16_f32 v128, v128, v129\n"
"v_mfma_f32_16x16x32_bf16 v[236:239], a[204:207], v[36:39], v[236:239]\n"
"v_exp_f32 v138, v138\n"
"v_mfma_f32_16x16x32_bf16 v[244:247], a[204:207], v[40:43], v[244:247]\n"
"v_exp_f32 v139, v139\n"
"v_mfma_f32_16x16x32_bf16 v[252:255], a[204:207], v[44:47], v[252:255]\n"
"v_or3_b32 v198, v198, v122, v123\n"
"v_cvt_pk_bf16_f32 v129, v130, v131\n"
"v_mfma_f32_16x16x32_bf16 v[208:211], a[208:211], v[48:51], v[208:211]\n"
"s_add_u32 m0, s73, 53248\n"
"v_exp_f32 v140, v140\n"
"v_mfma_f32_16x16x32_bf16 v[216:219], a[208:211], v[52:55], v[216:219]\n"
"s_mov_b32 s55, 53248\n"
"v_exp_f32 v141, v141\n"
"v_mfma_f32_16x16x32_bf16 v[224:227], a[208:211], v[56:59], v[224:227]\n"
"v_exp_f32 v142, v142\n"
"v_mfma_f32_16x16x32_bf16 v[232:235], a[208:211], v[60:63], v[232:235]\n"
"buffer_load_dwordx4 v197, s[28:31], s55 offen offset:2048 nt lds\n"
"v_mfma_f32_16x16x32_bf16 v[240:243], a[208:211], v[64:67], v[240:243]\n"
"v_exp_f32 v143, v143\n"
"v_mfma_f32_16x16x32_bf16 v[248:251], a[208:211], v[68:71], v[248:251]\n"
"v_or3_b32 v198, v198, v128, v129\n"
"v_cvt_pk_bf16_f32 v130, v132, v133\n"
"v_mfma_f32_16x16x32_bf16 v[212:215], a[212:215], v[48:51], v[212:215]\n"
"v_cvt_pk_bf16_f32 v131, v134, v135\n"
"v_cvt_pk_bf16_f32 v136, v136, v137\n"
"v_mfma_f32_16x16x32_bf16 v[220:223], a[212:215], v[52:55], v[220:223]\n"
"v_or3_b32 v198, v198, v130, v131\n"
"v_cvt_pk_bf16_f32 v137, v138, v139\n"
"v_mfma_f32_16x16x32_bf16 v[228:231], a[212:215], v[56:59], v[228:231]\n"
"v_or3_b32 v198, v198, v136, v137\n"
"v_cvt_pk_bf16_f32 v138, v140, v141\n"
"v_mfma_f32_16x16x32_bf16 v[236:239], a[212:215], v[60:63], v[236:239]\n"
"v_cvt_pk_bf16_f32 v139, v142, v143\n"
"v_or3_b32 v198, v198, v138, v139\n"
"v_mfma_f32_16x16x32_bf16 v[244:247], a[212:215], v[64:67], v[244:247]\n"
"v_and_b32 v199, 0x40004000, v198\n"
"v_cmp_ne_u32 vcc, 0, v199\n"
"v_mfma_f32_16x16x32_bf16 v[252:255], a[212:215], v[68:71], v[252:255]\n"
"ds_read_b64_tr_b16 a[228:229], v194\n"
"v_mfma_f32_16x16x32_bf16 v[208:211], a[216:219], v[72:75], v[208:211]\n"
"s_add_u32 m0, s73, 98304\n"
"ds_read_b64_tr_b16 a[230:231], v194 offset:4096\n"
"s_mov_b32 s55, 65536\n"
"v_mfma_f32_16x16x32_bf16 v[216:219], a[216:219], v[76:79], v[216:219]\n"
"ds_read_b64_tr_b16 a[232:233], v193 offset:256\n"
"v_mfma_f32_16x16x32_bf16 v[224:227], a[216:219], v[80:83], v[224:227]\n"
"ds_read_b64_tr_b16 a[234:235], v193 offset:4352\n"
"v_mfma_f32_16x16x32_bf16 v[232:235], a[216:219], v[84:87], v[232:235]\n"
"buffer_load_dwordx4 v195, s[24:27], s55 offen nt lds\n"
"v_mfma_f32_16x16x32_bf16 v[240:243], a[216:219], v[88:91], v[240:243]\n"
"ds_read_b64_tr_b16 a[236:237], v194 offset:256\n"
"v_mfma_f32_16x16x32_bf16 v[248:251], a[216:219], v[92:95], v[248:251]\n"
"ds_read_b64_tr_b16 a[238:239], v194 offset:4352\n"
"v_mfma_f32_16x16x32_bf16 v[212:215], a[220:223], v[72:75], v[212:215]\n"
"ds_read_b64_tr_b16 a[240:241], v193 offset:512\n"
"v_mfma_f32_16x16x32_bf16 v[220:223], a[220:223], v[76:79], v[220:223]\n"
"ds_read_b64_tr_b16 a[242:243], v193 offset:4608\n"
"v_mfma_f32_16x16x32_bf16 v[228:231], a[220:223], v[80:83], v[228:231]\n"
"ds_read_b64_tr_b16 a[244:245], v194 offset:512\n"
"v_mfma_f32_16x16x32_bf16 v[236:239], a[220:223], v[84:87], v[236:239]\n"
"ds_read_b64_tr_b16 a[246:247], v194 offset:4608\n"
"v_mfma_f32_16x16x32_bf16 v[244:247], a[220:223], v[88:91], v[244:247]\n"
"ds_read_b64_tr_b16 a[248:249], v193 offset:768\n"
"v_mfma_f32_16x16x32_bf16 v[252:255], a[220:223], v[92:95], v[252:255]\n"
"s_cbranch_vccnz .Lrare5\n"
".Lrare5_ret:\n"
"ds_read_b64_tr_b16 a[250:251], v193 offset:4864\n"
"v_add_u32 v193, 32768, v193\n"
"v_mfma_f32_16x16x32_bf16 v[168:171], v[204:207], v[96:99], v[168:171]\n"
"s_add_u32 m0, s73, 98304\n"
"ds_read_b64_tr_b16 a[252:253], v194 offset:768\n"
"s_mov_b32 s55, 65536\n"
"v_mfma_f32_16x16x32_bf16 v[172:175], v[204:207], v[104:107], v[172:175]\n"
"ds_read_b64_tr_b16 a[254:255], v194 offset:4864\n"
"v_add_u32 v194, 32768, v194\n"
"v_mfma_f32_16x16x32_bf16 v[176:179], v[204:207], v[112:115], v[176:179]\n"
"v_exp_f32 v208, v208\n"
"v_mfma_f32_16x16x32_bf16 v[180:183], v[204:207], v[120:123], v[180:183]\n"
"buffer_load_dwordx4 v196, s[24:27], s55 offen offset:2048 nt lds\n"
"v_mfma_f32_16x16x32_bf16 v[184:187], v[204:207], v[128:131], v[184:187]\n"
"v_exp_f32 v209, v209\n"
"v_mfma_f32_16x16x32_bf16 v[188:191], v[204:207], v[136:139], v[188:191]\n"
"v_exp_f32 v210, v210\n"
"v_mfma_f32_16x16x32_bf16 a[0:3], a[224:227], v[96:99], a[0:3]\n"
"v_exp_f32 v211, v211\n"
"v_mfma_f32_16x16x32_bf16 a[4:7], a[224:227], v[104:107], a[4:7]\n"
"v_exp_f32 v212, v212\n"
"v_mfma_f32_16x16x32_bf16 a[8:11], a[224:227], v[112:115], a[8:11]\n"
"v_exp_f32 v213, v213\n"
"v_mfma_f32_16x16x32_bf16 a[12:15], a[224:227], v[120:123], a[12:15]\n"
"v_exp_f32 v214, v214\n"
"v_mfma_f32_16x16x32_bf16 a[16:19], a[224:227], v[128:131], a[16:19]\n"
"v_exp_f32 v215, v215\n"
"v_mfma_f32_16x16x32_bf16 a[20:23], a[224:227], v[136:139], a[20:23]\n"
"s_waitcnt lgkmcnt(0)\n"
"v_exp_f32 v216, v216\n"
"v_mfma_f32_16x16x32_bf16 a[24:27], a[228:231], v[96:99], a[24:27]\n"
"s_add_u32 m0, s73, 102400\n"
"v_exp_f32 v217, v217\n"
"v_mfma_f32_16x16x32_bf16 a[28:31], a[228:231], v[104:107], a[28:31]\n"
"s_mov_b32 s55, 69632\n"
"v_cvt_pk_bf16_f32 v208, v208, v209\n"
"v_cvt_pk_bf16_f32 v209, v210, v211\n"
"v_mfma_f32_16x16x32_bf16 a[32:35], a[228:231], v[112:115], a[32:35]\n"
"s_add_u32 s64, s64, 8192\n"
"v_exp_f32 v218, v218\n"
"v_mfma_f32_16x16x32_bf16 a[36:39], a[228:231], v[120:123], a[36:39]\n"
"buffer_load_dwordx4 v195, s[24:27], s55 offen nt lds\n"
"v_mfma_f32_16x16x32_bf16 a[40:43], a[228:231], v[128:131], a[40:43]\n"
"s_add_u32 s72, s72, 112\n"
"v_exp_f32 v219, v219\n"
"v_mfma_f32_16x16x32_bf16 a[44:47], a[228:231], v[136:139], a[44:47]\n"
"v_exp_f32 v220, v220\n"
"v_mfma_f32_16x16x32_bf16 a[48:51], a[232:235], v[96:99], a[48:51]\n"
"v_exp_f32 v221, v221\n"
"v_mfma_f32_16x16x32_bf16 a[52:55], a[232:235], v[104:107], a[52:55]\n"
"v_exp_f32 v222, v222\n"
"v_mfma_f32_16x16x32_bf16 a[56:59], a[232:235], v[112:115], a[56:59]\n"
"v_exp_f32 v223, v223\n"
"v_mfma_f32_16x16x32_bf16 a[60:63], a[232:235], v[120:123], a[60:63]\n"
"v_exp_f32 v224, v224\n"
"v_mfma_f32_16x16x32_bf16 a[64:67], a[232:235], v[128:131], a[64:67]\n"
"v_exp_f32 v225, v225\n"
"v_mfma_f32_16x16x32_bf16 a[68:71], a[232:235], v[136:139], a[68:71]\n"
"v_or_b32 v198, v208, v209\n"
"v_cvt_pk_bf16_f32 v210, v212, v213\n"
"v_mfma_f32_16x16x32_bf16 a[72:75], a[236:239], v[96:99], a[72:75]\n"
"v_cvt_pk_bf16_f32 v211, v214, v215\n"
"v_cvt_pk_bf16_f32 v216, v216, v217\n"
"v_mfma_f32_16x16x32_bf16 a[76:79], a[236:239], v[104:107], a[76:79]\n"
"v_exp_f32 v226, v226\n"
"v_mfma_f32_16x16x32_bf16 a[80:83], a[236:239], v[112:115], a[80:83]\n"
"v_exp_f32 v227, v227\n"
"v_mfma_f32_16x16x32_bf16 a[84:87], a[236:239], v[120:123], a[84:87]\n"
"v_or3_b32 v198, v198, v210, v211\n"
"v_cvt_pk_bf16_f32 v217, v218, v219\n"
"v_mfma_f32_16x16x32_bf16 a[88:91], a[236:239], v[128:131], a[88:91]\n"
"v_exp_f32 v228, v228\n"
"v_mfma_f32_16x16x32_bf16 a[92:95], a[236:239], v[136:139], a[92:95]\n"
"v_exp_f32 v229, v229\n"
"v_mfma_f32_16x16x32_bf16 a[96:99], a[240:243], v[96:99], a[96:99]\n"
"s_memrealtime s[70:71]\n"
"v_or3_b32 v198, v198, v216, v217\n"
"v_cvt_pk_bf16_f32 v218, v220, v221\n"
"v_mfma_f32_16x16x32_bf16 a[100:103], a[240:243], v[104:107], a[100:103]\n"
"v_cvt_pk_bf16_f32 v219, v222, v223\n"
"v_cvt_pk_bf16_f32 v224, v224, v225\n"
"v_mfma_f32_16x16x32_bf16 a[104:107], a[240:243], v[112:115], a[104:107]\n"
"v_or3_b32 v198, v198, v218, v219\n"
"v_cvt_pk_bf16_f32 v225, v226, v227\n"
"v_mfma_f32_16x16x32_bf16 a[108:111], a[240:243], v[120:123], a[108:111]\n"
"v_or3_b32 v198, v198, v224, v225\n"
"v_cvt_pk_bf16_f32 v226, v228, v229\n"
"v_mfma_f32_16x16x32_bf16 a[112:115], a[240:243], v[128:131], a[112:115]\n"
"v_exp_f32 v230, v230\n"
"v_mfma_f32_16x16x32_bf16 a[116:119], a[240:243], v[136:139], a[116:119]\n"
"v_exp_f32 v231, v231\n"
"v_mfma_f32_16x16x32_bf16 a[120:123], a[244:247], v[96:99], a[120:123]\n"
"v_mfma_f32_16x16x32_bf16 a[124:127], a[244:247], v[104:107], a[124:127]\n"
"v_mfma_f32_16x16x32_bf16 a[128:131], a[244:247], v[112:115], a[128:131]\n"
"v_mfma_f32_16x16x32_bf16 a[132:135], a[244:247], v[120:123], a[132:135]\n"
"v_mfma_f32_16x16x32_bf16 a[136:139], a[244:247], v[128:131], a[136:139]\n"
"v_mfma_f32_16x16x32_bf16 a[140:143], a[244:247], v[136:139], a[140:143]\n"
"s_waitcnt vmcnt(19) lgkmcnt(0)\n"
"s_sub_u32 s71, s72, s70\n"
"s_sub_u32 s71, s71, 16\n"
"s_cmp_lt_u32 s71, 400\n"
"s_cbranch_scc1 .Lpg3\n"
".Lpg3x:\n"
"s_barrier\n"
"v_mfma_f32_16x16x32_bf16 a[144:147], a[248:251], v[96:99], a[144:147]\n"
"s_add_u32 m0, s73, 102400\n"
"ds_read_b128 a[192:195], v192\n"
"v_cvt_pk_bf16_f32 v227, v230, v231\n"
"v_mfma_f32_16x16x32_bf16 a[148:151], a[248:251], v[104:107], a[148:151]\n"
"s_mov_b32 s55, 69632\n"
"ds_read_b128 a[196:199], v192 offset:4096\n"
"v_or3_b32 v198, v198, v226, v227\n"
"v_mfma_f32_16x16x32_bf16 a[152:155], a[248:251], v[112:115], a[152:155]\n"
"ds_read_b128 a[200:203], v192 offset:256\n"
"v_mfma_f32_16x16x32_bf16 a[156:159], a[248:251], v[120:123], a[156:159]\n"
"buffer_load_dwordx4 v196, s[24:27], s55 offen offset:2048 nt lds\n"
"v_mfma_f32_16x16x32_bf16 a[160:163], a[248:251], v[128:131], a[160:163]\n"
"ds_read_b128 a[204:207], v192 offset:4352\n"
"v_mfma_f32_16x16x32_bf16 a[164:167], a[248:251], v[136:139], a[164:167]\n"
"ds_read_b128 a[208:211], v192 offset:512\n"
"v_mfma_f32_16x16x32_bf16 a[168:171], a[252:255], v[96:99], a[168:171]\n"
"ds_read_b128 a[212:215], v192 offset:4608\n"
"v_mfma_f32_16x16x32_bf16 a[172:175], a[252:255], v[104:107], a[172:175]\n"
"ds_read_b128 a[216:219], v192 offset:768\n"
"v_mfma_f32_16x16x32_bf16 a[176:179], a[252:255], v[112:115], a[176:179]\n"
"ds_read_b128 a[220:223], v192 offset:4864\n"
"v_add_u32 v192, 32768, v192\n"
"v_mfma_f32_16x16x32_bf16 a[180:183], a[252:255], v[120:123], a[180:183]\n"
"v_exp_f32 v232, v232\n"
"v_mfma_f32_16x16x32_bf16 a[184:187], a[252:255], v[128:131], a[184:187]\n"
"v_exp_f32 v233, v233\n"
"v_mfma_f32_16x16x32_bf16 a[188:191], a[252:255], v[136:139], a[188:191]\n"
"s_waitcnt lgkmcnt(5)\n"
"v_exp_f32 v234, v234\n"
"v_mfma_f32_16x16x32_bf16 v[96:99], a[192:195], v[0:3], v[144:147]\n"
"s_add_u32 m0, s73, 81920\n"
"v_exp_f32 v235, v235\n"
"v_mfma_f32_16x16x32_bf16 v[104:107], a[192:195], v[4:7], v[148:151]\n"
"s_mov_b32 s55, 57344\n"
"v_exp_f32 v236, v236\n"
"v_mfma_f32_16x16x32_bf16 v[112:115], a[192:195], v[8:11], v[152:155]\n"
"ds_read_b64_tr_b16 a[224:225], v193\n"
"v_cvt_pk_bf16_f32 v232, v232, v233\n"
"v_mfma_f32_16x16x32_bf16 v[120:123], a[192:195], v[12:15], v[156:159]\n"
"buffer_load_dwordx4 v197, s[28:31], s55 offen nt lds\n"
"v_mfma_f32_16x16x32_bf16 v[128:131], a[192:195], v[16:19], v[160:163]\n"
"v_exp_f32 v237, v237\n"
"v_mfma_f32_16x16x32_bf16 v[136:139], a[192:195], v[20:23], v[164:167]\n"
"ds_read_b64_tr_b16 a[226:227], v193 offset:4096\n"
"v_cvt_pk_bf16_f32 v233, v234, v235\n"
"v_mfma_f32_16x16x32_bf16 v[100:103], a[196:199], v[0:3], v[144:147]\n"
"v_exp_f32 v238, v238\n"
"v_mfma_f32_16x16x32_bf16 v[108:111], a[196:199], v[4:7], v[148:151]\n"
"v_exp_f32 v239, v239\n"
"v_mfma_f32_16x16x32_bf16 v[116:119], a[196:199], v[8:11], v[152:155]\n"
"v_exp_f32 v240, v240\n"
"v_mfma_f32_16x16x32_bf16 v[124:127], a[196:199], v[12:15], v[156:159]\n"
"v_exp_f32 v241, v241\n"
"v_mfma_f32_16x16x32_bf16 v[132:135], a[196:199], v[16:19], v[160:163]\n"
"v_exp_f32 v242, v242\n"
"v_mfma_f32_16x16x32_bf16 v[140:143], a[196:199], v[20:23], v[164:167]\n"
"v_exp_f32 v243, v243\n"
"v_mfma_f32_16x16x32_bf16 v[96:99], a[200:203], v[24:27], v[96:99]\n"
"s_add_u32 m0, s73, 81920\n"
"v_exp_f32 v244, v244\n"
"v_mfma_f32_16x16x32_bf16 v[104:107], a[200:203], v[28:31], v[104:107]\n"
"s_mov_b32 s55, 57344\n"
"v_exp_f32 v245, v245\n"
"v_mfma_f32_16x16x32_bf16 v[112:115], a[200:203], v[32:35], v[112:115]\n"
"v_exp_f32 v246, v246\n"
"v_mfma_f32_16x16x32_bf16 v[120:123], a[200:203], v[36:39], v[120:123]\n"
"buffer_load_dwordx4 v197, s[28:31], s55 offen offset:2048 nt lds\n"
"v_mfma_f32_16x16x32_bf16 v[128:131], a[200:203], v[40:43], v[128:131]\n"
"v_exp_f32 v247, v247\n"
"v_mfma_f32_16x16x32_bf16 v[136:139], a[200:203], v[44:47], v[136:139]\n"
"s_waitcnt lgkmcnt(0)\n"
"v_exp_f32 v248, v248\n"
"v_mfma_f32_16x16x32_bf16 v[100:103], a[204:207], v[24:27], v[100:103]\n"
"v_exp_f32 v249, v249\n"
"v_mfma_f32_16x16x32_bf16 v[108:111], a[204:207], v[28:31], v[108:111]\n"
"v_or3_b32 v198, v198, v232, v233\n"
"v_cvt_pk_bf16_f32 v234, v236, v237\n"
"v_mfma_f32_16x16x32_bf16 v[116:119], a[204:207], v[32:35], v[116:119]\n"
"v_cvt_pk_bf16_f32 v235, v238, v239\n"
"v_cvt_pk_bf16_f32 v240, v240, v241\n"
"v_mfma_f32_16x16x32_bf16 v[124:127], a[204:207], v[36:39], v[124:127]\n"
"v_exp_f32 v250, v250\n"
"v_mfma_f32_16x16x32_bf16 v[132:135], a[204:207], v[40:43], v[132:135]\n"
"v_exp_f32 v251, v251\n"
"v_mfma_f32_16x16x32_bf16 v[140:143], a[204:207], v[44:47], v[140:143]\n"
"v_or3_b32 v198, v198, v234, v235\n"
"v_cvt_pk_bf16_f32 v241, v242, v243\n"
"v_mfma_f32_16x16x32_bf16 v[96:99], a[208:211], v[48:51], v[96:99]\n"
"s_add_u32 m0, s73, 86016\n"
"v_exp_f32 v252, v252\n"
"v_mfma_f32_16x16x32_bf16 v[104:107], a[208:211], v[52:55], v[104:107]\n"
"s_mov_b32 s55, 61440\n"
"v_exp_f32 v253, v253\n"
"v_mfma_f32_16x16x32_bf16 v[112:115], a[208:211], v[56:59], v[112:115]\n"
"v_exp_f32 v254, v254\n"
"v_mfma_f32_16x16x32_bf16 v[120:123], a[208:211], v[60:63], v[120:123]\n"
"buffer_load_dwordx4 v197, s[28:31], s55 offen nt lds\n"
"v_mfma_f32_16x16x32_bf16 v[128:131], a[208:211], v[64:67], v[128:131]\n"
"v_exp_f32 v255, v255\n"
"v_mfma_f32_16x16x32_bf16 v[136:139], a[208:211], v[68:71], v[136:139]\n"
"v_or3_b32 v198, v198, v240, v241\n"
"v_cvt_pk_bf16_f32 v242, v244, v245\n"
"v_mfma_f32_16x16x32_bf16 v[100:103], a[212:215], v[48:51], v[100:103]\n"
"v_cvt_pk_bf16_f32 v243, v246, v247\n"
"v_cvt_pk_bf16_f32 v248, v248, v249\n"
"v_mfma_f32_16x16x32_bf16 v[108:111], a[212:215], v[52:55], v[108:111]\n"
"v_or3_b32 v198, v198, v242, v243\n"
"v_cvt_pk_bf16_f32 v249, v250, v251\n"
"v_mfma_f32_16x16x32_bf16 v[116:119], a[212:215], v[56:59], v[116:119]\n"
"v_or3_b32 v198, v198, v248, v249\n"
"v_cvt_pk_bf16_f32 v250, v252, v253\n"
"v_mfma_f32_16x16x32_bf16 v[124:127], a[212:215], v[60:63], v[124:127]\n"
"v_cvt_pk_bf16_f32 v251, v254, v255\n"
"v_or3_b32 v198, v198, v250, v251\n"
"v_mfma_f32_16x16x32_bf16 v[132:135], a[212:215], v[64:67], v[132:135]\n"
"v_and_b32 v199, 0x40004000, v198\n"
"v_cmp_ne_u32 vcc, 0, v199\n"
"v_mfma_f32_16x16x32_bf16 v[140:143], a[212:215], v[68:71], v[140:143]\n"
"ds_read_b64_tr_b16 a[228:229], v194\n"
"v_mfma_f32_16x16x32_bf16 v[96:99], a[216:219], v[72:75], v[96:99]\n"
"s_add_u32 m0, s73, 86016\n"
"ds_read_b64_tr_b16 a[230:231], v194 offset:4096\n"
"s_mov_b32 s55, 61440\n"
"v_mfma_f32_16x16x32_bf16 v[104:107], a[216:219], v[76:79], v[104:107]\n"
"ds_read_b64_tr_b16 a[232:233], v193 offset:256\n"
"v_mfma_f32_16x16x32_bf16 v[112:115], a[216:219], v[80:83], v[112:115]\n"
"ds_read_b64_tr_b16 a[234:235], v193 offset:4352\n"
"v_mfma_f32_16x16x32_bf16 v[120:123], a[216:219], v[84:87], v[120:123]\n"
"buffer_load_dwordx4 v197, s[28:31], s55 offen offset:2048 nt lds\n"
"v_mfma_f32_16x16x32_bf16 v[128:131], a[216:219], v[88:91], v[128:131]\n"
"ds_read_b64_tr_b16 a[236:237], v194 offset:256\n"
"v_mfma_f32_16x16x32_bf16 v[136:139], a[216:219], v[92:95], v[136:139]\n"
"ds_read_b64_tr_b16 a[238:239], v194 offset:4352\n"
"v_mfma_f32_16x16x32_bf16 v[100:103], a[220:223], v[72:75], v[100:103]\n"
"ds_read_b64_tr_b16 a[240:241], v193 offset:512\n"
"v_mfma_f32_16x16x32_bf16 v[108:111], a[220:223], v[76:79], v[108:111]\n"
"ds_read_b64_tr_b16 a[242:243], v193 offset:4608\n"
"v_mfma_f32_16x16x32_bf16 v[116:119], a[220:223], v[80:83], v[116:119]\n"
"ds_read_b64_tr_b16 a[244:245], v194 offset:512\n"
"v_mfma_f32_16x16x32_bf16 v[124:127], a[220:223], v[84:87], v[124:127]\n"
"ds_read_b64_tr_b16 a[246:247], v194 offset:4608\n"
"v_mfma_f32_16x16x32_bf16 v[132:135], a[220:223], v[88:91], v[132:135]\n"
"ds_read_b64_tr_b16 a[248:249], v193 offset:768\n"
"v_mfma_f32_16x16x32_bf16 v[140:143], a[220:223], v[92:95], v[140:143]\n"
"s_cbranch_vccnz .Lrare6\n"
".Lrare6_ret:\n"
"ds_read_b64_tr_b16 a[250:251], v193 offset:4864\n"
"v_add_u32 v193, 0xfffe0000, v193\n"
"v_mfma_f32_16x16x32_bf16 v[168:171], v[204:207], v[208:211], v[168:171]\n"
"s_add_u32 m0, s73, 131072\n"
"ds_read_b64_tr_b16 a[252:253], v194 offset:768\n"
"s_mov_b32 s55, 73728\n"
"v_mfma_f32_16x16x32_bf16 v[172:175], v[204:207], v[216:219], v[172:175]\n"
"ds_read_b64_tr_b16 a[254:255], v194 offset:4864\n"
"v_add_u32 v194, 0xfffe0000, v194\n"
"v_mfma_f32_16x16x32_bf16 v[176:179], v[204:207], v[224:227], v[176:179]\n"
"v_exp_f32 v96, v96\n"
"v_mfma_f32_16x16x32_bf16 v[180:183], v[204:207], v[232:235], v[180:183]\n"
"buffer_load_dwordx4 v195, s[24:27], s55 offen nt lds\n"
"v_mfma_f32_16x16x32_bf16 v[184:187], v[204:207], v[240:243], v[184:187]\n"
"v_exp_f32 v97, v97\n"
"v_mfma_f32_16x16x32_bf16 v[188:191], v[204:207], v[248:251], v[188:191]\n"
"v_exp_f32 v98, v98\n"
"v_mfma_f32_16x16x32_bf16 a[0:3], a[224:227], v[208:211], a[0:3]\n"
"v_exp_f32 v99, v99\n"
"v_mfma_f32_16x16x32_bf16 a[4:7], a[224:227], v[216:219], a[4:7]\n"
"v_exp_f32 v100, v100\n"
"v_mfma_f32_16x16x32_bf16 a[8:11], a[224:227], v[224:227], a[8:11]\n"
"v_exp_f32 v101, v101\n"
"v_mfma_f32_16x16x32_bf16 a[12:15], a[224:227], v[232:235], a[12:15]\n"
"v_exp_f32 v102, v102\n"
"v_mfma_f32_16x16x32_bf16 a[16:19], a[224:227], v[240:243], a[16:19]\n"
"v_exp_f32 v103, v103\n"
"v_mfma_f32_16x16x32_bf16 a[20:23], a[224:227], v[248:251], a[20:23]\n"
"s_waitcnt lgkmcnt(0)\n"
"v_exp_f32 v104, v104\n"
"v_mfma_f32_16x16x32_bf16 a[24:27], a[228:231], v[208:211], a[24:27]\n"
"s_add_u32 m0, s73, 131072\n"
"v_exp_f32 v105, v105\n"
"v_mfma_f32_16x16x32_bf16 a[28:31], a[228:231], v[216:219], a[28:31]\n"
"s_mov_b32 s55, 73728\n"
"v_cvt_pk_bf16_f32 v96, v96, v97\n"
"v_cvt_pk_bf16_f32 v97, v98, v99\n"
"v_mfma_f32_16x16x32_bf16 a[32:35], a[228:231], v[224:227], a[32:35]\n"
"s_add_u32 s64, s64, 8192\n"
"v_exp_f32 v106, v106\n"
"v_mfma_f32_16x16x32_bf16 a[36:39], a[228:231], v[232:235], a[36:39]\n"
"buffer_load_dwordx4 v196, s[24:27], s55 offen offset:2048 nt lds\n"
"v_mfma_f32_16x16x32_bf16 a[40:43], a[228:231], v[240:243], a[40:43]\n"
"s_add_u32 s72, s72, 112\n"
"v_exp_f32 v107, v107\n"
"v_mfma_f32_16x16x32_bf16 a[44:47], a[228:231], v[248:251], a[44:47]\n"
"v_exp_f32 v108, v108\n"
"v_mfma_f32_16x16x32_bf16 a[48:51], a[232:235], v[208:211], a[48:51]\n"
"v_exp_f32 v109, v109\n"
"v_mfma_f32_16x16x32_bf16 a[52:55], a[232:235], v[216:219], a[52:55]\n"
"v_exp_f32 v110, v110\n"
"v_mfma_f32_16x16x32_bf16 a[56:59], a[232:235], v[224:227], a[56:59]\n"
"v_exp_f32 v111, v111\n"
"v_mfma_f32_16x16x32_bf16 a[60:63], a[232:235], v[232:235], a[60:63]\n"
"v_exp_f32 v112, v112\n"
"v_mfma_f32_16x16x32_bf16 a[64:67], a[232:235], v[240:243], a[64:67]\n"
"v_exp_f32 v113, v113\n"
"v_mfma_f32_16x16x32_bf16 a[68:71], a[232:235], v[248:251], a[68:71]\n"
"v_or_b32 v198, v96, v97\n"
"v_cvt_pk_bf16_f32 v98, v100, v101\n"
"v_mfma_f32_16x16x32_bf16 a[72:75], a[236:239], v[208:211], a[72:75]\n"
"v_cvt_pk_bf16_f32 v99, v102, v103\n"
"v_cvt_pk_bf16_f32 v104, v104, v105\n"
"v_mfma_f32_16x16x32_bf16 a[76:79], a[236:239], v[216:219], a[76:79]\n"
"v_exp_f32 v114, v114\n"
"v_mfma_f32_16x16x32_bf16 a[80:83], a[236:239], v[224:227], a[80:83]\n"
"v_exp_f32 v115, v115\n"
"v_mfma_f32_16x16x32_bf16 a[84:87], a[236:239], v[232:235], a[84:87]\n"
"v_or3_b32 v198, v198, v98, v99\n"
"v_cvt_pk_bf16_f32 v105, v106, v107\n"
"v_mfma_f32_16x16x32_bf16 a[88:91], a[236:239], v[240:243], a[88:91]\n"
"v_exp_f32 v116, v116\n"
"v_mfma_f32_16x16x32_bf16 a[92:95], a[236:239], v[248:251], a[92:95]\n"
"v_exp_f32 v117, v117\n"
"v_mfma_f32_16x16x32_bf16 a[96:99], a[240:243], v[208:211], a[96:99]\n"
"s_memrealtime s[70:71]\n"
"v_or3_b32 v198, v198, v104, v105\n"
"v_cvt_pk_bf16_f32 v106, v108, v109\n"
"v_mfma_f32_16x16x32_bf16 a[100:103], a[240:243], v[216:219], a[100:103]\n"
"v_cvt_pk_bf16_f32 v107, v110, v111\n"
"v_cvt_pk_bf16_f32 v112, v112, v113\n"
"v_mfma_f32_16x16x32_bf16 a[104:107], a[240:243], v[224:227], a[104:107]\n"
"v_or3_b32 v198, v198, v106, v107\n"
"v_cvt_pk_bf16_f32 v113, v114, v115\n"
"v_mfma_f32_16x16x32_bf16 a[108:111], a[240:243], v[232:235], a[108:111]\n"
"v_or3_b32 v198, v198, v112, v113\n"
"v_cvt_pk_bf16_f32 v114, v116, v117\n"
"v_mfma_f32_16x16x32_bf16 a[112:115], a[240:243], v[240:243], a[112:115]\n"
"v_exp_f32 v118, v118\n"
"v_mfma_f32_16x16x32_bf16 a[116:119], a[240:243], v[248:251], a[116:119]\n"
"v_exp_f32 v119, v119\n"
"v_mfma_f32_16x16x32_bf16 a[120:123], a[244:247], v[208:211], a[120:123]\n"
"v_mfma_f32_16x16x32_bf16 a[124:127], a[244:247], v[216:219], a[124:127]\n"
"v_mfma_f32_16x16x32_bf16 a[128:131], a[244:247], v[224:227], a[128:131]\n"
"v_mfma_f32_16x16x32_bf16 a[132:135], a[244:247], v[232:235], a[132:135]\n"
"v_mfma_f32_16x16x32_bf16 a[136:139], a[244:247], v[240:243], a[136:139]\n"
"v_mfma_f32_16x16x32_bf16 a[140:143], a[244:247], v[248:251], a[140:143]\n"
"s_waitcnt vmcnt(18) lgkmcnt(0)\n"
"s_sub_u32 s71, s72, s70\n"
"s_sub_u32 s71, s71, 16\n"
"s_cmp_lt_u32 s71, 400\n"
"s_cbranch_scc1 .Lpg4\n"
".Lpg4x:\n"
"s_barrier\n"
"v_mfma_f32_16x16x32_bf16 a[144:147], a[248:251], v[208:211], a[144:147]\n"
"s_add_u32 m0, s73, 135168\n"
"ds_read_b128 a[192:195], v192\n"
"v_cvt_pk_bf16_f32 v115, v118, v119\n"
"v_mfma_f32_16x16x32_bf16 a[148:151], a[248:251], v[216:219], a[148:151]\n"
"s_mov_b32 s55, 77824\n"
"ds_read_b128 a[196:199], v192 offset:4096\n"
"v_or3_b32 v198, v198, v114, v115\n"
"v_mfma_f32_16x16x32_bf16 a[152:155], a[248:251], v[224:227], a[152:155]\n"
"ds_read_b128 a[200:203], v192 offset:256\n"
"v_mfma_f32_16x16x32_bf16 a[156:159], a[248:251], v[232:235], a[156:159]\n"
"buffer_load_dwordx4 v195, s[24:27], s55 offen nt lds\n"
"v_mfma_f32_16x16x32_bf16 a[160:163], a[248:251], v[240:243], a[160:163]\n"
"ds_read_b128 a[204:207], v192 offset:4352\n"
"v_mfma_f32_16x16x32_bf16 a[164:167], a[248:251], v[248:251], a[164:167]\n"
"ds_read_b128 a[208:211], v192 offset:512\n"
"v_mfma_f32_16x16x32_bf16 a[168:171], a[252:255], v[208:211], a[168:171]\n"
"ds_read_b128 a[212:215], v192 offset:4608\n"
"v_mfma_f32_16x16x32_bf16 a[172:175], a[252:255], v[216:219], a[172:175]\n"
"ds_read_b128 a[216:219], v192 offset:768\n"
"v_mfma_f32_16x16x32_bf16 a[176:179], a[252:255], v[224:227], a[176:179]\n"
"ds_read_b128 a[220:223], v192 offset:4864\n"
"v_add_u32 v192, 32768, v192\n"
"v_mfma_f32_16x16x32_bf16 a[180:183], a[252:255], v[232:235], a[180:183]\n"
"v_exp_f32 v120, v120\n"
"v_mfma_f32_16x16x32_bf16 a[184:187], a[252:255], v[240:243], a[184:187]\n"
"v_exp_f32 v121, v121\n"
"v_mfma_f32_16x16x32_bf16 a[188:191], a[252:255], v[248:251], a[188:191]\n"
"s_waitcnt lgkmcnt(5)\n"
"v_exp_f32 v122, v122\n"
"v_mfma_f32_16x16x32_bf16 v[208:211], a[192:195], v[0:3], v[144:147]\n"
"s_add_u32 m0, s73, 135168\n"
"v_exp_f32 v123, v123\n"
"v_mfma_f32_16x16x32_bf16 v[216:219], a[192:195], v[4:7], v[148:151]\n"
"s_mov_b32 s55, 77824\n"
"v_exp_f32 v124, v124\n"
"v_mfma_f32_16x16x32_bf16 v[224:227], a[192:195], v[8:11], v[152:155]\n"
"ds_read_b64_tr_b16 a[224:225], v193\n"
"v_cvt_pk_bf16_f32 v120, v120, v121\n"
"v_mfma_f32_16x16x32_bf16 v[232:235], a[192:195], v[12:15], v[156:159]\n"
"buffer_load_dwordx4 v196, s[24:27], s55 offen offset:2048 nt lds\n"
"v_mfma_f32_16x16x32_bf16 v[240:243], a[192:195], v[16:19], v[160:163]\n"
"v_exp_f32 v125, v125\n"
"v_mfma_f32_16x16x32_bf16 v[248:251], a[192:195], v[20:23], v[164:167]\n"
"ds_read_b64_tr_b16 a[226:227], v193 offset:4096\n"
"v_cvt_pk_bf16_f32 v121, v122, v123\n"
"v_mfma_f32_16x16x32_bf16 v[212:215], a[196:199], v[0:3], v[144:147]\n"
"v_exp_f32 v126, v126\n"
"v_mfma_f32_16x16x32_bf16 v[220:223], a[196:199], v[4:7], v[148:151]\n"
"v_exp_f32 v127, v127\n"
"v_mfma_f32_16x16x32_bf16 v[228:231], a[196:199], v[8:11], v[152:155]\n"
"v_exp_f32 v128, v128\n"
"v_mfma_f32_16x16x32_bf16 v[236:239], a[196:199], v[12:15], v[156:159]\n"
"v_exp_f32 v129, v129\n"
"v_mfma_f32_16x16x32_bf16 v[244:247], a[196:199], v[16:19], v[160:163]\n"
"v_exp_f32 v130, v130\n"
"v_mfma_f32_16x16x32_bf16 v[252:255], a[196:199], v[20:23], v[164:167]\n"
"v_exp_f32 v131, v131\n"
"v_mfma_f32_16x16x32_bf16 v[208:211], a[200:203], v[24:27], v[208:211]\n"
"s_add_u32 m0, s73, 114688\n"
"v_exp_f32 v132, v132\n"
"v_mfma_f32_16x16x32_bf16 v[216:219], a[200:203], v[28:31], v[216:219]\n"
"s_mov_b32 s55, 65536\n"
"v_exp_f32 v133, v133\n"
"v_mfma_f32_16x16x32_bf16 v[224:227], a[200:203], v[32:35], v[224:227]\n"
"v_exp_f32 v134, v134\n"
"v_mfma_f32_16x16x32_bf16 v[232:235], a[200:203], v[36:39], v[232:235]\n"
"buffer_load_dwordx4 v197, s[28:31], s55 offen nt lds\n"
"v_mfma_f32_16x16x32_bf16 v[240:243], a[200:203], v[40:43], v[240:243]\n"
"v_exp_f32 v135, v135\n"
"v_mfma_f32_16x16x32_bf16 v[248:251], a[200:203], v[44:47], v[248:251]\n"
"s_waitcnt lgkmcnt(0)\n"
"v_exp_f32 v136, v136\n"
"v_mfma_f32_16x16x32_bf16 v[212:215], a[204:207], v[24:27], v[212:215]\n"
"v_exp_f32 v137, v137\n"
"v_mfma_f32_16x16x32_bf16 v[220:223], a[204:207], v[28:31], v[220:223]\n"
"v_or3_b32 v198, v198, v120, v121\n"
"v_cvt_pk_bf16_f32 v122, v124, v125\n"
"v_mfma_f32_16x16x32_bf16 v[228:231], a[204:207], v[32:35], v[228:231]\n"
"v_cvt_pk_bf16_f32 v123, v126, v127\n"
"v_cvt_pk_bf16_f32 v128, v128, v129\n"
"v_mfma_f32_16x16x32_bf16 v[236:239], a[204:207], v[36:39], v[236:239]\n"
"v_exp_f32 v138, v138\n"
"v_mfma_f32_16x16x32_bf16 v[244:247], a[204:207], v[40:43], v[244:247]\n"
"v_exp_f32 v139, v139\n"
"v_mfma_f32_16x16x32_bf16 v[252:255], a[204:207], v[44:47], v[252:255]\n"
"v_or3_b32 v198, v198, v122, v123\n"
"v_cvt_pk_bf16_f32 v129, v130, v131\n"
"v_mfma_f32_16x16x32_bf16 v[208:211], a[208:211], v[48:51], v[208:211]\n"
"s_add_u32 m0, s73, 114688\n"
"v_exp_f32 v140, v140\n"
"v_mfma_f32_16x16x32_bf16 v[216:219], a[208:211], v[52:55], v[216:219]\n"
"s_mov_b32 s55, 65536\n"
"v_exp_f32 v141, v141\n"
"v_mfma_f32_16x16x32_bf16 v[224:227], a[208:211], v[56:59], v[224:227]\n"
"v_exp_f32 v142, v142\n"
"v_mfma_f32_16x16x32_bf16 v[232:235], a[208:211], v[60:63], v[232:235]\n"
"buffer_load_dwordx4 v197, s[28:31], s55 offen offset:2048 nt lds\n"
"v_mfma_f32_16x16x32_bf16 v[240:243], a[208:211], v[64:67], v[240:243]\n"
"v_exp_f32 v143, v143\n"
"v_mfma_f32_16x16x32_bf16 v[248:251], a[208:211], v[68:71], v[248:251]\n"
"v_or3_b32 v198, v198, v128, v129\n"
"v_cvt_pk_bf16_f32 v130, v132, v133\n"
"v_mfma_f32_16x16x32_bf16 v[212:215], a[212:215], v[48:51], v[212:215]\n"
"v_cvt_pk_bf16_f32 v131, v134, v135\n"
"v_cvt_pk_bf16_f32 v136, v136, v137\n"
"v_mfma_f32_16x16x32_bf16 v[220:223], a[212:215], v[52:55], v[220:223]\n"
"v_or3_b32 v198, v198, v130, v131\n"
"v_cvt_pk_bf16_f32 v137, v138, v139\n"
"v_mfma_f32_16x16x32_bf16 v[228:231], a[212:215], v[56:59], v[228:231]\n"
"v_or3_b32 v198, v198, v136, v137\n"
"v_cvt_pk_bf16_f32 v138, v140, v141\n"
"v_mfma_f32_16x16x32_bf16 v[236:239], a[212:215], v[60:63], v[236:239]\n"
"v_cvt_pk_bf16_f32 v139, v142, v143\n"
"v_or3_b32 v198, v198, v138, v139\n"
"v_mfma_f32_16x16x32_bf16 v[244:247], a[212:215], v[64:67], v[244:247]\n"
"v_and_b32 v199, 0x40004000, v198\n"
"v_cmp_ne_u32 vcc, 0, v199\n"
"v_mfma_f32_16x16x32_bf16 v[252:255], a[212:215], v[68:71], v[252:255]\n"
"ds_read_b64_tr_b16 a[228:229], v194\n"
"v_mfma_f32_16x16x32_bf16 v[208:211], a[216:219], v[72:75], v[208:211]\n"
"s_add_u32 m0, s73, 118784\n"
"ds_read_b64_tr_b16 a[230:231], v194 offset:4096\n"
"s_mov_b32 s55, 69632\n"
"v_mfma_f32_16x16x32_bf16 v[216:219], a[216:219], v[76:79], v[216:219]\n"
"ds_read_b64_tr_b16 a[232:233], v193 offset:256\n"
"v_mfma_f32_16x16x32_bf16 v[224:227], a[216:219], v[80:83], v[224:227]\n"
"ds_read_b64_tr_b16 a[234:235], v193 offset:4352\n"
"v_mfma_f32_16x16x32_bf16 v[232:235], a[216:219], v[84:87], v[232:235]\n"
"buffer_load_dwordx4 v197, s[28:31], s55 offen nt lds\n"
"v_mfma_f32_16x16x32_bf16 v[240:243], a[216:219], v[88:91], v[240:243]\n"
"ds_read_b64_tr_b16 a[236:237], v194 offset:256\n"
"v_mfma_f32_16x16x32_bf16 v[248:251], a[216:219], v[92:95], v[248:251]\n"
"ds_read_b64_tr_b16 a[238:239], v194 offset:4352\n"
"v_mfma_f32_16x16x32_bf16 v[212:215], a[220:223], v[72:75], v[212:215]\n"
"ds_read_b64_tr_b16 a[240:241], v193 offset:512\n"
"v_mfma_f32_16x16x32_bf16 v[220:223], a[220:223], v[76:79], v[220:223]\n"
"ds_read_b64_tr_b16 a[242:243], v193 offset:4608\n"
"v_mfma_f32_16x16x32_bf16 v[228:231], a[220:223], v[80:83], v[228:231]\n"
"ds_read_b64_tr_b16 a[244:245], v194 offset:512\n"
"v_mfma_f32_16x16x32_bf16 v[236:239], a[220:223], v[84:87], v[236:239]\n"
"ds_read_b64_tr_b16 a[246:247], v194 offset:4608\n"
"v_mfma_f32_16x16x32_bf16 v[244:247], a[220:223], v[88:91], v[244:247]\n"
"ds_read_b64_tr_b16 a[248:249], v193 offset:768\n"
"v_mfma_f32_16x16x32_bf16 v[252:255], a[220:223], v[92:95], v[252:255]\n"
"s_cbranch_vccnz .Lrare7\n"
".Lrare7_ret:\n"
"ds_read_b64_tr_b16 a[250:251], v193 offset:4864\n"
"v_add_u32 v193, 32768, v193\n"
"v_mfma_f32_16x16x32_bf16 v[168:171], v[204:207], v[96:99], v[168:171]\n"
"s_add_u32 m0, s73, 118784\n"
"ds_read_b64_tr_b16 a[252:253], v194 offset:768\n"
"s_mov_b32 s55, 69632\n"
"v_mfma_f32_16x16x32_bf16 v[172:175], v[204:207], v[104:107], v[172:175]\n"
"ds_read_b64_tr_b16 a[254:255], v194 offset:4864\n"
"v_add_u32 v194, 32768, v194\n"
"v_mfma_f32_16x16x32_bf16 v[176:179], v[204:207], v[112:115], v[176:179]\n"
"v_exp_f32 v208, v208\n"
"v_mfma_f32_16x16x32_bf16 v[180:183], v[204:207], v[120:123], v[180:183]\n"
"buffer_load_dwordx4 v197, s[28:31], s55 offen offset:2048 nt lds\n"
"v_mfma_f32_16x16x32_bf16 v[184:187], v[204:207], v[128:131], v[184:187]\n"
"v_exp_f32 v209, v209\n"
"v_mfma_f32_16x16x32_bf16 v[188:191], v[204:207], v[136:139], v[188:191]\n"
"v_exp_f32 v210, v210\n"
"v_mfma_f32_16x16x32_bf16 a[0:3], a[224:227], v[96:99], a[0:3]\n"
"v_exp_f32 v211, v211\n"
"v_mfma_f32_16x16x32_bf16 a[4:7], a[224:227], v[104:107], a[4:7]\n"
"v_exp_f32 v212, v212\n"
"v_mfma_f32_16x16x32_bf16 a[8:11], a[224:227], v[112:115], a[8:11]\n"
"v_exp_f32 v213, v213\n"
"v_mfma_f32_16x16x32_bf16 a[12:15], a[224:227], v[120:123], a[12:15]\n"
"v_exp_f32 v214, v214\n"
"v_mfma_f32_16x16x32_bf16 a[16:19], a[224:227], v[128:131], a[16:19]\n"
"v_exp_f32 v215, v215\n"
"v_mfma_f32_16x16x32_bf16 a[20:23], a[224:227], v[136:139], a[20:23]\n"
"s_waitcnt lgkmcnt(0)\n"
"v_exp_f32 v216, v216\n"
"v_mfma_f32_16x16x32_bf16 a[24:27], a[228:231], v[96:99], a[24:27]\n"
"s_add_u32 m0, s73, 0\n"
"v_exp_f32 v217, v217\n"
"v_mfma_f32_16x16x32_bf16 a[28:31], a[228:231], v[104:107], a[28:31]\n"
"s_mov_b32 s55, 81920\n"
"v_cvt_pk_bf16_f32 v208, v208, v209\n"
"v_cvt_pk_bf16_f32 v209, v210, v211\n"
"v_mfma_f32_16x16x32_bf16 a[32:35], a[228:231], v[112:115], a[32:35]\n"
"s_add_u32 s64, s64, 8192\n"
"v_exp_f32 v218, v218\n"
"v_mfma_f32_16x16x32_bf16 a[36:39], a[228:231], v[120:123], a[36:39]\n"
"buffer_load_dwordx4 v195, s[24:27], s55 offen nt lds\n"
"v_mfma_f32_16x16x32_bf16 a[40:43], a[228:231], v[128:131], a[40:43]\n"
"s_add_u32 s72, s72, 112\n"
"v_exp_f32 v219, v219\n"
"v_mfma_f32_16x16x32_bf16 a[44:47], a[228:231], v[136:139], a[44:47]\n"
"v_exp_f32 v220, v220\n"
"v_mfma_f32_16x16x32_bf16 a[48:51], a[232:235], v[96:99], a[48:51]\n"
"v_exp_f32 v221, v221\n"
"v_mfma_f32_16x16x32_bf16 a[52:55], a[232:235], v[104:107], a[52:55]\n"
"v_exp_f32 v222, v222\n"
"v_mfma_f32_16x16x32_bf16 a[56:59], a[232:235], v[112:115], a[56:59]\n"
"v_exp_f32 v223, v223\n"
"v_mfma_f32_16x16x32_bf16 a[60:63], a[232:235], v[120:123], a[60:63]\n"
"v_exp_f32 v224, v224\n"
"v_mfma_f32_16x16x32_bf16 a[64:67], a[232:235], v[128:131], a[64:67]\n"
"v_exp_f32 v225, v225\n"
"v_mfma_f32_16x16x32_bf16 a[68:71], a[232:235], v[136:139], a[68:71]\n"
"v_or_b32 v198, v208, v209\n"
"v_cvt_pk_bf16_f32 v210, v212, v213\n"
"v_mfma_f32_16x16x32_bf16 a[72:75], a[236:239], v[96:99], a[72:75]\n"
"v_cvt_pk_bf16_f32 v211, v214, v215\n"
"v_cvt_pk_bf16_f32 v216, v216, v217\n"
"v_mfma_f32_16x16x32_bf16 a[76:79], a[236:239], v[104:107], a[76:79]\n"
"v_exp_f32 v226, v226\n"
"v_mfma_f32_16x16x32_bf16 a[80:83], a[236:239], v[112:115], a[80:83]\n"
"v_exp_f32 v227, v227\n"
"v_mfma_f32_16x16x32_bf16 a[84:87], a[236:239], v[120:123], a[84:87]\n"
"v_or3_b32 v198, v198, v210, v211\n"
"v_cvt_pk_bf16_f32 v217, v218, v219\n"
"v_mfma_f32_16x16x32_bf16 a[88:91], a[236:239], v[128:131], a[88:91]\n"
"v_exp_f32 v228, v228\n"
"v_mfma_f32_16x16x32_bf16 a[92:95], a[236:239], v[136:139], a[92:95]\n"
"v_exp_f32 v229, v229\n"
"v_mfma_f32_16x16x32_bf16 a[96:99], a[240:243], v[96:99], a[96:99]\n"
"s_memrealtime s[70:71]\n"
"v_or3_b32 v198, v198, v216, v217\n"
"v_cvt_pk_bf16_f32 v218, v220, v221\n"
"v_mfma_f32_16x16x32_bf16 a[100:103], a[240:243], v[104:107], a[100:103]\n"
"v_cvt_pk_bf16_f32 v219, v222, v223\n"
"v_cvt_pk_bf16_f32 v224, v224, v225\n"
"v_mfma_f32_16x16x32_bf16 a[104:107], a[240:243], v[112:115], a[104:107]\n"
"v_or3_b32 v198, v198, v218, v219\n"
"v_cvt_pk_bf16_f32 v225, v226, v227\n"
"v_mfma_f32_16x16x32_bf16 a[108:111], a[240:243], v[120:123], a[108:111]\n"
"v_or3_b32 v198, v198, v224, v225\n"
"v_cvt_pk_bf16_f32 v226, v228, v229\n"
"v_mfma_f32_16x16x32_bf16 a[112:115], a[240:243], v[128:131], a[112:115]\n"
"v_exp_f32 v230, v230\n"
"v_mfma_f32_16x16x32_bf16 a[116:119], a[240:243], v[136:139], a[116:119]\n"
"v_exp_f32 v231, v231\n"
"v_mfma_f32_16x16x32_bf16 a[120:123], a[244:247], v[96:99], a[120:123]\n"
"v_mfma_f32_16x16x32_bf16 a[124:127], a[244:247], v[104:107], a[124:127]\n"
"v_mfma_f32_16x16x32_bf16 a[128:131], a[244:247], v[112:115], a[128:131]\n"
"v_mfma_f32_16x16x32_bf16 a[132:135], a[244:247], v[120:123], a[132:135]\n"
"v_mfma_f32_16x16x32_bf16 a[136:139], a[244:247], v[128:131], a[136:139]\n"
"v_mfma_f32_16x16x32_bf16 a[140:143], a[244:247], v[136:139], a[140:143]\n"
"s_waitcnt vmcnt(17) lgkmcnt(0)\n"
"s_sub_u32 s71, s72, s70\n"
"s_sub_u32 s71, s71, 16\n"
"s_cmp_lt_u32 s71, 400\n"
"s_cbranch_scc1 .Lpg5\n"
".Lpg5x:\n"
"s_barrier\n"
"v_mfma_f32_16x16x32_bf16 a[144:147], a[248:251], v[96:99], a[144:147]\n"
"s_add_u32 m0, s73, 0\n"
"ds_read_b128 a[192:195], v192\n"
"v_cvt_pk_bf16_f32 v227, v230, v231\n"
"v_mfma_f32_16x16x32_bf16 a[148:151], a[248:251], v[104:107], a[148:151]\n"
"s_mov_b32 s55, 81920\n"
"ds_read_b128 a[196:199], v192 offset:4096\n"
"v_or3_b32 v198, v198, v226, v227\n"
"v_mfma_f32_16x16x32_bf16 a[152:155], a[248:251], v[112:115], a[152:155]\n"
"ds_read_b128 a[200:203], v192 offset:256\n"
"v_mfma_f32_16x16x32_bf16 a[156:159], a[248:251], v[120:123], a[156:159]\n"
"buffer_load_dwordx4 v196, s[24:27], s55 offen offset:2048 nt lds\n"
"v_mfma_f32_16x16x32_bf16 a[160:163], a[248:251], v[128:131], a[160:163]\n"
"ds_read_b128 a[204:207], v192 offset:4352\n"
"v_mfma_f32_16x16x32_bf16 a[164:167], a[248:251], v[136:139], a[164:167]\n"
"ds_read_b128 a[208:211], v192 offset:512\n"
"v_mfma_f32_16x16x32_bf16 a[168:171], a[252:255], v[96:99], a[168:171]\n"
"ds_read_b128 a[212:215], v192 offset:4608\n"
"v_mfma_f32_16x16x32_bf16 a[172:175], a[252:255], v[104:107], a[172:175]\n"
"ds_read_b128 a[216:219], v192 offset:768\n"
"v_mfma_f32_16x16x32_bf16 a[176:179], a[252:255], v[112:115], a[176:179]\n"
"ds_read_b128 a[220:223], v192 offset:4864\n"
"v_add_u32 v192, 32768, v192\n"
"v_mfma_f32_16x16x32_bf16 a[180:183], a[252:255], v[120:123], a[180:183]\n"
"v_exp_f32 v232, v232\n"
"v_mfma_f32_16x16x32_bf16 a[184:187], a[252:255], v[128:131], a[184:187]\n"
"v_exp_f32 v233, v233\n"
"v_mfma_f32_16x16x32_bf16 a[188:191], a[252:255], v[136:139], a[188:191]\n"
"s_waitcnt lgkmcnt(5)\n"
"v_exp_f32 v234, v234\n"
"v_mfma_f32_16x16x32_bf16 v[96:99], a[192:195], v[0:3], v[144:147]\n"
"s_add_u32 m0, s73, 4096\n"
"v_exp_f32 v235, v235\n"
"v_mfma_f32_16x16x32_bf16 v[104:107], a[192:195], v[4:7], v[148:151]\n"
"s_mov_b32 s55, 86016\n"
"v_exp_f32 v236, v236\n"
"v_mfma_f32_16x16x32_bf16 v[112:115], a[192:195], v[8:11], v[152:155]\n"
"ds_read_b64_tr_b16 a[224:225], v193\n"
"v_cvt_pk_bf16_f32 v232, v232, v233\n"
"v_mfma_f32_16x16x32_bf16 v[120:123], a[192:195], v[12:15], v[156:159]\n"
"buffer_load_dwordx4 v195, s[24:27], s55 offen nt lds\n"
"v_mfma_f32_16x16x32_bf16 v[128:131], a[192:195], v[16:19], v[160:163]\n"
"v_exp_f32 v237, v237\n"
"v_mfma_f32_16x16x32_bf16 v[136:139], a[192:195], v[20:23], v[164:167]\n"
"ds_read_b64_tr_b16 a[226:227], v193 offset:4096\n"
"v_cvt_pk_bf16_f32 v233, v234, v235\n"
"v_mfma_f32_16x16x32_bf16 v[100:103], a[196:199], v[0:3], v[144:147]\n"
"v_exp_f32 v238, v238\n"
"v_mfma_f32_16x16x32_bf16 v[108:111], a[196:199], v[4:7], v[148:151]\n"
"v_exp_f32 v239, v239\n"
"v_mfma_f32_16x16x32_bf16 v[116:119], a[196:199], v[8:11], v[152:155]\n"
"v_exp_f32 v240, v240\n"
"v_mfma_f32_16x16x32_bf16 v[124:127], a[196:199], v[12:15], v[156:159]\n"
"v_exp_f32 v241, v241\n"
"v_mfma_f32_16x16x32_bf16 v[132:135], a[196:199], v[16:19], v[160:163]\n"
"v_exp_f32 v242, v242\n"
"v_mfma_f32_16x16x32_bf16 v[140:143], a[196:199], v[20:23], v[164:167]\n"
"v_exp_f32 v243, v243\n"
"v_mfma_f32_16x16x32_bf16 v[96:99], a[200:203], v[24:27], v[96:99]\n"
"s_add_u32 m0, s73, 4096\n"
"v_exp_f32 v244, v244\n"
"v_mfma_f32_16x16x32_bf16 v[104:107], a[200:203], v[28:31], v[104:107]\n"
"s_mov_b32 s55, 86016\n"
"v_exp_f32 v245, v245\n"
"v_mfma_f32_16x16x32_bf16 v[112:115], a[200:203], v[32:35], v[112:115]\n"
"v_exp_f32 v246, v246\n"
"v_mfma_f32_16x16x32_bf16 v[120:123], a[200:203], v[36:39], v[120:123]\n"
"buffer_load_dwordx4 v196, s[24:27], s55 offen offset:2048 nt lds\n"
"v_mfma_f32_16x16x32_bf16 v[128:131], a[200:203], v[40:43], v[128:131]\n"
"v_exp_f32 v247, v247\n"
"v_mfma_f32_16x16x32_bf16 v[136:139], a[200:203], v[44:47], v[136:139]\n"
"s_waitcnt lgkmcnt(0)\n"
"v_exp_f32 v248, v248\n"
"v_mfma_f32_16x16x32_bf16 v[100:103], a[204:207], v[24:27], v[100:103]\n"
"v_exp_f32 v249, v249\n"
"v_mfma_f32_16x16x32_bf16 v[108:111], a[204:207], v[28:31], v[108:111]\n"
"v_or3_b32 v198, v198, v232, v233\n"
"v_cvt_pk_bf16_f32 v234, v236, v237\n"
"v_mfma_f32_16x16x32_bf16 v[116:119], a[204:207], v[32:35], v[116:119]\n"
"v_cvt_pk_bf16_f32 v235, v238, v239\n"
"v_cvt_pk_bf16_f32 v240, v240, v241\n"
"v_mfma_f32_16x16x32_bf16 v[124:127], a[204:207], v[36:39], v[124:127]\n"
"v_exp_f32 v250, v250\n"
"v_mfma_f32_16x16x32_bf16 v[132:135], a[204:207], v[40:43], v[132:135]\n"
"v_exp_f32 v251, v251\n"
"v_mfma_f32_16x16x32_bf16 v[140:143], a[204:207], v[44:47], v[140:143]\n"
"v_or3_b32 v198, v198, v234, v235\n"
"v_cvt_pk_bf16_f32 v241, v242, v243\n"
"v_mfma_f32_16x16x32_bf16 v[96:99], a[208:211], v[48:51], v[96:99]\n"
"s_add_u32 m0, s73, 147456\n"
"v_exp_f32 v252, v252\n"
"v_mfma_f32_16x16x32_bf16 v[104:107], a[208:211], v[52:55], v[104:107]\n"
"s_mov_b32 s55, 73728\n"
"v_exp_f32 v253, v253\n"
"v_mfma_f32_16x16x32_bf16 v[112:115], a[208:211], v[56:59], v[112:115]\n"
"v_exp_f32 v254, v254\n"
"v_mfma_f32_16x16x32_bf16 v[120:123], a[208:211], v[60:63], v[120:123]\n"
"buffer_load_dwordx4 v197, s[28:31], s55 offen nt lds\n"
"v_mfma_f32_16x16x32_bf16 v[128:131], a[208:211], v[64:67], v[128:131]\n"
"v_exp_f32 v255, v255\n"
"v_mfma_f32_16x16x32_bf16 v[136:139], a[208:211], v[68:71], v[136:139]\n"
"v_or3_b32 v198, v198, v240, v241\n"
"v_cvt_pk_bf16_f32 v242, v244, v245\n"
"v_mfma_f32_16x16x32_bf16 v[100:103], a[212:215], v[48:51], v[100:103]\n"
"v_cvt_pk_bf16_f32 v243, v246, v247\n"
"v_cvt_pk_bf16_f32 v248, v248, v249\n"
"v_mfma_f32_16x16x32_bf16 v[108:111], a[212:215], v[52:55], v[108:111]\n"
"v_or3_b32 v198, v198, v242, v243\n"
"v_cvt_pk_bf16_f32 v249, v250, v251\n"
"v_mfma_f32_16x16x32_bf16 v[116:119], a[212:215], v[56:59], v[116:119]\n"
"v_or3_b32 v198, v198, v248, v249\n"
"v_cvt_pk_bf16_f32 v250, v252, v253\n"
"v_mfma_f32_16x16x32_bf16 v[124:127], a[212:215], v[60:63], v[124:127]\n"
"v_cvt_pk_bf16_f32 v251, v254, v255\n"
"v_or3_b32 v198, v198, v250, v251\n"
"v_mfma_f32_16x16x32_bf16 v[132:135], a[212:215], v[64:67], v[132:135]\n"
"v_and_b32 v199, 0x40004000, v198\n"
"v_cmp_ne_u32 vcc, 0, v199\n"
"v_mfma_f32_16x16x32_bf16 v[140:143], a[212:215], v[68:71], v[140:143]\n"
"ds_read_b64_tr_b16 a[228:229], v194\n"
"v_mfma_f32_16x16x32_bf16 v[96:99], a[216:219], v[72:75], v[96:99]\n"
"s_add_u32 m0, s73, 147456\n"
"ds_read_b64_tr_b16 a[230:231], v194 offset:4096\n"
"s_mov_b32 s55, 73728\n"
"v_mfma_f32_16x16x32_bf16 v[104:107], a[216:219], v[76:79], v[104:107]\n"
"ds_read_b64_tr_b16 a[232:233], v193 offset:256\n"
"v_mfma_f32_16x16x32_bf16 v[112:115], a[216:219], v[80:83], v[112:115]\n"
"ds_read_b64_tr_b16 a[234:235], v193 offset:4352\n"
"v_mfma_f32_16x16x32_bf16 v[120:123], a[216:219], v[84:87], v[120:123]\n"
"buffer_load_dwordx4 v197, s[28:31], s55 offen offset:2048 nt lds\n"
"v_mfma_f32_16x16x32_bf16 v[128:131], a[216:219], v[88:91], v[128:131]\n"
"ds_read_b64_tr_b16 a[236:237], v194 offset:256\n"
"v_mfma_f32_16x16x32_bf16 v[136:139], a[216:219], v[92:95], v[136:139]\n"
"ds_read_b64_tr_b16 a[238:239], v194 offset:4352\n"
"v_mfma_f32_16x16x32_bf16 v[100:103], a[220:223], v[72:75], v[100:103]\n"
"ds_read_b64_tr_b16 a[240:241], v193 offset:512\n"
"v_mfma_f32_16x16x32_bf16 v[108:111], a[220:223], v[76:79], v[108:111]\n"
"ds_read_b64_tr_b16 a[242:243], v193 offset:4608\n"
"v_mfma_f32_16x16x32_bf16 v[116:119], a[220:223], v[80:83], v[116:119]\n"
"ds_read_b64_tr_b16 a[244:245], v194 offset:512\n"
"v_mfma_f32_16x16x32_bf16 v[124:127], a[220:223], v[84:87], v[124:127]\n"
"ds_read_b64_tr_b16 a[246:247], v194 offset:4608\n"
"v_mfma_f32_16x16x32_bf16 v[132:135], a[220:223], v[88:91], v[132:135]\n"
"ds_read_b64_tr_b16 a[248:249], v193 offset:768\n"
"v_mfma_f32_16x16x32_bf16 v[140:143], a[220:223], v[92:95], v[140:143]\n"
"s_cbranch_vccnz .Lrare8\n"
".Lrare8_ret:\n"
"ds_read_b64_tr_b16 a[250:251], v193 offset:4864\n"
"v_add_u32 v193, 32768, v193\n"
"v_mfma_f32_16x16x32_bf16 v[168:171], v[204:207], v[208:211], v[168:171]\n"
"s_add_u32 m0, s73, 151552\n"
"ds_read_b64_tr_b16 a[252:253], v194 offset:768\n"
"s_mov_b32 s55, 77824\n"
"v_mfma_f32_16x16x32_bf16 v[172:175], v[204:207], v[216:219], v[172:175]\n"
"ds_read_b64_tr_b16 a[254:255], v194 offset:4864\n"
"v_add_u32 v194, 32768, v194\n"
"v_mfma_f32_16x16x32_bf16 v[176:179], v[204:207], v[224:227], v[176:179]\n"
"v_exp_f32 v96, v96\n"
"v_mfma_f32_16x16x32_bf16 v[180:183], v[204:207], v[232:235], v[180:183]\n"
"buffer_load_dwordx4 v197, s[28:31], s55 offen nt lds\n"
"v_mfma_f32_16x16x32_bf16 v[184:187], v[204:207], v[240:243], v[184:187]\n"
"v_exp_f32 v97, v97\n"
"v_mfma_f32_16x16x32_bf16 v[188:191], v[204:207], v[248:251], v[188:191]\n"
"v_exp_f32 v98, v98\n"
"v_mfma_f32_16x16x32_bf16 a[0:3], a[224:227], v[208:211], a[0:3]\n"
"v_exp_f32 v99, v99\n"
"v_mfma_f32_16x16x32_bf16 a[4:7], a[224:227], v[216:219], a[4:7]\n"
"v_exp_f32 v100, v100\n"
"v_mfma_f32_16x16x32_bf16 a[8:11], a[224:227], v[224:227], a[8:11]\n"
"v_exp_f32 v101, v101\n"
"v_mfma_f32_16x16x32_bf16 a[12:15], a[224:227], v[232:235], a[12:15]\n"
"v_exp_f32 v102, v102\n"
"v_mfma_f32_16x16x32_bf16 a[16:19], a[224:227], v[240:243], a[16:19]\n"
"v_exp_f32 v103, v103\n"
"v_mfma_f32_16x16x32_bf16 a[20:23], a[224:227], v[248:251], a[20:23]\n"
"s_waitcnt lgkmcnt(0)\n"
"v_exp_f32 v104, v104\n"
"v_mfma_f32_16x16x32_bf16 a[24:27], a[228:231], v[208:211], a[24:27]\n"
"s_add_u32 m0, s73, 151552\n"
"v_exp_f32 v105, v105\n"
"v_mfma_f32_16x16x32_bf16 a[28:31], a[228:231], v[216:219], a[28:31]\n"
"s_mov_b32 s55, 77824\n"
"v_cvt_pk_bf16_f32 v96, v96, v97\n"
"v_cvt_pk_bf16_f32 v97, v98, v99\n"
"v_mfma_f32_16x16x32_bf16 a[32:35], a[228:231], v[224:227], a[32:35]\n"
"s_add_u32 s64, s64, 8192\n"
"v_exp_f32 v106, v106\n"
"v_mfma_f32_16x16x32_bf16 a[36:39], a[228:231], v[232:235], a[36:39]\n"
"buffer_load_dwordx4 v197, s[28:31], s55 offen offset:2048 nt lds\n"
"v_mfma_f32_16x16x32_bf16 a[40:43], a[228:231], v[240:243], a[40:43]\n"
"s_add_u32 s72, s72, 112\n"
"v_exp_f32 v107, v107\n"
"v_mfma_f32_16x16x32_bf16 a[44:47], a[228:231], v[248:251], a[44:47]\n"
"v_exp_f32 v108, v108\n"
"v_mfma_f32_16x16x32_bf16 a[48:51], a[232:235], v[208:211], a[48:51]\n"
"v_exp_f32 v109, v109\n"
"v_mfma_f32_16x16x32_bf16 a[52:55], a[232:235], v[216:219], a[52:55]\n"
"v_exp_f32 v110, v110\n"
"v_mfma_f32_16x16x32_bf16 a[56:59], a[232:235], v[224:227], a[56:59]\n"
"v_exp_f32 v111, v111\n"
"v_mfma_f32_16x16x32_bf16 a[60:63], a[232:235], v[232:235], a[60:63]\n"
"v_exp_f32 v112, v112\n"
"v_mfma_f32_16x16x32_bf16 a[64:67], a[232:235], v[240:243], a[64:67]\n"
"v_exp_f32 v113, v113\n"
"v_mfma_f32_16x16x32_bf16 a[68:71], a[232:235], v[248:251], a[68:71]\n"
"v_or_b32 v198, v96, v97\n"
"v_cvt_pk_bf16_f32 v98, v100, v101\n"
"v_mfma_f32_16x16x32_bf16 a[72:75], a[236:239], v[208:211], a[72:75]\n"
"v_cvt_pk_bf16_f32 v99, v102, v103\n"
"v_cvt_pk_bf16_f32 v104, v104, v105\n"
"v_mfma_f32_16x16x32_bf16 a[76:79], a[236:239], v[216:219], a[76:79]\n"
"v_exp_f32 v114, v114\n"
"v_mfma_f32_16x16x32_bf16 a[80:83], a[236:239], v[224:227], a[80:83]\n"
"v_exp_f32 v115, v115\n"
"v_mfma_f32_16x16x32_bf16 a[84:87], a[236:239], v[232:235], a[84:87]\n"
"v_or3_b32 v198, v198, v98, v99\n"
"v_cvt_pk_bf16_f32 v105, v106, v107\n"
"v_mfma_f32_16x16x32_bf16 a[88:91], a[236:239], v[240:243], a[88:91]\n"
"v_exp_f32 v116, v116\n"
"v_mfma_f32_16x16x32_bf16 a[92:95], a[236:239], v[248:251], a[92:95]\n"
"v_exp_f32 v117, v117\n"
"v_mfma_f32_16x16x32_bf16 a[96:99], a[240:243], v[208:211], a[96:99]\n"
"s_memrealtime s[70:71]\n"
"v_or3_b32 v198, v198, v104, v105\n"
"v_cvt_pk_bf16_f32 v106, v108, v109\n"
"v_mfma_f32_16x16x32_bf16 a[100:103], a[240:243], v[216:219], a[100:103]\n"
"v_cvt_pk_bf16_f32 v107, v110, v111\n"
"v_cvt_pk_bf16_f32 v112, v112, v113\n"
"v_mfma_f32_16x16x32_bf16 a[104:107], a[240:243], v[224:227], a[104:107]\n"
"v_or3_b32 v198, v198, v106, v107\n"
"v_cvt_pk_bf16_f32 v113, v114, v115\n"
"v_mfma_f32_16x16x32_bf16 a[108:111], a[240:243], v[232:235], a[108:111]\n"
"v_or3_b32 v198, v198, v112, v113\n"
"v_cvt_pk_bf16_f32 v114, v116, v117\n"
"v_mfma_f32_16x16x32_bf16 a[112:115], a[240:243], v[240:243], a[112:115]\n"
"v_exp_f32 v118, v118\n"
"v_mfma_f32_16x16x32_bf16 a[116:119], a[240:243], v[248:251], a[116:119]\n"
"v_exp_f32 v119, v119\n"
"v_mfma_f32_16x16x32_bf16 a[120:123], a[244:247], v[208:211], a[120:123]\n"
"v_mfma_f32_16x16x32_bf16 a[124:127], a[244:247], v[216:219], a[124:127]\n"
"v_mfma_f32_16x16x32_bf16 a[128:131], a[244:247], v[224:227], a[128:131]\n"
"v_mfma_f32_16x16x32_bf16 a[132:135], a[244:247], v[232:235], a[132:135]\n"
"v_mfma_f32_16x16x32_bf16 a[136:139], a[244:247], v[240:243], a[136:139]\n"
"v_mfma_f32_16x16x32_bf16 a[140:143], a[244:247], v[248:251], a[140:143]\n"
"s_waitcnt vmcnt(16) lgkmcnt(0)\n"
"s_sub_u32 s71, s72, s70\n"
"s_sub_u32 s71, s71, 16\n"
"s_cmp_lt_u32 s71, 400\n"
"s_cbranch_scc1 .Lpg6\n"
".Lpg6x:\n"
"s_barrier\n"
"v_mfma_f32_16x16x32_bf16 a[144:147], a[248:251], v[208:211], a[144:147]\n"
"s_add_u32 m0, s73, 32768\n"
"ds_read_b128 a[192:195], v192\n"
"v_cvt_pk_bf16_f32 v115, v118, v119\n"
"v_mfma_f32_16x16x32_bf16 a[148:151], a[248:251], v[216:219], a[148:151]\n"
"s_mov_b32 s55, 90112\n"
"ds_read_b128 a[196:199], v192 offset:4096\n"
"v_or3_b32 v198, v198, v114, v115\n"
"v_mfma_f32_16x16x32_bf16 a[152:155], a[248:251], v[224:227], a[152:155]\n"
"ds_read_b128 a[200:203], v192 offset:256\n"
"v_mfma_f32_16x16x32_bf16 a[156:159], a[248:251], v[232:235], a[156:159]\n"
"buffer_load_dwordx4 v195, s[24:27], s55 offen nt lds\n"
"v_mfma_f32_16x16x32_bf16 a[160:163], a[248:251], v[240:243], a[160:163]\n"
"ds_read_b128 a[204:207], v192 offset:4352\n"
"v_mfma_f32_16x16x32_bf16 a[164:167], a[248:251], v[248:251], a[164:167]\n"
"ds_read_b128 a[208:211], v192 offset:512\n"
"v_mfma_f32_16x16x32_bf16 a[168:171], a[252:255], v[208:211], a[168:171]\n"
"ds_read_b128 a[212:215], v192 offset:4608\n"
"v_mfma_f32_16x16x32_bf16 a[172:175], a[252:255], v[216:219], a[172:175]\n"
"ds_read_b128 a[216:219], v192 offset:768\n"
"v_mfma_f32_16x16x32_bf16 a[176:179], a[252:255], v[224:227], a[176:179]\n"
"ds_read_b128 a[220:223], v192 offset:4864\n"
"v_add_u32 v192, 32768, v192\n"
"v_mfma_f32_16x16x32_bf16 a[180:183], a[252:255], v[232:235], a[180:183]\n"
"v_exp_f32 v120, v120\n"
"v_mfma_f32_16x16x32_bf16 a[184:187], a[252:255], v[240:243], a[184:187]\n"
"v_exp_f32 v121, v121\n"
"v_mfma_f32_16x16x32_bf16 a[188:191], a[252:255], v[248:251], a[188:191]\n"
"s_waitcnt lgkmcnt(5)\n"
"v_exp_f32 v122, v122\n"
"v_mfma_f32_16x16x32_bf16 v[208:211], a[192:195], v[0:3], v[144:147]\n"
"s_add_u32 m0, s73, 32768\n"
"v_exp_f32 v123, v123\n"
"v_mfma_f32_16x16x32_bf16 v[216:219], a[192:195], v[4:7], v[148:151]\n"
"s_mov_b32 s55, 90112\n"
"v_exp_f32 v124, v124\n"
"v_mfma_f32_16x16x32_bf16 v[224:227], a[192:195], v[8:11], v[152:155]\n"
"ds_read_b64_tr_b16 a[224:225], v193\n"
"v_cvt_pk_bf16_f32 v120, v120, v121\n"
"v_mfma_f32_16x16x32_bf16 v[232:235], a[192:195], v[12:15], v[156:159]\n"
"buffer_load_dwordx4 v196, s[24:27], s55 offen offset:2048 nt lds\n"
"v_mfma_f32_16x16x32_bf16 v[240:243], a[192:195], v[16:19], v[160:163]\n"
"v_exp_f32 v125, v125\n"
"v_mfma_f32_16x16x32_bf16 v[248:251], a[192:195], v[20:23], v[164:167]\n"
"ds_read_b64_tr_b16 a[226:227], v193 offset:4096\n"
"v_cvt_pk_bf16_f32 v121, v122, v123\n"
"v_mfma_f32_16x16x32_bf16 v[212:215], a[196:199], v[0:3], v[144:147]\n"
"v_exp_f32 v126, v126\n"
"v_mfma_f32_16x16x32_bf16 v[220:223], a[196:199], v[4:7], v[148:151]\n"
"v_exp_f32 v127, v127\n"
"v_mfma_f32_16x16x32_bf16 v[228:231], a[196:199], v[8:11], v[152:155]\n"
"v_exp_f32 v128, v128\n"
"v_mfma_f32_16x16x32_bf16 v[236:239], a[196:199], v[12:15], v[156:159]\n"
"v_exp_f32 v129, v129\n"
"v_mfma_f32_16x16x32_bf16 v[244:247], a[196:199], v[16:19], v[160:163]\n"
"v_exp_f32 v130, v130\n"
"v_mfma_f32_16x16x32_bf16 v[252:255], a[196:199], v[20:23], v[164:167]\n"
"v_exp_f32 v131, v131\n"
"v_mfma_f32_16x16x32_bf16 v[208:211], a[200:203], v[24:27], v[208:211]\n"
"s_add_u32 m0, s73, 36864\n"
"v_exp_f32 v132, v132\n"
"v_mfma_f32_16x16x32_bf16 v[216:219], a[200:203], v[28:31], v[216:219]\n"
"s_mov_b32 s55, 94208\n"
"v_exp_f32 v133, v133\n"
"v_mfma_f32_16x16x32_bf16 v[224:227], a[200:203], v[32:35], v[224:227]\n"
"v_exp_f32 v134, v134\n"
"v_mfma_f32_16x16x32_bf16 v[232:235], a[200:203], v[36:39], v[232:235]\n"
"buffer_load_dwordx4 v195, s[24:27], s55 offen nt lds\n"
"v_mfma_f32_16x16x32_bf16 v[240:243], a[200:203], v[40:43], v[240:243]\n"
"v_exp_f32 v135, v135\n"
"v_mfma_f32_16x16x32_bf16 v[248:251], a[200:203], v[44:47], v[248:251]\n"
"s_waitcnt lgkmcnt(0)\n"
"v_exp_f32 v136, v136\n"
"v_mfma_f32_16x16x32_bf16 v[212:215], a[204:207], v[24:27], v[212:215]\n"
"v_exp_f32 v137, v137\n"
"v_mfma_f32_16x16x32_bf16 v[220:223], a[204:207], v[28:31], v[220:223]\n"
"v_or3_b32 v198, v198, v120, v121\n"
"v_cvt_pk_bf16_f32 v122, v124, v125\n"
"v_mfma_f32_16x16x32_bf16 v[228:231], a[204:207], v[32:35], v[228:231]\n"
"v_cvt_pk_bf16_f32 v123, v126, v127\n"
"v_cvt_pk_bf16_f32 v128, v128, v129\n"
"v_mfma_f32_16x16x32_bf16 v[236:239], a[204:207], v[36:39], v[236:239]\n"
"v_exp_f32 v138, v138\n"
"v_mfma_f32_16x16x32_bf16 v[244:247], a[204:207], v[40:43], v[244:247]\n"
"v_exp_f32 v139, v139\n"
"v_mfma_f32_16x16x32_bf16 v[252:255], a[204:207], v[44:47], v[252:255]\n"
"v_or3_b32 v198, v198, v122, v123\n"
"v_cvt_pk_bf16_f32 v129, v130, v131\n"
"v_mfma_f32_16x16x32_bf16 v[208:211], a[208:211], v[48:51], v[208:211]\n"
"s_add_u32 m0, s73, 36864\n"
"v_exp_f32 v140, v140\n"
"v_mfma_f32_16x16x32_bf16 v[216:219], a[208:211], v[52:55], v[216:219]\n"
"s_mov_b32 s55, 94208\n"
"v_exp_f32 v141, v141\n"
"v_mfma_f32_16x16x32_bf16 v[224:227], a[208:211], v[56:59], v[224:227]\n"
"v_exp_f32 v142, v142\n"
"v_mfma_f32_16x16x32_bf16 v[232:235], a[208:211], v[60:63], v[232:235]\n"
"buffer_load_dwordx4 v196, s[24:27], s55 offen offset:2048 nt lds\n"
"v_mfma_f32_16x16x32_bf16 v[240:243], a[208:211], v[64:67], v[240:243]\n"
"v_exp_f32 v143, v143\n"
"v_mfma_f32_16x16x32_bf16 v[248:251], a[208:211], v[68:71], v[248:251]\n"
"v_or3_b32 v198, v198, v128, v129\n"
"v_cvt_pk_bf16_f32 v130, v132, v133\n"
"v_mfma_f32_16x16x32_bf16 v[212:215], a[212:215], v[48:51], v[212:215]\n"
"v_cvt_pk_bf16_f32 v131, v134, v135\n"
"v_cvt_pk_bf16_f32 v136, v136, v137\n"
"v_mfma_f32_16x16x32_bf16 v[220:223], a[212:215], v[52:55], v[220:223]\n"
"v_or3_b32 v198, v198, v130, v131\n"
"v_cvt_pk_bf16_f32 v137, v138, v139\n"
"v_mfma_f32_16x16x32_bf16 v[228:231], a[212:215], v[56:59], v[228:231]\n"
"v_or3_b32 v198, v198, v136, v137\n"
"v_cvt_pk_bf16_f32 v138, v140, v141\n"
"v_mfma_f32_16x16x32_bf16 v[236:239], a[212:215], v[60:63], v[236:239]\n"
"v_cvt_pk_bf16_f32 v139, v142, v143\n"
"v_or3_b32 v198, v198, v138, v139\n"
"v_mfma_f32_16x16x32_bf16 v[244:247], a[212:215], v[64:67], v[244:247]\n"
"v_and_b32 v199, 0x40004000, v198\n"
"v_cmp_ne_u32 vcc, 0, v199\n"
"v_mfma_f32_16x16x32_bf16 v[252:255], a[212:215], v[68:71], v[252:255]\n"
"ds_read_b64_tr_b16 a[228:229], v194\n"
"v_mfma_f32_16x16x32_bf16 v[208:211], a[216:219], v[72:75], v[208:211]\n"
"s_add_u32 m0, s73, 16384\n"
"ds_read_b64_tr_b16 a[230:231], v194 offset:4096\n"
"s_mov_b32 s55, 81920\n"
"v_mfma_f32_16x16x32_bf16 v[216:219], a[216:219], v[76:79], v[216:219]\n"
"ds_read_b64_tr_b16 a[232:233], v193 offset:256\n"
"v_mfma_f32_16x16x32_bf16 v[224:227], a[216:219], v[80:83], v[224:227]\n"
"ds_read_b64_tr_b16 a[234:235], v193 offset:4352\n"
"v_mfma_f32_16x16x32_bf16 v[232:235], a[216:219], v[84:87], v[232:235]\n"
"buffer_load_dwordx4 v197, s[28:31], s55 offen nt lds\n"
"v_mfma_f32_16x16x32_bf16 v[240:243], a[216:219], v[88:91], v[240:243]\n"
"ds_read_b64_tr_b16 a[236:237], v194 offset:256\n"
"v_mfma_f32_16x16x32_bf16 v[248:251], a[216:219], v[92:95], v[248:251]\n"
"ds_read_b64_tr_b16 a[238:239], v194 offset:4352\n"
"v_mfma_f32_16x16x32_bf16 v[212:215], a[220:223], v[72:75], v[212:215]\n"
"ds_read_b64_tr_b16 a[240:241], v193 offset:512\n"
"v_mfma_f32_16x16x32_bf16 v[220:223], a[220:223], v[76:79], v[220:223]\n"
"ds_read_b64_tr_b16 a[242:243], v193 offset:4608\n"
"v_mfma_f32_16x16x32_bf16 v[228:231], a[220:223], v[80:83], v[228:231]\n"
"ds_read_b64_tr_b16 a[244:245], v194 offset:512\n"
"v_mfma_f32_16x16x32_bf16 v[236:239], a[220:223], v[84:87], v[236:239]\n"
"ds_read_b64_tr_b16 a[246:247], v194 offset:4608\n"
"v_mfma_f32_16x16x32_bf16 v[244:247], a[220:223], v[88:91], v[244:247]\n"
"ds_read_b64_tr_b16 a[248:249], v193 offset:768\n"
"v_mfma_f32_16x16x32_bf16 v[252:255], a[220:223], v[92:95], v[252:255]\n"
"s_cbranch_vccnz .Lrare9\n"
".Lrare9_ret:\n"
"ds_read_b64_tr_b16 a[250:251], v193 offset:4864\n"
"v_add_u32 v193, 32768, v193\n"
"v_mfma_f32_16x16x32_bf16 v[168:171], v[204:207], v[96:99], v[168:171]\n"
"s_add_u32 m0, s73, 16384\n"
"ds_read_b64_tr_b16 a[252:253], v194 offset:768\n"
"s_mov_b32 s55, 81920\n"
"v_mfma_f32_16x16x32_bf16 v[172:175], v[204:207], v[104:107], v[172:175]\n"
"ds_read_b64_tr_b16 a[254:255], v194 offset:4864\n"
"v_add_u32 v194, 32768, v194\n"
"v_mfma_f32_16x16x32_bf16 v[176:179], v[204:207], v[112:115], v[176:179]\n"
"v_exp_f32 v208, v208\n"
"v_mfma_f32_16x16x32_bf16 v[180:183], v[204:207], v[120:123], v[180:183]\n"
"buffer_load_dwordx4 v197, s[28:31], s55 offen offset:2048 nt lds\n"
"v_mfma_f32_16x16x32_bf16 v[184:187], v[204:207], v[128:131], v[184:187]\n"
"v_exp_f32 v209, v209\n"
"v_mfma_f32_16x16x32_bf16 v[188:191], v[204:207], v[136:139], v[188:191]\n"
"v_exp_f32 v210, v210\n"
"v_mfma_f32_16x16x32_bf16 a[0:3], a[224:227], v[96:99], a[0:3]\n"
"v_exp_f32 v211, v211\n"
"v_mfma_f32_16x16x32_bf16 a[4:7], a[224:227], v[104:107], a[4:7]\n"
"v_exp_f32 v212, v212\n"
"v_mfma_f32_16x16x32_bf16 a[8:11], a[224:227], v[112:115], a[8:11]\n"
"v_exp_f32 v213, v213\n"
"v_mfma_f32_16x16x32_bf16 a[12:15], a[224:227], v[120:123], a[12:15]\n"
"v_exp_f32 v214, v214\n"
"v_mfma_f32_16x16x32_bf16 a[16:19], a[224:227], v[128:131], a[16:19]\n"
"v_exp_f32 v215, v215\n"
"v_mfma_f32_16x16x32_bf16 a[20:23], a[224:227], v[136:139], a[20:23]\n"
"s_waitcnt lgkmcnt(0)\n"
"v_exp_f32 v216, v216\n"
"v_mfma_f32_16x16x32_bf16 a[24:27], a[228:231], v[96:99], a[24:27]\n"
"s_add_u32 m0, s73, 20480\n"
"v_exp_f32 v217, v217\n"
"v_mfma_f32_16x16x32_bf16 a[28:31], a[228:231], v[104:107], a[28:31]\n"
"s_mov_b32 s55, 86016\n"
"v_cvt_pk_bf16_f32 v208, v208, v209\n"
"v_cvt_pk_bf16_f32 v209, v210, v211\n"
"v_mfma_f32_16x16x32_bf16 a[32:35], a[228:231], v[112:115], a[32:35]\n"
"s_add_u32 s64, s64, 8192\n"
"v_exp_f32 v218, v218\n"
"v_mfma_f32_16x16x32_bf16 a[36:39], a[228:231], v[120:123], a[36:39]\n"
"buffer_load_dwordx4 v197, s[28:31], s55 offen nt lds\n"
"v_mfma_f32_16x16x32_bf16 a[40:43], a[228:231], v[128:131], a[40:43]\n"
"s_add_u32 s72, s72, 112\n"
"v_exp_f32 v219, v219\n"
"v_mfma_f32_16x16x32_bf16 a[44:47], a[228:231], v[136:139], a[44:47]\n"
"v_exp_f32 v220, v220\n"
"v_mfma_f32_16x16x32_bf16 a[48:51], a[232:235], v[96:99], a[48:51]\n"
"v_exp_f32 v221, v221\n"
"v_mfma_f32_16x16x32_bf16 a[52:55], a[232:235], v[104:107], a[52:55]\n"
"v_exp_f32 v222, v222\n"
"v_mfma_f32_16x16x32_bf16 a[56:59], a[232:235], v[112:115], a[56:59]\n"
"v_exp_f32 v223, v223\n"
"v_mfma_f32_16x16x32_bf16 a[60:63], a[232:235], v[120:123], a[60:63]\n"
"v_exp_f32 v224, v224\n"
"v_mfma_f32_16x16x32_bf16 a[64:67], a[232:235], v[128:131], a[64:67]\n"
"v_exp_f32 v225, v225\n"
"v_mfma_f32_16x16x32_bf16 a[68:71], a[232:235], v[136:139], a[68:71]\n"
"v_or_b32 v198, v208, v209\n"
"v_cvt_pk_bf16_f32 v210, v212, v213\n"
"v_mfma_f32_16x16x32_bf16 a[72:75], a[236:239], v[96:99], a[72:75]\n"
"v_cvt_pk_bf16_f32 v211, v214, v215\n"
"v_cvt_pk_bf16_f32 v216, v216, v217\n"
"v_mfma_f32_16x16x32_bf16 a[76:79], a[236:239], v[104:107], a[76:79]\n"
"v_exp_f32 v226, v226\n"
"v_mfma_f32_16x16x32_bf16 a[80:83], a[236:239], v[112:115], a[80:83]\n"
"v_exp_f32 v227, v227\n"
"v_mfma_f32_16x16x32_bf16 a[84:87], a[236:239], v[120:123], a[84:87]\n"
"v_or3_b32 v198, v198, v210, v211\n"
"v_cvt_pk_bf16_f32 v217, v218, v219\n"
"v_mfma_f32_16x16x32_bf16 a[88:91], a[236:239], v[128:131], a[88:91]\n"
"v_exp_f32 v228, v228\n"
"v_mfma_f32_16x16x32_bf16 a[92:95], a[236:239], v[136:139], a[92:95]\n"
"v_exp_f32 v229, v229\n"
"v_mfma_f32_16x16x32_bf16 a[96:99], a[240:243], v[96:99], a[96:99]\n"
"s_memrealtime s[70:71]\n"
"v_or3_b32 v198, v198, v216, v217\n"
"v_cvt_pk_bf16_f32 v218, v220, v221\n"
"v_mfma_f32_16x16x32_bf16 a[100:103], a[240:243], v[104:107], a[100:103]\n"
"v_cvt_pk_bf16_f32 v219, v222, v223\n"
"v_cvt_pk_bf16_f32 v224, v224, v225\n"
"v_mfma_f32_16x16x32_bf16 a[104:107], a[240:243], v[112:115], a[104:107]\n"
"v_or3_b32 v198, v198, v218, v219\n"
"v_cvt_pk_bf16_f32 v225, v226, v227\n"
"v_mfma_f32_16x16x32_bf16 a[108:111], a[240:243], v[120:123], a[108:111]\n"
"v_or3_b32 v198, v198, v224, v225\n"
"v_cvt_pk_bf16_f32 v226, v228, v229\n"
"v_mfma_f32_16x16x32_bf16 a[112:115], a[240:243], v[128:131], a[112:115]\n"
"v_exp_f32 v230, v230\n"
"v_mfma_f32_16x16x32_bf16 a[116:119], a[240:243], v[136:139], a[116:119]\n"
"v_exp_f32 v231, v231\n"
"v_mfma_f32_16x16x32_bf16 a[120:123], a[244:247], v[96:99], a[120:123]\n"
"v_mfma_f32_16x16x32_bf16 a[124:127], a[244:247], v[104:107], a[124:127]\n"
"v_mfma_f32_16x16x32_bf16 a[128:131], a[244:247], v[112:115], a[128:131]\n"
"v_mfma_f32_16x16x32_bf16 a[132:135], a[244:247], v[120:123], a[132:135]\n"
"v_mfma_f32_16x16x32_bf16 a[136:139], a[244:247], v[128:131], a[136:139]\n"
"v_mfma_f32_16x16x32_bf16 a[140:143], a[244:247], v[136:139], a[140:143]\n"
"s_waitcnt vmcnt(15) lgkmcnt(0)\n"
"s_sub_u32 s71, s72, s70\n"
"s_sub_u32 s71, s71, 16\n"
"s_cmp_lt_u32 s71, 400\n"
"s_cbranch_scc1 .Lpg7\n"
".Lpg7x:\n"
"s_barrier\n"
"v_mfma_f32_16x16x32_bf16 a[144:147], a[248:251], v[96:99], a[144:147]\n"
"s_add_u32 m0, s73, 20480\n"
"ds_read_b128 a[192:195], v192\n"
"v_cvt_pk_bf16_f32 v227, v230, v231\n"
"v_mfma_f32_16x16x32_bf16 a[148:151], a[248:251], v[104:107], a[148:151]\n"
"s_mov_b32 s55, 86016\n"
"ds_read_b128 a[196:199], v192 offset:4096\n"
"v_or3_b32 v198, v198, v226, v227\n"
"v_mfma_f32_16x16x32_bf16 a[152:155], a[248:251], v[112:115], a[152:155]\n"
"ds_read_b128 a[200:203], v192 offset:256\n"
"v_mfma_f32_16x16x32_bf16 a[156:159], a[248:251], v[120:123], a[156:159]\n"
"buffer_load_dwordx4 v197, s[28:31], s55 offen offset:2048 nt lds\n"
"v_mfma_f32_16x16x32_bf16 a[160:163], a[248:251], v[128:131], a[160:163]\n"
"ds_read_b128 a[204:207], v192 offset:4352\n"
"v_mfma_f32_16x16x32_bf16 a[164:167], a[248:251], v[136:139], a[164:167]\n"
"ds_read_b128 a[208:211], v192 offset:512\n"
"v_mfma_f32_16x16x32_bf16 a[168:171], a[252:255], v[96:99], a[168:171]\n"
"ds_read_b128 a[212:215], v192 offset:4608\n"
"v_mfma_f32_16x16x32_bf16 a[172:175], a[252:255], v[104:107], a[172:175]\n"
"ds_read_b128 a[216:219], v192 offset:768\n"
"v_mfma_f32_16x16x32_bf16 a[176:179], a[252:255], v[112:115], a[176:179]\n"
"ds_read_b128 a[220:223], v192 offset:4864\n"
"v_add_u32 v192, 0xfffe0000, v192\n"
"v_mfma_f32_16x16x32_bf16 a[180:183], a[252:255], v[120:123], a[180:183]\n"
"v_exp_f32 v232, v232\n"
"v_mfma_f32_16x16x32_bf16 a[184:187], a[252:255], v[128:131], a[184:187]\n"
"v_exp_f32 v233, v233\n"
"v_mfma_f32_16x16x32_bf16 a[188:191], a[252:255], v[136:139], a[188:191]\n"
"s_waitcnt lgkmcnt(5)\n"
"v_exp_f32 v234, v234\n"
"v_mfma_f32_16x16x32_bf16 v[96:99], a[192:195], v[0:3], v[144:147]\n"
"s_add_u32 m0, s73, 65536\n"
"v_exp_f32 v235, v235\n"
"v_mfma_f32_16x16x32_bf16 v[104:107], a[192:195], v[4:7], v[148:151]\n"
"s_mov_b32 s55, 98304\n"
"v_exp_f32 v236, v236\n"
"v_mfma_f32_16x16x32_bf16 v[112:115], a[192:195], v[8:11], v[152:155]\n"
"ds_read_b64_tr_b16 a[224:225], v193\n"
"v_cvt_pk_bf16_f32 v232, v232, v233\n"
"v_mfma_f32_16x16x32_bf16 v[120:123], a[192:195], v[12:15], v[156:159]\n"
"buffer_load_dwordx4 v195, s[24:27], s55 offen nt lds\n"
"v_mfma_f32_16x16x32_bf16 v[128:131], a[192:195], v[16:19], v[160:163]\n"
"v_exp_f32 v237, v237\n"
"v_mfma_f32_16x16x32_bf16 v[136:139], a[192:195], v[20:23], v[164:167]\n"
"ds_read_b64_tr_b16 a[226:227], v193 offset:4096\n"
"v_cvt_pk_bf16_f32 v233, v234, v235\n"
"v_mfma_f32_16x16x32_bf16 v[100:103], a[196:199], v[0:3], v[144:147]\n"
"v_exp_f32 v238, v238\n"
"v_mfma_f32_16x16x32_bf16 v[108:111], a[196:199], v[4:7], v[148:151]\n"
"v_exp_f32 v239, v239\n"
"v_mfma_f32_16x16x32_bf16 v[116:119], a[196:199], v[8:11], v[152:155]\n"
"v_exp_f32 v240, v240\n"
"v_mfma_f32_16x16x32_bf16 v[124:127], a[196:199], v[12:15], v[156:159]\n"
"v_exp_f32 v241, v241\n"
"v_mfma_f32_16x16x32_bf16 v[132:135], a[196:199], v[16:19], v[160:163]\n"
"v_exp_f32 v242, v242\n"
"v_mfma_f32_16x16x32_bf16 v[140:143], a[196:199], v[20:23], v[164:167]\n"
"v_exp_f32 v243, v243\n"
"v_mfma_f32_16x16x32_bf16 v[96:99], a[200:203], v[24:27], v[96:99]\n"
"s_add_u32 m0, s73, 65536\n"
"v_exp_f32 v244, v244\n"
"v_mfma_f32_16x16x32_bf16 v[104:107], a[200:203], v[28:31], v[104:107]\n"
"s_mov_b32 s55, 98304\n"
"v_exp_f32 v245, v245\n"
"v_mfma_f32_16x16x32_bf16 v[112:115], a[200:203], v[32:35], v[112:115]\n"
"v_exp_f32 v246, v246\n"
"v_mfma_f32_16x16x32_bf16 v[120:123], a[200:203], v[36:39], v[120:123]\n"
"buffer_load_dwordx4 v196, s[24:27], s55 offen offset:2048 nt lds\n"
"v_mfma_f32_16x16x32_bf16 v[128:131], a[200:203], v[40:43], v[128:131]\n"
"v_exp_f32 v247, v247\n"
"v_mfma_f32_16x16x32_bf16 v[136:139], a[200:203], v[44:47], v[136:139]\n"
"s_waitcnt lgkmcnt(0)\n"
"v_exp_f32 v248, v248\n"
"v_mfma_f32_16x16x32_bf16 v[100:103], a[204:207], v[24:27], v[100:103]\n"
"v_exp_f32 v249, v249\n"
"v_mfma_f32_16x16x32_bf16 v[108:111], a[204:207], v[28:31], v[108:111]\n"
"v_or3_b32 v198, v198, v232, v233\n"
"v_cvt_pk_bf16_f32 v234, v236, v237\n"
"v_mfma_f32_16x16x32_bf16 v[116:119], a[204:207], v[32:35], v[116:119]\n"
"v_cvt_pk_bf16_f32 v235, v238, v239\n"
"v_cvt_pk_bf16_f32 v240, v240, v241\n"
"v_mfma_f32_16x16x32_bf16 v[124:127], a[204:207], v[36:39], v[124:127]\n"
"v_exp_f32 v250, v250\n"
"v_mfma_f32_16x16x32_bf16 v[132:135], a[204:207], v[40:43], v[132:135]\n"
"v_exp_f32 v251, v251\n"
"v_mfma_f32_16x16x32_bf16 v[140:143], a[204:207], v[44:47], v[140:143]\n"
"v_or3_b32 v198, v198, v234, v235\n"
"v_cvt_pk_bf16_f32 v241, v242, v243\n"
"v_mfma_f32_16x16x32_bf16 v[96:99], a[208:211], v[48:51], v[96:99]\n"
"s_add_u32 m0, s73, 69632\n"
"v_exp_f32 v252, v252\n"
"v_mfma_f32_16x16x32_bf16 v[104:107], a[208:211], v[52:55], v[104:107]\n"
"s_mov_b32 s55, 102400\n"
"v_exp_f32 v253, v253\n"
"v_mfma_f32_16x16x32_bf16 v[112:115], a[208:211], v[56:59], v[112:115]\n"
"v_exp_f32 v254, v254\n"
"v_mfma_f32_16x16x32_bf16 v[120:123], a[208:211], v[60:63], v[120:123]\n"
"buffer_load_dwordx4 v195, s[24:27], s55 offen nt lds\n"
"v_mfma_f32_16x16x32_bf16 v[128:131], a[208:211], v[64:67], v[128:131]\n"
"v_exp_f32 v255, v255\n"
"v_mfma_f32_16x16x32_bf16 v[136:139], a[208:211], v[68:71], v[136:139]\n"
"v_or3_b32 v198, v198, v240, v241\n"
"v_cvt_pk_bf16_f32 v242, v244, v245\n"
"v_mfma_f32_16x16x32_bf16 v[100:103], a[212:215], v[48:51], v[100:103]\n"
"v_cvt_pk_bf16_f32 v243, v246, v247\n"
"v_cvt_pk_bf16_f32 v248, v248, v249\n"
"v_mfma_f32_16x16x32_bf16 v[108:111], a[212:215], v[52:55], v[108:111]\n"
"v_or3_b32 v198, v198, v242, v243\n"
"v_cvt_pk_bf16_f32 v249, v250, v251\n"
"v_mfma_f32_16x16x32_bf16 v[116:119], a[212:215], v[56:59], v[116:119]\n"
"v_or3_b32 v198, v198, v248, v249\n"
"v_cvt_pk_bf16_f32 v250, v252, v253\n"
"v_mfma_f32_16x16x32_bf16 v[124:127], a[212:215], v[60:63], v[124:127]\n"
"v_cvt_pk_bf16_f32 v251, v254, v255\n"
"v_or3_b32 v198, v198, v250, v251\n"
"v_mfma_f32_16x16x32_bf16 v[132:135], a[212:215], v[64:67], v[132:135]\n"
"v_and_b32 v199, 0x40004000, v198\n"
"v_cmp_ne_u32 vcc, 0, v199\n"
"v_mfma_f32_16x16x32_bf16 v[140:143], a[212:215], v[68:71], v[140:143]\n"
"ds_read_b64_tr_b16 a[228:229], v194\n"
"v_mfma_f32_16x16x32_bf16 v[96:99], a[216:219], v[72:75], v[96:99]\n"
"s_add_u32 m0, s73, 69632\n"
"ds_read_b64_tr_b16 a[230:231], v194 offset:4096\n"
"s_mov_b32 s55, 102400\n"
"v_mfma_f32_16x16x32_bf16 v[104:107], a[216:219], v[76:79], v[104:107]\n"
"ds_read_b64_tr_b16 a[232:233], v193 offset:256\n"
"v_mfma_f32_16x16x32_bf16 v[112:115], a[216:219], v[80:83], v[112:115]\n"
"ds_read_b64_tr_b16 a[234:235], v193 offset:4352\n"
"v_mfma_f32_16x16x32_bf16 v[120:123], a[216:219], v[84:87], v[120:123]\n"
"buffer_load_dwordx4 v196, s[24:27], s55 offen offset:2048 nt lds\n"
"v_mfma_f32_16x16x32_bf16 v[128:131], a[216:219], v[88:91], v[128:131]\n"
"ds_read_b64_tr_b16 a[236:237], v194 offset:256\n"
"v_mfma_f32_16x16x32_bf16 v[136:139], a[216:219], v[92:95], v[136:139]\n"
"ds_read_b64_tr_b16 a[238:239], v194 offset:4352\n"
"v_mfma_f32_16x16x32_bf16 v[100:103], a[220:223], v[72:75], v[100:103]\n"
"ds_read_b64_tr_b16 a[240:241], v193 offset:512\n"
"v_mfma_f32_16x16x32_bf16 v[108:111], a[220:223], v[76:79], v[108:111]\n"
"ds_read_b64_tr_b16 a[242:243], v193 offset:4608\n"
"v_mfma_f32_16x16x32_bf16 v[116:119], a[220:223], v[80:83], v[116:119]\n"
"ds_read_b64_tr_b16 a[244:245], v194 offset:512\n"
"v_mfma_f32_16x16x32_bf16 v[124:127], a[220:223], v[84:87], v[124:127]\n"
"ds_read_b64_tr_b16 a[246:247], v194 offset:4608\n"
"v_mfma_f32_16x16x32_bf16 v[132:135], a[220:223], v[88:91], v[132:135]\n"
"ds_read_b64_tr_b16 a[248:249], v193 offset:768\n"
"v_mfma_f32_16x16x32_bf16 v[140:143], a[220:223], v[92:95], v[140:143]\n"
"s_cbranch_vccnz .Lrare10\n"
".Lrare10_ret:\n"
"ds_read_b64_tr_b16 a[250:251], v193 offset:4864\n"
"v_add_u32 v193, 32768, v193\n"
"v_mfma_f32_16x16x32_bf16 v[168:171], v[204:207], v[208:211], v[168:171]\n"
"s_add_u32 m0, s73, 49152\n"
"ds_read_b64_tr_b16 a[252:253], v194 offset:768\n"
"s_mov_b32 s55, 90112\n"
"v_mfma_f32_16x16x32_bf16 v[172:175], v[204:207], v[216:219], v[172:175]\n"
"ds_read_b64_tr_b16 a[254:255], v194 offset:4864\n"
"v_add_u32 v194, 32768, v194\n"
"v_mfma_f32_16x16x32_bf16 v[176:179], v[204:207], v[224:227], v[176:179]\n"
"v_exp_f32 v96, v96\n"
"v_mfma_f32_16x16x32_bf16 v[180:183], v[204:207], v[232:235], v[180:183]\n"
"buffer_load_dwordx4 v197, s[28:31], s55 offen nt lds\n"
"v_mfma_f32_16x16x32_bf16 v[184:187], v[204:207], v[240:243], v[184:187]\n"
"v_exp_f32 v97, v97\n"
"v_mfma_f32_16x16x32_bf16 v[188:191], v[204:207], v[248:251], v[188:191]\n"
"v_exp_f32 v98, v98\n"
"v_mfma_f32_16x16x32_bf16 a[0:3], a[224:227], v[208:211], a[0:3]\n"
"v_exp_f32 v99, v99\n"
"v_mfma_f32_16x16x32_bf16 a[4:7], a[224:227], v[216:219], a[4:7]\n"
"v_exp_f32 v100, v100\n"
"v_mfma_f32_16x16x32_bf16 a[8:11], a[224:227], v[224:227], a[8:11]\n"
"v_exp_f32 v101, v101\n"
"v_mfma_f32_16x16x32_bf16 a[12:15], a[224:227], v[232:235], a[12:15]\n"
"v_exp_f32 v102, v102\n"
"v_mfma_f32_16x16x32_bf16 a[16:19], a[224:227], v[240:243], a[16:19]\n"
"v_exp_f32 v103, v103\n"
"v_mfma_f32_16x16x32_bf16 a[20:23], a[224:227], v[248:251], a[20:23]\n"
"s_waitcnt lgkmcnt(0)\n"
"v_exp_f32 v104, v104\n"
"v_mfma_f32_16x16x32_bf16 a[24:27], a[228:231], v[208:211], a[24:27]\n"
"s_add_u32 m0, s73, 49152\n"
"v_exp_f32 v105, v105\n"
"v_mfma_f32_16x16x32_bf16 a[28:31], a[228:231], v[216:219], a[28:31]\n"
"s_mov_b32 s55, 90112\n"
"v_cvt_pk_bf16_f32 v96, v96, v97\n"
"v_cvt_pk_bf16_f32 v97, v98, v99\n"
"v_mfma_f32_16x16x32_bf16 a[32:35], a[228:231], v[224:227], a[32:35]\n"
"s_add_u32 s64, s64, 8192\n"
"v_exp_f32 v106, v106\n"
"v_mfma_f32_16x16x32_bf16 a[36:39], a[228:231], v[232:235], a[36:39]\n"
"buffer_load_dwordx4 v197, s[28:31], s55 offen offset:2048 nt lds\n"
"v_mfma_f32_16x16x32_bf16 a[40:43], a[228:231], v[240:243], a[40:43]\n"
"s_add_u32 s72, s72, 112\n"
"v_exp_f32 v107, v107\n"
"v_mfma_f32_16x16x32_bf16 a[44:47], a[228:231], v[248:251], a[44:47]\n"
"v_exp_f32 v108, v108\n"
"v_mfma_f32_16x16x32_bf16 a[48:51], a[232:235], v[208:211], a[48:51]\n"
"v_exp_f32 v109, v109\n"
"v_mfma_f32_16x16x32_bf16 a[52:55], a[232:235], v[216:219], a[52:55]\n"
"v_exp_f32 v110, v110\n"
"v_mfma_f32_16x16x32_bf16 a[56:59], a[232:235], v[224:227], a[56:59]\n"
"v_exp_f32 v111, v111\n"
"v_mfma_f32_16x16x32_bf16 a[60:63], a[232:235], v[232:235], a[60:63]\n"
"v_exp_f32 v112, v112\n"
"v_mfma_f32_16x16x32_bf16 a[64:67], a[232:235], v[240:243], a[64:67]\n"
"v_exp_f32 v113, v113\n"
"v_mfma_f32_16x16x32_bf16 a[68:71], a[232:235], v[248:251], a[68:71]\n"
"v_or_b32 v198, v96, v97\n"
"v_cvt_pk_bf16_f32 v98, v100, v101\n"
"v_mfma_f32_16x16x32_bf16 a[72:75], a[236:239], v[208:211], a[72:75]\n"
"v_cvt_pk_bf16_f32 v99, v102, v103\n"
"v_cvt_pk_bf16_f32 v104, v104, v105\n"
"v_mfma_f32_16x16x32_bf16 a[76:79], a[236:239], v[216:219], a[76:79]\n"
"v_exp_f32 v114, v114\n"
"v_mfma_f32_16x16x32_bf16 a[80:83], a[236:239], v[224:227], a[80:83]\n"
"v_exp_f32 v115, v115\n"
"v_mfma_f32_16x16x32_bf16 a[84:87], a[236:239], v[232:235], a[84:87]\n"
"v_or3_b32 v198, v198, v98, v99\n"
"v_cvt_pk_bf16_f32 v105, v106, v107\n"
"v_mfma_f32_16x16x32_bf16 a[88:91], a[236:239], v[240:243], a[88:91]\n"
"v_exp_f32 v116, v116\n"
"v_mfma_f32_16x16x32_bf16 a[92:95], a[236:239], v[248:251], a[92:95]\n"
"v_exp_f32 v117, v117\n"
"v_mfma_f32_16x16x32_bf16 a[96:99], a[240:243], v[208:211], a[96:99]\n"
"s_memrealtime s[70:71]\n"
"v_or3_b32 v198, v198, v104, v105\n"
"v_cvt_pk_bf16_f32 v106, v108, v109\n"
"v_mfma_f32_16x16x32_bf16 a[100:103], a[240:243], v[216:219], a[100:103]\n"
"v_cvt_pk_bf16_f32 v107, v110, v111\n"
"v_cvt_pk_bf16_f32 v112, v112, v113\n"
"v_mfma_f32_16x16x32_bf16 a[104:107], a[240:243], v[224:227], a[104:107]\n"
"v_or3_b32 v198, v198, v106, v107\n"
"v_cvt_pk_bf16_f32 v113, v114, v115\n"
"v_mfma_f32_16x16x32_bf16 a[108:111], a[240:243], v[232:235], a[108:111]\n"
"v_or3_b32 v198, v198, v112, v113\n"
"v_cvt_pk_bf16_f32 v114, v116, v117\n"
"v_mfma_f32_16x16x32_bf16 a[112:115], a[240:243], v[240:243], a[112:115]\n"
"v_exp_f32 v118, v118\n"
"v_mfma_f32_16x16x32_bf16 a[116:119], a[240:243], v[248:251], a[116:119]\n"
"v_exp_f32 v119, v119\n"
"v_mfma_f32_16x16x32_bf16 a[120:123], a[244:247], v[208:211], a[120:123]\n"
"v_mfma_f32_16x16x32_bf16 a[124:127], a[244:247], v[216:219], a[124:127]\n"
"v_mfma_f32_16x16x32_bf16 a[128:131], a[244:247], v[224:227], a[128:131]\n"
"v_mfma_f32_16x16x32_bf16 a[132:135], a[244:247], v[232:235], a[132:135]\n"
"v_mfma_f32_16x16x32_bf16 a[136:139], a[244:247], v[240:243], a[136:139]\n"
"v_mfma_f32_16x16x32_bf16 a[140:143], a[244:247], v[248:251], a[140:143]\n"
"s_waitcnt vmcnt(14) lgkmcnt(0)\n"
"s_sub_u32 s71, s72, s70\n"
"s_sub_u32 s71, s71, 16\n"
"s_cmp_lt_u32 s71, 400\n"
"s_cbranch_scc1 .Lpg8\n"
".Lpg8x:\n"
"s_barrier\n"
"v_mfma_f32_16x16x32_bf16 a[144:147], a[248:251], v[208:211], a[144:147]\n"
"s_add_u32 m0, s73, 53248\n"
"ds_read_b128 a[192:195], v192\n"
"v_cvt_pk_bf16_f32 v115, v118, v119\n"
"v_mfma_f32_16x16x32_bf16 a[148:151], a[248:251], v[216:219], a[148:151]\n"
"s_mov_b32 s55, 94208\n"
"ds_read_b128 a[196:199], v192 offset:4096\n"
"v_or3_b32 v198, v198, v114, v115\n"
"v_mfma_f32_16x16x32_bf16 a[152:155], a[248:251], v[224:227], a[152:155]\n"
"ds_read_b128 a[200:203], v192 offset:256\n"
"v_mfma_f32_16x16x32_bf16 a[156:159], a[248:251], v[232:235], a[156:159]\n"
"buffer_load_dwordx4 v197, s[28:31], s55 offen nt lds\n"
"v_mfma_f32_16x16x32_bf16 a[160:163], a[248:251], v[240:243], a[160:163]\n"
"ds_read_b128 a[204:207], v192 offset:4352\n"
"v_mfma_f32_16x16x32_bf16 a[164:167], a[248:251], v[248:251], a[164:167]\n"
"ds_read_b128 a[208:211], v192 offset:512\n"
"v_mfma_f32_16x16x32_bf16 a[168:171], a[252:255], v[208:211], a[168:171]\n"
"ds_read_b128 a[212:215], v192 offset:4608\n"
"v_mfma_f32_16x16x32_bf16 a[172:175], a[252:255], v[216:219], a[172:175]\n"
"ds_read_b128 a[216:219], v192 offset:768\n"
"v_mfma_f32_16x16x32_bf16 a[176:179], a[252:255], v[224:227], a[176:179]\n"
"ds_read_b128 a[220:223], v192 offset:4864\n"
"v_add_u32 v192, 32768, v192\n"
"v_mfma_f32_16x16x32_bf16 a[180:183], a[252:255], v[232:235], a[180:183]\n"
"v_exp_f32 v120, v120\n"
"v_mfma_f32_16x16x32_bf16 a[184:187], a[252:255], v[240:243], a[184:187]\n"
"v_exp_f32 v121, v121\n"
"v_mfma_f32_16x16x32_bf16 a[188:191], a[252:255], v[248:251], a[188:191]\n"
"s_waitcnt lgkmcnt(5)\n"
"v_exp_f32 v122, v122\n"
"v_mfma_f32_16x16x32_bf16 v[208:211], a[192:195], v[0:3], v[144:147]\n"
"v_exp_f32 v123, v123\n"
"v_mfma_f32_16x16x32_bf16 v[216:219], a[192:195], v[4:7], v[148:151]\n"
"v_exp_f32 v124, v124\n"
"v_mfma_f32_16x16x32_bf16 v[224:227], a[192:195], v[8:11], v[152:155]\n"
"s_add_u32 m0, s73, 53248\n"
"ds_read_b64_tr_b16 a[224:225], v193\n"
"v_cvt_pk_bf16_f32 v120, v120, v121\n"
"v_mfma_f32_16x16x32_bf16 v[232:235], a[192:195], v[12:15], v[156:159]\n"
"s_mov_b32 s55, 94208\n"
"v_exp_f32 v125, v125\n"
"v_mfma_f32_16x16x32_bf16 v[240:243], a[192:195], v[16:19], v[160:163]\n"
"v_exp_f32 v126, v126\n"
"v_mfma_f32_16x16x32_bf16 v[248:251], a[192:195], v[20:23], v[164:167]\n"
"buffer_load_dwordx4 v197, s[28:31], s55 offen offset:2048 nt lds\n"
"v_mfma_f32_16x16x32_bf16 v[212:215], a[196:199], v[0:3], v[144:147]\n"
"ds_read_b64_tr_b16 a[226:227], v193 offset:4096\n"
"v_cvt_pk_bf16_f32 v121, v122, v123\n"
"v_mfma_f32_16x16x32_bf16 v[220:223], a[196:199], v[4:7], v[148:151]\n"
"v_exp_f32 v127, v127\n"
"v_mfma_f32_16x16x32_bf16 v[228:231], a[196:199], v[8:11], v[152:155]\n"
"v_exp_f32 v128, v128\n"
"v_mfma_f32_16x16x32_bf16 v[236:239], a[196:199], v[12:15], v[156:159]\n"
"v_exp_f32 v129, v129\n"
"v_mfma_f32_16x16x32_bf16 v[244:247], a[196:199], v[16:19], v[160:163]\n"
"v_exp_f32 v130, v130\n"
"v_mfma_f32_16x16x32_bf16 v[252:255], a[196:199], v[20:23], v[164:167]\n"
"v_exp_f32 v131, v131\n"
"v_mfma_f32_16x16x32_bf16 v[208:211], a[200:203], v[24:27], v[208:211]\n"
"v_exp_f32 v132, v132\n"
"v_mfma_f32_16x16x32_bf16 v[216:219], a[200:203], v[28:31], v[216:219]\n"
"v_exp_f32 v133, v133\n"
"v_mfma_f32_16x16x32_bf16 v[224:227], a[200:203], v[32:35], v[224:227]\n"
"v_exp_f32 v134, v134\n"
"v_mfma_f32_16x16x32_bf16 v[232:235], a[200:203], v[36:39], v[232:235]\n"
"v_exp_f32 v135, v135\n"
"v_mfma_f32_16x16x32_bf16 v[240:243], a[200:203], v[40:43], v[240:243]\n"
"s_add_u32 m0, s73, 98304\n"
"v_exp_f32 v136, v136\n"
"v_mfma_f32_16x16x32_bf16 v[248:251], a[200:203], v[44:47], v[248:251]\n"
"s_waitcnt lgkmcnt(0)\n"
"v_exp_f32 v137, v137\n"
"v_mfma_f32_16x16x32_bf16 v[212:215], a[204:207], v[24:27], v[212:215]\n"
"s_mov_b32 s55, 106496\n"
"v_or3_b32 v198, v198, v120, v121\n"
"v_cvt_pk_bf16_f32 v122, v124, v125\n"
"v_mfma_f32_16x16x32_bf16 v[220:223], a[204:207], v[28:31], v[220:223]\n"
"buffer_load_dwordx4 v195, s[24:27], s55 offen nt lds\n"
"v_mfma_f32_16x16x32_bf16 v[228:231], a[204:207], v[32:35], v[228:231]\n"
"v_cvt_pk_bf16_f32 v123, v126, v127\n"
"v_cvt_pk_bf16_f32 v128, v128, v129\n"
"v_mfma_f32_16x16x32_bf16 v[236:239], a[204:207], v[36:39], v[236:239]\n"
"v_exp_f32 v138, v138\n"
"v_mfma_f32_16x16x32_bf16 v[244:247], a[204:207], v[40:43], v[244:247]\n"
"v_exp_f32 v139, v139\n"
"v_mfma_f32_16x16x32_bf16 v[252:255], a[204:207], v[44:47], v[252:255]\n"
"v_or3_b32 v198, v198, v122, v123\n"
"v_cvt_pk_bf16_f32 v129, v130, v131\n"
"v_mfma_f32_16x16x32_bf16 v[208:211], a[208:211], v[48:51], v[208:211]\n"
"v_exp_f32 v140, v140\n"
"v_mfma_f32_16x16x32_bf16 v[216:219], a[208:211], v[52:55], v[216:219]\n"
"v_exp_f32 v141, v141\n"
"v_mfma_f32_16x16x32_bf16 v[224:227], a[208:211], v[56:59], v[224:227]\n"
"v_exp_f32 v142, v142\n"
"v_mfma_f32_16x16x32_bf16 v[232:235], a[208:211], v[60:63], v[232:235]\n"
"v_exp_f32 v143, v143\n"
"v_mfma_f32_16x16x32_bf16 v[240:243], a[208:211], v[64:67], v[240:243]\n"
"v_or3_b32 v198, v198, v128, v129\n"
"v_cvt_pk_bf16_f32 v130, v132, v133\n"
"v_mfma_f32_16x16x32_bf16 v[248:251], a[208:211], v[68:71], v[248:251]\n"
"v_cvt_pk_bf16_f32 v131, v134, v135\n"
"v_cvt_pk_bf16_f32 v136, v136, v137\n"
"v_mfma_f32_16x16x32_bf16 v[212:215], a[212:215], v[48:51], v[212:215]\n"
"s_add_u32 m0, s73, 98304\n"
"v_or3_b32 v198, v198, v130, v131\n"
"v_cvt_pk_bf16_f32 v137, v138, v139\n"
"v_mfma_f32_16x16x32_bf16 v[220:223], a[212:215], v[52:55], v[220:223]\n"
"s_mov_b32 s55, 106496\n"
"v_or3_b32 v198, v198, v136, v137\n"
"v_cvt_pk_bf16_f32 v138, v140, v141\n"
"v_mfma_f32_16x16x32_bf16 v[228:231], a[212:215], v[56:59], v[228:231]\n"
"v_cvt_pk_bf16_f32 v139, v142, v143\n"
"v_or3_b32 v198, v198, v138, v139\n"
"v_mfma_f32_16x16x32_bf16 v[236:239], a[212:215], v[60:63], v[236:239]\n"
"buffer_load_dwordx4 v196, s[24:27], s55 offen offset:2048 nt lds\n"
"v_mfma_f32_16x16x32_bf16 v[244:247], a[212:215], v[64:67], v[244:247]\n"
"v_and_b32 v199, 0x40004000, v198\n"
"v_cmp_ne_u32 vcc, 0, v199\n"
"v_mfma_f32_16x16x32_bf16 v[252:255], a[212:215], v[68:71], v[252:255]\n"
"ds_read_b64_tr_b16 a[228:229], v194\n"
"v_mfma_f32_16x16x32_bf16 v[208:211], a[216:219], v[72:75], v[208:211]\n"
"ds_read_b64_tr_b16 a[230:231], v194 offset:4096\n"
"v_mfma_f32_16x16x32_bf16 v[216:219], a[216:219], v[76:79], v[216:219]\n"
"ds_read_b64_tr_b16 a[232:233], v193 offset:256\n"
"v_mfma_f32_16x16x32_bf16 v[224:227], a[216:219], v[80:83], v[224:227]\n"
"ds_read_b64_tr_b16 a[234:235], v193 offset:4352\n"
"v_mfma_f32_16x16x32_bf16 v[232:235], a[216:219], v[84:87], v[232:235]\n"
"ds_read_b64_tr_b16 a[236:237], v194 offset:256\n"
"v_mfma_f32_16x16x32_bf16 v[240:243], a[216:219], v[88:91], v[240:243]\n"
"ds_read_b64_tr_b16 a[238:239], v194 offset:4352\n"
"v_mfma_f32_16x16x32_bf16 v[248:251], a[216:219], v[92:95], v[248:251]\n"
"ds_read_b64_tr_b16 a[240:241], v193 offset:512\n"
"v_mfma_f32_16x16x32_bf16 v[212:215], a[220:223], v[72:75], v[212:215]\n"
"ds_read_b64_tr_b16 a[242:243], v193 offset:4608\n"
"v_mfma_f32_16x16x32_bf16 v[220:223], a[220:223], v[76:79], v[220:223]\n"
"ds_read_b64_tr_b16 a[244:245], v194 offset:512\n"
"v_mfma_f32_16x16x32_bf16 v[228:231], a[220:223], v[80:83], v[228:231]\n"
"s_add_u32 m0, s73, 102400\n"
"ds_read_b64_tr_b16 a[246:247], v194 offset:4608\n"
"s_mov_b32 s55, 110592\n"
"v_mfma_f32_16x16x32_bf16 v[236:239], a[220:223], v[84:87], v[236:239]\n"
"ds_read_b64_tr_b16 a[248:249], v193 offset:768\n"
"v_mfma_f32_16x16x32_bf16 v[244:247], a[220:223], v[88:91], v[244:247]\n"
"ds_read_b64_tr_b16 a[250:251], v193 offset:4864\n"
"v_add_u32 v193, 0xfffe0000, v193\n"
"v_mfma_f32_16x16x32_bf16 v[252:255], a[220:223], v[92:95], v[252:255]\n"
"s_cbranch_vccnz .Lrare11\n"
".Lrare11_ret:\n"
"ds_read_b64_tr_b16 a[252:253], v194 offset:768\n"
"v_mfma_f32_16x16x32_bf16 v[168:171], v[204:207], v[96:99], v[168:171]\n"
"buffer_load_dwordx4 v195, s[24:27], s55 offen nt lds\n"
"v_mfma_f32_16x16x32_bf16 v[172:175], v[204:207], v[104:107], v[172:175]\n"
"ds_read_b64_tr_b16 a[254:255], v194 offset:4864\n"
"v_add_u32 v194, 0xfffe0000, v194\n"
"v_mfma_f32_16x16x32_bf16 v[176:179], v[204:207], v[112:115], v[176:179]\n"
"v_exp_f32 v208, v208\n"
"v_mfma_f32_16x16x32_bf16 v[180:183], v[204:207], v[120:123], v[180:183]\n"
"v_exp_f32 v209, v209\n"
"v_mfma_f32_16x16x32_bf16 v[184:187], v[204:207], v[128:131], v[184:187]\n"
"v_exp_f32 v210, v210\n"
"v_mfma_f32_16x16x32_bf16 v[188:191], v[204:207], v[136:139], v[188:191]\n"
"v_exp_f32 v211, v211\n"
"v_mfma_f32_16x16x32_bf16 a[0:3], a[224:227], v[96:99], a[0:3]\n"
"v_exp_f32 v212, v212\n"
"v_mfma_f32_16x16x32_bf16 a[4:7], a[224:227], v[104:107], a[4:7]\n"
"v_exp_f32 v213, v213\n"
"v_mfma_f32_16x16x32_bf16 a[8:11], a[224:227], v[112:115], a[8:11]\n"
"v_exp_f32 v214, v214\n"
"v_mfma_f32_16x16x32_bf16 a[12:15], a[224:227], v[120:123], a[12:15]\n"
"v_exp_f32 v215, v215\n"
"v_mfma_f32_16x16x32_bf16 a[16:19], a[224:227], v[128:131], a[16:19]\n"
"s_add_u32 m0, s73, 102400\n"
"v_cvt_pk_bf16_f32 v208, v208, v209\n"
"v_mfma_f32_16x16x32_bf16 a[20:23], a[224:227], v[136:139], a[20:23]\n"
"s_waitcnt lgkmcnt(0)\n"
"v_exp_f32 v216, v216\n"
"v_mfma_f32_16x16x32_bf16 a[24:27], a[228:231], v[96:99], a[24:27]\n"
"s_mov_b32 s55, 110592\n"
"v_exp_f32 v217, v217\n"
"v_mfma_f32_16x16x32_bf16 a[28:31], a[228:231], v[104:107], a[28:31]\n"
"buffer_load_dwordx4 v196, s[24:27], s55 offen offset:2048 nt lds\n"
"v_mfma_f32_16x16x32_bf16 a[32:35], a[228:231], v[112:115], a[32:35]\n"
"s_add_u32 s64, s64, 8192\n"
"v_exp_f32 v218, v218\n"
"v_mfma_f32_16x16x32_bf16 a[36:39], a[228:231], v[120:123], a[36:39]\n"
"s_add_u32 s72, s72, 112\n"
"v_exp_f32 v219, v219\n"
"v_mfma_f32_16x16x32_bf16 a[40:43], a[228:231], v[128:131], a[40:43]\n"
"v_cvt_pk_bf16_f32 v209, v210, v211\n"
"v_or_b32 v198, v208, v209\n"
"v_mfma_f32_16x16x32_bf16 a[44:47], a[228:231], v[136:139], a[44:47]\n"
"v_exp_f32 v220, v220\n"
"v_mfma_f32_16x16x32_bf16 a[48:51], a[232:235], v[96:99], a[48:51]\n"
"v_exp_f32 v221, v221\n"
"v_mfma_f32_16x16x32_bf16 a[52:55], a[232:235], v[104:107], a[52:55]\n"
"v_exp_f32 v222, v222\n"
"v_mfma_f32_16x16x32_bf16 a[56:59], a[232:235], v[112:115], a[56:59]\n"
"v_exp_f32 v223, v223\n"
"v_mfma_f32_16x16x32_bf16 a[60:63], a[232:235], v[120:123], a[60:63]\n"
"v_exp_f32 v224, v224\n"
"v_mfma_f32_16x16x32_bf16 a[64:67], a[232:235], v[128:131], a[64:67]\n"
"v_exp_f32 v225, v225\n"
"v_mfma_f32_16x16x32_bf16 a[68:71], a[232:235], v[136:139], a[68:71]\n"
"v_cvt_pk_bf16_f32 v210, v212, v213\n"
"v_cvt_pk_bf16_f32 v211, v214, v215\n"
"v_mfma_f32_16x16x32_bf16 a[72:75], a[236:239], v[96:99], a[72:75]\n"
"v_cvt_pk_bf16_f32 v216, v216, v217\n"
"v_or3_b32 v198, v198, v210, v211\n"
"v_mfma_f32_16x16x32_bf16 a[76:79], a[236:239], v[104:107], a[76:79]\n"
"v_exp_f32 v226, v226\n"
"v_mfma_f32_16x16x32_bf16 a[80:83], a[236:239], v[112:115], a[80:83]\n"
"v_exp_f32 v227, v227\n"
"v_mfma_f32_16x16x32_bf16 a[84:87], a[236:239], v[120:123], a[84:87]\n"
"v_cvt_pk_bf16_f32 v217, v218, v219\n"
"v_or3_b32 v198, v198, v216, v217\n"
"v_mfma_f32_16x16x32_bf16 a[88:91], a[236:239], v[128:131], a[88:91]\n"
"v_exp_f32 v228, v228\n"
"v_mfma_f32_16x16x32_bf16 a[92:95], a[236:239], v[136:139], a[92:95]\n"
"v_exp_f32 v229, v229\n"
"v_mfma_f32_16x16x32_bf16 a[96:99], a[240:243], v[96:99], a[96:99]\n"
"s_memrealtime s[70:71]\n"
"v_cvt_pk_bf16_f32 v218, v220, v221\n"
"v_cvt_pk_bf16_f32 v219, v222, v223\n"
"v_mfma_f32_16x16x32_bf16 a[100:103], a[240:243], v[104:107], a[100:103]\n"
"v_cvt_pk_bf16_f32 v224, v224, v225\n"
"v_or3_b32 v198, v198, v218, v219\n"
"v_mfma_f32_16x16x32_bf16 a[104:107], a[240:243], v[112:115], a[104:107]\n"
"v_cvt_pk_bf16_f32 v225, v226, v227\n"
"v_or3_b32 v198, v198, v224, v225\n"
"v_mfma_f32_16x16x32_bf16 a[108:111], a[240:243], v[120:123], a[108:111]\n"
"v_exp_f32 v230, v230\n"
"v_mfma_f32_16x16x32_bf16 a[112:115], a[240:243], v[128:131], a[112:115]\n"
"v_exp_f32 v231, v231\n"
"v_mfma_f32_16x16x32_bf16 a[116:119], a[240:243], v[136:139], a[116:119]\n"
"v_cvt_pk_bf16_f32 v226, v228, v229\n"
"v_mfma_f32_16x16x32_bf16 a[120:123], a[244:247], v[96:99], a[120:123]\n"
"v_mfma_f32_16x16x32_bf16 a[124:127], a[244:247], v[104:107], a[124:127]\n"
"v_mfma_f32_16x16x32_bf16 a[128:131], a[244:247], v[112:115], a[128:131]\n"
"v_mfma_f32_16x16x32_bf16 a[132:135], a[244:247], v[120:123], a[132:135]\n"
"v_mfma_f32_16x16x32_bf16 a[136:139], a[244:247], v[128:131], a[136:139]\n"
"v_mfma_f32_16x16x32_bf16 a[140:143], a[244:247], v[136:139], a[140:143]\n"
"s_waitcnt vmcnt(12) lgkmcnt(0)\n"
"s_sub_u32 s71, s72, s70\n"
"s_sub_u32 s71, s71, 16\n"
"s_cmp_lt_u32 s71, 400\n"
"s_cbranch_scc1 .Lpg9\n"
".Lpg9x:\n"
"s_barrier\n"
"v_mfma_f32_16x16x32_bf16 a[144:147], a[248:251], v[96:99], a[144:147]\n"
"s_add_u32 m0, s73, 81920\n"
"ds_read_b128 a[192:195], v192\n"
"v_cvt_pk_bf16_f32 v227, v230, v231\n"
"v_mfma_f32_16x16x32_bf16 a[148:151], a[248:251], v[104:107], a[148:151]\n"
"s_mov_b32 s55, 98304\n"
"ds_read_b128 a[196:199], v192 offset:4096\n"
"v_or3_b32 v198, v198, v226, v227\n"
"v_mfma_f32_16x16x32_bf16 a[152:155], a[248:251], v[112:115], a[152:155]\n"
"ds_read_b128 a[200:203], v192 offset:256\n"
"v_mfma_f32_16x16x32_bf16 a[156:159], a[248:251], v[120:123], a[156:159]\n"
"buffer_load_dwordx4 v197, s[28:31], s55 offen nt lds\n"
"v_mfma_f32_16x16x32_bf16 a[160:163], a[248:251], v[128:131], a[160:163]\n"
"ds_read_b128 a[204:207], v192 offset:4352\n"
"v_mfma_f32_16x16x32_bf16 a[164:167], a[248:251], v[136:139], a[164:167]\n"
"ds_read_b128 a[208:211], v192 offset:512\n"
"v_mfma_f32_16x16x32_bf16 a[168:171], a[252:255], v[96:99], a[168:171]\n"
"ds_read_b128 a[212:215], v192 offset:4608\n"
"v_mfma_f32_16x16x32_bf16 a[172:175], a[252:255], v[104:107], a[172:175]\n"
"ds_read_b128 a[216:219], v192 offset:768\n"
"v_mfma_f32_16x16x32_bf16 a[176:179], a[252:255], v[112:115], a[176:179]\n"
"ds_read_b128 a[220:223], v192 offset:4864\n"
"v_add_u32 v192, 32768, v192\n"
"v_mfma_f32_16x16x32_bf16 a[180:183], a[252:255], v[120:123], a[180:183]\n"
"v_exp_f32 v232, v232\n"
"v_mfma_f32_16x16x32_bf16 a[184:187], a[252:255], v[128:131], a[184:187]\n"
"v_exp_f32 v233, v233\n"
"v_mfma_f32_16x16x32_bf16 a[188:191], a[252:255], v[136:139], a[188:191]\n"
"s_waitcnt lgkmcnt(5)\n"
"v_exp_f32 v234, v234\n"
"v_mfma_f32_16x16x32_bf16 v[96:99], a[192:195], v[0:3], v[144:147]\n"
"v_exp_f32 v235, v235\n"
"v_mfma_f32_16x16x32_bf16 v[104:107], a[192:195], v[4:7], v[148:151]\n"
"v_exp_f32 v236, v236\n"
"v_mfma_f32_16x16x32_bf16 v[112:115], a[192:195], v[8:11], v[152:155]\n"
"s_add_u32 m0, s73, 81920\n"
"ds_read_b64_tr_b16 a[224:225], v193\n"
"v_cvt_pk_bf16_f32 v232, v232, v233\n"
"v_mfma_f32_16x16x32_bf16 v[120:123], a[192:195], v[12:15], v[156:159]\n"
"s_mov_b32 s55, 98304\n"
"v_exp_f32 v237, v237\n"
"v_mfma_f32_16x16x32_bf16 v[128:131], a[192:195], v[16:19], v[160:163]\n"
"v_exp_f32 v238, v238\n"
"v_mfma_f32_16x16x32_bf16 v[136:139], a[192:195], v[20:23], v[164:167]\n"
"buffer_load_dwordx4 v197, s[28:31], s55 offen offset:2048 nt lds\n"
"v_mfma_f32_16x16x32_bf16 v[100:103], a[196:199], v[0:3], v[144:147]\n"
"ds_read_b64_tr_b16 a[226:227], v193 offset:4096\n"
"v_cvt_pk_bf16_f32 v233, v234, v235\n"
"v_mfma_f32_16x16x32_bf16 v[108:111], a[196:199], v[4:7], v[148:151]\n"
"v_exp_f32 v239, v239\n"
"v_mfma_f32_16x16x32_bf16 v[116:119], a[196:199], v[8:11], v[152:155]\n"
"v_exp_f32 v240, v240\n"
"v_mfma_f32_16x16x32_bf16 v[124:127], a[196:199], v[12:15], v[156:159]\n"
"v_exp_f32 v241, v241\n"
"v_mfma_f32_16x16x32_bf16 v[132:135], a[196:199], v[16:19], v[160:163]\n"
"v_exp_f32 v242, v242\n"
"v_mfma_f32_16x16x32_bf16 v[140:143], a[196:199], v[20:23], v[164:167]\n"
"v_exp_f32 v243, v243\n"
"v_mfma_f32_16x16x32_bf16 v[96:99], a[200:203], v[24:27], v[96:99]\n"
"v_exp_f32 v244, v244\n"
"v_mfma_f32_16x16x32_bf16 v[104:107], a[200:203], v[28:31], v[104:107]\n"
"v_exp_f32 v245, v245\n"
"v_mfma_f32_16x16x32_bf16 v[112:115], a[200:203], v[32:35], v[112:115]\n"
"v_exp_f32 v246, v246\n"
"v_mfma_f32_16x16x32_bf16 v[120:123], a[200:203], v[36:39], v[120:123]\n"
"v_exp_f32 v247, v247\n"
"v_mfma_f32_16x16x32_bf16 v[128:131], a[200:203], v[40:43], v[128:131]\n"
"s_add_u32 m0, s73, 86016\n"
"v_exp_f32 v248, v248\n"
"v_mfma_f32_16x16x32_bf16 v[136:139], a[200:203], v[44:47], v[136:139]\n"
"s_waitcnt lgkmcnt(0)\n"
"v_exp_f32 v249, v249\n"
"v_mfma_f32_16x16x32_bf16 v[100:103], a[204:207], v[24:27], v[100:103]\n"
"s_mov_b32 s55, 102400\n"
"v_or3_b32 v198, v198, v232, v233\n"
"v_cvt_pk_bf16_f32 v234, v236, v237\n"
"v_mfma_f32_16x16x32_bf16 v[108:111], a[204:207], v[28:31], v[108:111]\n"
"buffer_load_dwordx4 v197, s[28:31], s55 offen nt lds\n"
"v_mfma_f32_16x16x32_bf16 v[116:119], a[204:207], v[32:35], v[116:119]\n"
"v_cvt_pk_bf16_f32 v235, v238, v239\n"
"v_cvt_pk_bf16_f32 v240, v240, v241\n"
"v_mfma_f32_16x16x32_bf16 v[124:127], a[204:207], v[36:39], v[124:127]\n"
"v_exp_f32 v250, v250\n"
"v_mfma_f32_16x16x32_bf16 v[132:135], a[204:207], v[40:43], v[132:135]\n"
"v_exp_f32 v251, v251\n"
"v_mfma_f32_16x16x32_bf16 v[140:143], a[204:207], v[44:47], v[140:143]\n"
"v_or3_b32 v198, v198, v234, v235\n"
"v_cvt_pk_bf16_f32 v241, v242, v243\n"
"v_mfma_f32_16x16x32_bf16 v[96:99], a[208:211], v[48:51], v[96:99]\n"
"v_exp_f32 v252, v252\n"
"v_mfma_f32_16x16x32_bf16 v[104:107], a[208:211], v[52:55], v[104:107]\n"
"v_exp_f32 v253, v253\n"
"v_mfma_f32_16x16x32_bf16 v[112:115], a[208:211], v[56:59], v[112:115]\n"
"v_exp_f32 v254, v254\n"
"v_mfma_f32_16x16x32_bf16 v[120:123], a[208:211], v[60:63], v[120:123]\n"
"v_exp_f32 v255, v255\n"
"v_mfma_f32_16x16x32_bf16 v[128:131], a[208:211], v[64:67], v[128:131]\n"
"v_or3_b32 v198, v198, v240, v241\n"
"v_cvt_pk_bf16_f32 v242, v244, v245\n"
"v_mfma_f32_16x16x32_bf16 v[136:139], a[208:211], v[68:71], v[136:139]\n"
"v_cvt_pk_bf16_f32 v243, v246, v247\n"
"v_cvt_pk_bf16_f32 v248, v248, v249\n"
"v_mfma_f32_16x16x32_bf16 v[100:103], a[212:215], v[48:51], v[100:103]\n"
"s_add_u32 m0, s73, 86016\n"
"v_or3_b32 v198, v198, v242, v243\n"
"v_cvt_pk_bf16_f32 v249, v250, v251\n"
"v_mfma_f32_16x16x32_bf16 v[108:111], a[212:215], v[52:55], v[108:111]\n"
"s_mov_b32 s55, 102400\n"
"v_or3_b32 v198, v198, v248, v249\n"
"v_cvt_pk_bf16_f32 v250, v252, v253\n"
"v_mfma_f32_16x16x32_bf16 v[116:119], a[212:215], v[56:59], v[116:119]\n"
"v_cvt_pk_bf16_f32 v251, v254, v255\n"
"v_or3_b32 v198, v198, v250, v251\n"
"v_mfma_f32_16x16x32_bf16 v[124:127], a[212:215], v[60:63], v[124:127]\n"
"buffer_load_dwordx4 v197, s[28:31], s55 offen offset:2048 nt lds\n"
"v_mfma_f32_16x16x32_bf16 v[132:135], a[212:215], v[64:67], v[132:135]\n"
"v_and_b32 v199, 0x40004000, v198\n"
"v_cmp_ne_u32 vcc, 0, v199\n"
"v_mfma_f32_16x16x32_bf16 v[140:143], a[212:215], v[68:71], v[140:143]\n"
"ds_read_b64_tr_b16 a[228:229], v194\n"
"v_mfma_f32_16x16x32_bf16 v[96:99], a[216:219], v[72:75], v[96:99]\n"
"ds_read_b64_tr_b16 a[230:231], v194 offset:4096\n"
"v_mfma_f32_16x16x32_bf16 v[104:107], a[216:219], v[76:79], v[104:107]\n"
"ds_read_b64_tr_b16 a[232:233], v193 offset:256\n"
"v_mfma_f32_16x16x32_bf16 v[112:115], a[216:219], v[80:83], v[112:115]\n"
"ds_read_b64_tr_b16 a[234:235], v193 offset:4352\n"
"v_mfma_f32_16x16x32_bf16 v[120:123], a[216:219], v[84:87], v[120:123]\n"
"ds_read_b64_tr_b16 a[236:237], v194 offset:256\n"
"v_mfma_f32_16x16x32_bf16 v[128:131], a[216:219], v[88:91], v[128:131]\n"
"ds_read_b64_tr_b16 a[238:239], v194 offset:4352\n"
"v_mfma_f32_16x16x32_bf16 v[136:139], a[216:219], v[92:95], v[136:139]\n"
"ds_read_b64_tr_b16 a[240:241], v193 offset:512\n"
"v_mfma_f32_16x16x32_bf16 v[100:103], a[220:223], v[72:75], v[100:103]\n"
"ds_read_b64_tr_b16 a[242:243], v193 offset:4608\n"
"v_mfma_f32_16x16x32_bf16 v[108:111], a[220:223], v[76:79], v[108:111]\n"
"ds_read_b64_tr_b16 a[244:245], v194 offset:512\n"
"v_mfma_f32_16x16x32_bf16 v[116:119], a[220:223], v[80:83], v[116:119]\n"
"s_add_u32 m0, s73, 131072\n"
"ds_read_b64_tr_b16 a[246:247], v194 offset:4608\n"
"s_mov_b32 s55, 114688\n"
"v_mfma_f32_16x16x32_bf16 v[124:127], a[220:223], v[84:87], v[124:127]\n"
"ds_read_b64_tr_b16 a[248:249], v193 offset:768\n"
"v_mfma_f32_16x16x32_bf16 v[132:135], a[220:223], v[88:91], v[132:135]\n"
"ds_read_b64_tr_b16 a[250:251], v193 offset:4864\n"
"v_add_u32 v193, 32768, v193\n"
"v_mfma_f32_16x16x32_bf16 v[140:143], a[220:223], v[92:95], v[140:143]\n"
"s_cbranch_vccnz .Lrare12\n"
".Lrare12_ret:\n"
"ds_read_b64_tr_b16 a[252:253], v194 offset:768\n"
"v_mfma_f32_16x16x32_bf16 v[168:171], v[204:207], v[208:211], v[168:171]\n"
"buffer_load_dwordx4 v195, s[24:27], s55 offen nt lds\n"
"v_mfma_f32_16x16x32_bf16 v[172:175], v[204:207], v[216:219], v[172:175]\n"
"ds_read_b64_tr_b16 a[254:255], v194 offset:4864\n"
"v_add_u32 v194, 32768, v194\n"
"v_mfma_f32_16x16x32_bf16 v[176:179], v[204:207], v[224:227], v[176:179]\n"
"v_exp_f32 v96, v96\n"
"v_mfma_f32_16x16x32_bf16 v[180:183], v[204:207], v[232:235], v[180:183]\n"
"v_exp_f32 v97, v97\n"
"v_mfma_f32_16x16x32_bf16 v[184:187], v[204:207], v[240:243], v[184:187]\n"
"v_exp_f32 v98, v98\n"
"v_mfma_f32_16x16x32_bf16 v[188:191], v[204:207], v[248:251], v[188:191]\n"
"v_exp_f32 v99, v99\n"
"v_mfma_f32_16x16x32_bf16 a[0:3], a[224:227], v[208:211], a[0:3]\n"
"v_exp_f32 v100, v100\n"
"v_mfma_f32_16x16x32_bf16 a[4:7], a[224:227], v[216:219], a[4:7]\n"
"v_exp_f32 v101, v101\n"
"v_mfma_f32_16x16x32_bf16 a[8:11], a[224:227], v[224:227], a[8:11]\n"
"v_exp_f32 v102, v102\n"
"v_mfma_f32_16x16x32_bf16 a[12:15], a[224:227], v[232:235], a[12:15]\n"
"v_exp_f32 v103, v103\n"
"v_mfma_f32_16x16x32_bf16 a[16:19], a[224:227], v[240:243], a[16:19]\n"
"s_add_u32 m0, s73, 131072\n"
"v_cvt_pk_bf16_f32 v96, v96, v97\n"
"v_mfma_f32_16x16x32_bf16 a[20:23], a[224:227], v[248:251], a[20:23]\n"
"s_waitcnt lgkmcnt(0)\n"
"v_exp_f32 v104, v104\n"
"v_mfma_f32_16x16x32_bf16 a[24:27], a[228:231], v[208:211], a[24:27]\n"
"s_mov_b32 s55, 114688\n"
"v_exp_f32 v105, v105\n"
"v_mfma_f32_16x16x32_bf16 a[28:31], a[228:231], v[216:219], a[28:31]\n"
"buffer_load_dwordx4 v196, s[24:27], s55 offen offset:2048 nt lds\n"
"v_mfma_f32_16x16x32_bf16 a[32:35], a[228:231], v[224:227], a[32:35]\n"
"s_add_u32 s64, s64, 8192\n"
"v_exp_f32 v106, v106\n"
"v_mfma_f32_16x16x32_bf16 a[36:39], a[228:231], v[232:235], a[36:39]\n"
"s_add_u32 s72, s72, 112\n"
"v_exp_f32 v107, v107\n"
"v_mfma_f32_16x16x32_bf16 a[40:43], a[228:231], v[240:243], a[40:43]\n"
"v_cvt_pk_bf16_f32 v97, v98, v99\n"
"v_or_b32 v198, v96, v97\n"
"v_mfma_f32_16x16x32_bf16 a[44:47], a[228:231], v[248:251], a[44:47]\n"
"v_exp_f32 v108, v108\n"
"v_mfma_f32_16x16x32_bf16 a[48:51], a[232:235], v[208:211], a[48:51]\n"
"v_exp_f32 v109, v109\n"
"v_mfma_f32_16x16x32_bf16 a[52:55], a[232:235], v[216:219], a[52:55]\n"
"v_exp_f32 v110, v110\n"
"v_mfma_f32_16x16x32_bf16 a[56:59], a[232:235], v[224:227], a[56:59]\n"
"v_exp_f32 v111, v111\n"
"v_mfma_f32_16x16x32_bf16 a[60:63], a[232:235], v[232:235], a[60:63]\n"
"v_exp_f32 v112, v112\n"
"v_mfma_f32_16x16x32_bf16 a[64:67], a[232:235], v[240:243], a[64:67]\n"
"v_exp_f32 v113, v113\n"
"v_mfma_f32_16x16x32_bf16 a[68:71], a[232:235], v[248:251], a[68:71]\n"
"v_cvt_pk_bf16_f32 v98, v100, v101\n"
"v_cvt_pk_bf16_f32 v99, v102, v103\n"
"v_mfma_f32_16x16x32_bf16 a[72:75], a[236:239], v[208:211], a[72:75]\n"
"v_cvt_pk_bf16_f32 v104, v104, v105\n"
"v_or3_b32 v198, v198, v98, v99\n"
"v_mfma_f32_16x16x32_bf16 a[76:79], a[236:239], v[216:219], a[76:79]\n"
"v_exp_f32 v114, v114\n"
"v_mfma_f32_16x16x32_bf16 a[80:83], a[236:239], v[224:227], a[80:83]\n"
"v_exp_f32 v115, v115\n"
"v_mfma_f32_16x16x32_bf16 a[84:87], a[236:239], v[232:235], a[84:87]\n"
"v_cvt_pk_bf16_f32 v105, v106, v107\n"
"v_or3_b32 v198, v198, v104, v105\n"
"v_mfma_f32_16x16x32_bf16 a[88:91], a[236:239], v[240:243], a[88:91]\n"
"v_exp_f32 v116, v116\n"
"v_mfma_f32_16x16x32_bf16 a[92:95], a[236:239], v[248:251], a[92:95]\n"
"v_exp_f32 v117, v117\n"
"v_mfma_f32_16x16x32_bf16 a[96:99], a[240:243], v[208:211], a[96:99]\n"
"s_memrealtime s[70:71]\n"
"v_cvt_pk_bf16_f32 v106, v108, v109\n"
"v_cvt_pk_bf16_f32 v107, v110, v111\n"
"v_mfma_f32_16x16x32_bf16 a[100:103], a[240:243], v[216:219], a[100:103]\n"
"v_cvt_pk_bf16_f32 v112, v112, v113\n"
"v_or3_b32 v198, v198, v106, v107\n"
"v_mfma_f32_16x16x32_bf16 a[104:107], a[240:243], v[224:227], a[104:107]\n"
"v_cvt_pk_bf16_f32 v113, v114, v115\n"
"v_or3_b32 v198, v198, v112, v113\n"
"v_mfma_f32_16x16x32_bf16 a[108:111], a[240:243], v[232:235], a[108:111]\n"
"v_exp_f32 v118, v118\n"
"v_mfma_f32_16x16x32_bf16 a[112:115], a[240:243], v[240:243], a[112:115]\n"
"v_exp_f32 v119, v119\n"
"v_mfma_f32_16x16x32_bf16 a[116:119], a[240:243], v[248:251], a[116:119]\n"
"v_cvt_pk_bf16_f32 v114, v116, v117\n"
"v_mfma_f32_16x16x32_bf16 a[120:123], a[244:247], v[208:211], a[120:123]\n"
"v_mfma_f32_16x16x32_bf16 a[124:127], a[244:247], v[216:219], a[124:127]\n"
"v_mfma_f32_16x16x32_bf16 a[128:131], a[244:247], v[224:227], a[128:131]\n"
"v_mfma_f32_16x16x32_bf16 a[132:135], a[244:247], v[232:235], a[132:135]\n"
"v_mfma_f32_16x16x32_bf16 a[136:139], a[244:247], v[240:243], a[136:139]\n"
"v_mfma_f32_16x16x32_bf16 a[140:143], a[244:247], v[248:251], a[140:143]\n"
"s_waitcnt vmcnt(10) lgkmcnt(0)\n"
"s_sub_u32 s71, s72, s70\n"
"s_sub_u32 s71, s71, 16\n"
"s_cmp_lt_u32 s71, 400\n"
"s_cbranch_scc1 .Lpg10\n"
".Lpg10x:\n"
"s_barrier\n"
"v_mfma_f32_16x16x32_bf16 a[144:147], a[248:251], v[208:211], a[144:147]\n"
"s_add_u32 m0, s73, 135168\n"
"ds_read_b128 a[192:195], v192\n"
"v_cvt_pk_bf16_f32 v115, v118, v119\n"
"v_mfma_f32_16x16x32_bf16 a[148:151], a[248:251], v[216:219], a[148:151]\n"
"s_mov_b32 s55, 118784\n"
"ds_read_b128 a[196:199], v192 offset:4096\n"
"v_or3_b32 v198, v198, v114, v115\n"
"v_mfma_f32_16x16x32_bf16 a[152:155], a[248:251], v[224:227], a[152:155]\n"
"ds_read_b128 a[200:203], v192 offset:256\n"
"v_mfma_f32_16x16x32_bf16 a[156:159], a[248:251], v[232:235], a[156:159]\n"
"buffer_load_dwordx4 v195, s[24:27], s55 offen nt lds\n"
"v_mfma_f32_16x16x32_bf16 a[160:163], a[248:251], v[240:243], a[160:163]\n"
"ds_read_b128 a[204:207], v192 offset:4352\n"
"v_mfma_f32_16x16x32_bf16 a[164:167], a[248:251], v[248:251], a[164:167]\n"
"ds_read_b128 a[208:211], v192 offset:512\n"
"v_mfma_f32_16x16x32_bf16 a[168:171], a[252:255], v[208:211], a[168:171]\n"
"ds_read_b128 a[212:215], v192 offset:4608\n"
"v_mfma_f32_16x16x32_bf16 a[172:175], a[252:255], v[216:219], a[172:175]\n"
"ds_read_b128 a[216:219], v192 offset:768\n"
"v_mfma_f32_16x16x32_bf16 a[176:179], a[252:255], v[224:227], a[176:179]\n"
"ds_read_b128 a[220:223], v192 offset:4864\n"
"v_add_u32 v192, 32768, v192\n"
"v_mfma_f32_16x16x32_bf16 a[180:183], a[252:255], v[232:235], a[180:183]\n"
"v_exp_f32 v120, v120\n"
"v_mfma_f32_16x16x32_bf16 a[184:187], a[252:255], v[240:243], a[184:187]\n"
"v_exp_f32 v121, v121\n"
"v_mfma_f32_16x16x32_bf16 a[188:191], a[252:255], v[248:251], a[188:191]\n"
"s_waitcnt lgkmcnt(5)\n"
"v_exp_f32 v122, v122\n"
"v_mfma_f32_16x16x32_bf16 v[208:211], a[192:195], v[0:3], v[144:147]\n"
"v_exp_f32 v123, v123\n"
"v_mfma_f32_16x16x32_bf16 v[216:219], a[192:195], v[4:7], v[148:151]\n"
"v_exp_f32 v124, v124\n"
"v_mfma_f32_16x16x32_bf16 v[224:227], a[192:195], v[8:11], v[152:155]\n"
"s_add_u32 m0, s73, 135168\n"
"ds_read_b64_tr_b16 a[224:225], v193\n"
"v_cvt_pk_bf16_f32 v120, v120, v121\n"
"v_mfma_f32_16x16x32_bf16 v[232:235], a[192:195], v[12:15], v[156:159]\n"
"s_mov_b32 s55, 118784\n"
"v_exp_f32 v125, v125\n"
"v_mfma_f32_16x16x32_bf16 v[240:243], a[192:195], v[16:19], v[160:163]\n"
"v_exp_f32 v126, v126\n"
"v_mfma_f32_16x16x32_bf16 v[248:251], a[192:195], v[20:23], v[164:167]\n"
"buffer_load_dwordx4 v196, s[24:27], s55 offen offset:2048 nt lds\n"
"v_mfma_f32_16x16x32_bf16 v[212:215], a[196:199], v[0:3], v[144:147]\n"
"ds_read_b64_tr_b16 a[226:227], v193 offset:4096\n"
"v_cvt_pk_bf16_f32 v121, v122, v123\n"
"v_mfma_f32_16x16x32_bf16 v[220:223], a[196:199], v[4:7], v[148:151]\n"
"v_exp_f32 v127, v127\n"
"v_mfma_f32_16x16x32_bf16 v[228:231], a[196:199], v[8:11], v[152:155]\n"
"v_exp_f32 v128, v128\n"
"v_mfma_f32_16x16x32_bf16 v[236:239], a[196:199], v[12:15], v[156:159]\n"
"v_exp_f32 v129, v129\n"
"v_mfma_f32_16x16x32_bf16 v[244:247], a[196:199], v[16:19], v[160:163]\n"
"v_exp_f32 v130, v130\n"
"v_mfma_f32_16x16x32_bf16 v[252:255], a[196:199], v[20:23], v[164:167]\n"
"v_exp_f32 v131, v131\n"
"v_mfma_f32_16x16x32_bf16 v[208:211], a[200:203], v[24:27], v[208:211]\n"
"v_exp_f32 v132, v132\n"
"v_mfma_f32_16x16x32_bf16 v[216:219], a[200:203], v[28:31], v[216:219]\n"
"v_exp_f32 v133, v133\n"
"v_mfma_f32_16x16x32_bf16 v[224:227], a[200:203], v[32:35], v[224:227]\n"
"v_exp_f32 v134, v134\n"
"v_mfma_f32_16x16x32_bf16 v[232:235], a[200:203], v[36:39], v[232:235]\n"
"v_exp_f32 v135, v135\n"
"v_mfma_f32_16x16x32_bf16 v[240:243], a[200:203], v[40:43], v[240:243]\n"
"s_add_u32 m0, s73, 114688\n"
"v_exp_f32 v136, v136\n"
"v_mfma_f32_16x16x32_bf16 v[248:251], a[200:203], v[44:47], v[248:251]\n"
"s_waitcnt lgkmcnt(0)\n"
"v_exp_f32 v137, v137\n"
"v_mfma_f32_16x16x32_bf16 v[212:215], a[204:207], v[24:27], v[212:215]\n"
"s_mov_b32 s55, 106496\n"
"v_or3_b32 v198, v198, v120, v121\n"
"v_cvt_pk_bf16_f32 v122, v124, v125\n"
"v_mfma_f32_16x16x32_bf16 v[220:223], a[204:207], v[28:31], v[220:223]\n"
"buffer_load_dwordx4 v197, s[28:31], s55 offen nt lds\n"
"v_mfma_f32_16x16x32_bf16 v[228:231], a[204:207], v[32:35], v[228:231]\n"
"v_cvt_pk_bf16_f32 v123, v126, v127\n"
"v_cvt_pk_bf16_f32 v128, v128, v129\n"
"v_mfma_f32_16x16x32_bf16 v[236:239], a[204:207], v[36:39], v[236:239]\n"
"v_exp_f32 v138, v138\n"
"v_mfma_f32_16x16x32_bf16 v[244:247], a[204:207], v[40:43], v[244:247]\n"
"v_exp_f32 v139, v139\n"
"v_mfma_f32_16x16x32_bf16 v[252:255], a[204:207], v[44:47], v[252:255]\n"
"v_or3_b32 v198, v198, v122, v123\n"
"v_cvt_pk_bf16_f32 v129, v130, v131\n"
"v_mfma_f32_16x16x32_bf16 v[208:211], a[208:211], v[48:51], v[208:211]\n"
"v_exp_f32 v140, v140\n"
"v_mfma_f32_16x16x32_bf16 v[216:219], a[208:211], v[52:55], v[216:219]\n"
"v_exp_f32 v141, v141\n"
"v_mfma_f32_16x16x32_bf16 v[224:227], a[208:211], v[56:59], v[224:227]\n"
"v_exp_f32 v142, v142\n"
"v_mfma_f32_16x16x32_bf16 v[232:235], a[208:211], v[60:63], v[232:235]\n"
"v_exp_f32 v143, v143\n"
"v_mfma_f32_16x16x32_bf16 v[240:243], a[208:211], v[64:67], v[240:243]\n"
"v_or3_b32 v198, v198, v128, v129\n"
"v_cvt_pk_bf16_f32 v130, v132, v133\n"
"v_mfma_f32_16x16x32_bf16 v[248:251], a[208:211], v[68:71], v[248:251]\n"
"v_cvt_pk_bf16_f32 v131, v134, v135\n"
"v_cvt_pk_bf16_f32 v136, v136, v137\n"
"v_mfma_f32_16x16x32_bf16 v[212:215], a[212:215], v[48:51], v[212:215]\n"
"s_add_u32 m0, s73, 114688\n"
"v_or3_b32 v198, v198, v130, v131\n"
"v_cvt_pk_bf16_f32 v137, v138, v139\n"
"v_mfma_f32_16x16x32_bf16 v[220:223], a[212:215], v[52:55], v[220:223]\n"
"s_mov_b32 s55, 106496\n"
"v_or3_b32 v198, v198, v136, v137\n"
"v_cvt_pk_bf16_f32 v138, v140, v141\n"
"v_mfma_f32_16x16x32_bf16 v[228:231], a[212:215], v[56:59], v[228:231]\n"
"v_cvt_pk_bf16_f32 v139, v142, v143\n"
"v_or3_b32 v198, v198, v138, v139\n"
"v_mfma_f32_16x16x32_bf16 v[236:239], a[212:215], v[60:63], v[236:239]\n"
"buffer_load_dwordx4 v197, s[28:31], s55 offen offset:2048 nt lds\n"
"v_mfma_f32_16x16x32_bf16 v[244:247], a[212:215], v[64:67], v[244:247]\n"
"v_and_b32 v199, 0x40004000, v198\n"
"v_cmp_ne_u32 vcc, 0, v199\n"
"v_mfma_f32_16x16x32_bf16 v[252:255], a[212:215], v[68:71], v[252:255]\n"
"ds_read_b64_tr_b16 a[228:229], v194\n"
"v_mfma_f32_16x16x32_bf16 v[208:211], a[216:219], v[72:75], v[208:211]\n"
"ds_read_b64_tr_b16 a[230:231], v194 offset:4096\n"
"v_mfma_f32_16x16x32_bf16 v[216:219], a[216:219], v[76:79], v[216:219]\n"
"ds_read_b64_tr_b16 a[232:233], v193 offset:256\n"
"v_mfma_f32_16x16x32_bf16 v[224:227], a[216:219], v[80:83], v[224:227]\n"
"ds_read_b64_tr_b16 a[234:235], v193 offset:4352\n"
"v_mfma_f32_16x16x32_bf16 v[232:235], a[216:219], v[84:87], v[232:235]\n"
"ds_read_b64_tr_b16 a[236:237], v194 offset:256\n"
"v_mfma_f32_16x16x32_bf16 v[240:243], a[216:219], v[88:91], v[240:243]\n"
"ds_read_b64_tr_b16 a[238:239], v194 offset:4352\n"
"v_mfma_f32_16x16x32_bf16 v[248:251], a[216:219], v[92:95], v[248:251]\n"
"ds_read_b64_tr_b16 a[240:241], v193 offset:512\n"
"v_mfma_f32_16x16x32_bf16 v[212:215], a[220:223], v[72:75], v[212:215]\n"
"ds_read_b64_tr_b16 a[242:243], v193 offset:4608\n"
"v_mfma_f32_16x16x32_bf16 v[220:223], a[220:223], v[76:79], v[220:223]\n"
"ds_read_b64_tr_b16 a[244:245], v194 offset:512\n"
"v_mfma_f32_16x16x32_bf16 v[228:231], a[220:223], v[80:83], v[228:231]\n"
"s_add_u32 m0, s73, 118784\n"
"ds_read_b64_tr_b16 a[246:247], v194 offset:4608\n"
"s_mov_b32 s55, 110592\n"
"v_mfma_f32_16x16x32_bf16 v[236:239], a[220:223], v[84:87], v[236:239]\n"
"ds_read_b64_tr_b16 a[248:249], v193 offset:768\n"
"v_mfma_f32_16x16x32_bf16 v[244:247], a[220:223], v[88:91], v[244:247]\n"
"ds_read_b64_tr_b16 a[250:251], v193 offset:4864\n"
"v_add_u32 v193, 32768, v193\n"
"v_mfma_f32_16x16x32_bf16 v[252:255], a[220:223], v[92:95], v[252:255]\n"
"s_cbranch_vccnz .Lrare13\n"
".Lrare13_ret:\n"
"ds_read_b64_tr_b16 a[252:253], v194 offset:768\n"
"v_mfma_f32_16x16x32_bf16 v[168:171], v[204:207], v[96:99], v[168:171]\n"
"buffer_load_dwordx4 v197, s[28:31], s55 offen nt lds\n"
"v_mfma_f32_16x16x32_bf16 v[172:175], v[204:207], v[104:107], v[172:175]\n"
"ds_read_b64_tr_b16 a[254:255], v194 offset:4864\n"
"v_add_u32 v194, 32768, v194\n"
"v_mfma_f32_16x16x32_bf16 v[176:179], v[204:207], v[112:115], v[176:179]\n"
"v_exp_f32 v208, v208\n"
"v_mfma_f32_16x16x32_bf16 v[180:183], v[204:207], v[120:123], v[180:183]\n"
"v_exp_f32 v209, v209\n"
"v_mfma_f32_16x16x32_bf16 v[184:187], v[204:207], v[128:131], v[184:187]\n"
"v_exp_f32 v210, v210\n"
"v_mfma_f32_16x16x32_bf16 v[188:191], v[204:207], v[136:139], v[188:191]\n"
"v_exp_f32 v211, v211\n"
"v_mfma_f32_16x16x32_bf16 a[0:3], a[224:227], v[96:99], a[0:3]\n"
"v_exp_f32 v212, v212\n"
"v_mfma_f32_16x16x32_bf16 a[4:7], a[224:227], v[104:107], a[4:7]\n"
"v_exp_f32 v213, v213\n"
"v_mfma_f32_16x16x32_bf16 a[8:11], a[224:227], v[112:115], a[8:11]\n"
"v_exp_f32 v214, v214\n"
"v_mfma_f32_16x16x32_bf16 a[12:15], a[224:227], v[120:123], a[12:15]\n"
"v_exp_f32 v215, v215\n"
"v_mfma_f32_16x16x32_bf16 a[16:19], a[224:227], v[128:131], a[16:19]\n"
"s_add_u32 m0, s73, 118784\n"
"v_cvt_pk_bf16_f32 v208, v208, v209\n"
"v_mfma_f32_16x16x32_bf16 a[20:23], a[224:227], v[136:139], a[20:23]\n"
"s_waitcnt lgkmcnt(0)\n"
"v_exp_f32 v216, v216\n"
"v_mfma_f32_16x16x32_bf16 a[24:27], a[228:231], v[96:99], a[24:27]\n"
"s_mov_b32 s55, 110592\n"
"v_exp_f32 v217, v217\n"
"v_mfma_f32_16x16x32_bf16 a[28:31], a[228:231], v[104:107], a[28:31]\n"
"buffer_load_dwordx4 v197, s[28:31], s55 offen offset:2048 nt lds\n"
"v_mfma_f32_16x16x32_bf16 a[32:35], a[228:231], v[112:115], a[32:35]\n"
"s_add_u32 s64, s64, 8192\n"
"v_exp_f32 v218, v218\n"
"v_mfma_f32_16x16x32_bf16 a[36:39], a[228:231], v[120:123], a[36:39]\n"
"s_add_u32 s72, s72, 112\n"
"v_exp_f32 v219, v219\n"
"v_mfma_f32_16x16x32_bf16 a[40:43], a[228:231], v[128:131], a[40:43]\n"
"v_cvt_pk_bf16_f32 v209, v210, v211\n"
"v_or_b32 v198, v208, v209\n"
"v_mfma_f32_16x16x32_bf16 a[44:47], a[228:231], v[136:139], a[44:47]\n"
"v_exp_f32 v220, v220\n"
"v_mfma_f32_16x16x32_bf16 a[48:51], a[232:235], v[96:99], a[48:51]\n"
"v_exp_f32 v221, v221\n"
"v_mfma_f32_16x16x32_bf16 a[52:55], a[232:235], v[104:107], a[52:55]\n"
"v_exp_f32 v222, v222\n"
"v_mfma_f32_16x16x32_bf16 a[56:59], a[232:235], v[112:115], a[56:59]\n"
"v_exp_f32 v223, v223\n"
"v_mfma_f32_16x16x32_bf16 a[60:63], a[232:235], v[120:123], a[60:63]\n"
"v_exp_f32 v224, v224\n"
"v_mfma_f32_16x16x32_bf16 a[64:67], a[232:235], v[128:131], a[64:67]\n"
"v_exp_f32 v225, v225\n"
"v_mfma_f32_16x16x32_bf16 a[68:71], a[232:235], v[136:139], a[68:71]\n"
"v_cvt_pk_bf16_f32 v210, v212, v213\n"
"v_cvt_pk_bf16_f32 v211, v214, v215\n"
"v_mfma_f32_16x16x32_bf16 a[72:75], a[236:239], v[96:99], a[72:75]\n"
"v_cvt_pk_bf16_f32 v216, v216, v217\n"
"v_or3_b32 v198, v198, v210, v211\n"
"v_mfma_f32_16x16x32_bf16 a[76:79], a[236:239], v[104:107], a[76:79]\n"
"v_exp_f32 v226, v226\n"
"v_mfma_f32_16x16x32_bf16 a[80:83], a[236:239], v[112:115], a[80:83]\n"
"v_exp_f32 v227, v227\n"
"v_mfma_f32_16x16x32_bf16 a[84:87], a[236:239], v[120:123], a[84:87]\n"
"v_cvt_pk_bf16_f32 v217, v218, v219\n"
"v_or3_b32 v198, v198, v216, v217\n"
"v_mfma_f32_16x16x32_bf16 a[88:91], a[236:239], v[128:131], a[88:91]\n"
"v_exp_f32 v228, v228\n"
"v_mfma_f32_16x16x32_bf16 a[92:95], a[236:239], v[136:139], a[92:95]\n"
"v_exp_f32 v229, v229\n"
"v_mfma_f32_16x16x32_bf16 a[96:99], a[240:243], v[96:99], a[96:99]\n"
"s_memrealtime s[70:71]\n"
"v_cvt_pk_bf16_f32 v218, v220, v221\n"
"v_cvt_pk_bf16_f32 v219, v222, v223\n"
"v_mfma_f32_16x16x32_bf16 a[100:103], a[240:243], v[104:107], a[100:103]\n"
"v_cvt_pk_bf16_f32 v224, v224, v225\n"
"v_or3_b32 v198, v198, v218, v219\n"
"v_mfma_f32_16x16x32_bf16 a[104:107], a[240:243], v[112:115], a[104:107]\n"
"v_cvt_pk_bf16_f32 v225, v226, v227\n"
"v_or3_b32 v198, v198, v224, v225\n"
"v_mfma_f32_16x16x32_bf16 a[108:111], a[240:243], v[120:123], a[108:111]\n"
"v_exp_f32 v230, v230\n"
"v_mfma_f32_16x16x32_bf16 a[112:115], a[240:243], v[128:131], a[112:115]\n"
"v_exp_f32 v231, v231\n"
"v_mfma_f32_16x16x32_bf16 a[116:119], a[240:243], v[136:139], a[116:119]\n"
"v_cvt_pk_bf16_f32 v226, v228, v229\n"
"v_mfma_f32_16x16x32_bf16 a[120:123], a[244:247], v[96:99], a[120:123]\n"
"v_mfma_f32_16x16x32_bf16 a[124:127], a[244:247], v[104:107], a[124:127]\n"
"v_mfma_f32_16x16x32_bf16 a[128:131], a[244:247], v[112:115], a[128:131]\n"
"v_mfma_f32_16x16x32_bf16 a[132:135], a[244:247], v[120:123], a[132:135]\n"
"v_mfma_f32_16x16x32_bf16 a[136:139], a[244:247], v[128:131], a[136:139]\n"
"v_mfma_f32_16x16x32_bf16 a[140:143], a[244:247], v[136:139], a[140:143]\n"
"s_waitcnt vmcnt(8) lgkmcnt(0)\n"
"s_sub_u32 s71, s72, s70\n"
"s_sub_u32 s71, s71, 16\n"
"s_cmp_lt_u32 s71, 400\n"
"s_cbranch_scc1 .Lpg11\n"
".Lpg11x:\n"
"s_barrier\n"
"v_mfma_f32_16x16x32_bf16 a[144:147], a[248:251], v[96:99], a[144:147]\n"
"s_add_u32 m0, s73, 0\n"
"ds_read_b128 a[192:195], v192\n"
"v_cvt_pk_bf16_f32 v227, v230, v231\n"
"v_mfma_f32_16x16x32_bf16 a[148:151], a[248:251], v[104:107], a[148:151]\n"
"s_mov_b32 s55, 122880\n"
"ds_read_b128 a[196:199], v192 offset:4096\n"
"v_or3_b32 v198, v198, v226, v227\n"
"v_mfma_f32_16x16x32_bf16 a[152:155], a[248:251], v[112:115], a[152:155]\n"
"ds_read_b128 a[200:203], v192 offset:256\n"
"v_mfma_f32_16x16x32_bf16 a[156:159], a[248:251], v[120:123], a[156:159]\n"
"buffer_load_dwordx4 v195, s[24:27], s55 offen nt lds\n"
"v_mfma_f32_16x16x32_bf16 a[160:163], a[248:251], v[128:131], a[160:163]\n"
"ds_read_b128 a[204:207], v192 offset:4352\n"
"v_mfma_f32_16x16x32_bf16 a[164:167], a[248:251], v[136:139], a[164:167]\n"
"ds_read_b128 a[208:211], v192 offset:512\n"
"v_mfma_f32_16x16x32_bf16 a[168:171], a[252:255], v[96:99], a[168:171]\n"
"ds_read_b128 a[212:215], v192 offset:4608\n"
"v_mfma_f32_16x16x32_bf16 a[172:175], a[252:255], v[104:107], a[172:175]\n"
"ds_read_b128 a[216:219], v192 offset:768\n"
"v_mfma_f32_16x16x32_bf16 a[176:179], a[252:255], v[112:115], a[176:179]\n"
"ds_read_b128 a[220:223], v192 offset:4864\n"
"v_add_u32 v192, 32768, v192\n"
"v_mfma_f32_16x16x32_bf16 a[180:183], a[252:255], v[120:123], a[180:183]\n"
"v_exp_f32 v232, v232\n"
"v_mfma_f32_16x16x32_bf16 a[184:187], a[252:255], v[128:131], a[184:187]\n"
"v_exp_f32 v233, v233\n"
"v_mfma_f32_16x16x32_bf16 a[188:191], a[252:255], v[136:139], a[188:191]\n"
"s_waitcnt lgkmcnt(5)\n"
"v_exp_f32 v234, v234\n"
"v_mfma_f32_16x16x32_bf16 v[96:99], a[192:195], v[0:3], v[144:147]\n"
"v_exp_f32 v235, v235\n"
"v_mfma_f32_16x16x32_bf16 v[104:107], a[192:195], v[4:7], v[148:151]\n"
"v_exp_f32 v236, v236\n"
"v_mfma_f32_16x16x32_bf16 v[112:115], a[192:195], v[8:11], v[152:155]\n"
"s_add_u32 m0, s73, 0\n"
"ds_read_b64_tr_b16 a[224:225], v193\n"
"v_cvt_pk_bf16_f32 v232, v232, v233\n"
"v_mfma_f32_16x16x32_bf16 v[120:123], a[192:195], v[12:15], v[156:159]\n"
"s_mov_b32 s55, 122880\n"
"v_exp_f32 v237, v237\n"
"v_mfma_f32_16x16x32_bf16 v[128:131], a[192:195], v[16:19], v[160:163]\n"
"v_exp_f32 v238, v238\n"
"v_mfma_f32_16x16x32_bf16 v[136:139], a[192:195], v[20:23], v[164:167]\n"
"buffer_load_dwordx4 v196, s[24:27], s55 offen offset:2048 nt lds\n"
"v_mfma_f32_16x16x32_bf16 v[100:103], a[196:199], v[0:3], v[144:147]\n"
"ds_read_b64_tr_b16 a[226:227], v193 offset:4096\n"
"v_cvt_pk_bf16_f32 v233, v234, v235\n"
"v_mfma_f32_16x16x32_bf16 v[108:111], a[196:199], v[4:7], v[148:151]\n"
"v_exp_f32 v239, v239\n"
"v_mfma_f32_16x16x32_bf16 v[116:119], a[196:199], v[8:11], v[152:155]\n"
"v_exp_f32 v240, v240\n"
"v_mfma_f32_16x16x32_bf16 v[124:127], a[196:199], v[12:15], v[156:159]\n"
"v_exp_f32 v241, v241\n"
"v_mfma_f32_16x16x32_bf16 v[132:135], a[196:199], v[16:19], v[160:163]\n"
"v_exp_f32 v242, v242\n"
"v_mfma_f32_16x16x32_bf16 v[140:143], a[196:199], v[20:23], v[164:167]\n"
"v_exp_f32 v243, v243\n"
"v_mfma_f32_16x16x32_bf16 v[96:99], a[200:203], v[24:27], v[96:99]\n"
"v_exp_f32 v244, v244\n"
"v_mfma_f32_16x16x32_bf16 v[104:107], a[200:203], v[28:31], v[104:107]\n"
"v_exp_f32 v245, v245\n"
"v_mfma_f32_16x16x32_bf16 v[112:115], a[200:203], v[32:35], v[112:115]\n"
"v_exp_f32 v246, v246\n"
"v_mfma_f32_16x16x32_bf16 v[120:123], a[200:203], v[36:39], v[120:123]\n"
"v_exp_f32 v247, v247\n"
"v_mfma_f32_16x16x32_bf16 v[128:131], a[200:203], v[40:43], v[128:131]\n"
"s_add_u32 m0, s73, 4096\n"
"v_exp_f32 v248, v248\n"
"v_mfma_f32_16x16x32_bf16 v[136:139], a[200:203], v[44:47], v[136:139]\n"
"s_waitcnt lgkmcnt(0)\n"
"v_exp_f32 v249, v249\n"
"v_mfma_f32_16x16x32_bf16 v[100:103], a[204:207], v[24:27], v[100:103]\n"
"s_mov_b32 s55, 126976\n"
"v_or3_b32 v198, v198, v232, v233\n"
"v_cvt_pk_bf16_f32 v234, v236, v237\n"
"v_mfma_f32_16x16x32_bf16 v[108:111], a[204:207], v[28:31], v[108:111]\n"
"buffer_load_dwordx4 v195, s[24:27], s55 offen nt lds\n"
"v_mfma_f32_16x16x32_bf16 v[116:119], a[204:207], v[32:35], v[116:119]\n"
"v_cvt_pk_bf16_f32 v235, v238, v239\n"
"v_cvt_pk_bf16_f32 v240, v240, v241\n"
"v_mfma_f32_16x16x32_bf16 v[124:127], a[204:207], v[36:39], v[124:127]\n"
"v_exp_f32 v250, v250\n"
"v_mfma_f32_16x16x32_bf16 v[132:135], a[204:207], v[40:43], v[132:135]\n"
"v_exp_f32 v251, v251\n"
"v_mfma_f32_16x16x32_bf16 v[140:143], a[204:207], v[44:47], v[140:143]\n"
"v_or3_b32 v198, v198, v234, v235\n"
"v_cvt_pk_bf16_f32 v241, v242, v243\n"
"v_mfma_f32_16x16x32_bf16 v[96:99], a[208:211], v[48:51], v[96:99]\n"
"v_exp_f32 v252, v252\n"
"v_mfma_f32_16x16x32_bf16 v[104:107], a[208:211], v[52:55], v[104:107]\n"
"v_exp_f32 v253, v253\n"
"v_mfma_f32_16x16x32_bf16 v[112:115], a[208:211], v[56:59], v[112:115]\n"
"v_exp_f32 v254, v254\n"
"v_mfma_f32_16x16x32_bf16 v[120:123], a[208:211], v[60:63], v[120:123]\n"
"v_exp_f32 v255, v255\n"
"v_mfma_f32_16x16x32_bf16 v[128:131], a[208:211], v[64:67], v[128:131]\n"
"v_or3_b32 v198, v198, v240, v241\n"
"v_cvt_pk_bf16_f32 v242, v244, v245\n"
"v_mfma_f32_16x16x32_bf16 v[136:139], a[208:211], v[68:71], v[136:139]\n"
"v_cvt_pk_bf16_f32 v243, v246, v247\n"
"v_cvt_pk_bf16_f32 v248, v248, v249\n"
"v_mfma_f32_16x16x32_bf16 v[100:103], a[212:215], v[48:51], v[100:103]\n"
"s_add_u32 m0, s73, 4096\n"
"v_or3_b32 v198, v198, v242, v243\n"
"v_cvt_pk_bf16_f32 v249, v250, v251\n"
"v_mfma_f32_16x16x32_bf16 v[108:111], a[212:215], v[52:55], v[108:111]\n"
"s_mov_b32 s55, 126976\n"
"v_or3_b32 v198, v198, v248, v249\n"
"v_cvt_pk_bf16_f32 v250, v252, v253\n"
"v_mfma_f32_16x16x32_bf16 v[116:119], a[212:215], v[56:59], v[116:119]\n"
"v_cvt_pk_bf16_f32 v251, v254, v255\n"
"v_or3_b32 v198, v198, v250, v251\n"
"v_mfma_f32_16x16x32_bf16 v[124:127], a[212:215], v[60:63], v[124:127]\n"
"buffer_load_dwordx4 v196, s[24:27], s55 offen offset:2048 nt lds\n"
"v_mfma_f32_16x16x32_bf16 v[132:135], a[212:215], v[64:67], v[132:135]\n"
"v_and_b32 v199, 0x40004000, v198\n"
"v_cmp_ne_u32 vcc, 0, v199\n"
"v_mfma_f32_16x16x32_bf16 v[140:143], a[212:215], v[68:71], v[140:143]\n"
"ds_read_b64_tr_b16 a[228:229], v194\n"
"v_mfma_f32_16x16x32_bf16 v[96:99], a[216:219], v[72:75], v[96:99]\n"
"ds_read_b64_tr_b16 a[230:231], v194 offset:4096\n"
"v_mfma_f32_16x16x32_bf16 v[104:107], a[216:219], v[76:79], v[104:107]\n"
"ds_read_b64_tr_b16 a[232:233], v193 offset:256\n"
"v_mfma_f32_16x16x32_bf16 v[112:115], a[216:219], v[80:83], v[112:115]\n"
"ds_read_b64_tr_b16 a[234:235], v193 offset:4352\n"
"v_mfma_f32_16x16x32_bf16 v[120:123], a[216:219], v[84:87], v[120:123]\n"
"ds_read_b64_tr_b16 a[236:237], v194 offset:256\n"
"v_mfma_f32_16x16x32_bf16 v[128:131], a[216:219], v[88:91], v[128:131]\n"
"ds_read_b64_tr_b16 a[238:239], v194 offset:4352\n"
"v_mfma_f32_16x16x32_bf16 v[136:139], a[216:219], v[92:95], v[136:139]\n"
"ds_read_b64_tr_b16 a[240:241], v193 offset:512\n"
"v_mfma_f32_16x16x32_bf16 v[100:103], a[220:223], v[72:75], v[100:103]\n"
"ds_read_b64_tr_b16 a[242:243], v193 offset:4608\n"
"v_mfma_f32_16x16x32_bf16 v[108:111], a[220:223], v[76:79], v[108:111]\n"
"ds_read_b64_tr_b16 a[244:245], v194 offset:512\n"
"v_mfma_f32_16x16x32_bf16 v[116:119], a[220:223], v[80:83], v[116:119]\n"
"s_add_u32 m0, s73, 147456\n"
"ds_read_b64_tr_b16 a[246:247], v194 offset:4608\n"
"s_mov_b32 s55, 114688\n"
"v_mfma_f32_16x16x32_bf16 v[124:127], a[220:223], v[84:87], v[124:127]\n"
"ds_read_b64_tr_b16 a[248:249], v193 offset:768\n"
"v_mfma_f32_16x16x32_bf16 v[132:135], a[220:223], v[88:91], v[132:135]\n"
"ds_read_b64_tr_b16 a[250:251], v193 offset:4864\n"
"v_add_u32 v193, 32768, v193\n"
"v_mfma_f32_16x16x32_bf16 v[140:143], a[220:223], v[92:95], v[140:143]\n"
"s_cbranch_vccnz .Lrare14\n"
".Lrare14_ret:\n"
"ds_read_b64_tr_b16 a[252:253], v194 offset:768\n"
"v_mfma_f32_16x16x32_bf16 v[168:171], v[204:207], v[208:211], v[168:171]\n"
"buffer_load_dwordx4 v197, s[28:31], s55 offen nt lds\n"
"v_mfma_f32_16x16x32_bf16 v[172:175], v[204:207], v[216:219], v[172:175]\n"
"ds_read_b64_tr_b16 a[254:255], v194 offset:4864\n"
"v_add_u32 v194, 32768, v194\n"
"v_mfma_f32_16x16x32_bf16 v[176:179], v[204:207], v[224:227], v[176:179]\n"
"v_exp_f32 v96, v96\n"
"v_mfma_f32_16x16x32_bf16 v[180:183], v[204:207], v[232:235], v[180:183]\n"
"v_exp_f32 v97, v97\n"
"v_mfma_f32_16x16x32_bf16 v[184:187], v[204:207], v[240:243], v[184:187]\n"
"v_exp_f32 v98, v98\n"
"v_mfma_f32_16x16x32_bf16 v[188:191], v[204:207], v[248:251], v[188:191]\n"
"v_exp_f32 v99, v99\n"
"v_mfma_f32_16x16x32_bf16 a[0:3], a[224:227], v[208:211], a[0:3]\n"
"v_exp_f32 v100, v100\n"
"v_mfma_f32_16x16x32_bf16 a[4:7], a[224:227], v[216:219], a[4:7]\n"
"v_exp_f32 v101, v101\n"
"v_mfma_f32_16x16x32_bf16 a[8:11], a[224:227], v[224:227], a[8:11]\n"
"v_exp_f32 v102, v102\n"
"v_mfma_f32_16x16x32_bf16 a[12:15], a[224:227], v[232:235], a[12:15]\n"
"v_exp_f32 v103, v103\n"
"v_mfma_f32_16x16x32_bf16 a[16:19], a[224:227], v[240:243], a[16:19]\n"
"s_add_u32 m0, s73, 147456\n"
"v_cvt_pk_bf16_f32 v96, v96, v97\n"
"v_mfma_f32_16x16x32_bf16 a[20:23], a[224:227], v[248:251], a[20:23]\n"
"s_waitcnt lgkmcnt(0)\n"
"v_exp_f32 v104, v104\n"
"v_mfma_f32_16x16x32_bf16 a[24:27], a[228:231], v[208:211], a[24:27]\n"
"s_mov_b32 s55, 114688\n"
"v_exp_f32 v105, v105\n"
"v_mfma_f32_16x16x32_bf16 a[28:31], a[228:231], v[216:219], a[28:31]\n"
"buffer_load_dwordx4 v197, s[28:31], s55 offen offset:2048 nt lds\n"
"v_mfma_f32_16x16x32_bf16 a[32:35], a[228:231], v[224:227], a[32:35]\n"
"s_add_u32 s64, s64, 8192\n"
"v_exp_f32 v106, v106\n"
"v_mfma_f32_16x16x32_bf16 a[36:39], a[228:231], v[232:235], a[36:39]\n"
"s_add_u32 s72, s72, 112\n"
"v_exp_f32 v107, v107\n"
"v_mfma_f32_16x16x32_bf16 a[40:43], a[228:231], v[240:243], a[40:43]\n"
"v_cvt_pk_bf16_f32 v97, v98, v99\n"
"v_or_b32 v198, v96, v97\n"
"v_mfma_f32_16x16x32_bf16 a[44:47], a[228:231], v[248:251], a[44:47]\n"
"v_exp_f32 v108, v108\n"
"v_mfma_f32_16x16x32_bf16 a[48:51], a[232:235], v[208:211], a[48:51]\n"
"v_exp_f32 v109, v109\n"
"v_mfma_f32_16x16x32_bf16 a[52:55], a[232:235], v[216:219], a[52:55]\n"
"v_exp_f32 v110, v110\n"
"v_mfma_f32_16x16x32_bf16 a[56:59], a[232:235], v[224:227], a[56:59]\n"
"v_exp_f32 v111, v111\n"
"v_mfma_f32_16x16x32_bf16 a[60:63], a[232:235], v[232:235], a[60:63]\n"
"v_exp_f32 v112, v112\n"
"v_mfma_f32_16x16x32_bf16 a[64:67], a[232:235], v[240:243], a[64:67]\n"
"v_exp_f32 v113, v113\n"
"v_mfma_f32_16x16x32_bf16 a[68:71], a[232:235], v[248:251], a[68:71]\n"
"v_cvt_pk_bf16_f32 v98, v100, v101\n"
"v_cvt_pk_bf16_f32 v99, v102, v103\n"
"v_mfma_f32_16x16x32_bf16 a[72:75], a[236:239], v[208:211], a[72:75]\n"
"v_cvt_pk_bf16_f32 v104, v104, v105\n"
"v_or3_b32 v198, v198, v98, v99\n"
"v_mfma_f32_16x16x32_bf16 a[76:79], a[236:239], v[216:219], a[76:79]\n"
"v_exp_f32 v114, v114\n"
"v_mfma_f32_16x16x32_bf16 a[80:83], a[236:239], v[224:227], a[80:83]\n"
"v_exp_f32 v115, v115\n"
"v_mfma_f32_16x16x32_bf16 a[84:87], a[236:239], v[232:235], a[84:87]\n"
"v_cvt_pk_bf16_f32 v105, v106, v107\n"
"v_or3_b32 v198, v198, v104, v105\n"
"v_mfma_f32_16x16x32_bf16 a[88:91], a[236:239], v[240:243], a[88:91]\n"
"v_exp_f32 v116, v116\n"
"v_mfma_f32_16x16x32_bf16 a[92:95], a[236:239], v[248:251], a[92:95]\n"
"v_exp_f32 v117, v117\n"
"v_mfma_f32_16x16x32_bf16 a[96:99], a[240:243], v[208:211], a[96:99]\n"
"s_memrealtime s[70:71]\n"
"v_cvt_pk_bf16_f32 v106, v108, v109\n"
"v_cvt_pk_bf16_f32 v107, v110, v111\n"
"v_mfma_f32_16x16x32_bf16 a[100:103], a[240:243], v[216:219], a[100:103]\n"
"v_cvt_pk_bf16_f32 v112, v112, v113\n"
"v_or3_b32 v198, v198, v106, v107\n"
"v_mfma_f32_16x16x32_bf16 a[104:107], a[240:243], v[224:227], a[104:107]\n"
"v_cvt_pk_bf16_f32 v113, v114, v115\n"
"v_or3_b32 v198, v198, v112, v113\n"
"v_mfma_f32_16x16x32_bf16 a[108:111], a[240:243], v[232:235], a[108:111]\n"
"v_exp_f32 v118, v118\n"
"v_mfma_f32_16x16x32_bf16 a[112:115], a[240:243], v[240:243], a[112:115]\n"
"v_exp_f32 v119, v119\n"
"v_mfma_f32_16x16x32_bf16 a[116:119], a[240:243], v[248:251], a[116:119]\n"
"v_cvt_pk_bf16_f32 v114, v116, v117\n"
"v_mfma_f32_16x16x32_bf16 a[120:123], a[244:247], v[208:211], a[120:123]\n"
"v_mfma_f32_16x16x32_bf16 a[124:127], a[244:247], v[216:219], a[124:127]\n"
"v_mfma_f32_16x16x32_bf16 a[128:131], a[244:247], v[224:227], a[128:131]\n"
"v_mfma_f32_16x16x32_bf16 a[132:135], a[244:247], v[232:235], a[132:135]\n"
"v_mfma_f32_16x16x32_bf16 a[136:139], a[244:247], v[240:243], a[136:139]\n"
"v_mfma_f32_16x16x32_bf16 a[140:143], a[244:247], v[248:251], a[140:143]\n"
"s_waitcnt vmcnt(6) lgkmcnt(0)\n"
"s_sub_u32 s71, s72, s70\n"
"s_sub_u32 s71, s71, 16\n"
"s_cmp_lt_u32 s71, 400\n"
"s_cbranch_scc1 .Lpg12\n"
".Lpg12x:\n"
"s_barrier\n"
"v_mfma_f32_16x16x32_bf16 a[144:147], a[248:251], v[208:211], a[144:147]\n"
"s_add_u32 m0, s73, 151552\n"
"ds_read_b128 a[192:195], v192\n"
"s_mov_b32 s55, 118784\n"
"v_mfma_f32_16x16x32_bf16 a[148:151], a[248:251], v[216:219], a[148:151]\n"
"ds_read_b128 a[196:199], v192 offset:4096\n"
"v_mbcnt_lo_u32_b32 v200, -1, 0\n"
"v_mfma_f32_16x16x32_bf16 a[152:155], a[248:251], v[224:227], a[152:155]\n"
"ds_read_b128 a[200:203], v192 offset:256\n"
"v_mbcnt_hi_u32_b32 v200, -1, v200\n"
"v_mfma_f32_16x16x32_bf16 a[156:159], a[248:251], v[232:235], a[156:159]\n"
"buffer_load_dwordx4 v197, s[28:31], s55 offen nt lds\n"
"v_mfma_f32_16x16x32_bf16 a[160:163], a[248:251], v[240:243], a[160:163]\n"
"ds_read_b128 a[204:207], v192 offset:4352\n"
"v_bfe_u32 v196, v200, 4, 1\n"
"v_mfma_f32_16x16x32_bf16 a[164:167], a[248:251], v[248:251], a[164:167]\n"
"ds_read_b128 a[208:211], v192 offset:512\n"
"v_cvt_pk_bf16_f32 v115, v118, v119\n"
"v_mfma_f32_16x16x32_bf16 a[168:171], a[252:255], v[208:211], a[168:171]\n"
"ds_read_b128 a[212:215], v192 offset:4608\n"
"v_or3_b32 v198, v198, v114, v115\n"
"v_mfma_f32_16x16x32_bf16 a[172:175], a[252:255], v[216:219], a[172:175]\n"
"ds_read_b128 a[216:219], v192 offset:768\n"
"v_cmp_eq_u32 s[82:83], 0, v200\n"
"v_mfma_f32_16x16x32_bf16 a[176:179], a[252:255], v[224:227], a[176:179]\n"
"s_add_u32 m0, s73, 151552\n"
"ds_read_b128 a[220:223], v192 offset:4864\n"
"v_mfma_f32_16x16x32_bf16 a[180:183], a[252:255], v[232:235], a[180:183]\n"
"s_mov_b32 s55, 118784\n"
"v_exp_f32 v120, v120\n"
"v_mfma_f32_16x16x32_bf16 a[184:187], a[252:255], v[240:243], a[184:187]\n"
"v_exp_f32 v121, v121\n"
"v_mfma_f32_16x16x32_bf16 a[188:191], a[252:255], v[248:251], a[188:191]\n"
"s_waitcnt lgkmcnt(5)\n"
"v_exp_f32 v122, v122\n"
"v_mfma_f32_16x16x32_bf16 v[208:211], a[192:195], v[0:3], v[144:147]\n"
"buffer_load_dwordx4 v197, s[28:31], s55 offen offset:2048 nt lds\n"
"v_mfma_f32_16x16x32_bf16 v[216:219], a[192:195], v[4:7], v[148:151]\n"
"v_exp_f32 v123, v123\n"
"v_mfma_f32_16x16x32_bf16 v[224:227], a[192:195], v[8:11], v[152:155]\n"
"ds_read_b64_tr_b16 a[224:225], v193\n"
"v_cvt_pk_bf16_f32 v120, v120, v121\n"
"v_mfma_f32_16x16x32_bf16 v[232:235], a[192:195], v[12:15], v[156:159]\n"
"v_exp_f32 v124, v124\n"
"v_mfma_f32_16x16x32_bf16 v[240:243], a[192:195], v[16:19], v[160:163]\n"
"v_exp_f32 v125, v125\n"
"v_mfma_f32_16x16x32_bf16 v[248:251], a[192:195], v[20:23], v[164:167]\n"
"s_add_u32 m0, s73, 16384\n"
"s_mov_b32 s55, 122880\n"
"ds_read_b64_tr_b16 a[226:227], v193 offset:4096\n"
"v_mfma_f32_16x16x32_bf16 v[212:215], a[196:199], v[0:3], v[144:147]\n"
"v_exp_f32 v126, v126\n"
"v_mfma_f32_16x16x32_bf16 v[220:223], a[196:199], v[4:7], v[148:151]\n"
"buffer_load_dwordx4 v197, s[28:31], s55 offen nt lds\n"
"v_mfma_f32_16x16x32_bf16 v[228:231], a[196:199], v[8:11], v[152:155]\n"
"v_exp_f32 v127, v127\n"
"v_mfma_f32_16x16x32_bf16 v[236:239], a[196:199], v[12:15], v[156:159]\n"
"v_exp_f32 v128, v128\n"
"v_mfma_f32_16x16x32_bf16 v[244:247], a[196:199], v[16:19], v[160:163]\n"
"v_exp_f32 v129, v129\n"
"v_mfma_f32_16x16x32_bf16 v[252:255], a[196:199], v[20:23], v[164:167]\n"
"v_exp_f32 v130, v130\n"
"v_mfma_f32_16x16x32_bf16 v[208:211], a[200:203], v[24:27], v[208:211]\n"
"v_exp_f32 v131, v131\n"
"v_mfma_f32_16x16x32_bf16 v[216:219], a[200:203], v[28:31], v[216:219]\n"
"s_add_u32 m0, s73, 16384\n"
"s_mov_b32 s55, 122880\n"
"v_cvt_pk_bf16_f32 v121, v122, v123\n"
"v_mfma_f32_16x16x32_bf16 v[224:227], a[200:203], v[32:35], v[224:227]\n"
"v_exp_f32 v132, v132\n"
"v_mfma_f32_16x16x32_bf16 v[232:235], a[200:203], v[36:39], v[232:235]\n"
"v_exp_f32 v133, v133\n"
"v_mfma_f32_16x16x32_bf16 v[240:243], a[200:203], v[40:43], v[240:243]\n"
"buffer_load_dwordx4 v197, s[28:31], s55 offen offset:2048 nt lds\n"
"v_mfma_f32_16x16x32_bf16 v[248:251], a[200:203], v[44:47], v[248:251]\n"
"s_waitcnt lgkmcnt(0)\n"
"v_exp_f32 v134, v134\n"
"v_mfma_f32_16x16x32_bf16 v[212:215], a[204:207], v[24:27], v[212:215]\n"
"v_exp_f32 v135, v135\n"
"v_mfma_f32_16x16x32_bf16 v[220:223], a[204:207], v[28:31], v[220:223]\n"
"v_exp_f32 v136, v136\n"
"v_mfma_f32_16x16x32_bf16 v[228:231], a[204:207], v[32:35], v[228:231]\n"
"v_exp_f32 v137, v137\n"
"v_mfma_f32_16x16x32_bf16 v[236:239], a[204:207], v[36:39], v[236:239]\n"
"s_add_u32 m0, s73, 20480\n"
"s_mov_b32 s55, 126976\n"
"v_or3_b32 v198, v198, v120, v121\n"
"v_mfma_f32_16x16x32_bf16 v[244:247], a[204:207], v[40:43], v[244:247]\n"
"v_cvt_pk_bf16_f32 v122, v124, v125\n"
"v_cvt_pk_bf16_f32 v123, v126, v127\n"
"v_mfma_f32_16x16x32_bf16 v[252:255], a[204:207], v[44:47], v[252:255]\n"
"v_cvt_pk_bf16_f32 v128, v128, v129\n"
"v_or3_b32 v198, v198, v122, v123\n"
"v_mfma_f32_16x16x32_bf16 v[208:211], a[208:211], v[48:51], v[208:211]\n"
"buffer_load_dwordx4 v197, s[28:31], s55 offen nt lds\n"
"v_mfma_f32_16x16x32_bf16 v[216:219], a[208:211], v[52:55], v[216:219]\n"
"v_exp_f32 v138, v138\n"
"v_mfma_f32_16x16x32_bf16 v[224:227], a[208:211], v[56:59], v[224:227]\n"
"v_exp_f32 v139, v139\n"
"v_mfma_f32_16x16x32_bf16 v[232:235], a[208:211], v[60:63], v[232:235]\n"
"v_cvt_pk_bf16_f32 v129, v130, v131\n"
"v_or3_b32 v198, v198, v128, v129\n"
"v_mfma_f32_16x16x32_bf16 v[240:243], a[208:211], v[64:67], v[240:243]\n"
"v_exp_f32 v140, v140\n"
"v_mfma_f32_16x16x32_bf16 v[248:251], a[208:211], v[68:71], v[248:251]\n"
"v_exp_f32 v141, v141\n"
"v_mfma_f32_16x16x32_bf16 v[212:215], a[212:215], v[48:51], v[212:215]\n"
"s_add_u32 m0, s73, 20480\n"
"s_lshl_b32 s57, s54, 1\n"
"v_xor_b32 v199, s57, v200\n"
"v_mfma_f32_16x16x32_bf16 v[220:223], a[212:215], v[52:55], v[220:223]\n"
"s_add_u32 s57, s45, 1\n"
"v_and_b32 v195, 1, v199\n"
"v_bfe_u32 v199, v199, 1, 1\n"
"v_mfma_f32_16x16x32_bf16 v[228:231], a[212:215], v[56:59], v[228:231]\n"
"s_mov_b32 s55, 126976\n"
"v_lshl_add_u32 v199, v196, 1, v199\n"
"v_add_u32 v199, s57, v199\n"
"v_mfma_f32_16x16x32_bf16 v[236:239], a[212:215], v[60:63], v[236:239]\n"
"buffer_load_dwordx4 v197, s[28:31], s55 offen offset:2048 nt lds\n"
"v_mfma_f32_16x16x32_bf16 v[244:247], a[212:215], v[64:67], v[244:247]\n"
"s_xor_b32 s57, s53, 1\n"
"v_lshlrev_b32 v195, 4, v195\n"
"v_and_b32 v199, 3, v199\n"
"v_mfma_f32_16x16x32_bf16 v[252:255], a[212:215], v[68:71], v[252:255]\n"
"s_add_u32 s74, s46, 1\n"
"v_lshl_add_u32 v195, v199, 5, v195\n"
"v_lshrrev_b32 v199, 5, v200\n"
"v_mfma_f32_16x16x32_bf16 v[208:211], a[216:219], v[72:75], v[208:211]\n"
"s_lshl_b32 s75, s45, 11\n"
"v_xor_b32 v199, s57, v199\n"
"v_lshl_add_u32 v195, v199, 7, v195\n"
"v_mfma_f32_16x16x32_bf16 v[216:219], a[216:219], v[76:79], v[216:219]\n"
"v_bfe_u32 v199, v200, 2, 2\n"
"v_lshl_add_u32 v195, v199, 10, v195\n"
"v_mfma_f32_16x16x32_bf16 v[224:227], a[216:219], v[80:83], v[224:227]\n"
"v_exp_f32 v142, v142\n"
"v_mfma_f32_16x16x32_bf16 v[232:235], a[216:219], v[84:87], v[232:235]\n"
"v_exp_f32 v143, v143\n"
"v_mfma_f32_16x16x32_bf16 v[240:243], a[216:219], v[88:91], v[240:243]\n"
"v_min_u32 v199, 2, v200\n"
"v_cvt_pk_bf16_f32 v130, v132, v133\n"
"v_mfma_f32_16x16x32_bf16 v[248:251], a[216:219], v[92:95], v[248:251]\n"
"v_cvt_pk_bf16_f32 v131, v134, v135\n"
"v_cvt_pk_bf16_f32 v136, v136, v137\n"
"v_mfma_f32_16x16x32_bf16 v[212:215], a[220:223], v[72:75], v[212:215]\n"
"v_add_u32 v199, s74, v199\n"
"v_or3_b32 v198, v198, v130, v131\n"
"v_mfma_f32_16x16x32_bf16 v[220:223], a[220:223], v[76:79], v[220:223]\n"
"v_cvt_pk_bf16_f32 v137, v138, v139\n"
"v_and_b32 v199, 3, v199\n"
"v_mfma_f32_16x16x32_bf16 v[228:231], a[220:223], v[80:83], v[228:231]\n"
"v_or3_b32 v198, v198, v136, v137\n"
"v_cvt_pk_bf16_f32 v138, v140, v141\n"
"v_mfma_f32_16x16x32_bf16 v[236:239], a[220:223], v[84:87], v[236:239]\n"
"v_cvt_pk_bf16_f32 v139, v142, v143\n"
"v_lshlrev_b32 v199, 9, v199\n"
"v_mfma_f32_16x16x32_bf16 v[244:247], a[220:223], v[88:91], v[244:247]\n"
"v_or3_b32 v198, v198, v138, v139\n"
"v_add_u32 v196, s75, v199\n"
"v_mfma_f32_16x16x32_bf16 v[252:255], a[220:223], v[92:95], v[252:255]\n"
"v_and_b32 v199, 0x40004000, v198\n"
"v_cmp_ne_u32 vcc, 0, v199\n"
"s_cbranch_vccnz .Lrare15\n"
".Lrare15_ret:\n"
"v_mfma_f32_16x16x32_bf16 v[168:171], v[204:207], v[96:99], v[168:171]\n"
"s_add_u32 s64, s64, 8192\n"
"ds_read_b64_tr_b16 a[228:229], v194\n"
"v_mov_b32 v197, 0x80000000\n"
"v_mfma_f32_16x16x32_bf16 v[172:175], v[204:207], v[104:107], v[172:175]\n"
"s_add_u32 s72, s72, 112\n"
"ds_read_b64_tr_b16 a[230:231], v194 offset:4096\n"
"v_cndmask_b32 v197, v197, 0, s[82:83]\n"
"v_mfma_f32_16x16x32_bf16 v[176:179], v[204:207], v[112:115], v[176:179]\n"
"ds_read_b64_tr_b16 a[232:233], v193 offset:256\n"
"v_add_u32 v192, 0xfffe0000, v192\n"
"v_mfma_f32_16x16x32_bf16 v[180:183], v[204:207], v[120:123], v[180:183]\n"
"ds_read_b64_tr_b16 a[234:235], v193 offset:4352\n"
"v_mfma_f32_16x16x32_bf16 v[184:187], v[204:207], v[128:131], v[184:187]\n"
"ds_read_b64_tr_b16 a[236:237], v194 offset:256\n"
"v_mfma_f32_16x16x32_bf16 v[188:191], v[204:207], v[136:139], v[188:191]\n"
"ds_read_b64_tr_b16 a[238:239], v194 offset:4352\n"
"v_mfma_f32_16x16x32_bf16 a[0:3], a[224:227], v[96:99], a[0:3]\n"
"ds_read_b64_tr_b16 a[240:241], v193 offset:512\n"
"v_mfma_f32_16x16x32_bf16 a[4:7], a[224:227], v[104:107], a[4:7]\n"
"ds_read_b64_tr_b16 a[242:243], v193 offset:4608\n"
"v_mfma_f32_16x16x32_bf16 a[8:11], a[224:227], v[112:115], a[8:11]\n"
"ds_read_b64_tr_b16 a[244:245], v194 offset:512\n"
"v_mfma_f32_16x16x32_bf16 a[12:15], a[224:227], v[120:123], a[12:15]\n"
"ds_read_b64_tr_b16 a[246:247], v194 offset:4608\n"
"v_mfma_f32_16x16x32_bf16 a[16:19], a[224:227], v[128:131], a[16:19]\n"
"ds_read_b64_tr_b16 a[248:249], v193 offset:768\n"
"v_mfma_f32_16x16x32_bf16 a[20:23], a[224:227], v[136:139], a[20:23]\n"
"s_waitcnt lgkmcnt(8)\n"
"ds_read_b64_tr_b16 a[250:251], v193 offset:4864\n"
"v_add_u32 v193, 32768, v193\n"
"v_mfma_f32_16x16x32_bf16 a[24:27], a[228:231], v[96:99], a[24:27]\n"
"ds_read_b64_tr_b16 a[252:253], v194 offset:768\n"
"v_mfma_f32_16x16x32_bf16 a[28:31], a[228:231], v[104:107], a[28:31]\n"
"ds_read_b64_tr_b16 a[254:255], v194 offset:4864\n"
"v_add_u32 v194, 32768, v194\n"
"v_mfma_f32_16x16x32_bf16 a[32:35], a[228:231], v[112:115], a[32:35]\n"
"v_exp_f32 v208, v208\n"
"v_mfma_f32_16x16x32_bf16 a[36:39], a[228:231], v[120:123], a[36:39]\n"
"v_exp_f32 v209, v209\n"
"v_mfma_f32_16x16x32_bf16 a[40:43], a[228:231], v[128:131], a[40:43]\n"
"v_exp_f32 v210, v210\n"
"v_mfma_f32_16x16x32_bf16 a[44:47], a[228:231], v[136:139], a[44:47]\n"
"s_waitcnt lgkmcnt(5)\n"
"v_exp_f32 v211, v211\n"
"v_mfma_f32_16x16x32_bf16 a[48:51], a[232:235], v[96:99], a[48:51]\n"
"v_exp_f32 v212, v212\n"
"v_mfma_f32_16x16x32_bf16 a[52:55], a[232:235], v[104:107], a[52:55]\n"
"v_exp_f32 v213, v213\n"
"v_mfma_f32_16x16x32_bf16 a[56:59], a[232:235], v[112:115], a[56:59]\n"
"v_exp_f32 v214, v214\n"
"v_mfma_f32_16x16x32_bf16 a[60:63], a[232:235], v[120:123], a[60:63]\n"
"v_exp_f32 v215, v215\n"
"v_mfma_f32_16x16x32_bf16 a[64:67], a[232:235], v[128:131], a[64:67]\n"
"v_exp_f32 v216, v216\n"
"v_mfma_f32_16x16x32_bf16 a[68:71], a[232:235], v[136:139], a[68:71]\n"
"v_exp_f32 v217, v217\n"
"v_mfma_f32_16x16x32_bf16 a[72:75], a[236:239], v[96:99], a[72:75]\n"
"v_cvt_pk_bf16_f32 v208, v208, v209\n"
"v_cvt_pk_bf16_f32 v209, v210, v211\n"
"v_mfma_f32_16x16x32_bf16 a[76:79], a[236:239], v[104:107], a[76:79]\n"
"v_exp_f32 v218, v218\n"
"v_mfma_f32_16x16x32_bf16 a[80:83], a[236:239], v[112:115], a[80:83]\n"
"v_exp_f32 v219, v219\n"
"v_mfma_f32_16x16x32_bf16 a[84:87], a[236:239], v[120:123], a[84:87]\n"
"v_exp_f32 v220, v220\n"
"v_mfma_f32_16x16x32_bf16 a[88:91], a[236:239], v[128:131], a[88:91]\n"
"v_exp_f32 v221, v221\n"
"v_mfma_f32_16x16x32_bf16 a[92:95], a[236:239], v[136:139], a[92:95]\n"
"v_exp_f32 v222, v222\n"
"v_mfma_f32_16x16x32_bf16 a[96:99], a[240:243], v[96:99], a[96:99]\n"
"s_memrealtime s[70:71]\n"
"v_exp_f32 v223, v223\n"
"v_mfma_f32_16x16x32_bf16 a[100:103], a[240:243], v[104:107], a[100:103]\n"
"v_exp_f32 v224, v224\n"
"v_mfma_f32_16x16x32_bf16 a[104:107], a[240:243], v[112:115], a[104:107]\n"
"v_exp_f32 v225, v225\n"
"v_mfma_f32_16x16x32_bf16 a[108:111], a[240:243], v[120:123], a[108:111]\n"
"v_or_b32 v198, v208, v209\n"
"v_cvt_pk_bf16_f32 v210, v212, v213\n"
"v_mfma_f32_16x16x32_bf16 a[112:115], a[240:243], v[128:131], a[112:115]\n"
"v_cvt_pk_bf16_f32 v211, v214, v215\n"
"v_cvt_pk_bf16_f32 v216, v216, v217\n"
"v_mfma_f32_16x16x32_bf16 a[116:119], a[240:243], v[136:139], a[116:119]\n"
"s_waitcnt lgkmcnt(0)\n"
"v_exp_f32 v226, v226\n"
"v_mfma_f32_16x16x32_bf16 a[120:123], a[244:247], v[96:99], a[120:123]\n"
"v_exp_f32 v227, v227\n"
"v_mfma_f32_16x16x32_bf16 a[124:127], a[244:247], v[104:107], a[124:127]\n"
"v_or3_b32 v198, v198, v210, v211\n"
"v_cvt_pk_bf16_f32 v217, v218, v219\n"
"v_mfma_f32_16x16x32_bf16 a[128:131], a[244:247], v[112:115], a[128:131]\n"
"v_exp_f32 v228, v228\n"
"v_mfma_f32_16x16x32_bf16 a[132:135], a[244:247], v[120:123], a[132:135]\n"
"v_exp_f32 v229, v229\n"
"v_mfma_f32_16x16x32_bf16 a[136:139], a[244:247], v[128:131], a[136:139]\n"
"v_or3_b32 v198, v198, v216, v217\n"
"v_cvt_pk_bf16_f32 v218, v220, v221\n"
"v_mfma_f32_16x16x32_bf16 a[140:143], a[244:247], v[136:139], a[140:143]\n"
"v_cvt_pk_bf16_f32 v219, v222, v223\n"
"v_cvt_pk_bf16_f32 v224, v224, v225\n"
"v_or3_b32 v198, v198, v218, v219\n"
"v_cvt_pk_bf16_f32 v225, v226, v227\n"
"v_or3_b32 v198, v198, v224, v225\n"
"v_exp_f32 v230, v230\n"
"v_exp_f32 v231, v231\n"
"v_cvt_pk_bf16_f32 v226, v228, v229\n"
"s_waitcnt vmcnt(4) lgkmcnt(0)\n"
"s_sub_u32 s71, s72, s70\n"
"s_sub_u32 s71, s71, 16\n"
"s_cmp_lt_u32 s71, 400\n"
"s_cbranch_scc1 .Lpg13\n"
".Lpg13x:\n"
"s_barrier\n"
"v_mfma_f32_16x16x32_bf16 a[144:147], a[248:251], v[96:99], a[144:147]\n"
"s_add_u32 m0, s59, 32768\n"
"ds_read_b128 a[192:195], v192\n"
"v_cvt_pk_bf16_f32 v227, v230, v231\n"
"v_mfma_f32_16x16x32_bf16 a[148:151], a[248:251], v[104:107], a[148:151]\n"
"s_add_u32 s55, s65, 0\n"
"ds_read_b128 a[196:199], v192 offset:4096\n"
"v_or3_b32 v198, v198, v226, v227\n"
"v_mfma_f32_16x16x32_bf16 a[152:155], a[248:251], v[112:115], a[152:155]\n"
"ds_read_b128 a[200:203], v192 offset:256\n"
"v_mbcnt_lo_u32_b32 v203, -1, 0\n"
"v_mfma_f32_16x16x32_bf16 a[156:159], a[248:251], v[120:123], a[156:159]\n"
"buffer_load_dwordx4 v195, s[32:35], s55 offen lds\n"
"v_mfma_f32_16x16x32_bf16 a[160:163], a[248:251], v[128:131], a[160:163]\n"
"s_add_u32 m0, s59, 34816\n"
"ds_read_b128 a[204:207], v192 offset:4352\n"
"s_add_u32 s55, s65, 8192\n"
"v_mfma_f32_16x16x32_bf16 a[164:167], a[248:251], v[136:139], a[164:167]\n"
"ds_read_b128 a[208:211], v192 offset:512\n"
"v_mbcnt_hi_u32_b32 v203, -1, v203\n"
"v_mfma_f32_16x16x32_bf16 a[168:171], a[252:255], v[96:99], a[168:171]\n"
"ds_read_b128 a[212:215], v192 offset:4608\n"
"v_mul_u32_u24 v203, 1028, v203\n"
"v_mfma_f32_16x16x32_bf16 a[172:175], a[252:255], v[104:107], a[172:175]\n"
"buffer_load_dwordx4 v195, s[32:35], s55 offen lds\n"
"v_mfma_f32_16x16x32_bf16 a[176:179], a[252:255], v[112:115], a[176:179]\n"
"s_add_u32 m0, s59, 36864\n"
"ds_read_b128 a[216:219], v192 offset:768\n"
"s_add_u32 s55, s65, 16384\n"
"v_mfma_f32_16x16x32_bf16 a[180:183], a[252:255], v[120:123], a[180:183]\n"
"ds_read_b128 a[220:223], v192 offset:4864\n"
"v_and_b32 v203, 0x7c80, v203\n"
"v_mfma_f32_16x16x32_bf16 a[184:187], a[252:255], v[128:131], a[184:187]\n"
"v_exp_f32 v232, v232\n"
"v_mfma_f32_16x16x32_bf16 a[188:191], a[252:255], v[136:139], a[188:191]\n"
"s_waitcnt lgkmcnt(5)\n"
"v_exp_f32 v233, v233\n"
"v_mfma_f32_16x16x32_bf16 v[96:99], a[192:195], v[0:3], v[144:147]\n"
"buffer_load_dwordx4 v195, s[32:35], s55 offen lds\n"
"v_mfma_f32_16x16x32_bf16 v[104:107], a[192:195], v[4:7], v[148:151]\n"
"s_add_u32 m0, s59, 38912\n"
"v_exp_f32 v234, v234\n"
"v_mfma_f32_16x16x32_bf16 v[112:115], a[192:195], v[8:11], v[152:155]\n"
"s_add_u32 s55, s65, 24576\n"
"ds_read_b64_tr_b16 a[224:225], v193\n"
"v_cvt_pk_bf16_f32 v232, v232, v233\n"
"v_mfma_f32_16x16x32_bf16 v[120:123], a[192:195], v[12:15], v[156:159]\n"
"buffer_load_dwordx4 v195, s[32:35], s55 offen lds\n"
"v_mfma_f32_16x16x32_bf16 v[128:131], a[192:195], v[16:19], v[160:163]\n"
"v_exp_f32 v235, v235\n"
"v_mfma_f32_16x16x32_bf16 v[136:139], a[192:195], v[20:23], v[164:167]\n"
"s_add_u32 m0, s59, 65536\n"
"ds_read_b64_tr_b16 a[226:227], v193 offset:4096\n"
"v_cvt_pk_bf16_f32 v233, v234, v235\n"
"v_mfma_f32_16x16x32_bf16 v[100:103], a[196:199], v[0:3], v[144:147]\n"
"s_add_u32 s55, s65, 32768\n"
"v_exp_f32 v236, v236\n"
"v_mfma_f32_16x16x32_bf16 v[108:111], a[196:199], v[4:7], v[148:151]\n"
"buffer_load_dwordx4 v195, s[32:35], s55 offen lds\n"
"v_mfma_f32_16x16x32_bf16 v[116:119], a[196:199], v[8:11], v[152:155]\n"
"v_exp_f32 v237, v237\n"
"v_mfma_f32_16x16x32_bf16 v[124:127], a[196:199], v[12:15], v[156:159]\n"
"s_add_u32 m0, s59, 67584\n"
"v_exp_f32 v238, v238\n"
"v_mfma_f32_16x16x32_bf16 v[132:135], a[196:199], v[16:19], v[160:163]\n"
"s_add_u32 s55, s65, 40960\n"
"v_exp_f32 v239, v239\n"
"v_mfma_f32_16x16x32_bf16 v[140:143], a[196:199], v[20:23], v[164:167]\n"
"buffer_load_dwordx4 v195, s[32:35], s55 offen lds\n"
"v_mfma_f32_16x16x32_bf16 v[96:99], a[200:203], v[24:27], v[96:99]\n"
"v_exp_f32 v240, v240\n"
"v_mfma_f32_16x16x32_bf16 v[104:107], a[200:203], v[28:31], v[104:107]\n"
"s_add_u32 m0, s59, 69632\n"
"v_exp_f32 v241, v241\n"
"v_mfma_f32_16x16x32_bf16 v[112:115], a[200:203], v[32:35], v[112:115]\n"
"s_add_u32 s55, s65, 49152\n"
"v_exp_f32 v242, v242\n"
"v_mfma_f32_16x16x32_bf16 v[120:123], a[200:203], v[36:39], v[120:123]\n"
"buffer_load_dwordx4 v195, s[32:35], s55 offen lds\n"
"v_mfma_f32_16x16x32_bf16 v[128:131], a[200:203], v[40:43], v[128:131]\n"
"v_exp_f32 v243, v243\n"
"v_mfma_f32_16x16x32_bf16 v[136:139], a[200:203], v[44:47], v[136:139]\n"
"s_waitcnt lgkmcnt(0)\n"
"v_exp_f32 v244, v244\n"
"v_mfma_f32_16x16x32_bf16 v[100:103], a[204:207], v[24:27], v[100:103]\n"
"s_add_u32 m0, s59, 71680\n"
"v_exp_f32 v245, v245\n"
"v_mfma_f32_16x16x32_bf16 v[108:111], a[204:207], v[28:31], v[108:111]\n"
"s_add_u32 s55, s65, 57344\n"
"v_exp_f32 v246, v246\n"
"v_mfma_f32_16x16x32_bf16 v[116:119], a[204:207], v[32:35], v[116:119]\n"
"buffer_load_dwordx4 v195, s[32:35], s55 offen lds\n"
"v_mfma_f32_16x16x32_bf16 v[124:127], a[204:207], v[36:39], v[124:127]\n"
"s_add_u32 m0, s59, 49152\n"
"v_exp_f32 v247, v247\n"
"v_mfma_f32_16x16x32_bf16 v[132:135], a[204:207], v[40:43], v[132:135]\n"
"s_add_u32 s55, s65, 65536\n"
"v_exp_f32 v248, v248\n"
"v_mfma_f32_16x16x32_bf16 v[140:143], a[204:207], v[44:47], v[140:143]\n"
"v_exp_f32 v249, v249\n"
"v_mfma_f32_16x16x32_bf16 v[96:99], a[208:211], v[48:51], v[96:99]\n"
"buffer_load_dwordx4 v195, s[32:35], s55 offen lds\n"
"v_mfma_f32_16x16x32_bf16 v[104:107], a[208:211], v[52:55], v[104:107]\n"
"s_add_u32 m0, s59, 51200\n"
"v_or3_b32 v198, v198, v232, v233\n"
"v_cvt_pk_bf16_f32 v234, v236, v237\n"
"v_mfma_f32_16x16x32_bf16 v[112:115], a[208:211], v[56:59], v[112:115]\n"
"s_add_u32 s55, s65, 73728\n"
"v_cvt_pk_bf16_f32 v235, v238, v239\n"
"v_cvt_pk_bf16_f32 v240, v240, v241\n"
"v_mfma_f32_16x16x32_bf16 v[120:123], a[208:211], v[60:63], v[120:123]\n"
"v_exp_f32 v250, v250\n"
"v_mfma_f32_16x16x32_bf16 v[128:131], a[208:211], v[64:67], v[128:131]\n"
"buffer_load_dwordx4 v195, s[32:35], s55 offen lds\n"
"v_mfma_f32_16x16x32_bf16 v[136:139], a[208:211], v[68:71], v[136:139]\n"
"s_add_u32 m0, s59, 53248\n"
"v_exp_f32 v251, v251\n"
"v_mfma_f32_16x16x32_bf16 v[100:103], a[212:215], v[48:51], v[100:103]\n"
"s_add_u32 s55, s65, 81920\n"
"v_or3_b32 v198, v198, v234, v235\n"
"v_cvt_pk_bf16_f32 v241, v242, v243\n"
"v_mfma_f32_16x16x32_bf16 v[108:111], a[212:215], v[52:55], v[108:111]\n"
"v_exp_f32 v252, v252\n"
"v_mfma_f32_16x16x32_bf16 v[116:119], a[212:215], v[56:59], v[116:119]\n"
"buffer_load_dwordx4 v195, s[32:35], s55 offen lds\n"
"v_mfma_f32_16x16x32_bf16 v[124:127], a[212:215], v[60:63], v[124:127]\n"
"s_add_u32 m0, s59, 55296\n"
"v_or3_b32 v198, v198, v240, v241\n"
"v_mfma_f32_16x16x32_bf16 v[132:135], a[212:215], v[64:67], v[132:135]\n"
"s_add_u32 s55, s65, 90112\n"
"v_exp_f32 v253, v253\n"
"v_mfma_f32_16x16x32_bf16 v[140:143], a[212:215], v[68:71], v[140:143]\n"
"v_exp_f32 v254, v254\n"
"v_mfma_f32_16x16x32_bf16 v[96:99], a[216:219], v[72:75], v[96:99]\n"
"buffer_load_dwordx4 v195, s[32:35], s55 offen lds\n"
"v_mfma_f32_16x16x32_bf16 v[104:107], a[216:219], v[76:79], v[104:107]\n"
"v_exp_f32 v255, v255\n"
"v_mfma_f32_16x16x32_bf16 v[112:115], a[216:219], v[80:83], v[112:115]\n"
"s_add_u32 m0, s59, 98304\n"
"v_cvt_pk_bf16_f32 v242, v244, v245\n"
"v_cvt_pk_bf16_f32 v243, v246, v247\n"
"v_mfma_f32_16x16x32_bf16 v[120:123], a[216:219], v[84:87], v[120:123]\n"
"s_add_u32 s55, s65, 98304\n"
"v_cvt_pk_bf16_f32 v248, v248, v249\n"
"v_or3_b32 v198, v198, v242, v243\n"
"v_mfma_f32_16x16x32_bf16 v[128:131], a[216:219], v[88:91], v[128:131]\n"
"buffer_load_dwordx4 v195, s[32:35], s55 offen lds\n"
"v_mfma_f32_16x16x32_bf16 v[136:139], a[216:219], v[92:95], v[136:139]\n"
"v_cvt_pk_bf16_f32 v249, v250, v251\n"
"v_or3_b32 v198, v198, v248, v249\n"
"v_mfma_f32_16x16x32_bf16 v[100:103], a[220:223], v[72:75], v[100:103]\n"
"s_add_u32 m0, s59, 100352\n"
"v_cvt_pk_bf16_f32 v250, v252, v253\n"
"v_cvt_pk_bf16_f32 v251, v254, v255\n"
"v_mfma_f32_16x16x32_bf16 v[108:111], a[220:223], v[76:79], v[108:111]\n"
"s_add_u32 s55, s65, 106496\n"
"v_or3_b32 v198, v198, v250, v251\n"
"v_and_b32 v199, 0x40004000, v198\n"
"v_mfma_f32_16x16x32_bf16 v[116:119], a[220:223], v[80:83], v[116:119]\n"
"buffer_load_dwordx4 v195, s[32:35], s55 offen lds\n"
"v_mfma_f32_16x16x32_bf16 v[124:127], a[220:223], v[84:87], v[124:127]\n"
"v_cmp_ne_u32 vcc, 0, v199\n"
"ds_read_b64_tr_b16 a[228:229], v194\n"
"v_mfma_f32_16x16x32_bf16 v[132:135], a[220:223], v[88:91], v[132:135]\n"
"s_add_u32 m0, s59, 102400\n"
"ds_read_b64_tr_b16 a[230:231], v194 offset:4096\n"
"s_add_u32 s55, s65, 114688\n"
"v_mfma_f32_16x16x32_bf16 v[140:143], a[220:223], v[92:95], v[140:143]\n"
"s_cbranch_vccnz .Lrare16\n"
".Lrare16_ret:\n"
"ds_read_b64_tr_b16 a[232:233], v193 offset:256\n"
"v_add_u32 v192, 32768, v192\n"
"v_mfma_f32_16x16x32_bf16 v[168:171], v[204:207], v[208:211], v[168:171]\n"
"buffer_load_dwordx4 v195, s[32:35], s55 offen lds\n"
"v_mfma_f32_16x16x32_bf16 v[172:175], v[204:207], v[216:219], v[172:175]\n"
"ds_read_b64_tr_b16 a[234:235], v193 offset:4352\n"
"v_mfma_f32_16x16x32_bf16 v[176:179], v[204:207], v[224:227], v[176:179]\n"
"s_add_u32 m0, s59, 104448\n"
"ds_read_b64_tr_b16 a[236:237], v194 offset:256\n"
"s_add_u32 s55, s65, 122880\n"
"v_mfma_f32_16x16x32_bf16 v[180:183], v[204:207], v[232:235], v[180:183]\n"
"ds_read_b64_tr_b16 a[238:239], v194 offset:4352\n"
"v_mfma_f32_16x16x32_bf16 v[184:187], v[204:207], v[240:243], v[184:187]\n"
"ds_read_b64_tr_b16 a[240:241], v193 offset:512\n"
"v_mfma_f32_16x16x32_bf16 v[188:191], v[204:207], v[248:251], v[188:191]\n"
"buffer_load_dwordx4 v195, s[32:35], s55 offen lds\n"
"v_mfma_f32_16x16x32_bf16 a[0:3], a[224:227], v[208:211], a[0:3]\n"
"s_add_u32 m0, s59, 81920\n"
"ds_read_b64_tr_b16 a[242:243], v193 offset:4608\n"
"s_add_u32 s55, s66, 0\n"
"v_mfma_f32_16x16x32_bf16 a[4:7], a[224:227], v[216:219], a[4:7]\n"
"ds_read_b64_tr_b16 a[244:245], v194 offset:512\n"
"v_mfma_f32_16x16x32_bf16 a[8:11], a[224:227], v[224:227], a[8:11]\n"
"ds_read_b64_tr_b16 a[246:247], v194 offset:4608\n"
"v_mfma_f32_16x16x32_bf16 a[12:15], a[224:227], v[232:235], a[12:15]\n"
"buffer_load_dwordx4 v195, s[32:35], s55 offen lds\n"
"v_mfma_f32_16x16x32_bf16 a[16:19], a[224:227], v[240:243], a[16:19]\n"
"s_add_u32 m0, s59, 83968\n"
"ds_read_b64_tr_b16 a[248:249], v193 offset:768\n"
"s_add_u32 s55, s66, 8192\n"
"v_mfma_f32_16x16x32_bf16 a[20:23], a[224:227], v[248:251], a[20:23]\n"
"s_waitcnt lgkmcnt(6)\n"
"ds_read_b64_tr_b16 a[250:251], v193 offset:4864\n"
"v_add_u32 v193, 0xfffe0000, v193\n"
"v_mfma_f32_16x16x32_bf16 a[24:27], a[228:231], v[208:211], a[24:27]\n"
"ds_read_b64_tr_b16 a[252:253], v194 offset:768\n"
"v_mfma_f32_16x16x32_bf16 a[28:31], a[228:231], v[216:219], a[28:31]\n"
"buffer_load_dwordx4 v195, s[32:35], s55 offen lds\n"
"v_mfma_f32_16x16x32_bf16 a[32:35], a[228:231], v[224:227], a[32:35]\n"
"s_add_u32 m0, s59, 86016\n"
"ds_read_b64_tr_b16 a[254:255], v194 offset:4864\n"
"s_add_u32 s55, s66, 16384\n"
"v_mfma_f32_16x16x32_bf16 a[36:39], a[228:231], v[232:235], a[36:39]\n"
"v_exp_f32 v96, v96\n"
"v_mfma_f32_16x16x32_bf16 a[40:43], a[228:231], v[240:243], a[40:43]\n"
"v_exp_f32 v97, v97\n"
"v_mfma_f32_16x16x32_bf16 a[44:47], a[228:231], v[248:251], a[44:47]\n"
"buffer_load_dwordx4 v195, s[32:35], s55 offen lds\n"
"v_mfma_f32_16x16x32_bf16 a[48:51], a[232:235], v[208:211], a[48:51]\n"
"s_add_u32 m0, s59, 88064\n"
"s_add_u32 s55, s66, 24576\n"
"v_mfma_f32_16x16x32_bf16 a[52:55], a[232:235], v[216:219], a[52:55]\n"
"s_and_b32 s50, s44, 7\n"
"s_lshl_b32 s50, s50, 1\n"
"s_add_u32 s50, s50, s54\n"
"v_mfma_f32_16x16x32_bf16 a[56:59], a[232:235], v[224:227], a[56:59]\n"
"s_lshr_b32 s51, s50, 2\n"
"v_exp_f32 v98, v98\n"
"v_mfma_f32_16x16x32_bf16 a[60:63], a[232:235], v[232:235], a[60:63]\n"
"buffer_load_dwordx4 v195, s[32:35], s55 offen lds\n"
"v_mfma_f32_16x16x32_bf16 a[64:67], a[232:235], v[240:243], a[64:67]\n"
"s_lshl_b32 s51, s51, 18\n"
"v_exp_f32 v99, v99\n"
"v_mfma_f32_16x16x32_bf16 a[68:71], a[232:235], v[248:251], a[68:71]\n"
"s_waitcnt lgkmcnt(0)\n"
"v_exp_f32 v100, v100\n"
"v_mfma_f32_16x16x32_bf16 a[72:75], a[236:239], v[208:211], a[72:75]\n"
"s_and_b32 s50, s50, 3\n"
"v_exp_f32 v101, v101\n"
"v_mfma_f32_16x16x32_bf16 a[76:79], a[236:239], v[216:219], a[76:79]\n"
"s_lshl_b32 s50, s50, 15\n"
"v_exp_f32 v102, v102\n"
"v_mfma_f32_16x16x32_bf16 a[80:83], a[236:239], v[224:227], a[80:83]\n"
"s_add_u32 s50, s50, s51\n"
"v_exp_f32 v103, v103\n"
"v_mfma_f32_16x16x32_bf16 a[84:87], a[236:239], v[232:235], a[84:87]\n"
"s_add_u32 m0, s59, 131072\n"
"v_exp_f32 v104, v104\n"
"v_mfma_f32_16x16x32_bf16 a[88:91], a[236:239], v[240:243], a[88:91]\n"
"buffer_load_dword v203, s[32:35], s50 offen lds\n"
"v_mfma_f32_16x16x32_bf16 a[92:95], a[236:239], v[248:251], a[92:95]\n"
"s_add_u32 s64, s64, 8192\n"
"v_exp_f32 v105, v105\n"
"v_mfma_f32_16x16x32_bf16 a[96:99], a[240:243], v[208:211], a[96:99]\n"
"s_add_u32 s72, s72, 112\n"
"v_cvt_pk_bf16_f32 v96, v96, v97\n"
"v_cvt_pk_bf16_f32 v97, v98, v99\n"
"v_mfma_f32_16x16x32_bf16 a[100:103], a[240:243], v[216:219], a[100:103]\n"
"s_memrealtime s[70:71]\n"
"v_exp_f32 v106, v106\n"
"v_mfma_f32_16x16x32_bf16 a[104:107], a[240:243], v[224:227], a[104:107]\n"
"v_exp_f32 v107, v107\n"
"v_mfma_f32_16x16x32_bf16 a[108:111], a[240:243], v[232:235], a[108:111]\n"
"v_exp_f32 v108, v108\n"
"v_mfma_f32_16x16x32_bf16 a[112:115], a[240:243], v[240:243], a[112:115]\n"
"v_exp_f32 v109, v109\n"
"v_mfma_f32_16x16x32_bf16 a[116:119], a[240:243], v[248:251], a[116:119]\n"
"v_exp_f32 v110, v110\n"
"v_mfma_f32_16x16x32_bf16 a[120:123], a[244:247], v[208:211], a[120:123]\n"
"v_exp_f32 v111, v111\n"
"v_mfma_f32_16x16x32_bf16 a[124:127], a[244:247], v[216:219], a[124:127]\n"
"v_exp_f32 v112, v112\n"
"v_mfma_f32_16x16x32_bf16 a[128:131], a[244:247], v[224:227], a[128:131]\n"
"v_exp_f32 v113, v113\n"
"v_mfma_f32_16x16x32_bf16 a[132:135], a[244:247], v[232:235], a[132:135]\n"
"v_or_b32 v198, v96, v97\n"
"v_cvt_pk_bf16_f32 v98, v100, v101\n"
"v_mfma_f32_16x16x32_bf16 a[136:139], a[244:247], v[240:243], a[136:139]\n"
"v_cvt_pk_bf16_f32 v99, v102, v103\n"
"v_cvt_pk_bf16_f32 v104, v104, v105\n"
"v_mfma_f32_16x16x32_bf16 a[140:143], a[244:247], v[248:251], a[140:143]\n"
"v_exp_f32 v114, v114\n"
"v_exp_f32 v115, v115\n"
"v_or3_b32 v198, v198, v98, v99\n"
"v_cvt_pk_bf16_f32 v105, v106, v107\n"
"v_exp_f32 v116, v116\n"
"v_exp_f32 v117, v117\n"
"v_or3_b32 v198, v198, v104, v105\n"
"v_cvt_pk_bf16_f32 v106, v108, v109\n"
"v_cvt_pk_bf16_f32 v107, v110, v111\n"
"v_cvt_pk_bf16_f32 v112, v112, v113\n"
"v_or3_b32 v198, v198, v106, v107\n"
"v_cvt_pk_bf16_f32 v113, v114, v115\n"
"v_or3_b32 v198, v198, v112, v113\n"
"v_exp_f32 v118, v118\n"
"v_exp_f32 v119, v119\n"
"v_cvt_pk_bf16_f32 v114, v116, v117\n"
"v_add_u32 v194, 0xfffe0000, v194\n"
"s_waitcnt vmcnt(1) lgkmcnt(0)\n"
"s_sub_u32 s71, s72, s70\n"
"s_sub_u32 s71, s71, 16\n"
"s_cmp_lt_u32 s71, 400\n"
"s_cbranch_scc1 .Lpg14\n"
".Lpg14x:\n"
"s_barrier\n"
"v_mfma_f32_16x16x32_bf16 a[144:147], a[248:251], v[208:211], a[144:147]\n"
"ds_read_b64_tr_b16 a[224:225], v193\n"
"v_cvt_pk_bf16_f32 v115, v118, v119\n"
"v_mfma_f32_16x16x32_bf16 a[148:151], a[248:251], v[216:219], a[148:151]\n"
"ds_read_b64_tr_b16 a[226:227], v193 offset:4096\n"
"v_or3_b32 v198, v198, v114, v115\n"
"v_mfma_f32_16x16x32_bf16 a[152:155], a[248:251], v[224:227], a[152:155]\n"
"ds_read_b64_tr_b16 a[228:229], v194\n"
"v_mbcnt_lo_u32_b32 v192, -1, 0\n"
"v_mfma_f32_16x16x32_bf16 a[156:159], a[248:251], v[232:235], a[156:159]\n"
"v_exp_f32 v120, v120\n"
"v_mfma_f32_16x16x32_bf16 a[160:163], a[248:251], v[240:243], a[160:163]\n"
"ds_read_b64_tr_b16 a[230:231], v194 offset:4096\n"
"v_mbcnt_hi_u32_b32 v192, -1, v192\n"
"v_mfma_f32_16x16x32_bf16 a[164:167], a[248:251], v[248:251], a[164:167]\n"
"ds_read_b64_tr_b16 a[232:233], v193 offset:256\n"
"v_lshlrev_b32 v192, 4, v192\n"
"v_mfma_f32_16x16x32_bf16 a[168:171], a[252:255], v[208:211], a[168:171]\n"
"ds_read_b64_tr_b16 a[234:235], v193 offset:4352\n"
"v_mfma_f32_16x16x32_bf16 a[172:175], a[252:255], v[216:219], a[172:175]\n"
"ds_read_b64_tr_b16 a[236:237], v194 offset:256\n"
"v_mfma_f32_16x16x32_bf16 a[176:179], a[252:255], v[224:227], a[176:179]\n"
"v_exp_f32 v121, v121\n"
"v_mfma_f32_16x16x32_bf16 a[180:183], a[252:255], v[232:235], a[180:183]\n"
"ds_read_b64_tr_b16 a[238:239], v194 offset:4352\n"
"v_cvt_pk_bf16_f32 v120, v120, v121\n"
"v_mfma_f32_16x16x32_bf16 a[184:187], a[252:255], v[240:243], a[184:187]\n"
"ds_read_b64_tr_b16 a[240:241], v193 offset:512\n"
"v_mfma_f32_16x16x32_bf16 a[188:191], a[252:255], v[248:251], a[188:191]\n"
"ds_read_b64_tr_b16 a[242:243], v193 offset:4608\n"
"v_exp_f32 v122, v122\n"
"v_exp_f32 v123, v123\n"
"ds_read_b64_tr_b16 a[244:245], v194 offset:512\n"
"v_exp_f32 v124, v124\n"
"v_exp_f32 v125, v125\n"
"ds_read_b64_tr_b16 a[246:247], v194 offset:4608\n"
"v_exp_f32 v126, v126\n"
"v_exp_f32 v127, v127\n"
"ds_read_b64_tr_b16 a[248:249], v193 offset:768\n"
"v_exp_f32 v128, v128\n"
"v_exp_f32 v129, v129\n"
"ds_read_b64_tr_b16 a[250:251], v193 offset:4864\n"
"v_exp_f32 v130, v130\n"
"v_exp_f32 v131, v131\n"
"ds_read_b64_tr_b16 a[252:253], v194 offset:768\n"
"v_cvt_pk_bf16_f32 v121, v122, v123\n"
"v_exp_f32 v132, v132\n"
"ds_read_b64_tr_b16 a[254:255], v194 offset:4864\n"
"v_exp_f32 v133, v133\n"
"v_exp_f32 v134, v134\n"
"v_exp_f32 v135, v135\n"
"v_exp_f32 v136, v136\n"
"v_exp_f32 v137, v137\n"
"v_or3_b32 v198, v198, v120, v121\n"
"v_cvt_pk_bf16_f32 v122, v124, v125\n"
"v_cvt_pk_bf16_f32 v123, v126, v127\n"
"v_cvt_pk_bf16_f32 v128, v128, v129\n"
"v_exp_f32 v138, v138\n"
"v_exp_f32 v139, v139\n"
"v_or3_b32 v198, v198, v122, v123\n"
"v_cvt_pk_bf16_f32 v129, v130, v131\n"
"v_exp_f32 v140, v140\n"
"v_exp_f32 v141, v141\n"
"v_exp_f32 v142, v142\n"
"v_exp_f32 v143, v143\n"
"v_or3_b32 v198, v198, v128, v129\n"
"v_cvt_pk_bf16_f32 v130, v132, v133\n"
"v_cvt_pk_bf16_f32 v131, v134, v135\n"
"v_cvt_pk_bf16_f32 v136, v136, v137\n"
"v_or3_b32 v198, v198, v130, v131\n"
"v_cvt_pk_bf16_f32 v137, v138, v139\n"
"v_or3_b32 v198, v198, v136, v137\n"
"v_cvt_pk_bf16_f32 v138, v140, v141\n"
"v_cvt_pk_bf16_f32 v139, v142, v143\n"
"v_or3_b32 v198, v198, v138, v139\n"
"v_and_b32 v199, 0x40004000, v198\n"
"v_cmp_ne_u32 vcc, 0, v199\n"
"s_cbranch_vccnz .Lrare2\n"
".Lrare2_ret:\n"
"v_mfma_f32_16x16x32_bf16 v[168:171], v[204:207], v[96:99], v[168:171]\n"
"s_add_u32 s64, s64, 8192\n"
"s_add_u32 s61, s88, 0\n"
"v_add_u32 v193, 16384, v193\n"
"v_mfma_f32_16x16x32_bf16 v[172:175], v[204:207], v[104:107], v[172:175]\n"
"ds_read_b64_tr_b16 a[192:193], v193\n"
"v_add_u32 v194, 16384, v194\n"
"v_mfma_f32_16x16x32_bf16 v[176:179], v[204:207], v[112:115], v[176:179]\n"
"ds_read_b64_tr_b16 a[194:195], v193 offset:4096\n"
"v_mfma_f32_16x16x32_bf16 v[180:183], v[204:207], v[120:123], v[180:183]\n"
"v_rcp_f32 v200, v168\n"
"v_mfma_f32_16x16x32_bf16 v[184:187], v[204:207], v[128:131], v[184:187]\n"
"ds_read_b64_tr_b16 a[196:197], v194\n"
"v_mfma_f32_16x16x32_bf16 v[188:191], v[204:207], v[136:139], v[188:191]\n"
"s_waitcnt lgkmcnt(3)\n"
"v_rcp_f32 v202, v172\n"
"v_mfma_f32_16x16x32_bf16 a[0:3], a[224:227], v[96:99], a[0:3]\n"
"v_rcp_f32 v203, v176\n"
"v_mfma_f32_16x16x32_bf16 a[24:27], a[228:231], v[96:99], a[24:27]\n"
"v_rcp_f32 v204, v180\n"
"v_mfma_f32_16x16x32_bf16 a[4:7], a[224:227], v[104:107], a[4:7]\n"
"v_rcp_f32 v205, v184\n"
"v_mfma_f32_16x16x32_bf16 a[28:31], a[228:231], v[104:107], a[28:31]\n"
"v_rcp_f32 v206, v188\n"
"v_mfma_f32_16x16x32_bf16 a[8:11], a[224:227], v[112:115], a[8:11]\n"
"v_accvgpr_read_b32 v198, a0\n"
"v_accvgpr_read_b32 v199, a1\n"
"v_mfma_f32_16x16x32_bf16 a[32:35], a[228:231], v[112:115], a[32:35]\n"
"v_mul_f32 v198, v198, v200\n"
"v_mul_f32 v199, v199, v200\n"
"v_mfma_f32_16x16x32_bf16 a[12:15], a[224:227], v[120:123], a[12:15]\n"
"v_cvt_pk_bf16_f32 v208, v198, v199\n"
"v_accvgpr_read_b32 v198, a2\n"
"v_mfma_f32_16x16x32_bf16 a[36:39], a[228:231], v[120:123], a[36:39]\n"
"v_accvgpr_read_b32 v199, a3\n"
"v_mul_f32 v198, v198, v200\n"
"v_mfma_f32_16x16x32_bf16 a[16:19], a[224:227], v[128:131], a[16:19]\n"
"v_mul_f32 v199, v199, v200\n"
"v_cvt_pk_bf16_f32 v209, v198, v199\n"
"v_mfma_f32_16x16x32_bf16 a[40:43], a[228:231], v[128:131], a[40:43]\n"
"v_accvgpr_read_b32 v198, a24\n"
"v_accvgpr_read_b32 v199, a25\n"
"v_mfma_f32_16x16x32_bf16 a[20:23], a[224:227], v[136:139], a[20:23]\n"
"v_mul_f32 v198, v198, v200\n"
"v_mul_f32 v199, v199, v200\n"
"v_mfma_f32_16x16x32_bf16 a[44:47], a[228:231], v[136:139], a[44:47]\n"
"v_cvt_pk_bf16_f32 v210, v198, v199\n"
"v_accvgpr_read_b32 v198, a26\n"
"v_mfma_f32_16x16x32_bf16 a[48:51], a[232:235], v[96:99], a[48:51]\n"
"v_accvgpr_read_b32 v199, a27\n"
"v_mul_f32 v198, v198, v200\n"
"v_mfma_f32_16x16x32_bf16 a[72:75], a[236:239], v[96:99], a[72:75]\n"
"v_mul_f32 v199, v199, v200\n"
"v_cvt_pk_bf16_f32 v211, v198, v199\n"
"v_mfma_f32_16x16x32_bf16 a[52:55], a[232:235], v[104:107], a[52:55]\n"
"buffer_store_dwordx4 v[208:211], v192, s[92:95], s61 offen sc1\n"
"v_mfma_f32_16x16x32_bf16 a[76:79], a[236:239], v[104:107], a[76:79]\n"
"v_accvgpr_read_b32 v198, a4\n"
"v_accvgpr_read_b32 v199, a5\n"
"v_mfma_f32_16x16x32_bf16 a[56:59], a[232:235], v[112:115], a[56:59]\n"
"v_mul_f32 v198, v198, v202\n"
"v_mul_f32 v199, v199, v202\n"
"v_mfma_f32_16x16x32_bf16 a[80:83], a[236:239], v[112:115], a[80:83]\n"
"v_cvt_pk_bf16_f32 v212, v198, v199\n"
"v_accvgpr_read_b32 v198, a6\n"
"v_mfma_f32_16x16x32_bf16 a[60:63], a[232:235], v[120:123], a[60:63]\n"
"v_accvgpr_read_b32 v199, a7\n"
"v_mul_f32 v198, v198, v202\n"
"v_mfma_f32_16x16x32_bf16 a[84:87], a[236:239], v[120:123], a[84:87]\n"
"v_mul_f32 v199, v199, v202\n"
"v_cvt_pk_bf16_f32 v213, v198, v199\n"
"v_mfma_f32_16x16x32_bf16 a[64:67], a[232:235], v[128:131], a[64:67]\n"
"v_accvgpr_read_b32 v198, a28\n"
"v_accvgpr_read_b32 v199, a29\n"
"v_mfma_f32_16x16x32_bf16 a[88:91], a[236:239], v[128:131], a[88:91]\n"
"v_mul_f32 v198, v198, v202\n"
"v_mul_f32 v199, v199, v202\n"
"v_mfma_f32_16x16x32_bf16 a[68:71], a[232:235], v[136:139], a[68:71]\n"
"v_cvt_pk_bf16_f32 v214, v198, v199\n"
"v_accvgpr_read_b32 v198, a30\n"
"v_mfma_f32_16x16x32_bf16 a[92:95], a[236:239], v[136:139], a[92:95]\n"
"v_accvgpr_read_b32 v199, a31\n"
"v_mul_f32 v198, v198, v202\n"
"v_mfma_f32_16x16x32_bf16 a[96:99], a[240:243], v[96:99], a[96:99]\n"
"v_mul_f32 v199, v199, v202\n"
"v_cvt_pk_bf16_f32 v215, v198, v199\n"
"v_mfma_f32_16x16x32_bf16 a[120:123], a[244:247], v[96:99], a[120:123]\n"
"buffer_store_dwordx4 v[212:215], v192, s[92:95], s61 offen offset:1024 sc1\n"
"v_mfma_f32_16x16x32_bf16 a[100:103], a[240:243], v[104:107], a[100:103]\n"
"v_accvgpr_read_b32 v198, a8\n"
"v_accvgpr_read_b32 v199, a9\n"
"v_mfma_f32_16x16x32_bf16 a[124:127], a[244:247], v[104:107], a[124:127]\n"
"v_mul_f32 v198, v198, v203\n"
"v_mul_f32 v199, v199, v203\n"
"v_mfma_f32_16x16x32_bf16 a[104:107], a[240:243], v[112:115], a[104:107]\n"
"v_cvt_pk_bf16_f32 v216, v198, v199\n"
"v_accvgpr_read_b32 v198, a10\n"
"v_mfma_f32_16x16x32_bf16 a[128:131], a[244:247], v[112:115], a[128:131]\n"
"v_accvgpr_read_b32 v199, a11\n"
"v_mul_f32 v198, v198, v203\n"
"v_mfma_f32_16x16x32_bf16 a[108:111], a[240:243], v[120:123], a[108:111]\n"
"v_mul_f32 v199, v199, v203\n"
"v_cvt_pk_bf16_f32 v217, v198, v199\n"
"v_mfma_f32_16x16x32_bf16 a[132:135], a[244:247], v[120:123], a[132:135]\n"
"v_accvgpr_read_b32 v198, a32\n"
"v_accvgpr_read_b32 v199, a33\n"
"v_mfma_f32_16x16x32_bf16 a[112:115], a[240:243], v[128:131], a[112:115]\n"
"v_mul_f32 v198, v198, v203\n"
"v_mul_f32 v199, v199, v203\n"
"v_mfma_f32_16x16x32_bf16 a[136:139], a[244:247], v[128:131], a[136:139]\n"
"v_cvt_pk_bf16_f32 v218, v198, v199\n"
"v_accvgpr_read_b32 v198, a34\n"
"v_mfma_f32_16x16x32_bf16 a[116:119], a[240:243], v[136:139], a[116:119]\n"
"v_accvgpr_read_b32 v199, a35\n"
"v_mul_f32 v198, v198, v203\n"
"v_mfma_f32_16x16x32_bf16 a[140:143], a[244:247], v[136:139], a[140:143]\n"
"v_mul_f32 v199, v199, v203\n"
"v_cvt_pk_bf16_f32 v219, v198, v199\n"
"v_mfma_f32_16x16x32_bf16 a[144:147], a[248:251], v[96:99], a[144:147]\n"
"buffer_store_dwordx4 v[216:219], v192, s[92:95], s61 offen offset:2048 sc1\n"
"v_mfma_f32_16x16x32_bf16 a[168:171], a[252:255], v[96:99], a[168:171]\n"
"v_accvgpr_read_b32 v198, a12\n"
"v_accvgpr_read_b32 v199, a13\n"
"v_mfma_f32_16x16x32_bf16 a[148:151], a[248:251], v[104:107], a[148:151]\n"
"v_mul_f32 v198, v198, v204\n"
"v_mul_f32 v199, v199, v204\n"
"v_mfma_f32_16x16x32_bf16 a[172:175], a[252:255], v[104:107], a[172:175]\n"
"v_cvt_pk_bf16_f32 v220, v198, v199\n"
"v_accvgpr_read_b32 v198, a14\n"
"v_mfma_f32_16x16x32_bf16 a[152:155], a[248:251], v[112:115], a[152:155]\n"
"v_accvgpr_read_b32 v199, a15\n"
"v_mul_f32 v198, v198, v204\n"
"v_mfma_f32_16x16x32_bf16 a[176:179], a[252:255], v[112:115], a[176:179]\n"
"v_mul_f32 v199, v199, v204\n"
"v_cvt_pk_bf16_f32 v221, v198, v199\n"
"v_mfma_f32_16x16x32_bf16 a[156:159], a[248:251], v[120:123], a[156:159]\n"
"v_accvgpr_read_b32 v198, a36\n"
"v_accvgpr_read_b32 v199, a37\n"
"v_mfma_f32_16x16x32_bf16 a[180:183], a[252:255], v[120:123], a[180:183]\n"
"v_mul_f32 v198, v198, v204\n"
"v_mul_f32 v199, v199, v204\n"
"v_mfma_f32_16x16x32_bf16 a[160:163], a[248:251], v[128:131], a[160:163]\n"
"v_cvt_pk_bf16_f32 v222, v198, v199\n"
"v_accvgpr_read_b32 v198, a38\n"
"v_mfma_f32_16x16x32_bf16 a[184:187], a[252:255], v[128:131], a[184:187]\n"
"v_accvgpr_read_b32 v199, a39\n"
"v_mul_f32 v198, v198, v204\n"
"v_mfma_f32_16x16x32_bf16 a[164:167], a[248:251], v[136:139], a[164:167]\n"
"v_mul_f32 v199, v199, v204\n"
"v_cvt_pk_bf16_f32 v223, v198, v199\n"
"v_mfma_f32_16x16x32_bf16 a[188:191], a[252:255], v[136:139], a[188:191]\n"
"s_waitcnt lgkmcnt(0)\n"
"buffer_store_dwordx4 v[220:223], v192, s[92:95], s61 offen offset:3072 sc1\n"
"v_accvgpr_read_b32 v198, a16\n"
"v_accvgpr_read_b32 v199, a17\n"
"v_mul_f32 v198, v198, v205\n"
"v_mul_f32 v199, v199, v205\n"
"v_cvt_pk_bf16_f32 v224, v198, v199\n"
"v_accvgpr_read_b32 v198, a18\n"
"v_accvgpr_read_b32 v199, a19\n"
"v_mul_f32 v198, v198, v205\n"
"v_mul_f32 v199, v199, v205\n"
"v_cvt_pk_bf16_f32 v225, v198, v199\n"
"v_accvgpr_read_b32 v198, a40\n"
"v_accvgpr_read_b32 v199, a41\n"
"v_mul_f32 v198, v198, v205\n"
"v_mul_f32 v199, v199, v205\n"
"v_cvt_pk_bf16_f32 v226, v198, v199\n"
"v_accvgpr_read_b32 v198, a42\n"
"v_accvgpr_read_b32 v199, a43\n"
"v_mul_f32 v198, v198, v205\n"
"v_mul_f32 v199, v199, v205\n"
"v_cvt_pk_bf16_f32 v227, v198, v199\n"
"s_add_u32 s61, s88, 4096\n"
"buffer_store_dwordx4 v[224:227], v192, s[92:95], s61 offen sc1\n"
"v_accvgpr_read_b32 v198, a20\n"
"v_accvgpr_read_b32 v199, a21\n"
"v_mul_f32 v198, v198, v206\n"
"v_mul_f32 v199, v199, v206\n"
"v_cvt_pk_bf16_f32 v228, v198, v199\n"
"v_accvgpr_read_b32 v198, a22\n"
"v_accvgpr_read_b32 v199, a23\n"
"v_mul_f32 v198, v198, v206\n"
"v_mul_f32 v199, v199, v206\n"
"v_cvt_pk_bf16_f32 v229, v198, v199\n"
"v_accvgpr_read_b32 v198, a44\n"
"v_accvgpr_read_b32 v199, a45\n"
"v_mul_f32 v198, v198, v206\n"
"v_mul_f32 v199, v199, v206\n"
"v_cvt_pk_bf16_f32 v230, v198, v199\n"
"v_accvgpr_read_b32 v198, a46\n"
"v_accvgpr_read_b32 v199, a47\n"
"v_mul_f32 v198, v198, v206\n"
"v_mul_f32 v199, v199, v206\n"
"v_cvt_pk_bf16_f32 v231, v198, v199\n"
"buffer_store_dwordx4 v[228:231], v192, s[92:95], s61 offen offset:1024 sc1\n"
"v_accvgpr_read_b32 v198, a48\n"
"v_accvgpr_read_b32 v199, a49\n"
"v_mul_f32 v198, v198, v200\n"
"v_mul_f32 v199, v199, v200\n"
"v_cvt_pk_bf16_f32 v232, v198, v199\n"
"v_accvgpr_read_b32 v198, a50\n"
"v_accvgpr_read_b32 v199, a51\n"
"v_mul_f32 v198, v198, v200\n"
"v_mul_f32 v199, v199, v200\n"
"v_cvt_pk_bf16_f32 v233, v198, v199\n"
"v_accvgpr_read_b32 v198, a72\n"
"v_accvgpr_read_b32 v199, a73\n"
"v_mul_f32 v198, v198, v200\n"
"v_mul_f32 v199, v199, v200\n"
"v_cvt_pk_bf16_f32 v234, v198, v199\n"
"v_accvgpr_read_b32 v198, a74\n"
"v_accvgpr_read_b32 v199, a75\n"
"v_mul_f32 v198, v198, v200\n"
"v_mul_f32 v199, v199, v200\n"
"v_cvt_pk_bf16_f32 v235, v198, v199\n"
"s_add_u32 s61, s88, 6144\n"
"buffer_store_dwordx4 v[232:235], v192, s[92:95], s61 offen sc1\n"
"v_accvgpr_read_b32 v198, a52\n"
"v_accvgpr_read_b32 v199, a53\n"
"v_mul_f32 v198, v198, v202\n"
"v_mul_f32 v199, v199, v202\n"
"v_cvt_pk_bf16_f32 v236, v198, v199\n"
"v_accvgpr_read_b32 v198, a54\n"
"v_accvgpr_read_b32 v199, a55\n"
"v_mul_f32 v198, v198, v202\n"
"v_mul_f32 v199, v199, v202\n"
"v_cvt_pk_bf16_f32 v237, v198, v199\n"
"v_accvgpr_read_b32 v198, a76\n"
"v_accvgpr_read_b32 v199, a77\n"
"v_mul_f32 v198, v198, v202\n"
"v_mul_f32 v199, v199, v202\n"
"v_cvt_pk_bf16_f32 v238, v198, v199\n"
"v_accvgpr_read_b32 v198, a78\n"
"v_accvgpr_read_b32 v199, a79\n"
"v_mul_f32 v198, v198, v202\n"
"v_mul_f32 v199, v199, v202\n"
"v_cvt_pk_bf16_f32 v239, v198, v199\n"
"buffer_store_dwordx4 v[236:239], v192, s[92:95], s61 offen offset:1024 sc1\n"
"v_accvgpr_read_b32 v198, a56\n"
"v_accvgpr_read_b32 v199, a57\n"
"v_mul_f32 v198, v198, v203\n"
"v_mul_f32 v199, v199, v203\n"
"v_cvt_pk_bf16_f32 v240, v198, v199\n"
"v_accvgpr_read_b32 v198, a58\n"
"v_accvgpr_read_b32 v199, a59\n"
"v_mul_f32 v198, v198, v203\n"
"v_mul_f32 v199, v199, v203\n"
"v_cvt_pk_bf16_f32 v241, v198, v199\n"
"v_accvgpr_read_b32 v198, a80\n"
"v_accvgpr_read_b32 v199, a81\n"
"v_mul_f32 v198, v198, v203\n"
"v_mul_f32 v199, v199, v203\n"
"v_cvt_pk_bf16_f32 v242, v198, v199\n"
"v_accvgpr_read_b32 v198, a82\n"
"v_accvgpr_read_b32 v199, a83\n"
"v_mul_f32 v198, v198, v203\n"
"v_mul_f32 v199, v199, v203\n"
"v_cvt_pk_bf16_f32 v243, v198, v199\n"
"buffer_store_dwordx4 v[240:243], v192, s[92:95], s61 offen offset:2048 sc1\n"
"v_accvgpr_read_b32 v198, a60\n"
"v_accvgpr_read_b32 v199, a61\n"
"v_mul_f32 v198, v198, v204\n"
"v_mul_f32 v199, v199, v204\n"
"v_cvt_pk_bf16_f32 v244, v198, v199\n"
"v_accvgpr_read_b32 v198, a62\n"
"v_accvgpr_read_b32 v199, a63\n"
"v_mul_f32 v198, v198, v204\n"
"v_mul_f32 v199, v199, v204\n"
"v_cvt_pk_bf16_f32 v245, v198, v199\n"
"v_accvgpr_read_b32 v198, a84\n"
"v_accvgpr_read_b32 v199, a85\n"
"v_mul_f32 v198, v198, v204\n"
"v_mul_f32 v199, v199, v204\n"
"v_cvt_pk_bf16_f32 v246, v198, v199\n"
"v_accvgpr_read_b32 v198, a86\n"
"v_accvgpr_read_b32 v199, a87\n"
"v_mul_f32 v198, v198, v204\n"
"v_mul_f32 v199, v199, v204\n"
"v_cvt_pk_bf16_f32 v247, v198, v199\n"
"buffer_store_dwordx4 v[244:247], v192, s[92:95], s61 offen offset:3072 sc1\n"
"v_accvgpr_read_b32 v198, a64\n"
"v_accvgpr_read_b32 v199, a65\n"
"v_mul_f32 v198, v198, v205\n"
"v_mul_f32 v199, v199, v205\n"
"v_cvt_pk_bf16_f32 v248, v198, v199\n"
"v_accvgpr_read_b32 v198, a66\n"
"v_accvgpr_read_b32 v199, a67\n"
"v_mul_f32 v198, v198, v205\n"
"v_mul_f32 v199, v199, v205\n"
"v_cvt_pk_bf16_f32 v249, v198, v199\n"
"v_accvgpr_read_b32 v198, a88\n"
"v_accvgpr_read_b32 v199, a89\n"
"v_mul_f32 v198, v198, v205\n"
"v_mul_f32 v199, v199, v205\n"
"v_cvt_pk_bf16_f32 v250, v198, v199\n"
"v_accvgpr_read_b32 v198, a90\n"
"v_accvgpr_read_b32 v199, a91\n"
"v_mul_f32 v198, v198, v205\n"
"v_mul_f32 v199, v199, v205\n"
"v_cvt_pk_bf16_f32 v251, v198, v199\n"
"s_add_u32 s61, s88, 10240\n"
"buffer_store_dwordx4 v[248:251], v192, s[92:95], s61 offen sc1\n"
"v_accvgpr_read_b32 v198, a68\n"
"v_accvgpr_read_b32 v199, a69\n"
"v_mul_f32 v198, v198, v206\n"
"v_mul_f32 v199, v199, v206\n"
"v_cvt_pk_bf16_f32 v252, v198, v199\n"
"v_accvgpr_read_b32 v198, a70\n"
"v_accvgpr_read_b32 v199, a71\n"
"v_mul_f32 v198, v198, v206\n"
"v_mul_f32 v199, v199, v206\n"
"v_cvt_pk_bf16_f32 v253, v198, v199\n"
"v_accvgpr_read_b32 v198, a92\n"
"v_accvgpr_read_b32 v199, a93\n"
"v_mul_f32 v198, v198, v206\n"
"v_mul_f32 v199, v199, v206\n"
"v_cvt_pk_bf16_f32 v254, v198, v199\n"
"v_accvgpr_read_b32 v198, a94\n"
"v_accvgpr_read_b32 v199, a95\n"
"v_mul_f32 v198, v198, v206\n"
"v_mul_f32 v199, v199, v206\n"
"v_cvt_pk_bf16_f32 v255, v198, v199\n"
"buffer_store_dwordx4 v[252:255], v192, s[92:95], s61 offen offset:1024 sc1\n"
"v_accvgpr_read_b32 v198, a96\n"
"v_accvgpr_read_b32 v199, a97\n"
"v_mul_f32 v198, v198, v200\n"
"v_mul_f32 v199, v199, v200\n"
"v_cvt_pk_bf16_f32 v198, v198, v199\n"
"v_accvgpr_write_b32 a96, v198\n"
"v_accvgpr_read_b32 v198, a98\n"
"v_accvgpr_read_b32 v199, a99\n"
"v_mul_f32 v198, v198, v200\n"
"v_mul_f32 v199, v199, v200\n"
"v_cvt_pk_bf16_f32 v198, v198, v199\n"
"v_accvgpr_write_b32 a97, v198\n"
"v_accvgpr_read_b32 v198, a120\n"
"v_accvgpr_read_b32 v199, a121\n"
"v_mul_f32 v198, v198, v200\n"
"v_mul_f32 v199, v199, v200\n"
"v_cvt_pk_bf16_f32 v198, v198, v199\n"
"v_accvgpr_write_b32 a98, v198\n"
"v_accvgpr_read_b32 v198, a122\n"
"v_accvgpr_read_b32 v199, a123\n"
"v_mul_f32 v198, v198, v200\n"
"v_mul_f32 v199, v199, v200\n"
"v_cvt_pk_bf16_f32 v198, v198, v199\n"
"v_accvgpr_write_b32 a99, v198\n"
"s_add_u32 s61, s88, 12288\n"
"buffer_store_dwordx4 a[96:99], v192, s[92:95], s61 offen sc1\n"
"v_accvgpr_read_b32 v198, a100\n"
"v_accvgpr_read_b32 v199, a101\n"
"v_mul_f32 v198, v198, v202\n"
"v_mul_f32 v199, v199, v202\n"
"v_cvt_pk_bf16_f32 v198, v198, v199\n"
"v_accvgpr_write_b32 a100, v198\n"
"v_accvgpr_read_b32 v198, a102\n"
"v_accvgpr_read_b32 v199, a103\n"
"v_mul_f32 v198, v198, v202\n"
"v_mul_f32 v199, v199, v202\n"
"v_cvt_pk_bf16_f32 v198, v198, v199\n"
"v_accvgpr_write_b32 a101, v198\n"
"v_accvgpr_read_b32 v198, a124\n"
"v_accvgpr_read_b32 v199, a125\n"
"v_mul_f32 v198, v198, v202\n"
"v_mul_f32 v199, v199, v202\n"
"v_cvt_pk_bf16_f32 v198, v198, v199\n"
"v_accvgpr_write_b32 a102, v198\n"
"v_accvgpr_read_b32 v198, a126\n"
"v_accvgpr_read_b32 v199, a127\n"
"v_mul_f32 v198, v198, v202\n"
"v_mul_f32 v199, v199, v202\n"
"v_cvt_pk_bf16_f32 v198, v198, v199\n"
"v_accvgpr_write_b32 a103, v198\n"
"buffer_store_dwordx4 a[100:103], v192, s[92:95], s61 offen offset:1024 sc1\n"
"v_accvgpr_read_b32 v198, a104\n"
"v_accvgpr_read_b32 v199, a105\n"
"v_mul_f32 v198, v198, v203\n"
"v_mul_f32 v199, v199, v203\n"
"v_cvt_pk_bf16_f32 v198, v198, v199\n"
"v_accvgpr_write_b32 a104, v198\n"
"v_accvgpr_read_b32 v198, a106\n"
"v_accvgpr_read_b32 v199, a107\n"
"v_mul_f32 v198, v198, v203\n"
"v_mul_f32 v199, v199, v203\n"
"v_cvt_pk_bf16_f32 v198, v198, v199\n"
"v_accvgpr_write_b32 a105, v198\n"
"v_accvgpr_read_b32 v198, a128\n"
"v_accvgpr_read_b32 v199, a129\n"
"v_mul_f32 v198, v198, v203\n"
"v_mul_f32 v199, v199, v203\n"
"v_cvt_pk_bf16_f32 v198, v198, v199\n"
"v_accvgpr_write_b32 a106, v198\n"
"v_accvgpr_read_b32 v198, a130\n"
"v_accvgpr_read_b32 v199, a131\n"
"v_mul_f32 v198, v198, v203\n"
"v_mul_f32 v199, v199, v203\n"
"v_cvt_pk_bf16_f32 v198, v198, v199\n"
"v_accvgpr_write_b32 a107, v198\n"
"buffer_store_dwordx4 a[104:107], v192, s[92:95], s61 offen offset:2048 sc1\n"
"v_accvgpr_read_b32 v198, a108\n"
"v_accvgpr_read_b32 v199, a109\n"
"v_mul_f32 v198, v198, v204\n"
"v_mul_f32 v199, v199, v204\n"
"v_cvt_pk_bf16_f32 v198, v198, v199\n"
"v_accvgpr_write_b32 a108, v198\n"
"v_accvgpr_read_b32 v198, a110\n"
"v_accvgpr_read_b32 v199, a111\n"
"v_mul_f32 v198, v198, v204\n"
"v_mul_f32 v199, v199, v204\n"
"v_cvt_pk_bf16_f32 v198, v198, v199\n"
"v_accvgpr_write_b32 a109, v198\n"
"v_accvgpr_read_b32 v198, a132\n"
"v_accvgpr_read_b32 v199, a133\n"
"v_mul_f32 v198, v198, v204\n"
"v_mul_f32 v199, v199, v204\n"
"v_cvt_pk_bf16_f32 v198, v198, v199\n"
"v_accvgpr_write_b32 a110, v198\n"
"v_accvgpr_read_b32 v198, a134\n"
"v_accvgpr_read_b32 v199, a135\n"
"v_mul_f32 v198, v198, v204\n"
"v_mul_f32 v199, v199, v204\n"
"v_cvt_pk_bf16_f32 v198, v198, v199\n"
"v_accvgpr_write_b32 a111, v198\n"
"buffer_store_dwordx4 a[108:111], v192, s[92:95], s61 offen offset:3072 sc1\n"
"v_accvgpr_read_b32 v198, a112\n"
"v_accvgpr_read_b32 v199, a113\n"
"v_mul_f32 v198, v198, v205\n"
"v_mul_f32 v199, v199, v205\n"
"v_cvt_pk_bf16_f32 v198, v198, v199\n"
"v_accvgpr_write_b32 a112, v198\n"
"v_accvgpr_read_b32 v198, a114\n"
"v_accvgpr_read_b32 v199, a115\n"
"v_mul_f32 v198, v198, v205\n"
"v_mul_f32 v199, v199, v205\n"
"v_cvt_pk_bf16_f32 v198, v198, v199\n"
"v_accvgpr_write_b32 a113, v198\n"
"v_accvgpr_read_b32 v198, a136\n"
"v_accvgpr_read_b32 v199, a137\n"
"v_mul_f32 v198, v198, v205\n"
"v_mul_f32 v199, v199, v205\n"
"v_cvt_pk_bf16_f32 v198, v198, v199\n"
"v_accvgpr_write_b32 a114, v198\n"
"v_accvgpr_read_b32 v198, a138\n"
"v_accvgpr_read_b32 v199, a139\n"
"v_mul_f32 v198, v198, v205\n"
"v_mul_f32 v199, v199, v205\n"
"v_cvt_pk_bf16_f32 v198, v198, v199\n"
"v_accvgpr_write_b32 a115, v198\n"
"s_add_u32 s61, s88, 16384\n"
"buffer_store_dwordx4 a[112:115], v192, s[92:95], s61 offen sc1\n"
"v_accvgpr_read_b32 v198, a116\n"
"v_accvgpr_read_b32 v199, a117\n"
"v_mul_f32 v198, v198, v206\n"
"v_mul_f32 v199, v199, v206\n"
"v_cvt_pk_bf16_f32 v198, v198, v199\n"
"v_accvgpr_write_b32 a116, v198\n"
"v_accvgpr_read_b32 v198, a118\n"
"v_accvgpr_read_b32 v199, a119\n"
"v_mul_f32 v198, v198, v206\n"
"v_mul_f32 v199, v199, v206\n"
"v_cvt_pk_bf16_f32 v198, v198, v199\n"
"v_accvgpr_write_b32 a117, v198\n"
"v_accvgpr_read_b32 v198, a140\n"
"v_accvgpr_read_b32 v199, a141\n"
"v_mul_f32 v198, v198, v206\n"
"v_mul_f32 v199, v199, v206\n"
"v_cvt_pk_bf16_f32 v198, v198, v199\n"
"v_accvgpr_write_b32 a118, v198\n"
"v_accvgpr_read_b32 v198, a142\n"
"v_accvgpr_read_b32 v199, a143\n"
"v_mul_f32 v198, v198, v206\n"
"v_mul_f32 v199, v199, v206\n"
"v_cvt_pk_bf16_f32 v198, v198, v199\n"
"v_accvgpr_write_b32 a119, v198\n"
"buffer_store_dwordx4 a[116:119], v192, s[92:95], s61 offen offset:1024 sc1\n"
"v_accvgpr_read_b32 v198, a144\n"
"v_accvgpr_read_b32 v199, a145\n"
"v_mul_f32 v198, v198, v200\n"
"v_mul_f32 v199, v199, v200\n"
"v_cvt_pk_bf16_f32 v198, v198, v199\n"
"v_accvgpr_write_b32 a144, v198\n"
"v_accvgpr_read_b32 v198, a146\n"
"v_accvgpr_read_b32 v199, a147\n"
"v_mul_f32 v198, v198, v200\n"
"v_mul_f32 v199, v199, v200\n"
"v_cvt_pk_bf16_f32 v198, v198, v199\n"
"v_accvgpr_write_b32 a145, v198\n"
"v_accvgpr_read_b32 v198, a168\n"
"v_accvgpr_read_b32 v199, a169\n"
"v_mul_f32 v198, v198, v200\n"
"v_mul_f32 v199, v199, v200\n"
"v_cvt_pk_bf16_f32 v198, v198, v199\n"
"v_accvgpr_write_b32 a146, v198\n"
"v_accvgpr_read_b32 v198, a170\n"
"v_accvgpr_read_b32 v199, a171\n"
"v_mul_f32 v198, v198, v200\n"
"v_mul_f32 v199, v199, v200\n"
"v_cvt_pk_bf16_f32 v198, v198, v199\n"
"v_accvgpr_write_b32 a147, v198\n"
"s_add_u32 s61, s88, 18432\n"
"buffer_store_dwordx4 a[144:147], v192, s[92:95], s61 offen sc1\n"
"v_accvgpr_read_b32 v198, a148\n"
"v_accvgpr_read_b32 v199, a149\n"
"v_mul_f32 v198, v198, v202\n"
"v_mul_f32 v199, v199, v202\n"
"v_cvt_pk_bf16_f32 v198, v198, v199\n"
"v_accvgpr_write_b32 a148, v198\n"
"v_accvgpr_read_b32 v198, a150\n"
"v_accvgpr_read_b32 v199, a151\n"
"v_mul_f32 v198, v198, v202\n"
"v_mul_f32 v199, v199, v202\n"
"v_cvt_pk_bf16_f32 v198, v198, v199\n"
"v_accvgpr_write_b32 a149, v198\n"
"v_accvgpr_read_b32 v198, a172\n"
"v_accvgpr_read_b32 v199, a173\n"
"v_mul_f32 v198, v198, v202\n"
"v_mul_f32 v199, v199, v202\n"
"v_cvt_pk_bf16_f32 v198, v198, v199\n"
"v_accvgpr_write_b32 a150, v198\n"
"v_accvgpr_read_b32 v198, a174\n"
"v_accvgpr_read_b32 v199, a175\n"
"v_mul_f32 v198, v198, v202\n"
"v_mul_f32 v199, v199, v202\n"
"v_cvt_pk_bf16_f32 v198, v198, v199\n"
"v_accvgpr_write_b32 a151, v198\n"
"buffer_store_dwordx4 a[148:151], v192, s[92:95], s61 offen offset:1024 sc1\n"
"v_accvgpr_read_b32 v198, a152\n"
"v_accvgpr_read_b32 v199, a153\n"
"v_mul_f32 v198, v198, v203\n"
"v_mul_f32 v199, v199, v203\n"
"v_cvt_pk_bf16_f32 v198, v198, v199\n"
"v_accvgpr_write_b32 a152, v198\n"
"v_accvgpr_read_b32 v198, a154\n"
"v_accvgpr_read_b32 v199, a155\n"
"v_mul_f32 v198, v198, v203\n"
"v_mul_f32 v199, v199, v203\n"
"v_cvt_pk_bf16_f32 v198, v198, v199\n"
"v_accvgpr_write_b32 a153, v198\n"
"v_accvgpr_read_b32 v198, a176\n"
"v_accvgpr_read_b32 v199, a177\n"
"v_mul_f32 v198, v198, v203\n"
"v_mul_f32 v199, v199, v203\n"
"v_cvt_pk_bf16_f32 v198, v198, v199\n"
"v_accvgpr_write_b32 a154, v198\n"
"v_accvgpr_read_b32 v198, a178\n"
"v_accvgpr_read_b32 v199, a179\n"
"v_mul_f32 v198, v198, v203\n"
"v_mul_f32 v199, v199, v203\n"
"v_cvt_pk_bf16_f32 v198, v198, v199\n"
"v_accvgpr_write_b32 a155, v198\n"
"buffer_store_dwordx4 a[152:155], v192, s[92:95], s61 offen offset:2048 sc1\n"
"v_accvgpr_read_b32 v198, a156\n"
"v_accvgpr_read_b32 v199, a157\n"
"v_mul_f32 v198, v198, v204\n"
"v_mul_f32 v199, v199, v204\n"
"v_cvt_pk_bf16_f32 v198, v198, v199\n"
"v_accvgpr_write_b32 a156, v198\n"
"v_accvgpr_read_b32 v198, a158\n"
"v_accvgpr_read_b32 v199, a159\n"
"v_mul_f32 v198, v198, v204\n"
"v_mul_f32 v199, v199, v204\n"
"v_cvt_pk_bf16_f32 v198, v198, v199\n"
"v_accvgpr_write_b32 a157, v198\n"
"v_accvgpr_read_b32 v198, a180\n"
"v_accvgpr_read_b32 v199, a181\n"
"v_mul_f32 v198, v198, v204\n"
"v_mul_f32 v199, v199, v204\n"
"v_cvt_pk_bf16_f32 v198, v198, v199\n"
"v_accvgpr_write_b32 a158, v198\n"
"v_accvgpr_read_b32 v198, a182\n"
"v_accvgpr_read_b32 v199, a183\n"
"v_mul_f32 v198, v198, v204\n"
"v_mul_f32 v199, v199, v204\n"
"v_cvt_pk_bf16_f32 v198, v198, v199\n"
"v_accvgpr_write_b32 a159, v198\n"
"buffer_store_dwordx4 a[156:159], v192, s[92:95], s61 offen offset:3072 sc1\n"
"v_accvgpr_read_b32 v198, a160\n"
"v_accvgpr_read_b32 v199, a161\n"
"v_mul_f32 v198, v198, v205\n"
"v_mul_f32 v199, v199, v205\n"
"v_cvt_pk_bf16_f32 v198, v198, v199\n"
"v_accvgpr_write_b32 a160, v198\n"
"v_accvgpr_read_b32 v198, a162\n"
"v_accvgpr_read_b32 v199, a163\n"
"v_mul_f32 v198, v198, v205\n"
"v_mul_f32 v199, v199, v205\n"
"v_cvt_pk_bf16_f32 v198, v198, v199\n"
"v_accvgpr_write_b32 a161, v198\n"
"v_accvgpr_read_b32 v198, a184\n"
"v_accvgpr_read_b32 v199, a185\n"
"v_mul_f32 v198, v198, v205\n"
"v_mul_f32 v199, v199, v205\n"
"v_cvt_pk_bf16_f32 v198, v198, v199\n"
"v_accvgpr_write_b32 a162, v198\n"
"v_accvgpr_read_b32 v198, a186\n"
"v_accvgpr_read_b32 v199, a187\n"
"v_mul_f32 v198, v198, v205\n"
"v_mul_f32 v199, v199, v205\n"
"v_cvt_pk_bf16_f32 v198, v198, v199\n"
"v_accvgpr_write_b32 a163, v198\n"
"s_add_u32 s61, s88, 22528\n"
"buffer_store_dwordx4 a[160:163], v192, s[92:95], s61 offen sc1\n"
"v_accvgpr_read_b32 v198, a164\n"
"v_accvgpr_read_b32 v199, a165\n"
"v_mul_f32 v198, v198, v206\n"
"v_mul_f32 v199, v199, v206\n"
"v_cvt_pk_bf16_f32 v198, v198, v199\n"
"v_accvgpr_write_b32 a164, v198\n"
"v_accvgpr_read_b32 v198, a166\n"
"v_accvgpr_read_b32 v199, a167\n"
"v_mul_f32 v198, v198, v206\n"
"v_mul_f32 v199, v199, v206\n"
"v_cvt_pk_bf16_f32 v198, v198, v199\n"
"v_accvgpr_write_b32 a165, v198\n"
"v_accvgpr_read_b32 v198, a188\n"
"v_accvgpr_read_b32 v199, a189\n"
"v_mul_f32 v198, v198, v206\n"
"v_mul_f32 v199, v199, v206\n"
"v_cvt_pk_bf16_f32 v198, v198, v199\n"
"v_accvgpr_write_b32 a166, v198\n"
"v_accvgpr_read_b32 v198, a190\n"
"v_accvgpr_read_b32 v199, a191\n"
"v_mul_f32 v198, v198, v206\n"
"v_mul_f32 v199, v199, v206\n"
"v_cvt_pk_bf16_f32 v198, v198, v199\n"
"v_accvgpr_write_b32 a167, v198\n"
"buffer_store_dwordx4 a[164:167], v192, s[92:95], s61 offen offset:1024 sc1\n"
"v_mfma_f32_16x16x32_bf16 v[0:3], a[192:195], v[208:211], 0\n"
"s_add_u32 m0, s59, 131072\n"
"ds_read_b64_tr_b16 a[198:199], v194 offset:4096\n"
"s_add_u32 s55, s67, 0\n"
"v_mfma_f32_16x16x32_bf16 v[4:7], a[192:195], v[212:215], 0\n"
"buffer_load_dwordx4 v195, s[32:35], s55 offen lds\n"
"v_mfma_f32_16x16x32_bf16 v[8:11], a[192:195], v[216:219], 0\n"
"s_add_u32 m0, s59, 133120\n"
"ds_read_b64_tr_b16 a[200:201], v193 offset:256\n"
"s_add_u32 s55, s67, 8192\n"
"v_mfma_f32_16x16x32_bf16 v[12:15], a[192:195], v[220:223], 0\n"
"buffer_load_dwordx4 v195, s[32:35], s55 offen lds\n"
"v_mfma_f32_16x16x32_bf16 v[16:19], a[192:195], v[224:227], 0\n"
"s_add_u32 m0, s59, 135168\n"
"ds_read_b64_tr_b16 a[202:203], v193 offset:4352\n"
"s_add_u32 s55, s67, 16384\n"
"v_mfma_f32_16x16x32_bf16 v[20:23], a[192:195], v[228:231], 0\n"
"buffer_load_dwordx4 v195, s[32:35], s55 offen lds\n"
"ds_read_b64_tr_b16 a[204:205], v194 offset:256\n"
"s_add_u32 m0, s59, 137216\n"
"s_add_u32 s55, s67, 24576\n"
"buffer_load_dwordx4 v195, s[32:35], s55 offen lds\n"
"s_add_u32 m0, s59, 114688\n"
"s_add_u32 s55, s68, 0\n"
"buffer_load_dwordx4 v195, s[32:35], s55 offen lds\n"
"s_waitcnt lgkmcnt(3)\n"
"v_mfma_f32_16x16x32_bf16 v[24:27], a[196:199], v[208:211], 0\n"
"s_add_u32 m0, s59, 116736\n"
"ds_read_b64_tr_b16 a[206:207], v194 offset:4352\n"
"s_add_u32 s55, s68, 8192\n"
"v_mfma_f32_16x16x32_bf16 v[28:31], a[196:199], v[212:215], 0\n"
"buffer_load_dwordx4 v195, s[32:35], s55 offen lds\n"
"v_mfma_f32_16x16x32_bf16 v[32:35], a[196:199], v[216:219], 0\n"
"s_add_u32 m0, s59, 118784\n"
"ds_read_b64_tr_b16 a[208:209], v193 offset:512\n"
"s_add_u32 s55, s68, 16384\n"
"v_mfma_f32_16x16x32_bf16 v[36:39], a[196:199], v[220:223], 0\n"
"buffer_load_dwordx4 v195, s[32:35], s55 offen lds\n"
"v_mfma_f32_16x16x32_bf16 v[40:43], a[196:199], v[224:227], 0\n"
"s_add_u32 m0, s59, 120832\n"
"ds_read_b64_tr_b16 a[210:211], v193 offset:4608\n"
"s_add_u32 s55, s68, 24576\n"
"v_mfma_f32_16x16x32_bf16 v[44:47], a[196:199], v[228:231], 0\n"
"s_waitcnt lgkmcnt(3)\n"
"ds_read_b64_tr_b16 a[212:213], v194 offset:512\n"
"v_mfma_f32_16x16x32_bf16 v[48:51], a[200:203], v[208:211], 0\n"
"buffer_load_dwordx4 v195, s[32:35], s55 offen lds\n"
"v_mfma_f32_16x16x32_bf16 v[52:55], a[200:203], v[212:215], 0\n"
"s_add_u32 m0, s59, 0\n"
"ds_read_b64_tr_b16 a[214:215], v194 offset:4608\n"
"s_add_u32 s55, s66, 32768\n"
"v_mfma_f32_16x16x32_bf16 v[56:59], a[200:203], v[216:219], 0\n"
"buffer_load_dwordx4 v195, s[32:35], s55 offen lds\n"
"v_mfma_f32_16x16x32_bf16 v[60:63], a[200:203], v[220:223], 0\n"
"s_add_u32 m0, s59, 2048\n"
"ds_read_b64_tr_b16 a[216:217], v193 offset:768\n"
"s_add_u32 s55, s66, 40960\n"
"v_mfma_f32_16x16x32_bf16 v[64:67], a[200:203], v[224:227], 0\n"
"buffer_load_dwordx4 v195, s[32:35], s55 offen lds\n"
"v_mfma_f32_16x16x32_bf16 v[68:71], a[200:203], v[228:231], 0\n"
"s_waitcnt lgkmcnt(4)\n"
"ds_read_b64_tr_b16 a[218:219], v193 offset:4864\n"
"v_add_u32 v193, 32768, v193\n"
"v_mfma_f32_16x16x32_bf16 v[72:75], a[204:207], v[208:211], 0\n"
"s_add_u32 m0, s59, 4096\n"
"ds_read_b64_tr_b16 a[220:221], v194 offset:768\n"
"s_add_u32 s55, s66, 49152\n"
"v_mfma_f32_16x16x32_bf16 v[76:79], a[204:207], v[212:215], 0\n"
"buffer_load_dwordx4 v195, s[32:35], s55 offen lds\n"
"v_mfma_f32_16x16x32_bf16 v[80:83], a[204:207], v[216:219], 0\n"
"s_add_u32 m0, s59, 6144\n"
"ds_read_b64_tr_b16 a[222:223], v194 offset:4864\n"
"v_add_u32 v194, 32768, v194\n"
"v_mfma_f32_16x16x32_bf16 v[84:87], a[204:207], v[220:223], 0\n"
"s_add_u32 s55, s66, 57344\n"
"ds_read_b64_tr_b16 a[224:225], v193\n"
"v_mfma_f32_16x16x32_bf16 v[88:91], a[204:207], v[224:227], 0\n"
"buffer_load_dwordx4 v195, s[32:35], s55 offen lds\n"
"v_mfma_f32_16x16x32_bf16 v[92:95], a[204:207], v[228:231], 0\n"
"s_waitcnt lgkmcnt(5)\n"
"ds_read_b64_tr_b16 a[226:227], v193 offset:4096\n"
"s_add_u32 m0, s59, 147456\n"
"v_mfma_f32_16x16x32_bf16 v[96:99], a[208:211], v[208:211], 0\n"
"s_add_u32 s55, s67, 32768\n"
"ds_read_b64_tr_b16 a[228:229], v194\n"
"v_mfma_f32_16x16x32_bf16 v[100:103], a[208:211], v[212:215], 0\n"
"buffer_load_dwordx4 v195, s[32:35], s55 offen lds\n"
"v_mfma_f32_16x16x32_bf16 v[104:107], a[208:211], v[216:219], 0\n"
"s_add_u32 m0, s59, 149504\n"
"ds_read_b64_tr_b16 a[230:231], v194 offset:4096\n"
"s_add_u32 s55, s67, 40960\n"
"v_mfma_f32_16x16x32_bf16 v[108:111], a[208:211], v[220:223], 0\n"
"buffer_load_dwordx4 v195, s[32:35], s55 offen lds\n"
"v_mfma_f32_16x16x32_bf16 v[112:115], a[208:211], v[224:227], 0\n"
"s_add_u32 m0, s59, 151552\n"
"ds_read_b64_tr_b16 a[232:233], v193 offset:256\n"
"s_add_u32 s55, s67, 49152\n"
"v_mfma_f32_16x16x32_bf16 v[116:119], a[208:211], v[228:231], 0\n"
"buffer_load_dwordx4 v195, s[32:35], s55 offen lds\n"
"v_mfma_f32_16x16x32_bf16 v[120:123], a[212:215], v[208:211], 0\n"
"s_add_u32 m0, s59, 153600\n"
"ds_read_b64_tr_b16 a[234:235], v193 offset:4352\n"
"s_add_u32 s55, s67, 57344\n"
"v_mfma_f32_16x16x32_bf16 v[124:127], a[212:215], v[212:215], 0\n"
"buffer_load_dwordx4 v195, s[32:35], s55 offen lds\n"
"v_mfma_f32_16x16x32_bf16 v[128:131], a[212:215], v[216:219], 0\n"
"s_add_u32 s56, s96, 4\n"
"ds_read_b64_tr_b16 a[236:237], v194 offset:256\n"
"v_mfma_f32_16x16x32_bf16 v[132:135], a[212:215], v[220:223], 0\n"
"ds_read_b64_tr_b16 a[238:239], v194 offset:4352\n"
"v_mfma_f32_16x16x32_bf16 v[136:139], a[212:215], v[224:227], 0\n"
"ds_read_b64_tr_b16 a[240:241], v193 offset:512\n"
"v_mfma_f32_16x16x32_bf16 v[140:143], a[212:215], v[228:231], 0\n"
"s_waitcnt lgkmcnt(5)\n"
"ds_read_b64_tr_b16 a[242:243], v193 offset:4608\n"
"v_mfma_f32_16x16x32_bf16 v[144:147], a[216:219], v[208:211], 0\n"
"ds_read_b64_tr_b16 a[244:245], v194 offset:512\n"
"v_mfma_f32_16x16x32_bf16 v[148:151], a[216:219], v[212:215], 0\n"
"ds_read_b64_tr_b16 a[246:247], v194 offset:4608\n"
"v_mfma_f32_16x16x32_bf16 v[152:155], a[216:219], v[216:219], 0\n"
"ds_read_b64_tr_b16 a[248:249], v193 offset:768\n"
"v_mfma_f32_16x16x32_bf16 v[156:159], a[216:219], v[220:223], 0\n"
"ds_read_b64_tr_b16 a[250:251], v193 offset:4864\n"
"v_add_u32 v193, 0xffffc000, v193\n"
"v_mfma_f32_16x16x32_bf16 v[160:163], a[216:219], v[224:227], 0\n"
"ds_read_b64_tr_b16 a[252:253], v194 offset:768\n"
"v_mfma_f32_16x16x32_bf16 v[164:167], a[216:219], v[228:231], 0\n"
"ds_read_b64_tr_b16 a[254:255], v194 offset:4864\n"
"v_add_u32 v194, 0xffffc000, v194\n"
"v_mfma_f32_16x16x32_bf16 v[168:171], a[220:223], v[208:211], 0\n"
"ds_read_b64_tr_b16 a[192:193], v193\n"
"v_mfma_f32_16x16x32_bf16 v[172:175], a[220:223], v[212:215], 0\n"
"ds_read_b64_tr_b16 a[194:195], v193 offset:4096\n"
"v_mfma_f32_16x16x32_bf16 v[176:179], a[220:223], v[216:219], 0\n"
"ds_read_b64_tr_b16 a[196:197], v194\n"
"v_mfma_f32_16x16x32_bf16 v[180:183], a[220:223], v[220:223], 0\n"
"ds_read_b64_tr_b16 a[198:199], v194 offset:4096\n"
"v_mfma_f32_16x16x32_bf16 v[184:187], a[220:223], v[224:227], 0\n"
"ds_read_b64_tr_b16 a[200:201], v193 offset:256\n"
"v_mfma_f32_16x16x32_bf16 v[188:191], a[220:223], v[228:231], 0\n"
"s_waitcnt vmcnt(16)\n"
"ds_read_b64_tr_b16 a[202:203], v193 offset:4352\n"
"v_mov_b32 v198, s56\n"
"v_mfma_f32_16x16x32_bf16 v[0:3], a[224:227], v[232:235], v[0:3]\n"
"s_add_u32 s56, s96, 4\n"
"ds_read_b64_tr_b16 a[204:205], v194 offset:256\n"
"v_mov_b32 v199, s97\n"
"v_mfma_f32_16x16x32_bf16 v[4:7], a[224:227], v[236:239], v[4:7]\n"
"buffer_store_dwordx2 v[198:199], v197, s[92:95], s98 offen sc1\n"
"v_mfma_f32_16x16x32_bf16 v[8:11], a[224:227], v[240:243], v[8:11]\n"
"ds_read_b64_tr_b16 a[206:207], v194 offset:4352\n"
"v_mfma_f32_16x16x32_bf16 v[12:15], a[224:227], v[244:247], v[12:15]\n"
"ds_read_b64_tr_b16 a[208:209], v193 offset:512\n"
"v_mfma_f32_16x16x32_bf16 v[16:19], a[224:227], v[248:251], v[16:19]\n"
"ds_read_b64_tr_b16 a[210:211], v193 offset:4608\n"
"v_mfma_f32_16x16x32_bf16 v[20:23], a[224:227], v[252:255], v[20:23]\n"
"ds_read_b64_tr_b16 a[212:213], v194 offset:512\n"
"v_mfma_f32_16x16x32_bf16 v[24:27], a[228:231], v[232:235], v[24:27]\n"
"ds_read_b64_tr_b16 a[214:215], v194 offset:4608\n"
"v_mfma_f32_16x16x32_bf16 v[28:31], a[228:231], v[236:239], v[28:31]\n"
"ds_read_b64_tr_b16 a[216:217], v193 offset:768\n"
"v_mfma_f32_16x16x32_bf16 v[32:35], a[228:231], v[240:243], v[32:35]\n"
"ds_read_b64_tr_b16 a[218:219], v193 offset:4864\n"
"v_add_u32 v193, 49152, v193\n"
"v_mfma_f32_16x16x32_bf16 v[36:39], a[228:231], v[244:247], v[36:39]\n"
"ds_read_b64_tr_b16 a[220:221], v194 offset:768\n"
"v_mfma_f32_16x16x32_bf16 v[40:43], a[228:231], v[248:251], v[40:43]\n"
"ds_read_b64_tr_b16 a[222:223], v194 offset:4864\n"
"v_add_u32 v194, 49152, v194\n"
"v_mfma_f32_16x16x32_bf16 v[44:47], a[228:231], v[252:255], v[44:47]\n"
"s_waitcnt lgkmcnt(8)\n"
"ds_read_b64_tr_b16 a[224:225], v193\n"
"v_mfma_f32_16x16x32_bf16 v[48:51], a[232:235], v[232:235], v[48:51]\n"
"ds_read_b64_tr_b16 a[226:227], v193 offset:4096\n"
"v_mfma_f32_16x16x32_bf16 v[52:55], a[232:235], v[236:239], v[52:55]\n"
"ds_read_b64_tr_b16 a[228:229], v194\n"
"v_mfma_f32_16x16x32_bf16 v[56:59], a[232:235], v[240:243], v[56:59]\n"
"ds_read_b64_tr_b16 a[230:231], v194 offset:4096\n"
"v_mfma_f32_16x16x32_bf16 v[60:63], a[232:235], v[244:247], v[60:63]\n"
"v_mfma_f32_16x16x32_bf16 v[64:67], a[232:235], v[248:251], v[64:67]\n"
"v_mfma_f32_16x16x32_bf16 v[68:71], a[232:235], v[252:255], v[68:71]\n"
"ds_read_b64_tr_b16 a[232:233], v193 offset:256\n"
"v_mfma_f32_16x16x32_bf16 v[72:75], a[236:239], v[232:235], v[72:75]\n"
"ds_read_b64_tr_b16 a[234:235], v193 offset:4352\n"
"v_mfma_f32_16x16x32_bf16 v[76:79], a[236:239], v[236:239], v[76:79]\n"
"v_mfma_f32_16x16x32_bf16 v[80:83], a[236:239], v[240:243], v[80:83]\n"
"v_mfma_f32_16x16x32_bf16 v[84:87], a[236:239], v[244:247], v[84:87]\n"
"v_mfma_f32_16x16x32_bf16 v[88:91], a[236:239], v[248:251], v[88:91]\n"
"v_mfma_f32_16x16x32_bf16 v[92:95], a[236:239], v[252:255], v[92:95]\n"
"ds_read_b64_tr_b16 a[236:237], v194 offset:256\n"
"v_mfma_f32_16x16x32_bf16 v[96:99], a[240:243], v[232:235], v[96:99]\n"
"ds_read_b64_tr_b16 a[238:239], v194 offset:4352\n"
"v_mfma_f32_16x16x32_bf16 v[100:103], a[240:243], v[236:239], v[100:103]\n"
"v_mfma_f32_16x16x32_bf16 v[104:107], a[240:243], v[240:243], v[104:107]\n"
"v_mfma_f32_16x16x32_bf16 v[108:111], a[240:243], v[244:247], v[108:111]\n"
"v_mfma_f32_16x16x32_bf16 v[112:115], a[240:243], v[248:251], v[112:115]\n"
"v_mfma_f32_16x16x32_bf16 v[116:119], a[240:243], v[252:255], v[116:119]\n"
"ds_read_b64_tr_b16 a[240:241], v193 offset:512\n"
"v_mfma_f32_16x16x32_bf16 v[120:123], a[244:247], v[232:235], v[120:123]\n"
"ds_read_b64_tr_b16 a[242:243], v193 offset:4608\n"
"v_mfma_f32_16x16x32_bf16 v[124:127], a[244:247], v[236:239], v[124:127]\n"
"v_mfma_f32_16x16x32_bf16 v[128:131], a[244:247], v[240:243], v[128:131]\n"
"v_mfma_f32_16x16x32_bf16 v[132:135], a[244:247], v[244:247], v[132:135]\n"
"v_mfma_f32_16x16x32_bf16 v[136:139], a[244:247], v[248:251], v[136:139]\n"
"v_mfma_f32_16x16x32_bf16 v[140:143], a[244:247], v[252:255], v[140:143]\n"
"ds_read_b64_tr_b16 a[244:245], v194 offset:512\n"
"v_mfma_f32_16x16x32_bf16 v[144:147], a[248:251], v[232:235], v[144:147]\n"
"ds_read_b64_tr_b16 a[246:247], v194 offset:4608\n"
"v_mfma_f32_16x16x32_bf16 v[148:151], a[248:251], v[236:239], v[148:151]\n"
"v_mfma_f32_16x16x32_bf16 v[152:155], a[248:251], v[240:243], v[152:155]\n"
"v_mfma_f32_16x16x32_bf16 v[156:159], a[248:251], v[244:247], v[156:159]\n"
"v_mfma_f32_16x16x32_bf16 v[160:163], a[248:251], v[248:251], v[160:163]\n"
"v_mfma_f32_16x16x32_bf16 v[164:167], a[248:251], v[252:255], v[164:167]\n"
"ds_read_b64_tr_b16 a[248:249], v193 offset:768\n"
"v_mfma_f32_16x16x32_bf16 v[168:171], a[252:255], v[232:235], v[168:171]\n"
"ds_read_b64_tr_b16 a[250:251], v193 offset:4864\n"
"v_add_u32 v193, 0xffffc000, v193\n"
"v_mfma_f32_16x16x32_bf16 v[172:175], a[252:255], v[236:239], v[172:175]\n"
"v_mfma_f32_16x16x32_bf16 v[176:179], a[252:255], v[240:243], v[176:179]\n"
"v_mfma_f32_16x16x32_bf16 v[180:183], a[252:255], v[244:247], v[180:183]\n"
"v_mfma_f32_16x16x32_bf16 v[184:187], a[252:255], v[248:251], v[184:187]\n"
"v_mfma_f32_16x16x32_bf16 v[188:191], a[252:255], v[252:255], v[188:191]\n"
"ds_read_b64_tr_b16 a[252:253], v194 offset:768\n"
"v_mfma_f32_16x16x32_bf16 v[0:3], a[192:195], a[96:99], v[0:3]\n"
"ds_read_b64_tr_b16 a[254:255], v194 offset:4864\n"
"v_add_u32 v194, 0xffffc000, v194\n"
"v_mfma_f32_16x16x32_bf16 v[4:7], a[192:195], a[100:103], v[4:7]\n"
"v_mfma_f32_16x16x32_bf16 v[8:11], a[192:195], a[104:107], v[8:11]\n"
"v_mfma_f32_16x16x32_bf16 v[12:15], a[192:195], a[108:111], v[12:15]\n"
"v_mfma_f32_16x16x32_bf16 v[16:19], a[192:195], a[112:115], v[16:19]\n"
"v_mfma_f32_16x16x32_bf16 v[20:23], a[192:195], a[116:119], v[20:23]\n"
"ds_read_b64_tr_b16 a[192:193], v193\n"
"v_mfma_f32_16x16x32_bf16 v[24:27], a[196:199], a[96:99], v[24:27]\n"
"ds_read_b64_tr_b16 a[194:195], v193 offset:4096\n"
"v_mfma_f32_16x16x32_bf16 v[28:31], a[196:199], a[100:103], v[28:31]\n"
"v_mfma_f32_16x16x32_bf16 v[32:35], a[196:199], a[104:107], v[32:35]\n"
"v_mfma_f32_16x16x32_bf16 v[36:39], a[196:199], a[108:111], v[36:39]\n"
"v_mfma_f32_16x16x32_bf16 v[40:43], a[196:199], a[112:115], v[40:43]\n"
"v_mfma_f32_16x16x32_bf16 v[44:47], a[196:199], a[116:119], v[44:47]\n"
"ds_read_b64_tr_b16 a[196:197], v194\n"
"v_mfma_f32_16x16x32_bf16 v[48:51], a[200:203], a[96:99], v[48:51]\n"
"ds_read_b64_tr_b16 a[198:199], v194 offset:4096\n"
"v_mfma_f32_16x16x32_bf16 v[52:55], a[200:203], a[100:103], v[52:55]\n"
"v_mfma_f32_16x16x32_bf16 v[56:59], a[200:203], a[104:107], v[56:59]\n"
"v_mfma_f32_16x16x32_bf16 v[60:63], a[200:203], a[108:111], v[60:63]\n"
"v_mfma_f32_16x16x32_bf16 v[64:67], a[200:203], a[112:115], v[64:67]\n"
"v_mfma_f32_16x16x32_bf16 v[68:71], a[200:203], a[116:119], v[68:71]\n"
"ds_read_b64_tr_b16 a[200:201], v193 offset:256\n"
"v_mfma_f32_16x16x32_bf16 v[72:75], a[204:207], a[96:99], v[72:75]\n"
"ds_read_b64_tr_b16 a[202:203], v193 offset:4352\n"
"v_mfma_f32_16x16x32_bf16 v[76:79], a[204:207], a[100:103], v[76:79]\n"
"v_mfma_f32_16x16x32_bf16 v[80:83], a[204:207], a[104:107], v[80:83]\n"
"v_mfma_f32_16x16x32_bf16 v[84:87], a[204:207], a[108:111], v[84:87]\n"
"v_mfma_f32_16x16x32_bf16 v[88:91], a[204:207], a[112:115], v[88:91]\n"
"v_mfma_f32_16x16x32_bf16 v[92:95], a[204:207], a[116:119], v[92:95]\n"
"s_waitcnt lgkmcnt(2)\n"
"ds_read_b64_tr_b16 a[204:205], v194 offset:256\n"
"v_mfma_f32_16x16x32_bf16 v[96:99], a[208:211], a[96:99], v[96:99]\n"
"ds_read_b64_tr_b16 a[206:207], v194 offset:4352\n"
"v_mfma_f32_16x16x32_bf16 v[100:103], a[208:211], a[100:103], v[100:103]\n"
"v_mfma_f32_16x16x32_bf16 v[104:107], a[208:211], a[104:107], v[104:107]\n"
"v_mfma_f32_16x16x32_bf16 v[108:111], a[208:211], a[108:111], v[108:111]\n"
"v_mfma_f32_16x16x32_bf16 v[112:115], a[208:211], a[112:115], v[112:115]\n"
"v_mfma_f32_16x16x32_bf16 v[116:119], a[208:211], a[116:119], v[116:119]\n"
"ds_read_b64_tr_b16 a[208:209], v193 offset:512\n"
"v_mfma_f32_16x16x32_bf16 v[120:123], a[212:215], a[96:99], v[120:123]\n"
"ds_read_b64_tr_b16 a[210:211], v193 offset:4608\n"
"v_mfma_f32_16x16x32_bf16 v[124:127], a[212:215], a[100:103], v[124:127]\n"
"v_mfma_f32_16x16x32_bf16 v[128:131], a[212:215], a[104:107], v[128:131]\n"
"v_mfma_f32_16x16x32_bf16 v[132:135], a[212:215], a[108:111], v[132:135]\n"
"v_mfma_f32_16x16x32_bf16 v[136:139], a[212:215], a[112:115], v[136:139]\n"
"v_mfma_f32_16x16x32_bf16 v[140:143], a[212:215], a[116:119], v[140:143]\n"
"ds_read_b64_tr_b16 a[212:213], v194 offset:512\n"
"v_mfma_f32_16x16x32_bf16 v[144:147], a[216:219], a[96:99], v[144:147]\n"
"ds_read_b64_tr_b16 a[214:215], v194 offset:4608\n"
"v_mfma_f32_16x16x32_bf16 v[148:151], a[216:219], a[100:103], v[148:151]\n"
"v_mfma_f32_16x16x32_bf16 v[152:155], a[216:219], a[104:107], v[152:155]\n"
"v_mfma_f32_16x16x32_bf16 v[156:159], a[216:219], a[108:111], v[156:159]\n"
"buffer_load_dwordx2 v[198:199], v196, s[92:95], s99 offen sc1\n"
"v_mfma_f32_16x16x32_bf16 v[160:163], a[216:219], a[112:115], v[160:163]\n"
"v_mfma_f32_16x16x32_bf16 v[164:167], a[216:219], a[116:119], v[164:167]\n"
"ds_read_b64_tr_b16 a[216:217], v193 offset:768\n"
"v_mfma_f32_16x16x32_bf16 v[168:171], a[220:223], a[96:99], v[168:171]\n"
"ds_read_b64_tr_b16 a[218:219], v193 offset:4864\n"
"v_mfma_f32_16x16x32_bf16 v[172:175], a[220:223], a[100:103], v[172:175]\n"
"v_mfma_f32_16x16x32_bf16 v[176:179], a[220:223], a[104:107], v[176:179]\n"
"v_mfma_f32_16x16x32_bf16 v[180:183], a[220:223], a[108:111], v[180:183]\n"
"v_mfma_f32_16x16x32_bf16 v[184:187], a[220:223], a[112:115], v[184:187]\n"
"v_mfma_f32_16x16x32_bf16 v[188:191], a[220:223], a[116:119], v[188:191]\n"
"ds_read_b64_tr_b16 a[220:221], v194 offset:768\n"
"v_mfma_f32_16x16x32_bf16 v[0:3], a[224:227], a[144:147], v[0:3]\n"
"ds_read_b64_tr_b16 a[222:223], v194 offset:4864\n"
"v_mfma_f32_16x16x32_bf16 v[4:7], a[224:227], a[148:151], v[4:7]\n"
"v_mfma_f32_16x16x32_bf16 v[8:11], a[224:227], a[152:155], v[8:11]\n"
"v_mfma_f32_16x16x32_bf16 v[12:15], a[224:227], a[156:159], v[12:15]\n"
"v_mfma_f32_16x16x32_bf16 v[16:19], a[224:227], a[160:163], v[16:19]\n"
"v_mfma_f32_16x16x32_bf16 v[20:23], a[224:227], a[164:167], v[20:23]\n"
"v_mfma_f32_16x16x32_bf16 v[24:27], a[228:231], a[144:147], v[24:27]\n"
"v_mfma_f32_16x16x32_bf16 v[28:31], a[228:231], a[148:151], v[28:31]\n"
"v_mfma_f32_16x16x32_bf16 v[32:35], a[228:231], a[152:155], v[32:35]\n"
"v_mfma_f32_16x16x32_bf16 v[36:39], a[228:231], a[156:159], v[36:39]\n"
"v_mfma_f32_16x16x32_bf16 v[40:43], a[228:231], a[160:163], v[40:43]\n"
"v_mfma_f32_16x16x32_bf16 v[44:47], a[228:231], a[164:167], v[44:47]\n"
"v_mfma_f32_16x16x32_bf16 v[48:51], a[232:235], a[144:147], v[48:51]\n"
"v_mfma_f32_16x16x32_bf16 v[52:55], a[232:235], a[148:151], v[52:55]\n"
"v_mfma_f32_16x16x32_bf16 v[56:59], a[232:235], a[152:155], v[56:59]\n"
"v_mfma_f32_16x16x32_bf16 v[60:63], a[232:235], a[156:159], v[60:63]\n"
"v_mfma_f32_16x16x32_bf16 v[64:67], a[232:235], a[160:163], v[64:67]\n"
"v_mfma_f32_16x16x32_bf16 v[68:71], a[232:235], a[164:167], v[68:71]\n"
"v_mfma_f32_16x16x32_bf16 v[72:75], a[236:239], a[144:147], v[72:75]\n"
"v_mfma_f32_16x16x32_bf16 v[76:79], a[236:239], a[148:151], v[76:79]\n"
"v_mfma_f32_16x16x32_bf16 v[80:83], a[236:239], a[152:155], v[80:83]\n"
"v_mfma_f32_16x16x32_bf16 v[84:87], a[236:239], a[156:159], v[84:87]\n"
"v_mfma_f32_16x16x32_bf16 v[88:91], a[236:239], a[160:163], v[88:91]\n"
"v_mfma_f32_16x16x32_bf16 v[92:95], a[236:239], a[164:167], v[92:95]\n"
"v_mfma_f32_16x16x32_bf16 v[96:99], a[240:243], a[144:147], v[96:99]\n"
"v_mfma_f32_16x16x32_bf16 v[100:103], a[240:243], a[148:151], v[100:103]\n"
"v_mfma_f32_16x16x32_bf16 v[104:107], a[240:243], a[152:155], v[104:107]\n"
"v_mfma_f32_16x16x32_bf16 v[108:111], a[240:243], a[156:159], v[108:111]\n"
"v_mfma_f32_16x16x32_bf16 v[112:115], a[240:243], a[160:163], v[112:115]\n"
"v_mfma_f32_16x16x32_bf16 v[116:119], a[240:243], a[164:167], v[116:119]\n"
"v_mfma_f32_16x16x32_bf16 v[120:123], a[244:247], a[144:147], v[120:123]\n"
"v_mfma_f32_16x16x32_bf16 v[124:127], a[244:247], a[148:151], v[124:127]\n"
"v_mfma_f32_16x16x32_bf16 v[128:131], a[244:247], a[152:155], v[128:131]\n"
"v_mfma_f32_16x16x32_bf16 v[132:135], a[244:247], a[156:159], v[132:135]\n"
"v_mfma_f32_16x16x32_bf16 v[136:139], a[244:247], a[160:163], v[136:139]\n"
"v_mfma_f32_16x16x32_bf16 v[140:143], a[244:247], a[164:167], v[140:143]\n"
"v_mfma_f32_16x16x32_bf16 v[144:147], a[248:251], a[144:147], v[144:147]\n"
"v_mfma_f32_16x16x32_bf16 v[148:151], a[248:251], a[148:151], v[148:151]\n"
"v_mfma_f32_16x16x32_bf16 v[152:155], a[248:251], a[152:155], v[152:155]\n"
"v_mfma_f32_16x16x32_bf16 v[156:159], a[248:251], a[156:159], v[156:159]\n"
"v_mfma_f32_16x16x32_bf16 v[160:163], a[248:251], a[160:163], v[160:163]\n"
"s_waitcnt vmcnt(0)\n"
"v_mfma_f32_16x16x32_bf16 v[164:167], a[248:251], a[164:167], v[164:167]\n"
"v_subrev_u32 v198, s56, v198\n"
"v_cmp_ge_u32 vcc, 1, v198\n"
"v_mfma_f32_16x16x32_bf16 v[168:171], a[252:255], a[144:147], v[168:171]\n"
"v_cmp_eq_u32 s[82:83], s97, v199\n"
"v_mfma_f32_16x16x32_bf16 v[172:175], a[252:255], a[148:151], v[172:175]\n"
"s_and_b64 vcc, vcc, s[82:83]\n"
"s_cmp_eq_u64 vcc, -1\n"
"s_cbranch_scc0 .Lspin4\n"
".Lspin4_ret:\n"
"v_mfma_f32_16x16x32_bf16 v[176:179], a[252:255], a[152:155], v[176:179]\n"
"s_add_u32 s61, s89, 0\n"
"v_mfma_f32_16x16x32_bf16 v[180:183], a[252:255], a[156:159], v[180:183]\n"
"buffer_load_dwordx4 a[0:3], v192, s[92:95], s61 offen sc1\n"
"v_mfma_f32_16x16x32_bf16 v[184:187], a[252:255], a[160:163], v[184:187]\n"
"buffer_load_dwordx4 a[4:7], v192, s[92:95], s61 offen offset:1024 sc1\n"
"v_mfma_f32_16x16x32_bf16 v[188:191], a[252:255], a[164:167], v[188:191]\n"
"buffer_load_dwordx4 a[8:11], v192, s[92:95], s61 offen offset:2048 sc1\n"
"buffer_load_dwordx4 a[12:15], v192, s[92:95], s61 offen offset:3072 sc1\n"
"s_add_u32 s61, s89, 4096\n"
"buffer_load_dwordx4 a[16:19], v192, s[92:95], s61 offen sc1\n"
"buffer_load_dwordx4 a[20:23], v192, s[92:95], s61 offen offset:1024 sc1\n"
"s_waitcnt lgkmcnt(0)\n"
"s_barrier\n"
"s_add_u32 s61, s90, 0\n"
"buffer_load_dwordx4 a[24:27], v192, s[92:95], s61 offen sc1\n"
"buffer_load_dwordx4 a[28:31], v192, s[92:95], s61 offen offset:1024 sc1\n"
"buffer_load_dwordx4 a[32:35], v192, s[92:95], s61 offen offset:2048 sc1\n"
"buffer_load_dwordx4 a[36:39], v192, s[92:95], s61 offen offset:3072 sc1\n"
"s_add_u32 s61, s90, 4096\n"
"buffer_load_dwordx4 a[40:43], v192, s[92:95], s61 offen sc1\n"
"buffer_load_dwordx4 a[44:47], v192, s[92:95], s61 offen offset:1024 sc1\n"
"s_add_u32 s61, s91, 0\n"
"buffer_load_dwordx4 a[48:51], v192, s[92:95], s61 offen sc1\n"
"buffer_load_dwordx4 a[52:55], v192, s[92:95], s61 offen offset:1024 sc1\n"
"buffer_load_dwordx4 a[56:59], v192, s[92:95], s61 offen offset:2048 sc1\n"
"buffer_load_dwordx4 a[60:63], v192, s[92:95], s61 offen offset:3072 sc1\n"
"s_add_u32 s61, s91, 4096\n"
"buffer_load_dwordx4 a[64:67], v192, s[92:95], s61 offen sc1\n"
"buffer_load_dwordx4 a[68:71], v192, s[92:95], s61 offen offset:1024 sc1\n"
"s_waitcnt vmcnt(12)\n"
"v_mfma_f32_16x16x32_bf16 v[0:3], a[192:195], a[0:3], v[0:3]\n"
"v_add_u32 v193, 49152, v193\n"
"ds_read_b64_tr_b16 a[224:225], v193\n"
"v_mfma_f32_16x16x32_bf16 v[4:7], a[192:195], a[4:7], v[4:7]\n"
"ds_read_b64_tr_b16 a[226:227], v193 offset:4096\n"
"v_add_u32 v194, 49152, v194\n"
"v_mfma_f32_16x16x32_bf16 v[8:11], a[192:195], a[8:11], v[8:11]\n"
"ds_read_b64_tr_b16 a[228:229], v194\n"
"v_mfma_f32_16x16x32_bf16 v[12:15], a[192:195], a[12:15], v[12:15]\n"
"ds_read_b64_tr_b16 a[230:231], v194 offset:4096\n"
"v_mfma_f32_16x16x32_bf16 v[16:19], a[192:195], a[16:19], v[16:19]\n"
"ds_read_b64_tr_b16 a[232:233], v193 offset:256\n"
"v_mfma_f32_16x16x32_bf16 v[20:23], a[192:195], a[20:23], v[20:23]\n"
"ds_read_b64_tr_b16 a[234:235], v193 offset:4352\n"
"v_mfma_f32_16x16x32_bf16 v[24:27], a[196:199], a[0:3], v[24:27]\n"
"v_mfma_f32_16x16x32_bf16 v[28:31], a[196:199], a[4:7], v[28:31]\n"
"ds_read_b64_tr_b16 a[236:237], v194 offset:256\n"
"v_mfma_f32_16x16x32_bf16 v[32:35], a[196:199], a[8:11], v[32:35]\n"
"v_mfma_f32_16x16x32_bf16 v[36:39], a[196:199], a[12:15], v[36:39]\n"
"ds_read_b64_tr_b16 a[238:239], v194 offset:4352\n"
"v_mfma_f32_16x16x32_bf16 v[40:43], a[196:199], a[16:19], v[40:43]\n"
"v_mfma_f32_16x16x32_bf16 v[44:47], a[196:199], a[20:23], v[44:47]\n"
"ds_read_b64_tr_b16 a[240:241], v193 offset:512\n"
"v_mfma_f32_16x16x32_bf16 v[48:51], a[200:203], a[0:3], v[48:51]\n"
"v_mfma_f32_16x16x32_bf16 v[52:55], a[200:203], a[4:7], v[52:55]\n"
"ds_read_b64_tr_b16 a[242:243], v193 offset:4608\n"
"v_mfma_f32_16x16x32_bf16 v[56:59], a[200:203], a[8:11], v[56:59]\n"
"v_mfma_f32_16x16x32_bf16 v[60:63], a[200:203], a[12:15], v[60:63]\n"
"v_mfma_f32_16x16x32_bf16 v[64:67], a[200:203], a[16:19], v[64:67]\n"
"ds_read_b64_tr_b16 a[244:245], v194 offset:512\n"
"v_mfma_f32_16x16x32_bf16 v[68:71], a[200:203], a[20:23], v[68:71]\n"
"v_mfma_f32_16x16x32_bf16 v[72:75], a[204:207], a[0:3], v[72:75]\n"
"s_add_u32 m0, s59, 16384\n"
"s_add_u32 s55, s68, 32768\n"
"v_mfma_f32_16x16x32_bf16 v[76:79], a[204:207], a[4:7], v[76:79]\n"
"ds_read_b64_tr_b16 a[246:247], v194 offset:4608\n"
"v_mfma_f32_16x16x32_bf16 v[80:83], a[204:207], a[8:11], v[80:83]\n"
"ds_read_b64_tr_b16 a[248:249], v193 offset:768\n"
"v_mfma_f32_16x16x32_bf16 v[84:87], a[204:207], a[12:15], v[84:87]\n"
"buffer_load_dwordx4 v195, s[32:35], s55 offen lds\n"
"v_mfma_f32_16x16x32_bf16 v[88:91], a[204:207], a[16:19], v[88:91]\n"
"ds_read_b64_tr_b16 a[250:251], v193 offset:4864\n"
"v_mfma_f32_16x16x32_bf16 v[92:95], a[204:207], a[20:23], v[92:95]\n"
"v_add_u32 v193, 0xffffc000, v193\n"
"v_mfma_f32_16x16x32_bf16 v[96:99], a[208:211], a[0:3], v[96:99]\n"
"s_add_u32 m0, s59, 18432\n"
"v_mfma_f32_16x16x32_bf16 v[100:103], a[208:211], a[4:7], v[100:103]\n"
"s_add_u32 s55, s68, 40960\n"
"ds_read_b64_tr_b16 a[252:253], v194 offset:768\n"
"v_mfma_f32_16x16x32_bf16 v[104:107], a[208:211], a[8:11], v[104:107]\n"
"v_mfma_f32_16x16x32_bf16 v[108:111], a[208:211], a[12:15], v[108:111]\n"
"buffer_load_dwordx4 v195, s[32:35], s55 offen lds\n"
"v_mfma_f32_16x16x32_bf16 v[112:115], a[208:211], a[16:19], v[112:115]\n"
"ds_read_b64_tr_b16 a[254:255], v194 offset:4864\n"
"v_add_u32 v194, 0xffffc000, v194\n"
"v_mfma_f32_16x16x32_bf16 v[116:119], a[208:211], a[20:23], v[116:119]\n"
"v_mfma_f32_16x16x32_bf16 v[120:123], a[212:215], a[0:3], v[120:123]\n"
"v_mfma_f32_16x16x32_bf16 v[124:127], a[212:215], a[4:7], v[124:127]\n"
"s_add_u32 m0, s59, 20480\n"
"s_add_u32 s55, s68, 49152\n"
"v_mfma_f32_16x16x32_bf16 v[128:131], a[212:215], a[8:11], v[128:131]\n"
"v_mfma_f32_16x16x32_bf16 v[132:135], a[212:215], a[12:15], v[132:135]\n"
"v_mfma_f32_16x16x32_bf16 v[136:139], a[212:215], a[16:19], v[136:139]\n"
"buffer_load_dwordx4 v195, s[32:35], s55 offen lds\n"
"v_mfma_f32_16x16x32_bf16 v[140:143], a[212:215], a[20:23], v[140:143]\n"
"s_waitcnt vmcnt(9)\n"
"v_mfma_f32_16x16x32_bf16 v[144:147], a[216:219], a[0:3], v[144:147]\n"
"v_mfma_f32_16x16x32_bf16 v[148:151], a[216:219], a[4:7], v[148:151]\n"
"s_add_u32 m0, s59, 22528\n"
"s_add_u32 s55, s68, 57344\n"
"s_add_u32 s61, s89, 6144\n"
"v_mfma_f32_16x16x32_bf16 v[152:155], a[216:219], a[8:11], v[152:155]\n"
"v_mfma_f32_16x16x32_bf16 v[156:159], a[216:219], a[12:15], v[156:159]\n"
"v_mfma_f32_16x16x32_bf16 v[160:163], a[216:219], a[16:19], v[160:163]\n"
"buffer_load_dwordx4 v195, s[32:35], s55 offen lds\n"
"v_mfma_f32_16x16x32_bf16 v[164:167], a[216:219], a[20:23], v[164:167]\n"
"buffer_load_dwordx4 a[72:75], v192, s[92:95], s61 offen sc1\n"
"v_mfma_f32_16x16x32_bf16 v[168:171], a[220:223], a[0:3], v[168:171]\n"
"ds_read_b64_tr_b16 a[192:193], v193\n"
"v_mfma_f32_16x16x32_bf16 v[172:175], a[220:223], a[4:7], v[172:175]\n"
"ds_read_b64_tr_b16 a[194:195], v193 offset:4096\n"
"v_mfma_f32_16x16x32_bf16 v[176:179], a[220:223], a[8:11], v[176:179]\n"
"s_add_u32 m0, s59, 32768\n"
"v_mfma_f32_16x16x32_bf16 v[180:183], a[220:223], a[12:15], v[180:183]\n"
"buffer_load_dwordx4 a[76:79], v192, s[92:95], s61 offen offset:1024 sc1\n"
"v_mfma_f32_16x16x32_bf16 v[184:187], a[220:223], a[16:19], v[184:187]\n"
"s_add_u32 s55, s66, 65536\n"
"ds_read_b64_tr_b16 a[196:197], v194\n"
"v_mfma_f32_16x16x32_bf16 v[188:191], a[220:223], a[20:23], v[188:191]\n"
"s_waitcnt lgkmcnt(3)\n"
"v_mfma_f32_16x16x32_bf16 v[0:3], a[224:227], a[24:27], v[0:3]\n"
"buffer_load_dwordx4 v195, s[32:35], s55 offen lds\n"
"v_mfma_f32_16x16x32_bf16 v[4:7], a[224:227], a[28:31], v[4:7]\n"
"ds_read_b64_tr_b16 a[198:199], v194 offset:4096\n"
"v_mfma_f32_16x16x32_bf16 v[8:11], a[224:227], a[32:35], v[8:11]\n"
"buffer_load_dwordx4 a[80:83], v192, s[92:95], s61 offen offset:2048 sc1\n"
"v_mfma_f32_16x16x32_bf16 v[12:15], a[224:227], a[36:39], v[12:15]\n"
"s_add_u32 m0, s59, 34816\n"
"s_add_u32 s55, s66, 73728\n"
"ds_read_b64_tr_b16 a[200:201], v193 offset:256\n"
"v_mfma_f32_16x16x32_bf16 v[16:19], a[224:227], a[40:43], v[16:19]\n"
"ds_read_b64_tr_b16 a[202:203], v193 offset:4352\n"
"v_mfma_f32_16x16x32_bf16 v[20:23], a[224:227], a[44:47], v[20:23]\n"
"v_mfma_f32_16x16x32_bf16 v[24:27], a[228:231], a[24:27], v[24:27]\n"
"buffer_load_dwordx4 v195, s[32:35], s55 offen lds\n"
"v_mfma_f32_16x16x32_bf16 v[28:31], a[228:231], a[28:31], v[28:31]\n"
"buffer_load_dwordx4 a[84:87], v192, s[92:95], s61 offen offset:3072 sc1\n"
"v_mfma_f32_16x16x32_bf16 v[32:35], a[228:231], a[32:35], v[32:35]\n"
"s_add_u32 s61, s89, 10240\n"
"ds_read_b64_tr_b16 a[204:205], v194 offset:256\n"
"v_mfma_f32_16x16x32_bf16 v[36:39], a[228:231], a[36:39], v[36:39]\n"
"s_add_u32 m0, s59, 36864\n"
"s_add_u32 s55, s66, 81920\n"
"ds_read_b64_tr_b16 a[206:207], v194 offset:4352\n"
"v_mfma_f32_16x16x32_bf16 v[40:43], a[228:231], a[40:43], v[40:43]\n"
"v_mfma_f32_16x16x32_bf16 v[44:47], a[228:231], a[44:47], v[44:47]\n"
"buffer_load_dwordx4 a[88:91], v192, s[92:95], s61 offen sc1\n"
"v_mfma_f32_16x16x32_bf16 v[48:51], a[232:235], a[24:27], v[48:51]\n"
"buffer_load_dwordx4 v195, s[32:35], s55 offen lds\n"
"v_mfma_f32_16x16x32_bf16 v[52:55], a[232:235], a[28:31], v[52:55]\n"
"ds_read_b64_tr_b16 a[208:209], v193 offset:512\n"
"v_mfma_f32_16x16x32_bf16 v[56:59], a[232:235], a[32:35], v[56:59]\n"
"ds_read_b64_tr_b16 a[210:211], v193 offset:4608\n"
"v_mfma_f32_16x16x32_bf16 v[60:63], a[232:235], a[36:39], v[60:63]\n"
"s_add_u32 m0, s59, 38912\n"
"s_add_u32 s55, s66, 90112\n"
"v_mfma_f32_16x16x32_bf16 v[64:67], a[232:235], a[40:43], v[64:67]\n"
"buffer_load_dwordx4 a[92:95], v192, s[92:95], s61 offen offset:1024 sc1\n"
"v_mfma_f32_16x16x32_bf16 v[68:71], a[232:235], a[44:47], v[68:71]\n"
"s_add_u32 s61, s90, 6144\n"
"ds_read_b64_tr_b16 a[212:213], v194 offset:512\n"
"v_mfma_f32_16x16x32_bf16 v[72:75], a[236:239], a[24:27], v[72:75]\n"
"buffer_load_dwordx4 v195, s[32:35], s55 offen lds\n"
"v_mfma_f32_16x16x32_bf16 v[76:79], a[236:239], a[28:31], v[76:79]\n"
"ds_read_b64_tr_b16 a[214:215], v194 offset:4608\n"
"v_mfma_f32_16x16x32_bf16 v[80:83], a[236:239], a[32:35], v[80:83]\n"
"ds_read_b64_tr_b16 a[216:217], v193 offset:768\n"
"v_mfma_f32_16x16x32_bf16 v[84:87], a[236:239], a[36:39], v[84:87]\n"
"buffer_load_dwordx4 a[96:99], v192, s[92:95], s61 offen sc1\n"
"v_mfma_f32_16x16x32_bf16 v[88:91], a[236:239], a[40:43], v[88:91]\n"
"ds_read_b64_tr_b16 a[218:219], v193 offset:4864\n"
"v_mfma_f32_16x16x32_bf16 v[92:95], a[236:239], a[44:47], v[92:95]\n"
"v_add_u32 v193, 0xfffe4000, v193\n"
"v_mfma_f32_16x16x32_bf16 v[96:99], a[240:243], a[24:27], v[96:99]\n"
"v_mfma_f32_16x16x32_bf16 v[100:103], a[240:243], a[28:31], v[100:103]\n"
"ds_read_b64_tr_b16 a[220:221], v194 offset:768\n"
"v_mfma_f32_16x16x32_bf16 v[104:107], a[240:243], a[32:35], v[104:107]\n"
"buffer_load_dwordx4 a[100:103], v192, s[92:95], s61 offen offset:1024 sc1\n"
"v_mfma_f32_16x16x32_bf16 v[108:111], a[240:243], a[36:39], v[108:111]\n"
"ds_read_b64_tr_b16 a[222:223], v194 offset:4864\n"
"v_add_u32 v194, 0xfffe4000, v194\n"
"v_mfma_f32_16x16x32_bf16 v[112:115], a[240:243], a[40:43], v[112:115]\n"
"v_mfma_f32_16x16x32_bf16 v[116:119], a[240:243], a[44:47], v[116:119]\n"
"v_mfma_f32_16x16x32_bf16 v[120:123], a[244:247], a[24:27], v[120:123]\n"
"v_mfma_f32_16x16x32_bf16 v[124:127], a[244:247], a[28:31], v[124:127]\n"
"buffer_load_dwordx4 a[104:107], v192, s[92:95], s61 offen offset:2048 sc1\n"
"v_mfma_f32_16x16x32_bf16 v[128:131], a[244:247], a[32:35], v[128:131]\n"
"v_mfma_f32_16x16x32_bf16 v[132:135], a[244:247], a[36:39], v[132:135]\n"
"v_mfma_f32_16x16x32_bf16 v[136:139], a[244:247], a[40:43], v[136:139]\n"
"v_mfma_f32_16x16x32_bf16 v[140:143], a[244:247], a[44:47], v[140:143]\n"
"v_mfma_f32_16x16x32_bf16 v[144:147], a[248:251], a[24:27], v[144:147]\n"
"buffer_load_dwordx4 a[108:111], v192, s[92:95], s61 offen offset:3072 sc1\n"
"v_mfma_f32_16x16x32_bf16 v[148:151], a[248:251], a[28:31], v[148:151]\n"
"s_waitcnt vmcnt(18)\n"
"s_add_u32 s61, s90, 10240\n"
"v_mfma_f32_16x16x32_bf16 v[152:155], a[248:251], a[32:35], v[152:155]\n"
"v_mfma_f32_16x16x32_bf16 v[156:159], a[248:251], a[36:39], v[156:159]\n"
"v_mfma_f32_16x16x32_bf16 v[160:163], a[248:251], a[40:43], v[160:163]\n"
"v_mfma_f32_16x16x32_bf16 v[164:167], a[248:251], a[44:47], v[164:167]\n"
"buffer_load_dwordx4 a[112:115], v192, s[92:95], s61 offen sc1\n"
"v_mfma_f32_16x16x32_bf16 v[168:171], a[252:255], a[24:27], v[168:171]\n"
"ds_read_b64_tr_b16 a[224:225], v193\n"
"v_mfma_f32_16x16x32_bf16 v[172:175], a[252:255], a[28:31], v[172:175]\n"
"ds_read_b64_tr_b16 a[226:227], v193 offset:4096\n"
"v_mfma_f32_16x16x32_bf16 v[176:179], a[252:255], a[32:35], v[176:179]\n"
"v_mfma_f32_16x16x32_bf16 v[180:183], a[252:255], a[36:39], v[180:183]\n"
"v_mfma_f32_16x16x32_bf16 v[184:187], a[252:255], a[40:43], v[184:187]\n"
"buffer_load_dwordx4 a[116:119], v192, s[92:95], s61 offen offset:1024 sc1\n"
"v_mfma_f32_16x16x32_bf16 v[188:191], a[252:255], a[44:47], v[188:191]\n"
"s_waitcnt lgkmcnt(2)\n"
"s_add_u32 s61, s91, 6144\n"
"ds_read_b64_tr_b16 a[228:229], v194\n"
"v_mfma_f32_16x16x32_bf16 v[0:3], a[192:195], a[48:51], v[0:3]\n"
"ds_read_b64_tr_b16 a[230:231], v194 offset:4096\n"
"v_mfma_f32_16x16x32_bf16 v[4:7], a[192:195], a[52:55], v[4:7]\n"
"v_mfma_f32_16x16x32_bf16 v[8:11], a[192:195], a[56:59], v[8:11]\n"
"buffer_load_dwordx4 a[120:123], v192, s[92:95], s61 offen sc1\n"
"v_mfma_f32_16x16x32_bf16 v[12:15], a[192:195], a[60:63], v[12:15]\n"
"ds_read_b64_tr_b16 a[232:233], v193 offset:256\n"
"v_mfma_f32_16x16x32_bf16 v[16:19], a[192:195], a[64:67], v[16:19]\n"
"ds_read_b64_tr_b16 a[234:235], v193 offset:4352\n"
"v_mfma_f32_16x16x32_bf16 v[20:23], a[192:195], a[68:71], v[20:23]\n"
"v_mfma_f32_16x16x32_bf16 v[24:27], a[196:199], a[48:51], v[24:27]\n"
"v_mfma_f32_16x16x32_bf16 v[28:31], a[196:199], a[52:55], v[28:31]\n"
"buffer_load_dwordx4 a[124:127], v192, s[92:95], s61 offen offset:1024 sc1\n"
"v_mfma_f32_16x16x32_bf16 v[32:35], a[196:199], a[56:59], v[32:35]\n"
"ds_read_b64_tr_b16 a[236:237], v194 offset:256\n"
"v_mfma_f32_16x16x32_bf16 v[36:39], a[196:199], a[60:63], v[36:39]\n"
"ds_read_b64_tr_b16 a[238:239], v194 offset:4352\n"
"v_mfma_f32_16x16x32_bf16 v[40:43], a[196:199], a[64:67], v[40:43]\n"
"v_mfma_f32_16x16x32_bf16 v[44:47], a[196:199], a[68:71], v[44:47]\n"
"ds_read_b64_tr_b16 a[240:241], v193 offset:512\n"
"v_mfma_f32_16x16x32_bf16 v[48:51], a[200:203], a[48:51], v[48:51]\n"
"buffer_load_dwordx4 a[128:131], v192, s[92:95], s61 offen offset:2048 sc1\n"
"v_mfma_f32_16x16x32_bf16 v[52:55], a[200:203], a[52:55], v[52:55]\n"
"ds_read_b64_tr_b16 a[242:243], v193 offset:4608\n"
"v_mfma_f32_16x16x32_bf16 v[56:59], a[200:203], a[56:59], v[56:59]\n"
"v_mfma_f32_16x16x32_bf16 v[60:63], a[200:203], a[60:63], v[60:63]\n"
"v_mfma_f32_16x16x32_bf16 v[64:67], a[200:203], a[64:67], v[64:67]\n"
"ds_read_b64_tr_b16 a[244:245], v194 offset:512\n"
"v_mfma_f32_16x16x32_bf16 v[68:71], a[200:203], a[68:71], v[68:71]\n"
"buffer_load_dwordx4 a[132:135], v192, s[92:95], s61 offen offset:3072 sc1\n"
"v_mfma_f32_16x16x32_bf16 v[72:75], a[204:207], a[48:51], v[72:75]\n"
"s_add_u32 s61, s91, 10240\n"
"ds_read_b64_tr_b16 a[246:247], v194 offset:4608\n"
"v_mfma_f32_16x16x32_bf16 v[76:79], a[204:207], a[52:55], v[76:79]\n"
"v_mfma_f32_16x16x32_bf16 v[80:83], a[204:207], a[56:59], v[80:83]\n"
"ds_read_b64_tr_b16 a[248:249], v193 offset:768\n"
"v_mfma_f32_16x16x32_bf16 v[84:87], a[204:207], a[60:63], v[84:87]\n"
"v_mfma_f32_16x16x32_bf16 v[88:91], a[204:207], a[64:67], v[88:91]\n"
"buffer_load_dwordx4 a[136:139], v192, s[92:95], s61 offen sc1\n"
"v_mfma_f32_16x16x32_bf16 v[92:95], a[204:207], a[68:71], v[92:95]\n"
"ds_read_b64_tr_b16 a[250:251], v193 offset:4864\n"
"v_mfma_f32_16x16x32_bf16 v[96:99], a[208:211], a[48:51], v[96:99]\n"
"v_mfma_f32_16x16x32_bf16 v[100:103], a[208:211], a[52:55], v[100:103]\n"
"ds_read_b64_tr_b16 a[252:253], v194 offset:768\n"
"v_mfma_f32_16x16x32_bf16 v[104:107], a[208:211], a[56:59], v[104:107]\n"
"v_mfma_f32_16x16x32_bf16 v[108:111], a[208:211], a[60:63], v[108:111]\n"
"buffer_load_dwordx4 a[140:143], v192, s[92:95], s61 offen offset:1024 sc1\n"
"v_mfma_f32_16x16x32_bf16 v[112:115], a[208:211], a[64:67], v[112:115]\n"
"ds_read_b64_tr_b16 a[254:255], v194 offset:4864\n"
"v_mfma_f32_16x16x32_bf16 v[116:119], a[208:211], a[68:71], v[116:119]\n"
"v_mfma_f32_16x16x32_bf16 v[120:123], a[212:215], a[48:51], v[120:123]\n"
"v_mfma_f32_16x16x32_bf16 v[124:127], a[212:215], a[52:55], v[124:127]\n"
"v_mfma_f32_16x16x32_bf16 v[128:131], a[212:215], a[56:59], v[128:131]\n"
"v_mfma_f32_16x16x32_bf16 v[132:135], a[212:215], a[60:63], v[132:135]\n"
"v_mfma_f32_16x16x32_bf16 v[136:139], a[212:215], a[64:67], v[136:139]\n"
"v_mfma_f32_16x16x32_bf16 v[140:143], a[212:215], a[68:71], v[140:143]\n"
"v_mfma_f32_16x16x32_bf16 v[144:147], a[216:219], a[48:51], v[144:147]\n"
"v_mfma_f32_16x16x32_bf16 v[148:151], a[216:219], a[52:55], v[148:151]\n"
"v_mfma_f32_16x16x32_bf16 v[152:155], a[216:219], a[56:59], v[152:155]\n"
"v_mfma_f32_16x16x32_bf16 v[156:159], a[216:219], a[60:63], v[156:159]\n"
"v_mfma_f32_16x16x32_bf16 v[160:163], a[216:219], a[64:67], v[160:163]\n"
"v_mfma_f32_16x16x32_bf16 v[164:167], a[216:219], a[68:71], v[164:167]\n"
"v_mfma_f32_16x16x32_bf16 v[168:171], a[220:223], a[48:51], v[168:171]\n"
"v_mfma_f32_16x16x32_bf16 v[172:175], a[220:223], a[52:55], v[172:175]\n"
"v_mfma_f32_16x16x32_bf16 v[176:179], a[220:223], a[56:59], v[176:179]\n"
"v_mfma_f32_16x16x32_bf16 v[180:183], a[220:223], a[60:63], v[180:183]\n"
"v_mfma_f32_16x16x32_bf16 v[184:187], a[220:223], a[64:67], v[184:187]\n"
"v_mfma_f32_16x16x32_bf16 v[188:191], a[220:223], a[68:71], v[188:191]\n"
"s_waitcnt vmcnt(12) lgkmcnt(0)\n"
"s_barrier\n"
"v_mfma_f32_16x16x32_bf16 v[0:3], a[224:227], a[72:75], v[0:3]\n"
"s_add_u32 s57, s45, 1\n"
"v_mbcnt_lo_u32_b32 v211, -1, 0\n"
"v_mbcnt_hi_u32_b32 v211, -1, v211\n"
"v_mfma_f32_16x16x32_bf16 v[4:7], a[224:227], a[76:79], v[4:7]\n"
"v_min_u32 v211, 2, v211\n"
"v_add_u32 v211, s57, v211\n"
"v_mfma_f32_16x16x32_bf16 v[8:11], a[224:227], a[80:83], v[8:11]\n"
"s_lshl_b32 s57, s46, 9\n"
"s_add_u32 m0, s59, 65536\n"
"s_add_u32 s55, s67, 65536\n"
"v_mfma_f32_16x16x32_bf16 v[12:15], a[224:227], a[84:87], v[12:15]\n"
"s_add_u32 s61, s89, 12288\n"
"v_add_u32 v193, 147456, v193\n"
"ds_read_b64_tr_b16 a[192:193], v193\n"
"v_mfma_f32_16x16x32_bf16 v[16:19], a[224:227], a[88:91], v[16:19]\n"
"buffer_load_dwordx4 v195, s[32:35], s55 offen lds\n"
"v_mfma_f32_16x16x32_bf16 v[20:23], a[224:227], a[92:95], v[20:23]\n"
"v_add_u32 v194, 147456, v194\n"
"v_and_b32 v211, 3, v211\n"
"v_mfma_f32_16x16x32_bf16 v[24:27], a[228:231], a[72:75], v[24:27]\n"
"buffer_load_dwordx4 a[0:3], v192, s[92:95], s61 offen sc1\n"
"v_mfma_f32_16x16x32_bf16 v[28:31], a[228:231], a[76:79], v[28:31]\n"
"ds_read_b64_tr_b16 a[194:195], v193 offset:4096\n"
"s_add_u32 m0, s59, 67584\n"
"v_mfma_f32_16x16x32_bf16 v[32:35], a[228:231], a[80:83], v[32:35]\n"
"s_add_u32 s55, s67, 73728\n"
"ds_read_b64_tr_b16 a[196:197], v194\n"
"v_lshlrev_b32 v211, 11, v211\n"
"v_mfma_f32_16x16x32_bf16 v[36:39], a[228:231], a[84:87], v[36:39]\n"
"v_add_u32 v210, s57, v211\n"
"v_mfma_f32_16x16x32_bf16 v[40:43], a[228:231], a[88:91], v[40:43]\n"
"buffer_load_dwordx4 v195, s[32:35], s55 offen lds\n"
"v_mfma_f32_16x16x32_bf16 v[44:47], a[228:231], a[92:95], v[44:47]\n"
"ds_read_b64_tr_b16 a[198:199], v194 offset:4096\n"
"v_mfma_f32_16x16x32_bf16 v[48:51], a[232:235], a[72:75], v[48:51]\n"
"ds_read_b64_tr_b16 a[200:201], v193 offset:256\n"
"v_mfma_f32_16x16x32_bf16 v[52:55], a[232:235], a[76:79], v[52:55]\n"
"s_add_u32 m0, s59, 69632\n"
"v_mfma_f32_16x16x32_bf16 v[56:59], a[232:235], a[80:83], v[56:59]\n"
"buffer_load_dwordx4 a[4:7], v192, s[92:95], s61 offen offset:1024 sc1\n"
"v_mfma_f32_16x16x32_bf16 v[60:63], a[232:235], a[84:87], v[60:63]\n"
"s_add_u32 s55, s67, 81920\n"
"ds_read_b64_tr_b16 a[202:203], v193 offset:4352\n"
"v_mfma_f32_16x16x32_bf16 v[64:67], a[232:235], a[88:91], v[64:67]\n"
"buffer_load_dwordx4 v195, s[32:35], s55 offen lds\n"
"v_mfma_f32_16x16x32_bf16 v[68:71], a[232:235], a[92:95], v[68:71]\n"
"ds_read_b64_tr_b16 a[204:205], v194 offset:256\n"
"v_mfma_f32_16x16x32_bf16 v[72:75], a[236:239], a[72:75], v[72:75]\n"
"v_mfma_f32_16x16x32_bf16 v[76:79], a[236:239], a[76:79], v[76:79]\n"
"ds_read_b64_tr_b16 a[206:207], v194 offset:4352\n"
"v_mfma_f32_16x16x32_bf16 v[80:83], a[236:239], a[80:83], v[80:83]\n"
"s_add_u32 m0, s59, 71680\n"
"s_add_u32 s55, s67, 90112\n"
"v_mfma_f32_16x16x32_bf16 v[84:87], a[236:239], a[84:87], v[84:87]\n"
"buffer_load_dwordx4 a[8:11], v192, s[92:95], s61 offen offset:2048 sc1\n"
"v_mfma_f32_16x16x32_bf16 v[88:91], a[236:239], a[88:91], v[88:91]\n"
"ds_read_b64_tr_b16 a[208:209], v193 offset:512\n"
"v_mfma_f32_16x16x32_bf16 v[92:95], a[236:239], a[92:95], v[92:95]\n"
"buffer_load_dwordx4 v195, s[32:35], s55 offen lds\n"
"v_mfma_f32_16x16x32_bf16 v[96:99], a[240:243], a[72:75], v[96:99]\n"
"ds_read_b64_tr_b16 a[210:211], v193 offset:4608\n"
"v_mfma_f32_16x16x32_bf16 v[100:103], a[240:243], a[76:79], v[100:103]\n"
"v_mfma_f32_16x16x32_bf16 v[104:107], a[240:243], a[80:83], v[104:107]\n"
"ds_read_b64_tr_b16 a[212:213], v194 offset:512\n"
"v_mfma_f32_16x16x32_bf16 v[108:111], a[240:243], a[84:87], v[108:111]\n"
"s_add_u32 m0, s59, 49152\n"
"s_add_u32 s55, s68, 65536\n"
"v_mfma_f32_16x16x32_bf16 v[112:115], a[240:243], a[88:91], v[112:115]\n"
"ds_read_b64_tr_b16 a[214:215], v194 offset:4608\n"
"v_mfma_f32_16x16x32_bf16 v[116:119], a[240:243], a[92:95], v[116:119]\n"
"buffer_load_dwordx4 a[12:15], v192, s[92:95], s61 offen offset:3072 sc1\n"
"v_mfma_f32_16x16x32_bf16 v[120:123], a[244:247], a[72:75], v[120:123]\n"
"buffer_load_dwordx4 v195, s[32:35], s55 offen lds\n"
"v_mfma_f32_16x16x32_bf16 v[124:127], a[244:247], a[76:79], v[124:127]\n"
"ds_read_b64_tr_b16 a[216:217], v193 offset:768\n"
"v_mfma_f32_16x16x32_bf16 v[128:131], a[244:247], a[80:83], v[128:131]\n"
"v_mfma_f32_16x16x32_bf16 v[132:135], a[244:247], a[84:87], v[132:135]\n"
"ds_read_b64_tr_b16 a[218:219], v193 offset:4864\n"
"s_add_u32 m0, s59, 51200\n"
"v_mfma_f32_16x16x32_bf16 v[136:139], a[244:247], a[88:91], v[136:139]\n"
"s_add_u32 s55, s68, 73728\n"
"s_add_u32 s61, s89, 16384\n"
"v_add_u32 v193, 0xfffe0000, v193\n"
"v_mfma_f32_16x16x32_bf16 v[140:143], a[244:247], a[92:95], v[140:143]\n"
"ds_read_b64_tr_b16 a[220:221], v194 offset:768\n"
"v_mfma_f32_16x16x32_bf16 v[144:147], a[248:251], a[72:75], v[144:147]\n"
"buffer_load_dwordx4 v195, s[32:35], s55 offen lds\n"
"v_mfma_f32_16x16x32_bf16 v[148:151], a[248:251], a[76:79], v[148:151]\n"
"buffer_load_dwordx4 a[16:19], v192, s[92:95], s61 offen sc1\n"
"v_mfma_f32_16x16x32_bf16 v[152:155], a[248:251], a[80:83], v[152:155]\n"
"ds_read_b64_tr_b16 a[222:223], v194 offset:4864\n"
"v_add_u32 v194, 0xfffe0000, v194\n"
"v_mfma_f32_16x16x32_bf16 v[156:159], a[248:251], a[84:87], v[156:159]\n"
"v_mfma_f32_16x16x32_bf16 v[160:163], a[248:251], a[88:91], v[160:163]\n"
"s_add_u32 m0, s59, 53248\n"
"s_add_u32 s55, s68, 81920\n"
"v_mfma_f32_16x16x32_bf16 v[164:167], a[248:251], a[92:95], v[164:167]\n"
"v_mfma_f32_16x16x32_bf16 v[168:171], a[252:255], a[72:75], v[168:171]\n"
"v_mfma_f32_16x16x32_bf16 v[172:175], a[252:255], a[76:79], v[172:175]\n"
"buffer_load_dwordx4 v195, s[32:35], s55 offen lds\n"
"v_mfma_f32_16x16x32_bf16 v[176:179], a[252:255], a[80:83], v[176:179]\n"
"buffer_load_dwordx4 a[20:23], v192, s[92:95], s61 offen offset:1024 sc1\n"
"v_mfma_f32_16x16x32_bf16 v[180:183], a[252:255], a[84:87], v[180:183]\n"
"s_waitcnt vmcnt(19)\n"
"v_mfma_f32_16x16x32_bf16 v[184:187], a[252:255], a[88:91], v[184:187]\n"
"s_add_u32 m0, s59, 55296\n"
"s_add_u32 s55, s68, 90112\n"
"s_add_u32 s61, s90, 12288\n"
"v_mfma_f32_16x16x32_bf16 v[188:191], a[252:255], a[92:95], v[188:191]\n"
"s_waitcnt lgkmcnt(0)\n"
"v_mfma_f32_16x16x32_bf16 v[0:3], a[192:195], a[96:99], v[0:3]\n"
"v_mfma_f32_16x16x32_bf16 v[4:7], a[192:195], a[100:103], v[4:7]\n"
"buffer_load_dwordx4 v195, s[32:35], s55 offen lds\n"
"v_mfma_f32_16x16x32_bf16 v[8:11], a[192:195], a[104:107], v[8:11]\n"
"v_mfma_f32_16x16x32_bf16 v[12:15], a[192:195], a[108:111], v[12:15]\n"
"buffer_load_dwordx4 a[24:27], v192, s[92:95], s61 offen sc1\n"
"v_mfma_f32_16x16x32_bf16 v[16:19], a[192:195], a[112:115], v[16:19]\n"
"ds_read_b64_tr_b16 a[224:225], v193\n"
"v_mfma_f32_16x16x32_bf16 v[20:23], a[192:195], a[116:119], v[20:23]\n"
"s_add_u32 m0, s59, 98304\n"
"v_mfma_f32_16x16x32_bf16 v[24:27], a[196:199], a[96:99], v[24:27]\n"
"s_add_u32 s55, s66, 98304\n"
"ds_read_b64_tr_b16 a[226:227], v193 offset:4096\n"
"v_mfma_f32_16x16x32_bf16 v[28:31], a[196:199], a[100:103], v[28:31]\n"
"v_mfma_f32_16x16x32_bf16 v[32:35], a[196:199], a[104:107], v[32:35]\n"
"buffer_load_dwordx4 v195, s[32:35], s55 offen lds\n"
"v_mfma_f32_16x16x32_bf16 v[36:39], a[196:199], a[108:111], v[36:39]\n"
"ds_read_b64_tr_b16 a[228:229], v194\n"
"v_mfma_f32_16x16x32_bf16 v[40:43], a[196:199], a[112:115], v[40:43]\n"
"ds_read_b64_tr_b16 a[230:231], v194 offset:4096\n"
"v_mfma_f32_16x16x32_bf16 v[44:47], a[196:199], a[116:119], v[44:47]\n"
"buffer_load_dwordx4 a[28:31], v192, s[92:95], s61 offen offset:1024 sc1\n"
"v_mfma_f32_16x16x32_bf16 v[48:51], a[200:203], a[96:99], v[48:51]\n"
"s_add_u32 m0, s59, 100352\n"
"s_add_u32 s55, s66, 106496\n"
"ds_read_b64_tr_b16 a[232:233], v193 offset:256\n"
"v_mfma_f32_16x16x32_bf16 v[52:55], a[200:203], a[100:103], v[52:55]\n"
"v_mfma_f32_16x16x32_bf16 v[56:59], a[200:203], a[104:107], v[56:59]\n"
"v_mfma_f32_16x16x32_bf16 v[60:63], a[200:203], a[108:111], v[60:63]\n"
"buffer_load_dwordx4 v195, s[32:35], s55 offen lds\n"
"v_mfma_f32_16x16x32_bf16 v[64:67], a[200:203], a[112:115], v[64:67]\n"
"ds_read_b64_tr_b16 a[234:235], v193 offset:4352\n"
"v_mfma_f32_16x16x32_bf16 v[68:71], a[200:203], a[116:119], v[68:71]\n"
"ds_read_b64_tr_b16 a[236:237], v194 offset:256\n"
"v_mfma_f32_16x16x32_bf16 v[72:75], a[204:207], a[96:99], v[72:75]\n"
"buffer_load_dwordx4 a[32:35], v192, s[92:95], s61 offen offset:2048 sc1\n"
"v_mfma_f32_16x16x32_bf16 v[76:79], a[204:207], a[100:103], v[76:79]\n"
"s_add_u32 m0, s59, 102400\n"
"s_add_u32 s55, s66, 114688\n"
"ds_read_b64_tr_b16 a[238:239], v194 offset:4352\n"
"v_mfma_f32_16x16x32_bf16 v[80:83], a[204:207], a[104:107], v[80:83]\n"
"v_mfma_f32_16x16x32_bf16 v[84:87], a[204:207], a[108:111], v[84:87]\n"
"buffer_load_dwordx4 v195, s[32:35], s55 offen lds\n"
"v_mfma_f32_16x16x32_bf16 v[88:91], a[204:207], a[112:115], v[88:91]\n"
"ds_read_b64_tr_b16 a[240:241], v193 offset:512\n"
"v_mfma_f32_16x16x32_bf16 v[92:95], a[204:207], a[116:119], v[92:95]\n"
"v_mfma_f32_16x16x32_bf16 v[96:99], a[208:211], a[96:99], v[96:99]\n"
"s_add_u32 m0, s59, 104448\n"
"s_add_u32 s55, s66, 122880\n"
"v_mfma_f32_16x16x32_bf16 v[100:103], a[208:211], a[100:103], v[100:103]\n"
"ds_read_b64_tr_b16 a[242:243], v193 offset:4608\n"
"v_mfma_f32_16x16x32_bf16 v[104:107], a[208:211], a[104:107], v[104:107]\n"
"buffer_load_dwordx4 a[36:39], v192, s[92:95], s61 offen offset:3072 sc1\n"
"v_mfma_f32_16x16x32_bf16 v[108:111], a[208:211], a[108:111], v[108:111]\n"
"buffer_load_dwordx4 v195, s[32:35], s55 offen lds\n"
"v_mfma_f32_16x16x32_bf16 v[112:115], a[208:211], a[112:115], v[112:115]\n"
"s_add_u32 s61, s90, 16384\n"
"ds_read_b64_tr_b16 a[244:245], v194 offset:512\n"
"v_mfma_f32_16x16x32_bf16 v[116:119], a[208:211], a[116:119], v[116:119]\n"
"ds_read_b64_tr_b16 a[246:247], v194 offset:4608\n"
"v_mfma_f32_16x16x32_bf16 v[120:123], a[212:215], a[96:99], v[120:123]\n"
"ds_read_b64_tr_b16 a[248:249], v193 offset:768\n"
"v_mfma_f32_16x16x32_bf16 v[124:127], a[212:215], a[100:103], v[124:127]\n"
"v_mfma_f32_16x16x32_bf16 v[128:131], a[212:215], a[104:107], v[128:131]\n"
"v_mfma_f32_16x16x32_bf16 v[132:135], a[212:215], a[108:111], v[132:135]\n"
"buffer_load_dwordx4 a[40:43], v192, s[92:95], s61 offen sc1\n"
"v_mfma_f32_16x16x32_bf16 v[136:139], a[212:215], a[112:115], v[136:139]\n"
"ds_read_b64_tr_b16 a[250:251], v193 offset:4864\n"
"v_add_u32 v193, 16384, v193\n"
"v_mfma_f32_16x16x32_bf16 v[140:143], a[212:215], a[116:119], v[140:143]\n"
"ds_read_b64_tr_b16 a[252:253], v194 offset:768\n"
"v_mfma_f32_16x16x32_bf16 v[144:147], a[216:219], a[96:99], v[144:147]\n"
"v_mfma_f32_16x16x32_bf16 v[148:151], a[216:219], a[100:103], v[148:151]\n"
"ds_read_b64_tr_b16 a[254:255], v194 offset:4864\n"
"v_add_u32 v194, 16384, v194\n"
"v_mfma_f32_16x16x32_bf16 v[152:155], a[216:219], a[104:107], v[152:155]\n"
"v_mfma_f32_16x16x32_bf16 v[156:159], a[216:219], a[108:111], v[156:159]\n"
"v_mfma_f32_16x16x32_bf16 v[160:163], a[216:219], a[112:115], v[160:163]\n"
"v_mfma_f32_16x16x32_bf16 v[164:167], a[216:219], a[116:119], v[164:167]\n"
"buffer_load_dwordx4 a[44:47], v192, s[92:95], s61 offen offset:1024 sc1\n"
"v_mfma_f32_16x16x32_bf16 v[168:171], a[220:223], a[96:99], v[168:171]\n"
"s_add_u32 s61, s91, 12288\n"
"v_mfma_f32_16x16x32_bf16 v[172:175], a[220:223], a[100:103], v[172:175]\n"
"v_mfma_f32_16x16x32_bf16 v[176:179], a[220:223], a[104:107], v[176:179]\n"
"s_waitcnt vmcnt(24)\n"
"v_mfma_f32_16x16x32_bf16 v[180:183], a[220:223], a[108:111], v[180:183]\n"
"v_mfma_f32_16x16x32_bf16 v[184:187], a[220:223], a[112:115], v[184:187]\n"
"v_mfma_f32_16x16x32_bf16 v[188:191], a[220:223], a[116:119], v[188:191]\n"
"s_waitcnt lgkmcnt(0)\n"
"v_mfma_f32_16x16x32_bf16 v[0:3], a[224:227], a[120:123], v[0:3]\n"
"buffer_load_dwordx4 a[48:51], v192, s[92:95], s61 offen sc1\n"
"v_mfma_f32_16x16x32_bf16 v[4:7], a[224:227], a[124:127], v[4:7]\n"
"v_mfma_f32_16x16x32_bf16 v[8:11], a[224:227], a[128:131], v[8:11]\n"
"v_mfma_f32_16x16x32_bf16 v[12:15], a[224:227], a[132:135], v[12:15]\n"
"ds_read_b64_tr_b16 a[192:193], v193\n"
"v_mfma_f32_16x16x32_bf16 v[16:19], a[224:227], a[136:139], v[16:19]\n"
"v_mfma_f32_16x16x32_bf16 v[20:23], a[224:227], a[140:143], v[20:23]\n"
"v_mfma_f32_16x16x32_bf16 v[24:27], a[228:231], a[120:123], v[24:27]\n"
"ds_read_b64_tr_b16 a[194:195], v193 offset:4096\n"
"v_mfma_f32_16x16x32_bf16 v[28:31], a[228:231], a[124:127], v[28:31]\n"
"v_mfma_f32_16x16x32_bf16 v[32:35], a[228:231], a[128:131], v[32:35]\n"
"buffer_load_dwordx4 a[52:55], v192, s[92:95], s61 offen offset:1024 sc1\n"
"v_mfma_f32_16x16x32_bf16 v[36:39], a[228:231], a[132:135], v[36:39]\n"
"ds_read_b64_tr_b16 a[196:197], v194\n"
"v_mfma_f32_16x16x32_bf16 v[40:43], a[228:231], a[136:139], v[40:43]\n"
"ds_read_b64_tr_b16 a[198:199], v194 offset:4096\n"
"v_mfma_f32_16x16x32_bf16 v[44:47], a[228:231], a[140:143], v[44:47]\n"
"v_mfma_f32_16x16x32_bf16 v[48:51], a[232:235], a[120:123], v[48:51]\n"
"ds_read_b64_tr_b16 a[200:201], v193 offset:256\n"
"v_mfma_f32_16x16x32_bf16 v[52:55], a[232:235], a[124:127], v[52:55]\n"
"v_mfma_f32_16x16x32_bf16 v[56:59], a[232:235], a[128:131], v[56:59]\n"
"v_mfma_f32_16x16x32_bf16 v[60:63], a[232:235], a[132:135], v[60:63]\n"
"buffer_load_dwordx4 a[56:59], v192, s[92:95], s61 offen offset:2048 sc1\n"
"v_mfma_f32_16x16x32_bf16 v[64:67], a[232:235], a[136:139], v[64:67]\n"
"ds_read_b64_tr_b16 a[202:203], v193 offset:4352\n"
"v_mfma_f32_16x16x32_bf16 v[68:71], a[232:235], a[140:143], v[68:71]\n"
"ds_read_b64_tr_b16 a[204:205], v194 offset:256\n"
"v_mfma_f32_16x16x32_bf16 v[72:75], a[236:239], a[120:123], v[72:75]\n"
"v_mfma_f32_16x16x32_bf16 v[76:79], a[236:239], a[124:127], v[76:79]\n"
"ds_read_b64_tr_b16 a[206:207], v194 offset:4352\n"
"v_mfma_f32_16x16x32_bf16 v[80:83], a[236:239], a[128:131], v[80:83]\n"
"v_mfma_f32_16x16x32_bf16 v[84:87], a[236:239], a[132:135], v[84:87]\n"
"ds_read_b64_tr_b16 a[208:209], v193 offset:512\n"
"v_mfma_f32_16x16x32_bf16 v[88:91], a[236:239], a[136:139], v[88:91]\n"
"v_mfma_f32_16x16x32_bf16 v[92:95], a[236:239], a[140:143], v[92:95]\n"
"buffer_load_dwordx4 a[60:63], v192, s[92:95], s61 offen offset:3072 sc1\n"
"v_mfma_f32_16x16x32_bf16 v[96:99], a[240:243], a[120:123], v[96:99]\n"
"s_add_u32 s61, s91, 16384\n"
"ds_read_b64_tr_b16 a[210:211], v193 offset:4608\n"
"v_mfma_f32_16x16x32_bf16 v[100:103], a[240:243], a[124:127], v[100:103]\n"
"v_mfma_f32_16x16x32_bf16 v[104:107], a[240:243], a[128:131], v[104:107]\n"
"ds_read_b64_tr_b16 a[212:213], v194 offset:512\n"
"v_mfma_f32_16x16x32_bf16 v[108:111], a[240:243], a[132:135], v[108:111]\n"
"v_mfma_f32_16x16x32_bf16 v[112:115], a[240:243], a[136:139], v[112:115]\n"
"ds_read_b64_tr_b16 a[214:215], v194 offset:4608\n"
"v_mfma_f32_16x16x32_bf16 v[116:119], a[240:243], a[140:143], v[116:119]\n"
"v_mfma_f32_16x16x32_bf16 v[120:123], a[244:247], a[120:123], v[120:123]\n"
"buffer_load_dwordx4 a[64:67], v192, s[92:95], s61 offen sc1\n"
"v_mfma_f32_16x16x32_bf16 v[124:127], a[244:247], a[124:127], v[124:127]\n"
"ds_read_b64_tr_b16 a[216:217], v193 offset:768\n"
"v_mfma_f32_16x16x32_bf16 v[128:131], a[244:247], a[128:131], v[128:131]\n"
"v_mfma_f32_16x16x32_bf16 v[132:135], a[244:247], a[132:135], v[132:135]\n"
"ds_read_b64_tr_b16 a[218:219], v193 offset:4864\n"
"v_mfma_f32_16x16x32_bf16 v[136:139], a[244:247], a[136:139], v[136:139]\n"
"v_mfma_f32_16x16x32_bf16 v[140:143], a[244:247], a[140:143], v[140:143]\n"
"ds_read_b64_tr_b16 a[220:221], v194 offset:768\n"
"v_mfma_f32_16x16x32_bf16 v[144:147], a[248:251], a[120:123], v[144:147]\n"
"v_mfma_f32_16x16x32_bf16 v[148:151], a[248:251], a[124:127], v[148:151]\n"
"ds_read_b64_tr_b16 a[222:223], v194 offset:4864\n"
"v_mfma_f32_16x16x32_bf16 v[152:155], a[248:251], a[128:131], v[152:155]\n"
"buffer_load_dwordx4 a[68:71], v192, s[92:95], s61 offen offset:1024 sc1\n"
"v_mfma_f32_16x16x32_bf16 v[156:159], a[248:251], a[132:135], v[156:159]\n"
"v_mfma_f32_16x16x32_bf16 v[160:163], a[248:251], a[136:139], v[160:163]\n"
"v_mfma_f32_16x16x32_bf16 v[164:167], a[248:251], a[140:143], v[164:167]\n"
"v_mfma_f32_16x16x32_bf16 v[168:171], a[252:255], a[120:123], v[168:171]\n"
"v_mfma_f32_16x16x32_bf16 v[172:175], a[252:255], a[124:127], v[172:175]\n"
"v_mfma_f32_16x16x32_bf16 v[176:179], a[252:255], a[128:131], v[176:179]\n"
"v_mfma_f32_16x16x32_bf16 v[180:183], a[252:255], a[132:135], v[180:183]\n"
"v_mfma_f32_16x16x32_bf16 v[184:187], a[252:255], a[136:139], v[184:187]\n"
"v_mfma_f32_16x16x32_bf16 v[188:191], a[252:255], a[140:143], v[188:191]\n"
"s_waitcnt vmcnt(8) lgkmcnt(0)\n"
"s_barrier\n"
"v_mfma_f32_16x16x32_bf16 v[0:3], a[192:195], a[0:3], v[0:3]\n"
"v_add_u32 v193, 32768, v193\n"
"v_add_u32 v194, 32768, v194\n"
"v_mfma_f32_16x16x32_bf16 v[4:7], a[192:195], a[4:7], v[4:7]\n"
"s_add_u32 m0, s59, 81920\n"
"s_add_u32 s55, s67, 98304\n"
"s_add_u32 s61, s89, 18432\n"
"v_mfma_f32_16x16x32_bf16 v[8:11], a[192:195], a[8:11], v[8:11]\n"
"v_mfma_f32_16x16x32_bf16 v[12:15], a[192:195], a[12:15], v[12:15]\n"
"ds_read_b64_tr_b16 a[224:225], v193\n"
"v_mfma_f32_16x16x32_bf16 v[16:19], a[192:195], a[16:19], v[16:19]\n"
"buffer_load_dwordx4 v195, s[32:35], s55 offen lds\n"
"v_mfma_f32_16x16x32_bf16 v[20:23], a[192:195], a[20:23], v[20:23]\n"
"v_mfma_f32_16x16x32_bf16 v[24:27], a[196:199], a[0:3], v[24:27]\n"
"buffer_load_dwordx4 a[72:75], v192, s[92:95], s61 offen sc1\n"
"v_mfma_f32_16x16x32_bf16 v[28:31], a[196:199], a[4:7], v[28:31]\n"
"ds_read_b64_tr_b16 a[226:227], v193 offset:4096\n"
"s_add_u32 m0, s59, 83968\n"
"v_mfma_f32_16x16x32_bf16 v[32:35], a[196:199], a[8:11], v[32:35]\n"
"s_add_u32 s55, s67, 106496\n"
"ds_read_b64_tr_b16 a[228:229], v194\n"
"v_mfma_f32_16x16x32_bf16 v[36:39], a[196:199], a[12:15], v[36:39]\n"
"v_mfma_f32_16x16x32_bf16 v[40:43], a[196:199], a[16:19], v[40:43]\n"
"buffer_load_dwordx4 v195, s[32:35], s55 offen lds\n"
"v_mfma_f32_16x16x32_bf16 v[44:47], a[196:199], a[20:23], v[44:47]\n"
"ds_read_b64_tr_b16 a[230:231], v194 offset:4096\n"
"v_mfma_f32_16x16x32_bf16 v[48:51], a[200:203], a[0:3], v[48:51]\n"
"ds_read_b64_tr_b16 a[232:233], v193 offset:256\n"
"v_mfma_f32_16x16x32_bf16 v[52:55], a[200:203], a[4:7], v[52:55]\n"
"s_add_u32 m0, s59, 86016\n"
"v_mfma_f32_16x16x32_bf16 v[56:59], a[200:203], a[8:11], v[56:59]\n"
"buffer_load_dwordx4 a[76:79], v192, s[92:95], s61 offen offset:1024 sc1\n"
"v_mfma_f32_16x16x32_bf16 v[60:63], a[200:203], a[12:15], v[60:63]\n"
"s_add_u32 s55, s67, 114688\n"
"ds_read_b64_tr_b16 a[234:235], v193 offset:4352\n"
"v_mfma_f32_16x16x32_bf16 v[64:67], a[200:203], a[16:19], v[64:67]\n"
"buffer_load_dwordx4 v195, s[32:35], s55 offen lds\n"
"v_mfma_f32_16x16x32_bf16 v[68:71], a[200:203], a[20:23], v[68:71]\n"
"ds_read_b64_tr_b16 a[236:237], v194 offset:256\n"
"v_mfma_f32_16x16x32_bf16 v[72:75], a[204:207], a[0:3], v[72:75]\n"
"v_mfma_f32_16x16x32_bf16 v[76:79], a[204:207], a[4:7], v[76:79]\n"
"ds_read_b64_tr_b16 a[238:239], v194 offset:4352\n"
"v_mfma_f32_16x16x32_bf16 v[80:83], a[204:207], a[8:11], v[80:83]\n"
"s_add_u32 m0, s59, 88064\n"
"s_add_u32 s55, s67, 122880\n"
"v_mfma_f32_16x16x32_bf16 v[84:87], a[204:207], a[12:15], v[84:87]\n"
"buffer_load_dwordx4 a[80:83], v192, s[92:95], s61 offen offset:2048 sc1\n"
"v_mfma_f32_16x16x32_bf16 v[88:91], a[204:207], a[16:19], v[88:91]\n"
"ds_read_b64_tr_b16 a[240:241], v193 offset:512\n"
"v_mfma_f32_16x16x32_bf16 v[92:95], a[204:207], a[20:23], v[92:95]\n"
"buffer_load_dwordx4 v195, s[32:35], s55 offen lds\n"
"v_mfma_f32_16x16x32_bf16 v[96:99], a[208:211], a[0:3], v[96:99]\n"
"ds_read_b64_tr_b16 a[242:243], v193 offset:4608\n"
"v_mfma_f32_16x16x32_bf16 v[100:103], a[208:211], a[4:7], v[100:103]\n"
"v_mfma_f32_16x16x32_bf16 v[104:107], a[208:211], a[8:11], v[104:107]\n"
"ds_read_b64_tr_b16 a[244:245], v194 offset:512\n"
"v_mfma_f32_16x16x32_bf16 v[108:111], a[208:211], a[12:15], v[108:111]\n"
"s_add_u32 m0, s59, 131072\n"
"s_add_u32 s55, s68, 98304\n"
"v_mfma_f32_16x16x32_bf16 v[112:115], a[208:211], a[16:19], v[112:115]\n"
"ds_read_b64_tr_b16 a[246:247], v194 offset:4608\n"
"v_mfma_f32_16x16x32_bf16 v[116:119], a[208:211], a[20:23], v[116:119]\n"
"buffer_load_dwordx4 a[84:87], v192, s[92:95], s61 offen offset:3072 sc1\n"
"v_mfma_f32_16x16x32_bf16 v[120:123], a[212:215], a[0:3], v[120:123]\n"
"buffer_load_dwordx4 v195, s[32:35], s55 offen lds\n"
"v_mfma_f32_16x16x32_bf16 v[124:127], a[212:215], a[4:7], v[124:127]\n"
"ds_read_b64_tr_b16 a[248:249], v193 offset:768\n"
"v_mfma_f32_16x16x32_bf16 v[128:131], a[212:215], a[8:11], v[128:131]\n"
"v_mfma_f32_16x16x32_bf16 v[132:135], a[212:215], a[12:15], v[132:135]\n"
"ds_read_b64_tr_b16 a[250:251], v193 offset:4864\n"
"s_add_u32 m0, s59, 133120\n"
"v_mfma_f32_16x16x32_bf16 v[136:139], a[212:215], a[16:19], v[136:139]\n"
"s_add_u32 s55, s68, 106496\n"
"s_add_u32 s61, s89, 22528\n"
"v_add_u32 v193, 0xffffc000, v193\n"
"v_mfma_f32_16x16x32_bf16 v[140:143], a[212:215], a[20:23], v[140:143]\n"
"ds_read_b64_tr_b16 a[252:253], v194 offset:768\n"
"v_mfma_f32_16x16x32_bf16 v[144:147], a[216:219], a[0:3], v[144:147]\n"
"buffer_load_dwordx4 v195, s[32:35], s55 offen lds\n"
"v_mfma_f32_16x16x32_bf16 v[148:151], a[216:219], a[4:7], v[148:151]\n"
"buffer_load_dwordx4 a[88:91], v192, s[92:95], s61 offen sc1\n"
"v_mfma_f32_16x16x32_bf16 v[152:155], a[216:219], a[8:11], v[152:155]\n"
"ds_read_b64_tr_b16 a[254:255], v194 offset:4864\n"
"v_add_u32 v194, 0xffffc000, v194\n"
"v_mfma_f32_16x16x32_bf16 v[156:159], a[216:219], a[12:15], v[156:159]\n"
"v_mfma_f32_16x16x32_bf16 v[160:163], a[216:219], a[16:19], v[160:163]\n"
"s_add_u32 m0, s59, 135168\n"
"s_add_u32 s55, s68, 114688\n"
"v_mfma_f32_16x16x32_bf16 v[164:167], a[216:219], a[20:23], v[164:167]\n"
"v_mfma_f32_16x16x32_bf16 v[168:171], a[220:223], a[0:3], v[168:171]\n"
"v_mfma_f32_16x16x32_bf16 v[172:175], a[220:223], a[4:7], v[172:175]\n"
"buffer_load_dwordx4 v195, s[32:35], s55 offen lds\n"
"v_mfma_f32_16x16x32_bf16 v[176:179], a[220:223], a[8:11], v[176:179]\n"
"buffer_load_dwordx4 a[92:95], v192, s[92:95], s61 offen offset:1024 sc1\n"
"v_mfma_f32_16x16x32_bf16 v[180:183], a[220:223], a[12:15], v[180:183]\n"
"s_waitcnt vmcnt(19)\n"
"v_mfma_f32_16x16x32_bf16 v[184:187], a[220:223], a[16:19], v[184:187]\n"
"s_add_u32 m0, s59, 137216\n"
"s_add_u32 s55, s68, 122880\n"
"s_add_u32 s61, s90, 18432\n"
"v_mfma_f32_16x16x32_bf16 v[188:191], a[220:223], a[20:23], v[188:191]\n"
"s_waitcnt lgkmcnt(0)\n"
"v_mfma_f32_16x16x32_bf16 v[0:3], a[224:227], a[24:27], v[0:3]\n"
"v_mfma_f32_16x16x32_bf16 v[4:7], a[224:227], a[28:31], v[4:7]\n"
"buffer_load_dwordx4 v195, s[32:35], s55 offen lds\n"
"v_mfma_f32_16x16x32_bf16 v[8:11], a[224:227], a[32:35], v[8:11]\n"
"v_mfma_f32_16x16x32_bf16 v[12:15], a[224:227], a[36:39], v[12:15]\n"
"buffer_load_dwordx4 a[96:99], v192, s[92:95], s61 offen sc1\n"
"v_mfma_f32_16x16x32_bf16 v[16:19], a[224:227], a[40:43], v[16:19]\n"
"ds_read_b64_tr_b16 a[192:193], v193\n"
"v_mfma_f32_16x16x32_bf16 v[20:23], a[224:227], a[44:47], v[20:23]\n"
"v_mfma_f32_16x16x32_bf16 v[24:27], a[228:231], a[24:27], v[24:27]\n"
"ds_read_b64_tr_b16 a[194:195], v193 offset:4096\n"
"v_mfma_f32_16x16x32_bf16 v[28:31], a[228:231], a[28:31], v[28:31]\n"
"v_mfma_f32_16x16x32_bf16 v[32:35], a[228:231], a[32:35], v[32:35]\n"
"ds_read_b64_tr_b16 a[196:197], v194\n"
"v_mfma_f32_16x16x32_bf16 v[36:39], a[228:231], a[36:39], v[36:39]\n"
"v_mfma_f32_16x16x32_bf16 v[40:43], a[228:231], a[40:43], v[40:43]\n"
"ds_read_b64_tr_b16 a[198:199], v194 offset:4096\n"
"v_mfma_f32_16x16x32_bf16 v[44:47], a[228:231], a[44:47], v[44:47]\n"
"buffer_load_dwordx4 a[100:103], v192, s[92:95], s61 offen offset:1024 sc1\n"
"v_mfma_f32_16x16x32_bf16 v[48:51], a[232:235], a[24:27], v[48:51]\n"
"ds_read_b64_tr_b16 a[200:201], v193 offset:256\n"
"v_mfma_f32_16x16x32_bf16 v[52:55], a[232:235], a[28:31], v[52:55]\n"
"v_mfma_f32_16x16x32_bf16 v[56:59], a[232:235], a[32:35], v[56:59]\n"
"v_mfma_f32_16x16x32_bf16 v[60:63], a[232:235], a[36:39], v[60:63]\n"
"ds_read_b64_tr_b16 a[202:203], v193 offset:4352\n"
"v_mfma_f32_16x16x32_bf16 v[64:67], a[232:235], a[40:43], v[64:67]\n"
"v_mfma_f32_16x16x32_bf16 v[68:71], a[232:235], a[44:47], v[68:71]\n"
"ds_read_b64_tr_b16 a[204:205], v194 offset:256\n"
"v_mfma_f32_16x16x32_bf16 v[72:75], a[236:239], a[24:27], v[72:75]\n"
"buffer_load_dwordx4 a[104:107], v192, s[92:95], s61 offen offset:2048 sc1\n"
"v_mfma_f32_16x16x32_bf16 v[76:79], a[236:239], a[28:31], v[76:79]\n"
"ds_read_b64_tr_b16 a[206:207], v194 offset:4352\n"
"v_mfma_f32_16x16x32_bf16 v[80:83], a[236:239], a[32:35], v[80:83]\n"
"v_mfma_f32_16x16x32_bf16 v[84:87], a[236:239], a[36:39], v[84:87]\n"
"ds_read_b64_tr_b16 a[208:209], v193 offset:512\n"
"v_mfma_f32_16x16x32_bf16 v[88:91], a[236:239], a[40:43], v[88:91]\n"
"v_mfma_f32_16x16x32_bf16 v[92:95], a[236:239], a[44:47], v[92:95]\n"
"v_mfma_f32_16x16x32_bf16 v[96:99], a[240:243], a[24:27], v[96:99]\n"
"ds_read_b64_tr_b16 a[210:211], v193 offset:4608\n"
"v_mfma_f32_16x16x32_bf16 v[100:103], a[240:243], a[28:31], v[100:103]\n"
"v_mfma_f32_16x16x32_bf16 v[104:107], a[240:243], a[32:35], v[104:107]\n"
"buffer_load_dwordx4 a[108:111], v192, s[92:95], s61 offen offset:3072 sc1\n"
"v_mfma_f32_16x16x32_bf16 v[108:111], a[240:243], a[36:39], v[108:111]\n"
"s_add_u32 s61, s90, 22528\n"
"ds_read_b64_tr_b16 a[212:213], v194 offset:512\n"
"v_mfma_f32_16x16x32_bf16 v[112:115], a[240:243], a[40:43], v[112:115]\n"
"ds_read_b64_tr_b16 a[214:215], v194 offset:4608\n"
"v_mfma_f32_16x16x32_bf16 v[116:119], a[240:243], a[44:47], v[116:119]\n"
"v_mfma_f32_16x16x32_bf16 v[120:123], a[244:247], a[24:27], v[120:123]\n"
"ds_read_b64_tr_b16 a[216:217], v193 offset:768\n"
"v_mfma_f32_16x16x32_bf16 v[124:127], a[244:247], a[28:31], v[124:127]\n"
"v_mfma_f32_16x16x32_bf16 v[128:131], a[244:247], a[32:35], v[128:131]\n"
"v_mfma_f32_16x16x32_bf16 v[132:135], a[244:247], a[36:39], v[132:135]\n"
"buffer_load_dwordx4 a[112:115], v192, s[92:95], s61 offen sc1\n"
"v_mfma_f32_16x16x32_bf16 v[136:139], a[244:247], a[40:43], v[136:139]\n"
"ds_read_b64_tr_b16 a[218:219], v193 offset:4864\n"
"v_add_u32 v193, 32768, v193\n"
"v_mfma_f32_16x16x32_bf16 v[140:143], a[244:247], a[44:47], v[140:143]\n"
"ds_read_b64_tr_b16 a[220:221], v194 offset:768\n"
"v_mfma_f32_16x16x32_bf16 v[144:147], a[248:251], a[24:27], v[144:147]\n"
"v_mfma_f32_16x16x32_bf16 v[148:151], a[248:251], a[28:31], v[148:151]\n"
"ds_read_b64_tr_b16 a[222:223], v194 offset:4864\n"
"v_add_u32 v194, 32768, v194\n"
"v_mfma_f32_16x16x32_bf16 v[152:155], a[248:251], a[32:35], v[152:155]\n"
"v_mfma_f32_16x16x32_bf16 v[156:159], a[248:251], a[36:39], v[156:159]\n"
"v_mfma_f32_16x16x32_bf16 v[160:163], a[248:251], a[40:43], v[160:163]\n"
"v_mfma_f32_16x16x32_bf16 v[164:167], a[248:251], a[44:47], v[164:167]\n"
"buffer_load_dwordx4 a[116:119], v192, s[92:95], s61 offen offset:1024 sc1\n"
"v_mfma_f32_16x16x32_bf16 v[168:171], a[252:255], a[24:27], v[168:171]\n"
"s_add_u32 s61, s91, 18432\n"
"v_mfma_f32_16x16x32_bf16 v[172:175], a[252:255], a[28:31], v[172:175]\n"
"v_mfma_f32_16x16x32_bf16 v[176:179], a[252:255], a[32:35], v[176:179]\n"
"s_waitcnt vmcnt(20)\n"
"v_mfma_f32_16x16x32_bf16 v[180:183], a[252:255], a[36:39], v[180:183]\n"
"v_mfma_f32_16x16x32_bf16 v[184:187], a[252:255], a[40:43], v[184:187]\n"
"v_mfma_f32_16x16x32_bf16 v[188:191], a[252:255], a[44:47], v[188:191]\n"
"s_waitcnt lgkmcnt(0)\n"
"v_mfma_f32_16x16x32_bf16 v[0:3], a[192:195], a[48:51], v[0:3]\n"
"buffer_load_dwordx4 a[120:123], v192, s[92:95], s61 offen sc1\n"
"v_mfma_f32_16x16x32_bf16 v[4:7], a[192:195], a[52:55], v[4:7]\n"
"v_mfma_f32_16x16x32_bf16 v[8:11], a[192:195], a[56:59], v[8:11]\n"
"v_mfma_f32_16x16x32_bf16 v[12:15], a[192:195], a[60:63], v[12:15]\n"
"ds_read_b64_tr_b16 a[224:225], v193 offset:16384\n"
"v_mfma_f32_16x16x32_bf16 v[16:19], a[192:195], a[64:67], v[16:19]\n"
"v_mfma_f32_16x16x32_bf16 v[20:23], a[192:195], a[68:71], v[20:23]\n"
"v_mfma_f32_16x16x32_bf16 v[24:27], a[196:199], a[48:51], v[24:27]\n"
"ds_read_b64_tr_b16 a[226:227], v193 offset:20480\n"
"v_mfma_f32_16x16x32_bf16 v[28:31], a[196:199], a[52:55], v[28:31]\n"
"v_mfma_f32_16x16x32_bf16 v[32:35], a[196:199], a[56:59], v[32:35]\n"
"buffer_load_dwordx4 a[124:127], v192, s[92:95], s61 offen offset:1024 sc1\n"
"v_mfma_f32_16x16x32_bf16 v[36:39], a[196:199], a[60:63], v[36:39]\n"
"v_mfma_f32_16x16x32_bf16 v[40:43], a[196:199], a[64:67], v[40:43]\n"
"v_mfma_f32_16x16x32_bf16 v[44:47], a[196:199], a[68:71], v[44:47]\n"
"v_mfma_f32_16x16x32_bf16 v[48:51], a[200:203], a[48:51], v[48:51]\n"
"v_mfma_f32_16x16x32_bf16 v[52:55], a[200:203], a[52:55], v[52:55]\n"
"v_mfma_f32_16x16x32_bf16 v[56:59], a[200:203], a[56:59], v[56:59]\n"
"v_mfma_f32_16x16x32_bf16 v[60:63], a[200:203], a[60:63], v[60:63]\n"
"buffer_load_dwordx4 a[128:131], v192, s[92:95], s61 offen offset:2048 sc1\n"
"v_mfma_f32_16x16x32_bf16 v[64:67], a[200:203], a[64:67], v[64:67]\n"
"v_mfma_f32_16x16x32_bf16 v[68:71], a[200:203], a[68:71], v[68:71]\n"
"v_mfma_f32_16x16x32_bf16 v[72:75], a[204:207], a[48:51], v[72:75]\n"
"v_mfma_f32_16x16x32_bf16 v[76:79], a[204:207], a[52:55], v[76:79]\n"
"v_mfma_f32_16x16x32_bf16 v[80:83], a[204:207], a[56:59], v[80:83]\n"
"v_mfma_f32_16x16x32_bf16 v[84:87], a[204:207], a[60:63], v[84:87]\n"
"v_mfma_f32_16x16x32_bf16 v[88:91], a[204:207], a[64:67], v[88:91]\n"
"v_mfma_f32_16x16x32_bf16 v[92:95], a[204:207], a[68:71], v[92:95]\n"
"buffer_load_dwordx4 a[132:135], v192, s[92:95], s61 offen offset:3072 sc1\n"
"v_mfma_f32_16x16x32_bf16 v[96:99], a[208:211], a[48:51], v[96:99]\n"
"s_add_u32 s61, s91, 22528\n"
"v_mfma_f32_16x16x32_bf16 v[100:103], a[208:211], a[52:55], v[100:103]\n"
"v_mfma_f32_16x16x32_bf16 v[104:107], a[208:211], a[56:59], v[104:107]\n"
"v_mfma_f32_16x16x32_bf16 v[108:111], a[208:211], a[60:63], v[108:111]\n"
"v_mfma_f32_16x16x32_bf16 v[112:115], a[208:211], a[64:67], v[112:115]\n"
"v_mfma_f32_16x16x32_bf16 v[116:119], a[208:211], a[68:71], v[116:119]\n"
"v_mfma_f32_16x16x32_bf16 v[120:123], a[212:215], a[48:51], v[120:123]\n"
"buffer_load_dwordx4 a[136:139], v192, s[92:95], s61 offen sc1\n"
"v_mfma_f32_16x16x32_bf16 v[124:127], a[212:215], a[52:55], v[124:127]\n"
"v_mfma_f32_16x16x32_bf16 v[128:131], a[212:215], a[56:59], v[128:131]\n"
"v_mfma_f32_16x16x32_bf16 v[132:135], a[212:215], a[60:63], v[132:135]\n"
"v_mfma_f32_16x16x32_bf16 v[136:139], a[212:215], a[64:67], v[136:139]\n"
"v_mfma_f32_16x16x32_bf16 v[140:143], a[212:215], a[68:71], v[140:143]\n"
"v_mfma_f32_16x16x32_bf16 v[144:147], a[216:219], a[48:51], v[144:147]\n"
"v_mfma_f32_16x16x32_bf16 v[148:151], a[216:219], a[52:55], v[148:151]\n"
"v_mfma_f32_16x16x32_bf16 v[152:155], a[216:219], a[56:59], v[152:155]\n"
"buffer_load_dwordx4 a[140:143], v192, s[92:95], s61 offen offset:1024 sc1\n"
"v_mfma_f32_16x16x32_bf16 v[156:159], a[216:219], a[60:63], v[156:159]\n"
"v_mfma_f32_16x16x32_bf16 v[160:163], a[216:219], a[64:67], v[160:163]\n"
"v_mfma_f32_16x16x32_bf16 v[164:167], a[216:219], a[68:71], v[164:167]\n"
"v_mfma_f32_16x16x32_bf16 v[168:171], a[220:223], a[48:51], v[168:171]\n"
"v_mfma_f32_16x16x32_bf16 v[172:175], a[220:223], a[52:55], v[172:175]\n"
"v_mfma_f32_16x16x32_bf16 v[176:179], a[220:223], a[56:59], v[176:179]\n"
"v_mfma_f32_16x16x32_bf16 v[180:183], a[220:223], a[60:63], v[180:183]\n"
"v_mfma_f32_16x16x32_bf16 v[184:187], a[220:223], a[64:67], v[184:187]\n"
"v_mfma_f32_16x16x32_bf16 v[188:191], a[220:223], a[68:71], v[188:191]\n"
"s_waitcnt vmcnt(12) lgkmcnt(0)\n"
"s_barrier\n"
"ds_read_b64_tr_b16 a[228:229], v193\n"
"s_waitcnt vmcnt(0)\n"
"v_mfma_f32_16x16x32_bf16 v[0:3], a[224:227], a[72:75], v[0:3]\n"
"s_xor_b32 s56, s52, 0\n"
"ds_read_b64_tr_b16 a[230:231], v193 offset:4096\n"
"s_mul_i32 s57, s56, 24576\n"
"v_mfma_f32_16x16x32_bf16 v[4:7], a[224:227], a[76:79], v[4:7]\n"
"s_add_u32 s58, s57, 12288\n"
"ds_read_b64_tr_b16 a[232:233], v193 offset:49152\n"
"s_cmp_eq_u32 s56, 3\n"
"v_mfma_f32_16x16x32_bf16 v[8:11], a[224:227], a[80:83], v[8:11]\n"
"s_cselect_b32 s57, 114688, s57\n"
"ds_read_b64_tr_b16 a[234:235], v193 offset:53248\n"
"s_cselect_b32 s58, 147456, s58\n"
"v_mfma_f32_16x16x32_bf16 v[12:15], a[224:227], a[84:87], v[12:15]\n"
"s_xor_b32 s56, s52, 2\n"
"ds_read_b64_tr_b16 a[236:237], v194 offset:16384\n"
"v_add_u32 v208, s57, v192\n"
"v_mfma_f32_16x16x32_bf16 v[16:19], a[224:227], a[88:91], v[16:19]\n"
"s_mul_i32 s57, s56, 24576\n"
"ds_read_b64_tr_b16 a[238:239], v194 offset:20480\n"
"v_add_u32 v209, s58, v192\n"
"v_mfma_f32_16x16x32_bf16 v[20:23], a[224:227], a[92:95], v[20:23]\n"
"s_add_u32 s58, s57, 12288\n"
"ds_read_b64_tr_b16 a[240:241], v194\n"
"s_cmp_eq_u32 s56, 3\n"
"s_cselect_b32 s57, 114688, s57\n"
"s_cselect_b32 s58, 147456, s58\n"
"v_add_u32 v212, s57, v192\n"
"v_add_u32 v213, s58, v192\n"
"s_waitcnt lgkmcnt(5)\n"
"v_mfma_f32_16x16x32_bf16 v[0:3], a[228:231], a[96:99], v[0:3]\n"
"ds_read_b64_tr_b16 a[242:243], v194 offset:4096\n"
"v_mfma_f32_16x16x32_bf16 v[4:7], a[228:231], a[100:103], v[4:7]\n"
"ds_read_b64_tr_b16 a[244:245], v194 offset:49152\n"
"v_mfma_f32_16x16x32_bf16 v[8:11], a[228:231], a[104:107], v[8:11]\n"
"ds_read_b64_tr_b16 a[246:247], v194 offset:53248\n"
"v_mfma_f32_16x16x32_bf16 v[12:15], a[228:231], a[108:111], v[12:15]\n"
"ds_read_b64_tr_b16 a[248:249], v193 offset:16640\n"
"v_mfma_f32_16x16x32_bf16 v[16:19], a[228:231], a[112:115], v[16:19]\n"
"ds_read_b64_tr_b16 a[250:251], v193 offset:20736\n"
"v_mfma_f32_16x16x32_bf16 v[20:23], a[228:231], a[116:119], v[20:23]\n"
"s_waitcnt lgkmcnt(5)\n"
"ds_read_b64_tr_b16 a[252:253], v193 offset:256\n"
"v_mfma_f32_16x16x32_bf16 v[0:3], a[232:235], a[120:123], v[0:3]\n"
"ds_read_b64_tr_b16 a[254:255], v193 offset:4352\n"
"v_mfma_f32_16x16x32_bf16 v[4:7], a[232:235], a[124:127], v[4:7]\n"
"ds_read_b64_tr_b16 a[192:193], v193 offset:49408\n"
"v_mfma_f32_16x16x32_bf16 v[8:11], a[232:235], a[128:131], v[8:11]\n"
"ds_read_b64_tr_b16 a[194:195], v193 offset:53504\n"
"v_mfma_f32_16x16x32_bf16 v[12:15], a[232:235], a[132:135], v[12:15]\n"
"ds_read_b64_tr_b16 a[196:197], v194 offset:16640\n"
"v_mfma_f32_16x16x32_bf16 v[16:19], a[232:235], a[136:139], v[16:19]\n"
"ds_read_b64_tr_b16 a[198:199], v194 offset:20736\n"
"v_cvt_pk_bf16_f32 v224, v0, v1\n"
"v_mfma_f32_16x16x32_bf16 v[20:23], a[232:235], a[140:143], v[20:23]\n"
"ds_read_b64_tr_b16 a[200:201], v194 offset:256\n"
"v_cvt_pk_bf16_f32 v225, v2, v3\n"
"v_mfma_f32_16x16x32_bf16 v[24:27], a[236:239], a[72:75], v[24:27]\n"
"ds_read_b64_tr_b16 a[202:203], v194 offset:4352\n"
"v_cvt_pk_bf16_f32 v226, v4, v5\n"
"v_mfma_f32_16x16x32_bf16 v[28:31], a[236:239], a[76:79], v[28:31]\n"
"ds_read_b64_tr_b16 a[204:205], v194 offset:49408\n"
"v_cvt_pk_bf16_f32 v227, v6, v7\n"
"v_mfma_f32_16x16x32_bf16 v[32:35], a[236:239], a[80:83], v[32:35]\n"
"ds_read_b64_tr_b16 a[206:207], v194 offset:53504\n"
"v_cvt_pk_bf16_f32 v228, v8, v9\n"
"v_mfma_f32_16x16x32_bf16 v[36:39], a[236:239], a[84:87], v[36:39]\n"
"ds_read_b64_tr_b16 a[208:209], v193 offset:16896\n"
"v_cvt_pk_bf16_f32 v229, v10, v11\n"
"v_mfma_f32_16x16x32_bf16 v[40:43], a[236:239], a[88:91], v[40:43]\n"
"ds_read_b64_tr_b16 a[210:211], v193 offset:20992\n"
"v_cvt_pk_bf16_f32 v230, v12, v13\n"
"v_mfma_f32_16x16x32_bf16 v[44:47], a[236:239], a[92:95], v[44:47]\n"
"s_waitcnt lgkmcnt(8)\n"
"ds_read_b64_tr_b16 a[212:213], v193 offset:512\n"
"v_cvt_pk_bf16_f32 v231, v14, v15\n"
"v_mfma_f32_16x16x32_bf16 v[24:27], a[240:243], a[96:99], v[24:27]\n"
"ds_read_b64_tr_b16 a[214:215], v193 offset:4608\n"
"v_cvt_pk_bf16_f32 v232, v16, v17\n"
"v_mfma_f32_16x16x32_bf16 v[28:31], a[240:243], a[100:103], v[28:31]\n"
"ds_read_b64_tr_b16 a[216:217], v193 offset:49664\n"
"v_cvt_pk_bf16_f32 v233, v18, v19\n"
"v_mfma_f32_16x16x32_bf16 v[32:35], a[240:243], a[104:107], v[32:35]\n"
"ds_read_b64_tr_b16 a[218:219], v193 offset:53760\n"
"v_cvt_pk_bf16_f32 v234, v20, v21\n"
"v_mfma_f32_16x16x32_bf16 v[36:39], a[240:243], a[108:111], v[36:39]\n"
"v_cvt_pk_bf16_f32 v235, v22, v23\n"
"ds_read_b64_tr_b16 a[220:221], v194 offset:16896\n"
"v_mfma_f32_16x16x32_bf16 v[40:43], a[240:243], a[112:115], v[40:43]\n"
"ds_write_b64 v208, v[224:225]\n"
"v_mfma_f32_16x16x32_bf16 v[44:47], a[240:243], a[116:119], v[44:47]\n"
"ds_write_b64 v208, v[226:227] offset:8\n"
"v_mfma_f32_16x16x32_bf16 v[24:27], a[244:247], a[120:123], v[24:27]\n"
"ds_write_b64 v208, v[228:229] offset:1024\n"
"v_mfma_f32_16x16x32_bf16 v[28:31], a[244:247], a[124:127], v[28:31]\n"
"ds_write_b64 v208, v[230:231] offset:1032\n"
"v_mfma_f32_16x16x32_bf16 v[32:35], a[244:247], a[128:131], v[32:35]\n"
"ds_write_b64 v208, v[232:233] offset:2048\n"
"v_mfma_f32_16x16x32_bf16 v[36:39], a[244:247], a[132:135], v[36:39]\n"
"ds_write_b64 v208, v[234:235] offset:2056\n"
"v_mfma_f32_16x16x32_bf16 v[40:43], a[244:247], a[136:139], v[40:43]\n"
"v_cvt_pk_bf16_f32 v236, v24, v25\n"
"v_cvt_pk_bf16_f32 v237, v26, v27\n"
"v_mfma_f32_16x16x32_bf16 v[44:47], a[244:247], a[140:143], v[44:47]\n"
"v_cvt_pk_bf16_f32 v238, v28, v29\n"
"v_cvt_pk_bf16_f32 v239, v30, v31\n"
"v_mfma_f32_16x16x32_bf16 v[48:51], a[248:251], a[72:75], v[48:51]\n"
"v_cvt_pk_bf16_f32 v240, v32, v33\n"
"v_cvt_pk_bf16_f32 v241, v34, v35\n"
"v_mfma_f32_16x16x32_bf16 v[52:55], a[248:251], a[76:79], v[52:55]\n"
"v_cvt_pk_bf16_f32 v242, v36, v37\n"
"v_cvt_pk_bf16_f32 v243, v38, v39\n"
"v_mfma_f32_16x16x32_bf16 v[56:59], a[248:251], a[80:83], v[56:59]\n"
"v_cvt_pk_bf16_f32 v244, v40, v41\n"
"v_cvt_pk_bf16_f32 v245, v42, v43\n"
"v_mfma_f32_16x16x32_bf16 v[60:63], a[248:251], a[84:87], v[60:63]\n"
"v_cvt_pk_bf16_f32 v246, v44, v45\n"
"v_cvt_pk_bf16_f32 v247, v46, v47\n"
"v_mfma_f32_16x16x32_bf16 v[64:67], a[248:251], a[88:91], v[64:67]\n"
"ds_write_b64 v208, v[236:237] offset:3072\n"
"v_mfma_f32_16x16x32_bf16 v[68:71], a[248:251], a[92:95], v[68:71]\n"
"ds_write_b64 v208, v[238:239] offset:3080\n"
"v_mfma_f32_16x16x32_bf16 v[48:51], a[252:255], a[96:99], v[48:51]\n"
"ds_write_b64 v208, v[240:241] offset:4096\n"
"v_mfma_f32_16x16x32_bf16 v[52:55], a[252:255], a[100:103], v[52:55]\n"
"ds_write_b64 v208, v[242:243] offset:4104\n"
"v_mfma_f32_16x16x32_bf16 v[56:59], a[252:255], a[104:107], v[56:59]\n"
"ds_write_b64 v208, v[244:245] offset:5120\n"
"v_mfma_f32_16x16x32_bf16 v[60:63], a[252:255], a[108:111], v[60:63]\n"
"ds_write_b64 v208, v[246:247] offset:5128\n"
"v_mfma_f32_16x16x32_bf16 v[64:67], a[252:255], a[112:115], v[64:67]\n"
"ds_read_b64_tr_b16 a[222:223], v194 offset:20992\n"
"v_mfma_f32_16x16x32_bf16 v[68:71], a[252:255], a[116:119], v[68:71]\n"
"ds_read_b64_tr_b16 a[224:225], v194 offset:512\n"
"v_mfma_f32_16x16x32_bf16 v[48:51], a[192:195], a[120:123], v[48:51]\n"
"ds_read_b64_tr_b16 a[226:227], v194 offset:4608\n"
"v_mfma_f32_16x16x32_bf16 v[52:55], a[192:195], a[124:127], v[52:55]\n"
"ds_read_b64_tr_b16 a[228:229], v194 offset:49664\n"
"v_mfma_f32_16x16x32_bf16 v[56:59], a[192:195], a[128:131], v[56:59]\n"
"ds_read_b64_tr_b16 a[230:231], v194 offset:53760\n"
"v_mfma_f32_16x16x32_bf16 v[60:63], a[192:195], a[132:135], v[60:63]\n"
"ds_read_b64_tr_b16 a[232:233], v193 offset:17152\n"
"v_mfma_f32_16x16x32_bf16 v[64:67], a[192:195], a[136:139], v[64:67]\n"
"v_cvt_pk_bf16_f32 v224, v48, v49\n"
"v_cvt_pk_bf16_f32 v225, v50, v51\n"
"v_mfma_f32_16x16x32_bf16 v[68:71], a[192:195], a[140:143], v[68:71]\n"
"s_waitcnt lgkmcnt(7)\n"
"v_cvt_pk_bf16_f32 v226, v52, v53\n"
"v_cvt_pk_bf16_f32 v227, v54, v55\n"
"v_mfma_f32_16x16x32_bf16 v[72:75], a[196:199], a[72:75], v[72:75]\n"
"v_cvt_pk_bf16_f32 v228, v56, v57\n"
"v_cvt_pk_bf16_f32 v229, v58, v59\n"
"v_mfma_f32_16x16x32_bf16 v[76:79], a[196:199], a[76:79], v[76:79]\n"
"v_cvt_pk_bf16_f32 v230, v60, v61\n"
"v_cvt_pk_bf16_f32 v231, v62, v63\n"
"v_mfma_f32_16x16x32_bf16 v[80:83], a[196:199], a[80:83], v[80:83]\n"
"v_cvt_pk_bf16_f32 v232, v64, v65\n"
"v_cvt_pk_bf16_f32 v233, v66, v67\n"
"v_mfma_f32_16x16x32_bf16 v[84:87], a[196:199], a[84:87], v[84:87]\n"
"v_cvt_pk_bf16_f32 v234, v68, v69\n"
"v_cvt_pk_bf16_f32 v235, v70, v71\n"
"v_mfma_f32_16x16x32_bf16 v[88:91], a[196:199], a[88:91], v[88:91]\n"
"ds_read_b64_tr_b16 a[234:235], v193 offset:21248\n"
"v_mfma_f32_16x16x32_bf16 v[92:95], a[196:199], a[92:95], v[92:95]\n"
"ds_read_b64_tr_b16 a[236:237], v193 offset:768\n"
"v_mfma_f32_16x16x32_bf16 v[72:75], a[200:203], a[96:99], v[72:75]\n"
"ds_read_b64_tr_b16 a[238:239], v193 offset:4864\n"
"v_mfma_f32_16x16x32_bf16 v[76:79], a[200:203], a[100:103], v[76:79]\n"
"ds_read_b64_tr_b16 a[240:241], v193 offset:49920\n"
"v_mfma_f32_16x16x32_bf16 v[80:83], a[200:203], a[104:107], v[80:83]\n"
"ds_read_b64_tr_b16 a[242:243], v193 offset:54016\n"
"v_mfma_f32_16x16x32_bf16 v[84:87], a[200:203], a[108:111], v[84:87]\n"
"ds_read_b64_tr_b16 a[244:245], v194 offset:17152\n"
"v_mfma_f32_16x16x32_bf16 v[88:91], a[200:203], a[112:115], v[88:91]\n"
"ds_read_b64_tr_b16 a[246:247], v194 offset:21248\n"
"v_mfma_f32_16x16x32_bf16 v[92:95], a[200:203], a[116:119], v[92:95]\n"
"ds_read_b64_tr_b16 a[248:249], v194 offset:768\n"
"v_mfma_f32_16x16x32_bf16 v[72:75], a[204:207], a[120:123], v[72:75]\n"
"ds_read_b64_tr_b16 a[250:251], v194 offset:4864\n"
"v_mfma_f32_16x16x32_bf16 v[76:79], a[204:207], a[124:127], v[76:79]\n"
"ds_read_b64_tr_b16 a[252:253], v194 offset:49920\n"
"v_mfma_f32_16x16x32_bf16 v[80:83], a[204:207], a[128:131], v[80:83]\n"
"ds_read_b64_tr_b16 a[254:255], v194 offset:54016\n"
"v_mfma_f32_16x16x32_bf16 v[84:87], a[204:207], a[132:135], v[84:87]\n"
"ds_write_b64 v208, v[224:225] offset:6144\n"
"v_mfma_f32_16x16x32_bf16 v[88:91], a[204:207], a[136:139], v[88:91]\n"
"v_cvt_pk_bf16_f32 v236, v72, v73\n"
"v_cvt_pk_bf16_f32 v237, v74, v75\n"
"v_mfma_f32_16x16x32_bf16 v[92:95], a[204:207], a[140:143], v[92:95]\n"
"v_cvt_pk_bf16_f32 v238, v76, v77\n"
"v_cvt_pk_bf16_f32 v239, v78, v79\n"
"v_mfma_f32_16x16x32_bf16 v[96:99], a[208:211], a[72:75], v[96:99]\n"
"v_cvt_pk_bf16_f32 v240, v80, v81\n"
"v_cvt_pk_bf16_f32 v241, v82, v83\n"
"v_mfma_f32_16x16x32_bf16 v[100:103], a[208:211], a[76:79], v[100:103]\n"
"v_cvt_pk_bf16_f32 v242, v84, v85\n"
"v_cvt_pk_bf16_f32 v243, v86, v87\n"
"v_mfma_f32_16x16x32_bf16 v[104:107], a[208:211], a[80:83], v[104:107]\n"
"v_cvt_pk_bf16_f32 v244, v88, v89\n"
"v_cvt_pk_bf16_f32 v245, v90, v91\n"
"v_mfma_f32_16x16x32_bf16 v[108:111], a[208:211], a[84:87], v[108:111]\n"
"v_cvt_pk_bf16_f32 v246, v92, v93\n"
"v_cvt_pk_bf16_f32 v247, v94, v95\n"
"v_mfma_f32_16x16x32_bf16 v[112:115], a[208:211], a[88:91], v[112:115]\n"
"ds_write_b64 v208, v[226:227] offset:6152\n"
"v_mfma_f32_16x16x32_bf16 v[116:119], a[208:211], a[92:95], v[116:119]\n"
"ds_write_b64 v208, v[228:229] offset:7168\n"
"v_mfma_f32_16x16x32_bf16 v[96:99], a[212:215], a[96:99], v[96:99]\n"
"ds_write_b64 v208, v[230:231] offset:7176\n"
"v_mfma_f32_16x16x32_bf16 v[100:103], a[212:215], a[100:103], v[100:103]\n"
"ds_write_b64 v208, v[232:233] offset:8192\n"
"v_mfma_f32_16x16x32_bf16 v[104:107], a[212:215], a[104:107], v[104:107]\n"
"ds_write_b64 v208, v[234:235] offset:8200\n"
"v_mfma_f32_16x16x32_bf16 v[108:111], a[212:215], a[108:111], v[108:111]\n"
"ds_write_b64 v208, v[236:237] offset:9216\n"
"v_mfma_f32_16x16x32_bf16 v[112:115], a[212:215], a[112:115], v[112:115]\n"
"ds_write_b64 v208, v[238:239] offset:9224\n"
"v_mfma_f32_16x16x32_bf16 v[116:119], a[212:215], a[116:119], v[116:119]\n"
"ds_write_b64 v208, v[240:241] offset:10240\n"
"v_mfma_f32_16x16x32_bf16 v[96:99], a[216:219], a[120:123], v[96:99]\n"
"ds_write_b64 v208, v[242:243] offset:10248\n"
"v_mfma_f32_16x16x32_bf16 v[100:103], a[216:219], a[124:127], v[100:103]\n"
"ds_write_b64 v208, v[244:245] offset:11264\n"
"v_mfma_f32_16x16x32_bf16 v[104:107], a[216:219], a[128:131], v[104:107]\n"
"ds_write_b64 v208, v[246:247] offset:11272\n"
"v_mfma_f32_16x16x32_bf16 v[108:111], a[216:219], a[132:135], v[108:111]\n"
"v_mfma_f32_16x16x32_bf16 v[112:115], a[216:219], a[136:139], v[112:115]\n"
"v_mfma_f32_16x16x32_bf16 v[116:119], a[216:219], a[140:143], v[116:119]\n"
"s_waitcnt lgkmcnt(0)\n"
"s_barrier\n"
"v_mfma_f32_16x16x32_bf16 v[120:123], a[220:223], a[72:75], v[120:123]\n"
"s_add_u32 s56, s45, 1\n"
"ds_read_b128 v[0:3], v212\n"
"s_and_b32 s56, s56, 3\n"
"v_mfma_f32_16x16x32_bf16 v[124:127], a[220:223], a[76:79], v[124:127]\n"
"s_mul_i32 s56, s56, 12288\n"
"s_add_u32 s47, s56, s40\n"
"ds_read_b128 v[8:11], v212 offset:1024\n"
"v_mfma_f32_16x16x32_bf16 v[128:131], a[220:223], a[80:83], v[128:131]\n"
"s_add_u32 s56, s45, 2\n"
"ds_read_b128 v[16:19], v212 offset:2048\n"
"s_and_b32 s56, s56, 3\n"
"v_mfma_f32_16x16x32_bf16 v[132:135], a[220:223], a[84:87], v[132:135]\n"
"s_mul_i32 s56, s56, 12288\n"
"ds_read_b128 v[24:27], v212 offset:3072\n"
"s_add_u32 s48, s56, s40\n"
"v_mfma_f32_16x16x32_bf16 v[136:139], a[220:223], a[88:91], v[136:139]\n"
"s_add_u32 s56, s45, 3\n"
"ds_read_b128 v[32:35], v212 offset:4096\n"
"s_and_b32 s56, s56, 3\n"
"v_mfma_f32_16x16x32_bf16 v[140:143], a[220:223], a[92:95], v[140:143]\n"
"s_mul_i32 s56, s56, 12288\n"
"ds_read_b128 v[40:43], v212 offset:5120\n"
"s_add_u32 s49, s56, s40\n"
"v_mfma_f32_16x16x32_bf16 v[120:123], a[224:227], a[96:99], v[120:123]\n"
"s_add_u32 s56, s45, 1\n"
"ds_read_b128 v[48:51], v212 offset:6144\n"
"s_and_b32 s56, s56, 3\n"
"v_mfma_f32_16x16x32_bf16 v[124:127], a[224:227], a[100:103], v[124:127]\n"
"s_mul_i32 s56, s56, 3072\n"
"ds_read_b128 v[56:59], v212 offset:7168\n"
"s_add_u32 s70, s56, s41\n"
"v_mfma_f32_16x16x32_bf16 v[128:131], a[224:227], a[104:107], v[128:131]\n"
"s_add_u32 s56, s45, 2\n"
"ds_read_b128 v[64:67], v212 offset:8192\n"
"s_and_b32 s56, s56, 3\n"
"v_mfma_f32_16x16x32_bf16 v[132:135], a[224:227], a[108:111], v[132:135]\n"
"s_mul_i32 s56, s56, 3072\n"
"s_waitcnt lgkmcnt(8)\n"
"v_lshlrev_b32 v4, 16, v0\n"
"v_mfma_f32_16x16x32_bf16 v[136:139], a[224:227], a[112:115], v[136:139]\n"
"s_add_u32 s71, s56, s41\n"
"v_and_b32 v5, 0xffff0000, v0\n"
"v_lshlrev_b32 v6, 16, v1\n"
"v_mfma_f32_16x16x32_bf16 v[140:143], a[224:227], a[116:119], v[140:143]\n"
"s_add_u32 s56, s45, 3\n"
"v_and_b32 v7, 0xffff0000, v1\n"
"v_add_f32 v96, v96, v4\n"
"v_mfma_f32_16x16x32_bf16 v[120:123], a[228:231], a[120:123], v[120:123]\n"
"s_and_b32 s56, s56, 3\n"
"v_add_f32 v97, v97, v5\n"
"v_add_f32 v98, v98, v6\n"
"v_mfma_f32_16x16x32_bf16 v[124:127], a[228:231], a[124:127], v[124:127]\n"
"s_mul_i32 s56, s56, 3072\n"
"v_add_f32 v99, v99, v7\n"
"v_lshlrev_b32 v4, 16, v2\n"
"v_mfma_f32_16x16x32_bf16 v[128:131], a[228:231], a[128:131], v[128:131]\n"
"s_add_u32 s72, s56, s41\n"
"v_and_b32 v5, 0xffff0000, v2\n"
"v_lshlrev_b32 v6, 16, v3\n"
"v_mfma_f32_16x16x32_bf16 v[132:135], a[228:231], a[132:135], v[132:135]\n"
"s_or_b32 s56, s45, s46\n"
"v_and_b32 v7, 0xffff0000, v3\n"
"v_add_f32 v100, v100, v4\n"
"v_mfma_f32_16x16x32_bf16 v[136:139], a[228:231], a[136:139], v[136:139]\n"
"s_cmp_eq_u32 s56, 0\n"
"v_add_f32 v101, v101, v5\n"
"v_add_f32 v102, v102, v6\n"
"v_mfma_f32_16x16x32_bf16 v[140:143], a[228:231], a[140:143], v[140:143]\n"
"s_cselect_b32 s76, s42, 0x7ff00000\n"
"v_add_f32 v103, v103, v7\n"
"v_cvt_pk_bf16_f32 v216, v96, v97\n"
"v_mfma_f32_16x16x32_bf16 v[144:147], a[232:235], a[72:75], v[144:147]\n"
"s_add_u32 s77, s96, 5\n"
"v_cvt_pk_bf16_f32 v217, v98, v99\n"
"v_cvt_pk_bf16_f32 v218, v100, v101\n"
"v_mfma_f32_16x16x32_bf16 v[148:151], a[232:235], a[76:79], v[148:151]\n"
"s_mov_b32 s73, 7\n"
"v_cvt_pk_bf16_f32 v219, v102, v103\n"
"ds_read_b128 v[72:75], v212 offset:9216\n"
"v_mfma_f32_16x16x32_bf16 v[152:155], a[232:235], a[80:83], v[152:155]\n"
"buffer_store_dwordx4 v[216:219], v192, s[92:95], s47 offen sc1\n"
"v_mfma_f32_16x16x32_bf16 v[156:159], a[232:235], a[84:87], v[156:159]\n"
"s_mov_b32 s74, 0\n"
"s_waitcnt lgkmcnt(1)\n"
"v_lshlrev_b32 v12, 16, v8\n"
"v_mfma_f32_16x16x32_bf16 v[160:163], a[232:235], a[88:91], v[160:163]\n"
"s_mov_b32 s69, 0\n"
"v_and_b32 v13, 0xffff0000, v8\n"
"v_lshlrev_b32 v14, 16, v9\n"
"v_mfma_f32_16x16x32_bf16 v[164:167], a[232:235], a[92:95], v[164:167]\n"
"s_mov_b32 s60, 32768\n"
"v_and_b32 v15, 0xffff0000, v9\n"
"v_add_f32 v104, v104, v12\n"
"v_mfma_f32_16x16x32_bf16 v[144:147], a[236:239], a[96:99], v[144:147]\n"
"v_add_f32 v105, v105, v13\n"
"v_add_f32 v106, v106, v14\n"
"v_mfma_f32_16x16x32_bf16 v[148:151], a[236:239], a[100:103], v[148:151]\n"
"v_add_f32 v107, v107, v15\n"
"v_lshlrev_b32 v12, 16, v10\n"
"v_mfma_f32_16x16x32_bf16 v[152:155], a[236:239], a[104:107], v[152:155]\n"
"v_and_b32 v13, 0xffff0000, v10\n"
"v_lshlrev_b32 v14, 16, v11\n"
"v_mfma_f32_16x16x32_bf16 v[156:159], a[236:239], a[108:111], v[156:159]\n"
"v_and_b32 v15, 0xffff0000, v11\n"
"v_add_f32 v108, v108, v12\n"
"v_mfma_f32_16x16x32_bf16 v[160:163], a[236:239], a[112:115], v[160:163]\n"
"v_add_f32 v109, v109, v13\n"
"v_add_f32 v110, v110, v14\n"
"v_mfma_f32_16x16x32_bf16 v[164:167], a[236:239], a[116:119], v[164:167]\n"
"v_add_f32 v111, v111, v15\n"
"v_cvt_pk_bf16_f32 v220, v104, v105\n"
"v_mfma_f32_16x16x32_bf16 v[144:147], a[240:243], a[120:123], v[144:147]\n"
"v_cvt_pk_bf16_f32 v221, v106, v107\n"
"v_cvt_pk_bf16_f32 v222, v108, v109\n"
"v_mfma_f32_16x16x32_bf16 v[148:151], a[240:243], a[124:127], v[148:151]\n"
"v_cvt_pk_bf16_f32 v223, v110, v111\n"
"v_lshlrev_b32 v20, 16, v16\n"
"v_mfma_f32_16x16x32_bf16 v[152:155], a[240:243], a[128:131], v[152:155]\n"
"buffer_store_dwordx4 v[220:223], v192, s[92:95], s47 offen offset:1024 sc1\n"
"v_mfma_f32_16x16x32_bf16 v[156:159], a[240:243], a[132:135], v[156:159]\n"
"v_and_b32 v21, 0xffff0000, v16\n"
"v_lshlrev_b32 v22, 16, v17\n"
"v_mfma_f32_16x16x32_bf16 v[160:163], a[240:243], a[136:139], v[160:163]\n"
"v_and_b32 v23, 0xffff0000, v17\n"
"v_add_f32 v112, v112, v20\n"
"v_mfma_f32_16x16x32_bf16 v[164:167], a[240:243], a[140:143], v[164:167]\n"
"v_add_f32 v113, v113, v21\n"
"v_add_f32 v114, v114, v22\n"
"v_mfma_f32_16x16x32_bf16 v[168:171], a[244:247], a[72:75], v[168:171]\n"
"v_add_f32 v115, v115, v23\n"
"v_lshlrev_b32 v20, 16, v18\n"
"v_mfma_f32_16x16x32_bf16 v[172:175], a[244:247], a[76:79], v[172:175]\n"
"v_and_b32 v21, 0xffff0000, v18\n"
"v_lshlrev_b32 v22, 16, v19\n"
"v_mfma_f32_16x16x32_bf16 v[176:179], a[244:247], a[80:83], v[176:179]\n"
"v_and_b32 v23, 0xffff0000, v19\n"
"v_add_f32 v116, v116, v20\n"
"v_mfma_f32_16x16x32_bf16 v[180:183], a[244:247], a[84:87], v[180:183]\n"
"v_add_f32 v117, v117, v21\n"
"v_add_f32 v118, v118, v22\n"
"v_mfma_f32_16x16x32_bf16 v[184:187], a[244:247], a[88:91], v[184:187]\n"
"v_add_f32 v119, v119, v23\n"
"v_cvt_pk_bf16_f32 v224, v112, v113\n"
"v_mfma_f32_16x16x32_bf16 v[188:191], a[244:247], a[92:95], v[188:191]\n"
"v_cvt_pk_bf16_f32 v225, v114, v115\n"
"v_cvt_pk_bf16_f32 v226, v116, v117\n"
"v_mfma_f32_16x16x32_bf16 v[168:171], a[248:251], a[96:99], v[168:171]\n"
"v_cvt_pk_bf16_f32 v227, v118, v119\n"
"v_lshlrev_b32 v28, 16, v24\n"
"v_mfma_f32_16x16x32_bf16 v[172:175], a[248:251], a[100:103], v[172:175]\n"
"buffer_store_dwordx4 v[224:227], v192, s[92:95], s47 offen offset:2048 sc1\n"
"v_mfma_f32_16x16x32_bf16 v[176:179], a[248:251], a[104:107], v[176:179]\n"
"v_and_b32 v29, 0xffff0000, v24\n"
"v_lshlrev_b32 v30, 16, v25\n"
"v_mfma_f32_16x16x32_bf16 v[180:183], a[248:251], a[108:111], v[180:183]\n"
"v_and_b32 v31, 0xffff0000, v25\n"
"v_add_f32 v120, v120, v28\n"
"v_mfma_f32_16x16x32_bf16 v[184:187], a[248:251], a[112:115], v[184:187]\n"
"v_add_f32 v121, v121, v29\n"
"v_add_f32 v122, v122, v30\n"
"v_mfma_f32_16x16x32_bf16 v[188:191], a[248:251], a[116:119], v[188:191]\n"
"v_add_f32 v123, v123, v31\n"
"v_lshlrev_b32 v28, 16, v26\n"
"v_mfma_f32_16x16x32_bf16 v[168:171], a[252:255], a[120:123], v[168:171]\n"
"v_and_b32 v29, 0xffff0000, v26\n"
"v_lshlrev_b32 v30, 16, v27\n"
"v_mfma_f32_16x16x32_bf16 v[172:175], a[252:255], a[124:127], v[172:175]\n"
"v_and_b32 v31, 0xffff0000, v27\n"
"v_add_f32 v124, v124, v28\n"
"v_mfma_f32_16x16x32_bf16 v[176:179], a[252:255], a[128:131], v[176:179]\n"
"v_add_f32 v125, v125, v29\n"
"v_add_f32 v126, v126, v30\n"
"v_mfma_f32_16x16x32_bf16 v[180:183], a[252:255], a[132:135], v[180:183]\n"
"v_add_f32 v127, v127, v31\n"
"v_cvt_pk_bf16_f32 v228, v120, v121\n"
"v_mfma_f32_16x16x32_bf16 v[184:187], a[252:255], a[136:139], v[184:187]\n"
"v_cvt_pk_bf16_f32 v229, v122, v123\n"
"v_cvt_pk_bf16_f32 v230, v124, v125\n"
"v_mfma_f32_16x16x32_bf16 v[188:191], a[252:255], a[140:143], v[188:191]\n"
"v_cvt_pk_bf16_f32 v231, v126, v127\n"
"buffer_store_dwordx4 v[228:231], v192, s[92:95], s48 offen sc1\n"
"v_lshlrev_b32 v36, 16, v32\n"
"v_and_b32 v37, 0xffff0000, v32\n"
"v_lshlrev_b32 v38, 16, v33\n"
"v_and_b32 v39, 0xffff0000, v33\n"
"v_add_f32 v128, v128, v36\n"
"v_add_f32 v129, v129, v37\n"
"v_add_f32 v130, v130, v38\n"
"v_add_f32 v131, v131, v39\n"
"v_lshlrev_b32 v36, 16, v34\n"
"v_and_b32 v37, 0xffff0000, v34\n"
"v_lshlrev_b32 v38, 16, v35\n"
"v_and_b32 v39, 0xffff0000, v35\n"
"v_add_f32 v132, v132, v36\n"
"v_add_f32 v133, v133, v37\n"
"v_add_f32 v134, v134, v38\n"
"v_add_f32 v135, v135, v39\n"
"v_cvt_pk_bf16_f32 v232, v128, v129\n"
"v_cvt_pk_bf16_f32 v233, v130, v131\n"
"v_cvt_pk_bf16_f32 v234, v132, v133\n"
"v_cvt_pk_bf16_f32 v235, v134, v135\n"
"buffer_store_dwordx4 v[232:235], v192, s[92:95], s48 offen offset:1024 sc1\n"
"v_lshlrev_b32 v44, 16, v40\n"
"v_and_b32 v45, 0xffff0000, v40\n"
"v_lshlrev_b32 v46, 16, v41\n"
"v_and_b32 v47, 0xffff0000, v41\n"
"v_add_f32 v136, v136, v44\n"
"v_add_f32 v137, v137, v45\n"
"v_add_f32 v138, v138, v46\n"
"v_add_f32 v139, v139, v47\n"
"v_lshlrev_b32 v44, 16, v42\n"
"v_and_b32 v45, 0xffff0000, v42\n"
"v_lshlrev_b32 v46, 16, v43\n"
"v_and_b32 v47, 0xffff0000, v43\n"
"v_add_f32 v140, v140, v44\n"
"v_add_f32 v141, v141, v45\n"
"v_add_f32 v142, v142, v46\n"
"v_add_f32 v143, v143, v47\n"
"v_cvt_pk_bf16_f32 v236, v136, v137\n"
"v_cvt_pk_bf16_f32 v237, v138, v139\n"
"v_cvt_pk_bf16_f32 v238, v140, v141\n"
"v_cvt_pk_bf16_f32 v239, v142, v143\n"
"buffer_store_dwordx4 v[236:239], v192, s[92:95], s48 offen offset:2048 sc1\n"
"v_lshlrev_b32 v52, 16, v48\n"
"v_and_b32 v53, 0xffff0000, v48\n"
"v_lshlrev_b32 v54, 16, v49\n"
"v_and_b32 v55, 0xffff0000, v49\n"
"v_add_f32 v144, v144, v52\n"
"v_add_f32 v145, v145, v53\n"
"v_add_f32 v146, v146, v54\n"
"v_add_f32 v147, v147, v55\n"
"v_lshlrev_b32 v52, 16, v50\n"
"v_and_b32 v53, 0xffff0000, v50\n"
"v_lshlrev_b32 v54, 16, v51\n"
"v_and_b32 v55, 0xffff0000, v51\n"
"v_add_f32 v148, v148, v52\n"
"v_add_f32 v149, v149, v53\n"
"v_add_f32 v150, v150, v54\n"
"v_add_f32 v151, v151, v55\n"
"v_cvt_pk_bf16_f32 v240, v144, v145\n"
"v_cvt_pk_bf16_f32 v241, v146, v147\n"
"v_cvt_pk_bf16_f32 v242, v148, v149\n"
"v_cvt_pk_bf16_f32 v243, v150, v151\n"
"buffer_store_dwordx4 v[240:243], v192, s[92:95], s49 offen sc1\n"
"v_lshlrev_b32 v60, 16, v56\n"
"v_and_b32 v61, 0xffff0000, v56\n"
"v_lshlrev_b32 v62, 16, v57\n"
"v_and_b32 v63, 0xffff0000, v57\n"
"v_add_f32 v152, v152, v60\n"
"v_add_f32 v153, v153, v61\n"
"v_add_f32 v154, v154, v62\n"
"v_add_f32 v155, v155, v63\n"
"v_lshlrev_b32 v60, 16, v58\n"
"v_and_b32 v61, 0xffff0000, v58\n"
"v_lshlrev_b32 v62, 16, v59\n"
"v_and_b32 v63, 0xffff0000, v59\n"
"v_add_f32 v156, v156, v60\n"
"v_add_f32 v157, v157, v61\n"
"v_add_f32 v158, v158, v62\n"
"v_add_f32 v159, v159, v63\n"
"v_cvt_pk_bf16_f32 v244, v152, v153\n"
"v_cvt_pk_bf16_f32 v245, v154, v155\n"
"v_cvt_pk_bf16_f32 v246, v156, v157\n"
"v_cvt_pk_bf16_f32 v247, v158, v159\n"
"buffer_store_dwordx4 v[244:247], v192, s[92:95], s49 offen offset:1024 sc1\n"
"v_lshlrev_b32 v68, 16, v64\n"
"v_and_b32 v69, 0xffff0000, v64\n"
"v_lshlrev_b32 v70, 16, v65\n"
"v_and_b32 v71, 0xffff0000, v65\n"
"v_add_f32 v160, v160, v68\n"
"v_add_f32 v161, v161, v69\n"
"v_add_f32 v162, v162, v70\n"
"v_add_f32 v163, v163, v71\n"
"v_lshlrev_b32 v68, 16, v66\n"
"v_and_b32 v69, 0xffff0000, v66\n"
"v_lshlrev_b32 v70, 16, v67\n"
"v_and_b32 v71, 0xffff0000, v67\n"
"v_add_f32 v164, v164, v68\n"
"v_add_f32 v165, v165, v69\n"
"v_add_f32 v166, v166, v70\n"
"v_add_f32 v167, v167, v71\n"
"v_cvt_pk_bf16_f32 v248, v160, v161\n"
"v_cvt_pk_bf16_f32 v249, v162, v163\n"
"v_cvt_pk_bf16_f32 v250, v164, v165\n"
"v_cvt_pk_bf16_f32 v251, v166, v167\n"
"buffer_store_dwordx4 v[248:251], v192, s[92:95], s49 offen offset:2048 sc1\n"
"ds_read_b128 v[80:83], v212 offset:10240\n"
"ds_read_b128 v[88:91], v212 offset:11264\n"
"v_bfe_u32 v209, v192, 8, 1\n"
"s_waitcnt lgkmcnt(2)\n"
"v_lshlrev_b32 v76, 16, v72\n"
"v_and_b32 v77, 0xffff0000, v72\n"
"v_lshlrev_b32 v78, 16, v73\n"
"v_and_b32 v79, 0xffff0000, v73\n"
"v_lshlrev_b32 v209, 14, v209\n"
"v_and_b32 v208, 0xf0, v192\n"
"v_add_f32 v168, v168, v76\n"
"v_add_f32 v169, v169, v77\n"
"v_add_f32 v170, v170, v78\n"
"v_add_f32 v171, v171, v79\n"
"v_lshl_add_u32 v208, v208, 6, v209\n"
"v_lshlrev_b32 v76, 16, v74\n"
"v_and_b32 v77, 0xffff0000, v74\n"
"v_lshlrev_b32 v78, 16, v75\n"
"v_and_b32 v79, 0xffff0000, v75\n"
"v_lshrrev_b32 v209, 9, v192\n"
"v_add_f32 v172, v172, v76\n"
"v_add_f32 v173, v173, v77\n"
"v_add_f32 v174, v174, v78\n"
"v_add_f32 v175, v175, v79\n"
"v_lshl_add_u32 v208, v209, 4, v208\n"
"v_mov_b32 v44, s77\n"
"v_mov_b32 v45, s97\n"
"buffer_load_dwordx2 v[198:199], v210, s[92:95], s99 offen sc1\n"
"s_waitcnt lgkmcnt(1)\n"
"v_lshlrev_b32 v84, 16, v80\n"
"v_and_b32 v85, 0xffff0000, v80\n"
"v_lshlrev_b32 v86, 16, v81\n"
"v_and_b32 v87, 0xffff0000, v81\n"
"s_waitcnt lgkmcnt(0)\n"
"v_lshlrev_b32 v92, 16, v88\n"
"v_and_b32 v93, 0xffff0000, v88\n"
"v_lshlrev_b32 v94, 16, v89\n"
"v_and_b32 v95, 0xffff0000, v89\n"
"v_add_f32 v176, v176, v84\n"
"v_add_f32 v177, v177, v85\n"
"v_add_f32 v178, v178, v86\n"
"v_add_f32 v179, v179, v87\n"
"v_add_f32 v184, v184, v92\n"
"v_add_f32 v185, v185, v93\n"
"v_add_f32 v186, v186, v94\n"
"v_add_f32 v187, v187, v95\n"
"v_lshlrev_b32 v84, 16, v82\n"
"v_and_b32 v85, 0xffff0000, v82\n"
"v_lshlrev_b32 v86, 16, v83\n"
"v_and_b32 v87, 0xffff0000, v83\n"
"v_lshlrev_b32 v92, 16, v90\n"
"v_and_b32 v93, 0xffff0000, v90\n"
"v_lshlrev_b32 v94, 16, v91\n"
"v_and_b32 v95, 0xffff0000, v91\n"
"v_add_f32 v180, v180, v84\n"
"v_add_f32 v181, v181, v85\n"
"v_add_f32 v182, v182, v86\n"
"v_add_f32 v183, v183, v87\n"
"v_add_f32 v188, v188, v92\n"
"v_add_f32 v189, v189, v93\n"
"v_add_f32 v190, v190, v94\n"
"v_add_f32 v191, v191, v95\n"
"s_waitcnt lgkmcnt(0)\n"
"s_waitcnt vmcnt(1)\n"
"buffer_store_dwordx2 v[44:45], v197, s[92:95], s98 offen sc1\n"
"s_waitcnt vmcnt(1)\n"
".Lf_chk:\n"
"v_subrev_u32 v198, s77, v198\n"
"v_cmp_eq_u32 vcc, 0, v198\n"
"v_cmp_eq_u32 s[82:83], s97, v199\n"
"s_nop 1\n"
"s_and_b32 s75, vcc_lo, s82\n"
"s_and_b32 s75, s75, s73\n"
"s_bitcmp1_b32 s75, 0\n"
"s_cbranch_scc0 .Lf_l0\n"
"buffer_load_dwordx4 v[0:3], v192, s[92:95], s70 offen sc1\n"
"buffer_load_dwordx4 v[4:7], v192, s[92:95], s70 offen offset:1024 sc1\n"
"buffer_load_dwordx4 v[8:11], v192, s[92:95], s70 offen offset:2048 sc1\n"
".Lf_l0:\n"
"s_bitcmp1_b32 s75, 1\n"
"s_cbranch_scc0 .Lf_l1\n"
"buffer_load_dwordx4 v[12:15], v192, s[92:95], s71 offen sc1\n"
"buffer_load_dwordx4 v[16:19], v192, s[92:95], s71 offen offset:1024 sc1\n"
"buffer_load_dwordx4 v[20:23], v192, s[92:95], s71 offen offset:2048 sc1\n"
".Lf_l1:\n"
"s_bitcmp1_b32 s75, 2\n"
"s_cbranch_scc0 .Lf_l2\n"
"buffer_load_dwordx4 v[24:27], v192, s[92:95], s72 offen sc1\n"
"buffer_load_dwordx4 v[28:31], v192, s[92:95], s72 offen offset:1024 sc1\n"
"buffer_load_dwordx4 v[32:35], v192, s[92:95], s72 offen offset:2048 sc1\n"
".Lf_l2:\n"
"s_cmp_eq_u32 s69, 0\n"
"s_cbranch_scc0 .Lf_a0\n"
"s_bitcmp1_b32 s74, 0\n"
"s_cbranch_scc0 .Lf_a0\n"
"s_mov_b32 s69, 1\n"
"v_lshlrev_b32 v42, 16, v0\n"
"v_and_b32 v43, 0xffff0000, v0\n"
"v_add_f32 v168, v168, v42\n"
"v_add_f32 v169, v169, v43\n"
"v_lshlrev_b32 v42, 16, v1\n"
"v_and_b32 v43, 0xffff0000, v1\n"
"v_add_f32 v170, v170, v42\n"
"v_add_f32 v171, v171, v43\n"
"v_lshlrev_b32 v42, 16, v2\n"
"v_and_b32 v43, 0xffff0000, v2\n"
"v_add_f32 v172, v172, v42\n"
"v_add_f32 v173, v173, v43\n"
"v_lshlrev_b32 v42, 16, v3\n"
"v_and_b32 v43, 0xffff0000, v3\n"
"v_add_f32 v174, v174, v42\n"
"v_add_f32 v175, v175, v43\n"
"v_lshlrev_b32 v42, 16, v4\n"
"v_and_b32 v43, 0xffff0000, v4\n"
"v_add_f32 v176, v176, v42\n"
"v_add_f32 v177, v177, v43\n"
"v_lshlrev_b32 v42, 16, v5\n"
"v_and_b32 v43, 0xffff0000, v5\n"
"v_add_f32 v178, v178, v42\n"
"v_add_f32 v179, v179, v43\n"
"v_lshlrev_b32 v42, 16, v6\n"
"v_and_b32 v43, 0xffff0000, v6\n"
"v_add_f32 v180, v180, v42\n"
"v_add_f32 v181, v181, v43\n"
"v_lshlrev_b32 v42, 16, v7\n"
"v_and_b32 v43, 0xffff0000, v7\n"
"v_add_f32 v182, v182, v42\n"
"v_add_f32 v183, v183, v43\n"
"v_lshlrev_b32 v42, 16, v8\n"
"v_and_b32 v43, 0xffff0000, v8\n"
"v_add_f32 v184, v184, v42\n"
"v_add_f32 v185, v185, v43\n"
"v_lshlrev_b32 v42, 16, v9\n"
"v_and_b32 v43, 0xffff0000, v9\n"
"v_add_f32 v186, v186, v42\n"
"v_add_f32 v187, v187, v43\n"
"v_lshlrev_b32 v42, 16, v10\n"
"v_and_b32 v43, 0xffff0000, v10\n"
"v_add_f32 v188, v188, v42\n"
"v_add_f32 v189, v189, v43\n"
"v_lshlrev_b32 v42, 16, v11\n"
"v_and_b32 v43, 0xffff0000, v11\n"
"v_add_f32 v190, v190, v42\n"
"v_add_f32 v191, v191, v43\n"
".Lf_a0:\n"
"s_cmp_eq_u32 s69, 1\n"
"s_cbranch_scc0 .Lf_a1\n"
"s_bitcmp1_b32 s74, 1\n"
"s_cbranch_scc0 .Lf_a1\n"
"s_mov_b32 s69, 2\n"
"v_lshlrev_b32 v42, 16, v12\n"
"v_and_b32 v43, 0xffff0000, v12\n"
"v_add_f32 v168, v168, v42\n"
"v_add_f32 v169, v169, v43\n"
"v_lshlrev_b32 v42, 16, v13\n"
"v_and_b32 v43, 0xffff0000, v13\n"
"v_add_f32 v170, v170, v42\n"
"v_add_f32 v171, v171, v43\n"
"v_lshlrev_b32 v42, 16, v14\n"
"v_and_b32 v43, 0xffff0000, v14\n"
"v_add_f32 v172, v172, v42\n"
"v_add_f32 v173, v173, v43\n"
"v_lshlrev_b32 v42, 16, v15\n"
"v_and_b32 v43, 0xffff0000, v15\n"
"v_add_f32 v174, v174, v42\n"
"v_add_f32 v175, v175, v43\n"
"v_lshlrev_b32 v42, 16, v16\n"
"v_and_b32 v43, 0xffff0000, v16\n"
"v_add_f32 v176, v176, v42\n"
"v_add_f32 v177, v177, v43\n"
"v_lshlrev_b32 v42, 16, v17\n"
"v_and_b32 v43, 0xffff0000, v17\n"
"v_add_f32 v178, v178, v42\n"
"v_add_f32 v179, v179, v43\n"
"v_lshlrev_b32 v42, 16, v18\n"
"v_and_b32 v43, 0xffff0000, v18\n"
"v_add_f32 v180, v180, v42\n"
"v_add_f32 v181, v181, v43\n"
"v_lshlrev_b32 v42, 16, v19\n"
"v_and_b32 v43, 0xffff0000, v19\n"
"v_add_f32 v182, v182, v42\n"
"v_add_f32 v183, v183, v43\n"
"v_lshlrev_b32 v42, 16, v20\n"
"v_and_b32 v43, 0xffff0000, v20\n"
"v_add_f32 v184, v184, v42\n"
"v_add_f32 v185, v185, v43\n"
"v_lshlrev_b32 v42, 16, v21\n"
"v_and_b32 v43, 0xffff0000, v21\n"
"v_add_f32 v186, v186, v42\n"
"v_add_f32 v187, v187, v43\n"
"v_lshlrev_b32 v42, 16, v22\n"
"v_and_b32 v43, 0xffff0000, v22\n"
"v_add_f32 v188, v188, v42\n"
"v_add_f32 v189, v189, v43\n"
"v_lshlrev_b32 v42, 16, v23\n"
"v_and_b32 v43, 0xffff0000, v23\n"
"v_add_f32 v190, v190, v42\n"
"v_add_f32 v191, v191, v43\n"
".Lf_a1:\n"
"s_cmp_eq_u32 s69, 2\n"
"s_cbranch_scc0 .Lf_a2\n"
"s_bitcmp1_b32 s74, 2\n"
"s_cbranch_scc0 .Lf_a2\n"
"s_mov_b32 s69, 3\n"
"v_lshlrev_b32 v42, 16, v24\n"
"v_and_b32 v43, 0xffff0000, v24\n"
"v_add_f32 v168, v168, v42\n"
"v_add_f32 v169, v169, v43\n"
"v_lshlrev_b32 v42, 16, v25\n"
"v_and_b32 v43, 0xffff0000, v25\n"
"v_add_f32 v170, v170, v42\n"
"v_add_f32 v171, v171, v43\n"
"v_lshlrev_b32 v42, 16, v26\n"
"v_and_b32 v43, 0xffff0000, v26\n"
"v_add_f32 v172, v172, v42\n"
"v_add_f32 v173, v173, v43\n"
"v_lshlrev_b32 v42, 16, v27\n"
"v_and_b32 v43, 0xffff0000, v27\n"
"v_add_f32 v174, v174, v42\n"
"v_add_f32 v175, v175, v43\n"
"v_lshlrev_b32 v42, 16, v28\n"
"v_and_b32 v43, 0xffff0000, v28\n"
"v_add_f32 v176, v176, v42\n"
"v_add_f32 v177, v177, v43\n"
"v_lshlrev_b32 v42, 16, v29\n"
"v_and_b32 v43, 0xffff0000, v29\n"
"v_add_f32 v178, v178, v42\n"
"v_add_f32 v179, v179, v43\n"
"v_lshlrev_b32 v42, 16, v30\n"
"v_and_b32 v43, 0xffff0000, v30\n"
"v_add_f32 v180, v180, v42\n"
"v_add_f32 v181, v181, v43\n"
"v_lshlrev_b32 v42, 16, v31\n"
"v_and_b32 v43, 0xffff0000, v31\n"
"v_add_f32 v182, v182, v42\n"
"v_add_f32 v183, v183, v43\n"
"v_lshlrev_b32 v42, 16, v32\n"
"v_and_b32 v43, 0xffff0000, v32\n"
"v_add_f32 v184, v184, v42\n"
"v_add_f32 v185, v185, v43\n"
"v_lshlrev_b32 v42, 16, v33\n"
"v_and_b32 v43, 0xffff0000, v33\n"
"v_add_f32 v186, v186, v42\n"
"v_add_f32 v187, v187, v43\n"
"v_lshlrev_b32 v42, 16, v34\n"
"v_and_b32 v43, 0xffff0000, v34\n"
"v_add_f32 v188, v188, v42\n"
"v_add_f32 v189, v189, v43\n"
"v_lshlrev_b32 v42, 16, v35\n"
"v_and_b32 v43, 0xffff0000, v35\n"
"v_add_f32 v190, v190, v42\n"
"v_add_f32 v191, v191, v43\n"
".Lf_a2:\n"
"s_or_b32 s74, s74, s75\n"
"s_andn2_b32 s73, s73, s75\n"
"s_cbranch_scc0 .Lf_fin\n"
"s_sub_u32 s60, s60, 1\n"
"s_cbranch_scc1 .Lf_fin\n"
"s_sleep 1\n"
"buffer_load_dwordx2 v[198:199], v210, s[92:95], s99 offen sc1\n"
"s_waitcnt vmcnt(0)\n"
"s_branch .Lf_chk\n"
".Lf_fin:\n"
"s_add_u32 s56, s62, 1\n"
"v_mov_b32 v198, s56\n"
"s_waitcnt vmcnt(0)\n"
"buffer_store_dword v198, v197, s[92:95], s76 offen sc1\n"
"s_cmp_eq_u32 s69, 0\n"
"s_cbranch_scc0 .Lf_b0\n"
"s_bitcmp1_b32 s74, 0\n"
"s_cbranch_scc0 .Lf_b0\n"
"s_mov_b32 s69, 1\n"
"v_lshlrev_b32 v42, 16, v0\n"
"v_and_b32 v43, 0xffff0000, v0\n"
"v_add_f32 v168, v168, v42\n"
"v_add_f32 v169, v169, v43\n"
"v_lshlrev_b32 v42, 16, v1\n"
"v_and_b32 v43, 0xffff0000, v1\n"
"v_add_f32 v170, v170, v42\n"
"v_add_f32 v171, v171, v43\n"
"v_lshlrev_b32 v42, 16, v2\n"
"v_and_b32 v43, 0xffff0000, v2\n"
"v_add_f32 v172, v172, v42\n"
"v_add_f32 v173, v173, v43\n"
"v_lshlrev_b32 v42, 16, v3\n"
"v_and_b32 v43, 0xffff0000, v3\n"
"v_add_f32 v174, v174, v42\n"
"v_add_f32 v175, v175, v43\n"
"v_lshlrev_b32 v42, 16, v4\n"
"v_and_b32 v43, 0xffff0000, v4\n"
"v_add_f32 v176, v176, v42\n"
"v_add_f32 v177, v177, v43\n"
"v_lshlrev_b32 v42, 16, v5\n"
"v_and_b32 v43, 0xffff0000, v5\n"
"v_add_f32 v178, v178, v42\n"
"v_add_f32 v179, v179, v43\n"
"v_lshlrev_b32 v42, 16, v6\n"
"v_and_b32 v43, 0xffff0000, v6\n"
"v_add_f32 v180, v180, v42\n"
"v_add_f32 v181, v181, v43\n"
"v_lshlrev_b32 v42, 16, v7\n"
"v_and_b32 v43, 0xffff0000, v7\n"
"v_add_f32 v182, v182, v42\n"
"v_add_f32 v183, v183, v43\n"
"v_lshlrev_b32 v42, 16, v8\n"
"v_and_b32 v43, 0xffff0000, v8\n"
"v_add_f32 v184, v184, v42\n"
"v_add_f32 v185, v185, v43\n"
"v_lshlrev_b32 v42, 16, v9\n"
"v_and_b32 v43, 0xffff0000, v9\n"
"v_add_f32 v186, v186, v42\n"
"v_add_f32 v187, v187, v43\n"
"v_lshlrev_b32 v42, 16, v10\n"
"v_and_b32 v43, 0xffff0000, v10\n"
"v_add_f32 v188, v188, v42\n"
"v_add_f32 v189, v189, v43\n"
"v_lshlrev_b32 v42, 16, v11\n"
"v_and_b32 v43, 0xffff0000, v11\n"
"v_add_f32 v190, v190, v42\n"
"v_add_f32 v191, v191, v43\n"
".Lf_b0:\n"
"s_cmp_eq_u32 s69, 1\n"
"s_cbranch_scc0 .Lf_b1\n"
"s_bitcmp1_b32 s74, 1\n"
"s_cbranch_scc0 .Lf_b1\n"
"s_mov_b32 s69, 2\n"
"v_lshlrev_b32 v42, 16, v12\n"
"v_and_b32 v43, 0xffff0000, v12\n"
"v_add_f32 v168, v168, v42\n"
"v_add_f32 v169, v169, v43\n"
"v_lshlrev_b32 v42, 16, v13\n"
"v_and_b32 v43, 0xffff0000, v13\n"
"v_add_f32 v170, v170, v42\n"
"v_add_f32 v171, v171, v43\n"
"v_lshlrev_b32 v42, 16, v14\n"
"v_and_b32 v43, 0xffff0000, v14\n"
"v_add_f32 v172, v172, v42\n"
"v_add_f32 v173, v173, v43\n"
"v_lshlrev_b32 v42, 16, v15\n"
"v_and_b32 v43, 0xffff0000, v15\n"
"v_add_f32 v174, v174, v42\n"
"v_add_f32 v175, v175, v43\n"
"v_lshlrev_b32 v42, 16, v16\n"
"v_and_b32 v43, 0xffff0000, v16\n"
"v_add_f32 v176, v176, v42\n"
"v_add_f32 v177, v177, v43\n"
"v_lshlrev_b32 v42, 16, v17\n"
"v_and_b32 v43, 0xffff0000, v17\n"
"v_add_f32 v178, v178, v42\n"
"v_add_f32 v179, v179, v43\n"
"v_lshlrev_b32 v42, 16, v18\n"
"v_and_b32 v43, 0xffff0000, v18\n"
"v_add_f32 v180, v180, v42\n"
"v_add_f32 v181, v181, v43\n"
"v_lshlrev_b32 v42, 16, v19\n"
"v_and_b32 v43, 0xffff0000, v19\n"
"v_add_f32 v182, v182, v42\n"
"v_add_f32 v183, v183, v43\n"
"v_lshlrev_b32 v42, 16, v20\n"
"v_and_b32 v43, 0xffff0000, v20\n"
"v_add_f32 v184, v184, v42\n"
"v_add_f32 v185, v185, v43\n"
"v_lshlrev_b32 v42, 16, v21\n"
"v_and_b32 v43, 0xffff0000, v21\n"
"v_add_f32 v186, v186, v42\n"
"v_add_f32 v187, v187, v43\n"
"v_lshlrev_b32 v42, 16, v22\n"
"v_and_b32 v43, 0xffff0000, v22\n"
"v_add_f32 v188, v188, v42\n"
"v_add_f32 v189, v189, v43\n"
"v_lshlrev_b32 v42, 16, v23\n"
"v_and_b32 v43, 0xffff0000, v23\n"
"v_add_f32 v190, v190, v42\n"
"v_add_f32 v191, v191, v43\n"
".Lf_b1:\n"
"s_cmp_eq_u32 s69, 2\n"
"s_cbranch_scc0 .Lf_b2\n"
"s_bitcmp1_b32 s74, 2\n"
"s_cbranch_scc0 .Lf_b2\n"
"s_mov_b32 s69, 3\n"
"v_lshlrev_b32 v42, 16, v24\n"
"v_and_b32 v43, 0xffff0000, v24\n"
"v_add_f32 v168, v168, v42\n"
"v_add_f32 v169, v169, v43\n"
"v_lshlrev_b32 v42, 16, v25\n"
"v_and_b32 v43, 0xffff0000, v25\n"
"v_add_f32 v170, v170, v42\n"
"v_add_f32 v171, v171, v43\n"
"v_lshlrev_b32 v42, 16, v26\n"
"v_and_b32 v43, 0xffff0000, v26\n"
"v_add_f32 v172, v172, v42\n"
"v_add_f32 v173, v173, v43\n"
"v_lshlrev_b32 v42, 16, v27\n"
"v_and_b32 v43, 0xffff0000, v27\n"
"v_add_f32 v174, v174, v42\n"
"v_add_f32 v175, v175, v43\n"
"v_lshlrev_b32 v42, 16, v28\n"
"v_and_b32 v43, 0xffff0000, v28\n"
"v_add_f32 v176, v176, v42\n"
"v_add_f32 v177, v177, v43\n"
"v_lshlrev_b32 v42, 16, v29\n"
"v_and_b32 v43, 0xffff0000, v29\n"
"v_add_f32 v178, v178, v42\n"
"v_add_f32 v179, v179, v43\n"
"v_lshlrev_b32 v42, 16, v30\n"
"v_and_b32 v43, 0xffff0000, v30\n"
"v_add_f32 v180, v180, v42\n"
"v_add_f32 v181, v181, v43\n"
"v_lshlrev_b32 v42, 16, v31\n"
"v_and_b32 v43, 0xffff0000, v31\n"
"v_add_f32 v182, v182, v42\n"
"v_add_f32 v183, v183, v43\n"
"v_lshlrev_b32 v42, 16, v32\n"
"v_and_b32 v43, 0xffff0000, v32\n"
"v_add_f32 v184, v184, v42\n"
"v_add_f32 v185, v185, v43\n"
"v_lshlrev_b32 v42, 16, v33\n"
"v_and_b32 v43, 0xffff0000, v33\n"
"v_add_f32 v186, v186, v42\n"
"v_add_f32 v187, v187, v43\n"
"v_lshlrev_b32 v42, 16, v34\n"
"v_and_b32 v43, 0xffff0000, v34\n"
"v_add_f32 v188, v188, v42\n"
"v_add_f32 v189, v189, v43\n"
"v_lshlrev_b32 v42, 16, v35\n"
"v_and_b32 v43, 0xffff0000, v35\n"
"v_add_f32 v190, v190, v42\n"
"v_add_f32 v191, v191, v43\n"
".Lf_b2:\n"
"v_cvt_pk_bf16_f32 v48, v168, v169\n"
"v_cvt_pk_bf16_f32 v49, v170, v171\n"
"v_cvt_pk_bf16_f32 v50, v172, v173\n"
"v_cvt_pk_bf16_f32 v51, v174, v175\n"
"s_mov_b32 s55, 0\n"
"v_permlane16_swap_b32 v48, v50\n"
"v_permlane16_swap_b32 v49, v51\n"
"buffer_store_dwordx4 v[48:51], v208, s[36:39], s55 offen sc1\n"
"v_cvt_pk_bf16_f32 v52, v176, v177\n"
"v_cvt_pk_bf16_f32 v53, v178, v179\n"
"v_cvt_pk_bf16_f32 v54, v180, v181\n"
"v_cvt_pk_bf16_f32 v55, v182, v183\n"
"s_mov_b32 s55, 32768\n"
"v_permlane16_swap_b32 v52, v54\n"
"v_permlane16_swap_b32 v53, v55\n"
"buffer_store_dwordx4 v[52:55], v208, s[36:39], s55 offen sc1\n"
"v_cvt_pk_bf16_f32 v56, v184, v185\n"
"v_cvt_pk_bf16_f32 v57, v186, v187\n"
"v_cvt_pk_bf16_f32 v58, v188, v189\n"
"v_cvt_pk_bf16_f32 v59, v190, v191\n"
"s_mov_b32 s55, 65536\n"
"v_permlane16_swap_b32 v56, v58\n"
"v_permlane16_swap_b32 v57, v59\n"
"buffer_store_dwordx4 v[56:59], v208, s[36:39], s55 offen sc1\n"
"s_branch .Lend\n"
".Lrare2:\n"
"s_call_b64 s[80:81], .Lrr10\n"
"s_branch .Lrare2_ret\n"
".Lrare3:\n"
"s_call_b64 s[80:81], .Lrr11\n"
"s_branch .Lrare3_ret\n"
".Lrare4:\n"
"s_call_b64 s[80:81], .Lrr01\n"
"s_branch .Lrare4_ret\n"
".Lrare5:\n"
"s_call_b64 s[80:81], .Lrr11\n"
"s_branch .Lrare5_ret\n"
".Lrare6:\n"
"s_call_b64 s[80:81], .Lrr01\n"
"s_branch .Lrare6_ret\n"
".Lrare7:\n"
"s_call_b64 s[80:81], .Lrr11\n"
"s_branch .Lrare7_ret\n"
".Lrare8:\n"
"s_call_b64 s[80:81], .Lrr01\n"
"s_branch .Lrare8_ret\n"
".Lrare9:\n"
"s_call_b64 s[80:81], .Lrr11\n"
"s_branch .Lrare9_ret\n"
".Lrare10:\n"
"s_call_b64 s[80:81], .Lrr01\n"
"s_branch .Lrare10_ret\n"
".Lrare11:\n"
"s_call_b64 s[80:81], .Lrr11\n"
"s_branch .Lrare11_ret\n"
".Lrare12:\n"
"s_call_b64 s[80:81], .Lrr01\n"
"s_branch .Lrare12_ret\n"
".Lrare13:\n"
"s_call_b64 s[80:81], .Lrr11\n"
"s_branch .Lrare13_ret\n"
".Lrare14:\n"
"s_call_b64 s[80:81], .Lrr01\n"
"s_branch .Lrare14_ret\n"
".Lrare15:\n"
"s_call_b64 s[80:81], .Lrr11\n"
"s_branch .Lrare15_ret\n"
".Lrare16:\n"
"s_call_b64 s[80:81], .Lrr01\n"
"s_branch .Lrare16_ret\n"
".Lspin4:\n"
"s_mov_b32 s60, 32768\n"
".Lspin4_l:\n"
"s_sleep 1\n"
"buffer_load_dwordx2 v[198:199], v196, s[92:95], s99 offen sc1\n"
"s_waitcnt vmcnt(0)\n"
"s_add_u32 s56, s96, 4\n"
"v_subrev_u32 v198, s56, v198\n"
"v_cmp_ge_u32 vcc, 1, v198\n"
"v_cmp_eq_u32 s[82:83], s97, v199\n"
"s_nop 0\n"
"s_and_b64 vcc, vcc, s[82:83]\n"
"s_cmp_eq_u64 vcc, -1\n"
"s_cbranch_scc1 .Lspin4_ret\n"
"s_sub_u32 s60, s60, 1\n"
"s_cmp_lg_u32 s60, 0\n"
"s_cbranch_scc1 .Lspin4_l\n"
"s_branch .Lspin4_ret\n"
".Lpg1:\n"
"s_sleep 1\n"
"s_memrealtime s[70:71]\n"
"s_waitcnt lgkmcnt(0)\n"
"s_sub_u32 s71, s72, s70\n"
"s_sub_u32 s71, s71, 4\n"
"s_cmp_lt_u32 s71, 400\n"
"s_cbranch_scc1 .Lpg1\n"
"s_branch .Lpg1x\n"
".Lpg2:\n"
"s_sleep 1\n"
"s_memrealtime s[70:71]\n"
"s_waitcnt lgkmcnt(0)\n"
"s_sub_u32 s71, s72, s70\n"
"s_sub_u32 s71, s71, 4\n"
"s_cmp_lt_u32 s71, 400\n"
"s_cbranch_scc1 .Lpg2\n"
"s_branch .Lpg2x\n"
".Lpg3:\n"
"s_sleep 1\n"
"s_memrealtime s[70:71]\n"
"s_waitcnt lgkmcnt(0)\n"
"s_sub_u32 s71, s72, s70\n"
"s_sub_u32 s71, s71, 4\n"
"s_cmp_lt_u32 s71, 400\n"
"s_cbranch_scc1 .Lpg3\n"
"s_branch .Lpg3x\n"
".Lpg4:\n"
"s_sleep 1\n"
"s_memrealtime s[70:71]\n"
"s_waitcnt lgkmcnt(0)\n"
"s_sub_u32 s71, s72, s70\n"
"s_sub_u32 s71, s71, 4\n"
"s_cmp_lt_u32 s71, 400\n"
"s_cbranch_scc1 .Lpg4\n"
"s_branch .Lpg4x\n"
".Lpg5:\n"
"s_sleep 1\n"
"s_memrealtime s[70:71]\n"
"s_waitcnt lgkmcnt(0)\n"
"s_sub_u32 s71, s72, s70\n"
"s_sub_u32 s71, s71, 4\n"
"s_cmp_lt_u32 s71, 400\n"
"s_cbranch_scc1 .Lpg5\n"
"s_branch .Lpg5x\n"
".Lpg6:\n"
"s_sleep 1\n"
"s_memrealtime s[70:71]\n"
"s_waitcnt lgkmcnt(0)\n"
"s_sub_u32 s71, s72, s70\n"
"s_sub_u32 s71, s71, 4\n"
"s_cmp_lt_u32 s71, 400\n"
"s_cbranch_scc1 .Lpg6\n"
"s_branch .Lpg6x\n"
".Lpg7:\n"
"s_sleep 1\n"
"s_memrealtime s[70:71]\n"
"s_waitcnt lgkmcnt(0)\n"
"s_sub_u32 s71, s72, s70\n"
"s_sub_u32 s71, s71, 4\n"
"s_cmp_lt_u32 s71, 400\n"
"s_cbranch_scc1 .Lpg7\n"
"s_branch .Lpg7x\n"
".Lpg8:\n"
"s_sleep 1\n"
"s_memrealtime s[70:71]\n"
"s_waitcnt lgkmcnt(0)\n"
"s_sub_u32 s71, s72, s70\n"
"s_sub_u32 s71, s71, 4\n"
"s_cmp_lt_u32 s71, 400\n"
"s_cbranch_scc1 .Lpg8\n"
"s_branch .Lpg8x\n"
".Lpg9:\n"
"s_sleep 1\n"
"s_memrealtime s[70:71]\n"
"s_waitcnt lgkmcnt(0)\n"
"s_sub_u32 s71, s72, s70\n"
"s_sub_u32 s71, s71, 4\n"
"s_cmp_lt_u32 s71, 400\n"
"s_cbranch_scc1 .Lpg9\n"
"s_branch .Lpg9x\n"
".Lpg10:\n"
"s_sleep 1\n"
"s_memrealtime s[70:71]\n"
"s_waitcnt lgkmcnt(0)\n"
"s_sub_u32 s71, s72, s70\n"
"s_sub_u32 s71, s71, 4\n"
"s_cmp_lt_u32 s71, 400\n"
"s_cbranch_scc1 .Lpg10\n"
"s_branch .Lpg10x\n"
".Lpg11:\n"
"s_sleep 1\n"
"s_memrealtime s[70:71]\n"
"s_waitcnt lgkmcnt(0)\n"
"s_sub_u32 s71, s72, s70\n"
"s_sub_u32 s71, s71, 4\n"
"s_cmp_lt_u32 s71, 400\n"
"s_cbranch_scc1 .Lpg11\n"
"s_branch .Lpg11x\n"
".Lpg12:\n"
"s_sleep 1\n"
"s_memrealtime s[70:71]\n"
"s_waitcnt lgkmcnt(0)\n"
"s_sub_u32 s71, s72, s70\n"
"s_sub_u32 s71, s71, 4\n"
"s_cmp_lt_u32 s71, 400\n"
"s_cbranch_scc1 .Lpg12\n"
"s_branch .Lpg12x\n"
".Lpg13:\n"
"s_sleep 1\n"
"s_memrealtime s[70:71]\n"
"s_waitcnt lgkmcnt(0)\n"
"s_sub_u32 s71, s72, s70\n"
"s_sub_u32 s71, s71, 4\n"
"s_cmp_lt_u32 s71, 400\n"
"s_cbranch_scc1 .Lpg13\n"
"s_branch .Lpg13x\n"
".Lpg14:\n"
"s_sleep 1\n"
"s_memrealtime s[70:71]\n"
"s_waitcnt lgkmcnt(0)\n"
"s_sub_u32 s71, s72, s70\n"
"s_sub_u32 s71, s71, 4\n"
"s_cmp_lt_u32 s71, 400\n"
"s_cbranch_scc1 .Lpg14\n"
"s_branch .Lpg14x\n"
".Lrr01:\n"
"s_waitcnt vmcnt(0) lgkmcnt(0)\n"
"s_nop 15\n"
"s_nop 7\n"
"v_mbcnt_lo_u32_b32 v200, -1, 0\n"
"v_mbcnt_hi_u32_b32 v200, -1, v200\n"
"v_and_b32 v199, 15, v200\n"
"v_lshlrev_b32 v199, 8, v199\n"
"v_lshrrev_b32 v200, 4, v200\n"
"v_lshl_add_u32 v199, v200, 4, v199\n"
"v_add_u32 v200, 4096, v199\n"
"buffer_load_dwordx4 a[192:195], v199, s[24:27], s64 offen\n"
"buffer_load_dwordx4 a[196:199], v200, s[24:27], s64 offen\n"
"buffer_load_dwordx4 a[200:203], v199, s[24:27], s64 offen offset:64\n"
"buffer_load_dwordx4 a[204:207], v200, s[24:27], s64 offen offset:64\n"
"buffer_load_dwordx4 a[208:211], v199, s[24:27], s64 offen offset:128\n"
"buffer_load_dwordx4 a[212:215], v200, s[24:27], s64 offen offset:128\n"
"buffer_load_dwordx4 a[216:219], v199, s[24:27], s64 offen offset:192\n"
"buffer_load_dwordx4 a[220:223], v200, s[24:27], s64 offen offset:192\n"
"s_waitcnt vmcnt(0)\n"
"v_mfma_f32_16x16x32_bf16 v[208:211], a[192:195], v[0:3], 0\n"
"v_mfma_f32_16x16x32_bf16 v[216:219], a[192:195], v[4:7], 0\n"
"v_mfma_f32_16x16x32_bf16 v[224:227], a[192:195], v[8:11], 0\n"
"v_mfma_f32_16x16x32_bf16 v[232:235], a[192:195], v[12:15], 0\n"
"v_mfma_f32_16x16x32_bf16 v[240:243], a[192:195], v[16:19], 0\n"
"v_mfma_f32_16x16x32_bf16 v[248:251], a[192:195], v[20:23], 0\n"
"v_mfma_f32_16x16x32_bf16 v[212:215], a[196:199], v[0:3], 0\n"
"v_mfma_f32_16x16x32_bf16 v[220:223], a[196:199], v[4:7], 0\n"
"v_mfma_f32_16x16x32_bf16 v[228:231], a[196:199], v[8:11], 0\n"
"v_mfma_f32_16x16x32_bf16 v[236:239], a[196:199], v[12:15], 0\n"
"v_mfma_f32_16x16x32_bf16 v[244:247], a[196:199], v[16:19], 0\n"
"v_mfma_f32_16x16x32_bf16 v[252:255], a[196:199], v[20:23], 0\n"
"v_mfma_f32_16x16x32_bf16 v[208:211], a[200:203], v[24:27], v[208:211]\n"
"v_mfma_f32_16x16x32_bf16 v[216:219], a[200:203], v[28:31], v[216:219]\n"
"v_mfma_f32_16x16x32_bf16 v[224:227], a[200:203], v[32:35], v[224:227]\n"
"v_mfma_f32_16x16x32_bf16 v[232:235], a[200:203], v[36:39], v[232:235]\n"
"v_mfma_f32_16x16x32_bf16 v[240:243], a[200:203], v[40:43], v[240:243]\n"
"v_mfma_f32_16x16x32_bf16 v[248:251], a[200:203], v[44:47], v[248:251]\n"
"v_mfma_f32_16x16x32_bf16 v[212:215], a[204:207], v[24:27], v[212:215]\n"
"v_mfma_f32_16x16x32_bf16 v[220:223], a[204:207], v[28:31], v[220:223]\n"
"v_mfma_f32_16x16x32_bf16 v[228:231], a[204:207], v[32:35], v[228:231]\n"
"v_mfma_f32_16x16x32_bf16 v[236:239], a[204:207], v[36:39], v[236:239]\n"
"v_mfma_f32_16x16x32_bf16 v[244:247], a[204:207], v[40:43], v[244:247]\n"
"v_mfma_f32_16x16x32_bf16 v[252:255], a[204:207], v[44:47], v[252:255]\n"
"v_mfma_f32_16x16x32_bf16 v[208:211], a[208:211], v[48:51], v[208:211]\n"
"v_mfma_f32_16x16x32_bf16 v[216:219], a[208:211], v[52:55], v[216:219]\n"
"v_mfma_f32_16x16x32_bf16 v[224:227], a[208:211], v[56:59], v[224:227]\n"
"v_mfma_f32_16x16x32_bf16 v[232:235], a[208:211], v[60:63], v[232:235]\n"
"v_mfma_f32_16x16x32_bf16 v[240:243], a[208:211], v[64:67], v[240:243]\n"
"v_mfma_f32_16x16x32_bf16 v[248:251], a[208:211], v[68:71], v[248:251]\n"
"v_mfma_f32_16x16x32_bf16 v[212:215], a[212:215], v[48:51], v[212:215]\n"
"v_mfma_f32_16x16x32_bf16 v[220:223], a[212:215], v[52:55], v[220:223]\n"
"v_mfma_f32_16x16x32_bf16 v[228:231], a[212:215], v[56:59], v[228:231]\n"
"v_mfma_f32_16x16x32_bf16 v[236:239], a[212:215], v[60:63], v[236:239]\n"
"v_mfma_f32_16x16x32_bf16 v[244:247], a[212:215], v[64:67], v[244:247]\n"
"v_mfma_f32_16x16x32_bf16 v[252:255], a[212:215], v[68:71], v[252:255]\n"
"v_mfma_f32_16x16x32_bf16 v[208:211], a[216:219], v[72:75], v[208:211]\n"
"v_mfma_f32_16x16x32_bf16 v[216:219], a[216:219], v[76:79], v[216:219]\n"
"v_mfma_f32_16x16x32_bf16 v[224:227], a[216:219], v[80:83], v[224:227]\n"
"v_mfma_f32_16x16x32_bf16 v[232:235], a[216:219], v[84:87], v[232:235]\n"
"v_mfma_f32_16x16x32_bf16 v[240:243], a[216:219], v[88:91], v[240:243]\n"
"v_mfma_f32_16x16x32_bf16 v[248:251], a[216:219], v[92:95], v[248:251]\n"
"v_mfma_f32_16x16x32_bf16 v[212:215], a[220:223], v[72:75], v[212:215]\n"
"v_mfma_f32_16x16x32_bf16 v[220:223], a[220:223], v[76:79], v[220:223]\n"
"v_mfma_f32_16x16x32_bf16 v[228:231], a[220:223], v[80:83], v[228:231]\n"
"v_mfma_f32_16x16x32_bf16 v[236:239], a[220:223], v[84:87], v[236:239]\n"
"v_mfma_f32_16x16x32_bf16 v[244:247], a[220:223], v[88:91], v[244:247]\n"
"v_mfma_f32_16x16x32_bf16 v[252:255], a[220:223], v[92:95], v[252:255]\n"
"v_max3_f32 v202, v208, v209, v210\n"
"s_nop 1\n"
"v_max3_f32 v202, v202, v211, v212\n"
"v_max3_f32 v202, v202, v213, v214\n"
"v_max_f32 v202, v202, v215\n"
"v_mov_b32 v199, v202\n"
"s_nop 1\n"
"v_permlane32_swap_b32 v202, v199\n"
"v_max_f32 v202, v202, v199\n"
"v_mov_b32 v199, v202\n"
"s_nop 1\n"
"v_permlane16_swap_b32 v202, v199\n"
"v_max_f32 v202, v202, v199\n"
"v_sub_f32 v202, 0xc1800000, v202\n"
"v_min_f32 v202, v202, v144\n"
"v_sub_f32 v200, v202, v144\n"
"v_exp_f32 v199, v200\n"
"v_mov_b32 v144, v202\n"
"v_mov_b32 v145, v202\n"
"v_mov_b32 v146, v202\n"
"v_mov_b32 v147, v202\n"
"v_add_f32 v208, v208, v202\n"
"v_add_f32 v209, v209, v202\n"
"v_add_f32 v210, v210, v202\n"
"v_add_f32 v211, v211, v202\n"
"v_add_f32 v212, v212, v202\n"
"v_add_f32 v213, v213, v202\n"
"v_add_f32 v214, v214, v202\n"
"v_add_f32 v215, v215, v202\n"
"v_add_f32 v96, v96, v200\n"
"v_add_f32 v97, v97, v200\n"
"v_add_f32 v98, v98, v200\n"
"v_add_f32 v99, v99, v200\n"
"v_add_f32 v100, v100, v200\n"
"v_add_f32 v101, v101, v200\n"
"v_add_f32 v102, v102, v200\n"
"v_add_f32 v103, v103, v200\n"
"v_mul_f32 v168, v168, v199\n"
"v_mul_f32 v169, v169, v199\n"
"v_mul_f32 v170, v170, v199\n"
"v_mul_f32 v171, v171, v199\n"
"v_accvgpr_read_b32 v198, a0\n"
"v_mul_f32 v198, v198, v199\n"
"v_accvgpr_write_b32 a0, v198\n"
"v_accvgpr_read_b32 v198, a1\n"
"v_mul_f32 v198, v198, v199\n"
"v_accvgpr_write_b32 a1, v198\n"
"v_accvgpr_read_b32 v198, a2\n"
"v_mul_f32 v198, v198, v199\n"
"v_accvgpr_write_b32 a2, v198\n"
"v_accvgpr_read_b32 v198, a3\n"
"v_mul_f32 v198, v198, v199\n"
"v_accvgpr_write_b32 a3, v198\n"
"v_accvgpr_read_b32 v198, a24\n"
"v_mul_f32 v198, v198, v199\n"
"v_accvgpr_write_b32 a24, v198\n"
"v_accvgpr_read_b32 v198, a25\n"
"v_mul_f32 v198, v198, v199\n"
"v_accvgpr_write_b32 a25, v198\n"
"v_accvgpr_read_b32 v198, a26\n"
"v_mul_f32 v198, v198, v199\n"
"v_accvgpr_write_b32 a26, v198\n"
"v_accvgpr_read_b32 v198, a27\n"
"v_mul_f32 v198, v198, v199\n"
"v_accvgpr_write_b32 a27, v198\n"
"v_accvgpr_read_b32 v198, a48\n"
"v_mul_f32 v198, v198, v199\n"
"v_accvgpr_write_b32 a48, v198\n"
"v_accvgpr_read_b32 v198, a49\n"
"v_mul_f32 v198, v198, v199\n"
"v_accvgpr_write_b32 a49, v198\n"
"v_accvgpr_read_b32 v198, a50\n"
"v_mul_f32 v198, v198, v199\n"
"v_accvgpr_write_b32 a50, v198\n"
"v_accvgpr_read_b32 v198, a51\n"
"v_mul_f32 v198, v198, v199\n"
"v_accvgpr_write_b32 a51, v198\n"
"v_accvgpr_read_b32 v198, a72\n"
"v_mul_f32 v198, v198, v199\n"
"v_accvgpr_write_b32 a72, v198\n"
"v_accvgpr_read_b32 v198, a73\n"
"v_mul_f32 v198, v198, v199\n"
"v_accvgpr_write_b32 a73, v198\n"
"v_accvgpr_read_b32 v198, a74\n"
"v_mul_f32 v198, v198, v199\n"
"v_accvgpr_write_b32 a74, v198\n"
"v_accvgpr_read_b32 v198, a75\n"
"v_mul_f32 v198, v198, v199\n"
"v_accvgpr_write_b32 a75, v198\n"
"v_accvgpr_read_b32 v198, a96\n"
"v_mul_f32 v198, v198, v199\n"
"v_accvgpr_write_b32 a96, v198\n"
"v_accvgpr_read_b32 v198, a97\n"
"v_mul_f32 v198, v198, v199\n"
"v_accvgpr_write_b32 a97, v198\n"
"v_accvgpr_read_b32 v198, a98\n"
"v_mul_f32 v198, v198, v199\n"
"v_accvgpr_write_b32 a98, v198\n"
"v_accvgpr_read_b32 v198, a99\n"
"v_mul_f32 v198, v198, v199\n"
"v_accvgpr_write_b32 a99, v198\n"
"v_accvgpr_read_b32 v198, a120\n"
"v_mul_f32 v198, v198, v199\n"
"v_accvgpr_write_b32 a120, v198\n"
"v_accvgpr_read_b32 v198, a121\n"
"v_mul_f32 v198, v198, v199\n"
"v_accvgpr_write_b32 a121, v198\n"
"v_accvgpr_read_b32 v198, a122\n"
"v_mul_f32 v198, v198, v199\n"
"v_accvgpr_write_b32 a122, v198\n"
"v_accvgpr_read_b32 v198, a123\n"
"v_mul_f32 v198, v198, v199\n"
"v_accvgpr_write_b32 a123, v198\n"
"v_accvgpr_read_b32 v198, a144\n"
"v_mul_f32 v198, v198, v199\n"
"v_accvgpr_write_b32 a144, v198\n"
"v_accvgpr_read_b32 v198, a145\n"
"v_mul_f32 v198, v198, v199\n"
"v_accvgpr_write_b32 a145, v198\n"
"v_accvgpr_read_b32 v198, a146\n"
"v_mul_f32 v198, v198, v199\n"
"v_accvgpr_write_b32 a146, v198\n"
"v_accvgpr_read_b32 v198, a147\n"
"v_mul_f32 v198, v198, v199\n"
"v_accvgpr_write_b32 a147, v198\n"
"v_accvgpr_read_b32 v198, a168\n"
"v_mul_f32 v198, v198, v199\n"
"v_accvgpr_write_b32 a168, v198\n"
"v_accvgpr_read_b32 v198, a169\n"
"v_mul_f32 v198, v198, v199\n"
"v_accvgpr_write_b32 a169, v198\n"
"v_accvgpr_read_b32 v198, a170\n"
"v_mul_f32 v198, v198, v199\n"
"v_accvgpr_write_b32 a170, v198\n"
"v_accvgpr_read_b32 v198, a171\n"
"v_mul_f32 v198, v198, v199\n"
"v_accvgpr_write_b32 a171, v198\n"
"v_max3_f32 v202, v216, v217, v218\n"
"v_max3_f32 v202, v202, v219, v220\n"
"v_max3_f32 v202, v202, v221, v222\n"
"v_max_f32 v202, v202, v223\n"
"v_mov_b32 v199, v202\n"
"s_nop 1\n"
"v_permlane32_swap_b32 v202, v199\n"
"v_max_f32 v202, v202, v199\n"
"v_mov_b32 v199, v202\n"
"s_nop 1\n"
"v_permlane16_swap_b32 v202, v199\n"
"v_max_f32 v202, v202, v199\n"
"v_sub_f32 v202, 0xc1800000, v202\n"
"v_min_f32 v202, v202, v148\n"
"v_sub_f32 v200, v202, v148\n"
"v_exp_f32 v199, v200\n"
"v_mov_b32 v148, v202\n"
"v_mov_b32 v149, v202\n"
"v_mov_b32 v150, v202\n"
"v_mov_b32 v151, v202\n"
"v_add_f32 v216, v216, v202\n"
"v_add_f32 v217, v217, v202\n"
"v_add_f32 v218, v218, v202\n"
"v_add_f32 v219, v219, v202\n"
"v_add_f32 v220, v220, v202\n"
"v_add_f32 v221, v221, v202\n"
"v_add_f32 v222, v222, v202\n"
"v_add_f32 v223, v223, v202\n"
"v_add_f32 v104, v104, v200\n"
"v_add_f32 v105, v105, v200\n"
"v_add_f32 v106, v106, v200\n"
"v_add_f32 v107, v107, v200\n"
"v_add_f32 v108, v108, v200\n"
"v_add_f32 v109, v109, v200\n"
"v_add_f32 v110, v110, v200\n"
"v_add_f32 v111, v111, v200\n"
"v_mul_f32 v172, v172, v199\n"
"v_mul_f32 v173, v173, v199\n"
"v_mul_f32 v174, v174, v199\n"
"v_mul_f32 v175, v175, v199\n"
"v_accvgpr_read_b32 v198, a4\n"
"v_mul_f32 v198, v198, v199\n"
"v_accvgpr_write_b32 a4, v198\n"
"v_accvgpr_read_b32 v198, a5\n"
"v_mul_f32 v198, v198, v199\n"
"v_accvgpr_write_b32 a5, v198\n"
"v_accvgpr_read_b32 v198, a6\n"
"v_mul_f32 v198, v198, v199\n"
"v_accvgpr_write_b32 a6, v198\n"
"v_accvgpr_read_b32 v198, a7\n"
"v_mul_f32 v198, v198, v199\n"
"v_accvgpr_write_b32 a7, v198\n"
"v_accvgpr_read_b32 v198, a28\n"
"v_mul_f32 v198, v198, v199\n"
"v_accvgpr_write_b32 a28, v198\n"
"v_accvgpr_read_b32 v198, a29\n"
"v_mul_f32 v198, v198, v199\n"
"v_accvgpr_write_b32 a29, v198\n"
"v_accvgpr_read_b32 v198, a30\n"
"v_mul_f32 v198, v198, v199\n"
"v_accvgpr_write_b32 a30, v198\n"
"v_accvgpr_read_b32 v198, a31\n"
"v_mul_f32 v198, v198, v199\n"
"v_accvgpr_write_b32 a31, v198\n"
"v_accvgpr_read_b32 v198, a52\n"
"v_mul_f32 v198, v198, v199\n"
"v_accvgpr_write_b32 a52, v198\n"
"v_accvgpr_read_b32 v198, a53\n"
"v_mul_f32 v198, v198, v199\n"
"v_accvgpr_write_b32 a53, v198\n"
"v_accvgpr_read_b32 v198, a54\n"
"v_mul_f32 v198, v198, v199\n"
"v_accvgpr_write_b32 a54, v198\n"
"v_accvgpr_read_b32 v198, a55\n"
"v_mul_f32 v198, v198, v199\n"
"v_accvgpr_write_b32 a55, v198\n"
"v_accvgpr_read_b32 v198, a76\n"
"v_mul_f32 v198, v198, v199\n"
"v_accvgpr_write_b32 a76, v198\n"
"v_accvgpr_read_b32 v198, a77\n"
"v_mul_f32 v198, v198, v199\n"
"v_accvgpr_write_b32 a77, v198\n"
"v_accvgpr_read_b32 v198, a78\n"
"v_mul_f32 v198, v198, v199\n"
"v_accvgpr_write_b32 a78, v198\n"
"v_accvgpr_read_b32 v198, a79\n"
"v_mul_f32 v198, v198, v199\n"
"v_accvgpr_write_b32 a79, v198\n"
"v_accvgpr_read_b32 v198, a100\n"
"v_mul_f32 v198, v198, v199\n"
"v_accvgpr_write_b32 a100, v198\n"
"v_accvgpr_read_b32 v198, a101\n"
"v_mul_f32 v198, v198, v199\n"
"v_accvgpr_write_b32 a101, v198\n"
"v_accvgpr_read_b32 v198, a102\n"
"v_mul_f32 v198, v198, v199\n"
"v_accvgpr_write_b32 a102, v198\n"
"v_accvgpr_read_b32 v198, a103\n"
"v_mul_f32 v198, v198, v199\n"
"v_accvgpr_write_b32 a103, v198\n"
"v_accvgpr_read_b32 v198, a124\n"
"v_mul_f32 v198, v198, v199\n"
"v_accvgpr_write_b32 a124, v198\n"
"v_accvgpr_read_b32 v198, a125\n"
"v_mul_f32 v198, v198, v199\n"
"v_accvgpr_write_b32 a125, v198\n"
"v_accvgpr_read_b32 v198, a126\n"
"v_mul_f32 v198, v198, v199\n"
"v_accvgpr_write_b32 a126, v198\n"
"v_accvgpr_read_b32 v198, a127\n"
"v_mul_f32 v198, v198, v199\n"
"v_accvgpr_write_b32 a127, v198\n"
"v_accvgpr_read_b32 v198, a148\n"
"v_mul_f32 v198, v198, v199\n"
"v_accvgpr_write_b32 a148, v198\n"
"v_accvgpr_read_b32 v198, a149\n"
"v_mul_f32 v198, v198, v199\n"
"v_accvgpr_write_b32 a149, v198\n"
"v_accvgpr_read_b32 v198, a150\n"
"v_mul_f32 v198, v198, v199\n"
"v_accvgpr_write_b32 a150, v198\n"
"v_accvgpr_read_b32 v198, a151\n"
"v_mul_f32 v198, v198, v199\n"
"v_accvgpr_write_b32 a151, v198\n"
"v_accvgpr_read_b32 v198, a172\n"
"v_mul_f32 v198, v198, v199\n"
"v_accvgpr_write_b32 a172, v198\n"
"v_accvgpr_read_b32 v198, a173\n"
"v_mul_f32 v198, v198, v199\n"
"v_accvgpr_write_b32 a173, v198\n"
"v_accvgpr_read_b32 v198, a174\n"
"v_mul_f32 v198, v198, v199\n"
"v_accvgpr_write_b32 a174, v198\n"
"v_accvgpr_read_b32 v198, a175\n"
"v_mul_f32 v198, v198, v199\n"
"v_accvgpr_write_b32 a175, v198\n"
"v_max3_f32 v202, v224, v225, v226\n"
"v_max3_f32 v202, v202, v227, v228\n"
"v_max3_f32 v202, v202, v229, v230\n"
"v_max_f32 v202, v202, v231\n"
"v_mov_b32 v199, v202\n"
"s_nop 1\n"
"v_permlane32_swap_b32 v202, v199\n"
"v_max_f32 v202, v202, v199\n"
"v_mov_b32 v199, v202\n"
"s_nop 1\n"
"v_permlane16_swap_b32 v202, v199\n"
"v_max_f32 v202, v202, v199\n"
"v_sub_f32 v202, 0xc1800000, v202\n"
"v_min_f32 v202, v202, v152\n"
"v_sub_f32 v200, v202, v152\n"
"v_exp_f32 v199, v200\n"
"v_mov_b32 v152, v202\n"
"v_mov_b32 v153, v202\n"
"v_mov_b32 v154, v202\n"
"v_mov_b32 v155, v202\n"
"v_add_f32 v224, v224, v202\n"
"v_add_f32 v225, v225, v202\n"
"v_add_f32 v226, v226, v202\n"
"v_add_f32 v227, v227, v202\n"
"v_add_f32 v228, v228, v202\n"
"v_add_f32 v229, v229, v202\n"
"v_add_f32 v230, v230, v202\n"
"v_add_f32 v231, v231, v202\n"
"v_add_f32 v112, v112, v200\n"
"v_add_f32 v113, v113, v200\n"
"v_add_f32 v114, v114, v200\n"
"v_add_f32 v115, v115, v200\n"
"v_add_f32 v116, v116, v200\n"
"v_add_f32 v117, v117, v200\n"
"v_add_f32 v118, v118, v200\n"
"v_add_f32 v119, v119, v200\n"
"v_mul_f32 v176, v176, v199\n"
"v_mul_f32 v177, v177, v199\n"
"v_mul_f32 v178, v178, v199\n"
"v_mul_f32 v179, v179, v199\n"
"v_accvgpr_read_b32 v198, a8\n"
"v_mul_f32 v198, v198, v199\n"
"v_accvgpr_write_b32 a8, v198\n"
"v_accvgpr_read_b32 v198, a9\n"
"v_mul_f32 v198, v198, v199\n"
"v_accvgpr_write_b32 a9, v198\n"
"v_accvgpr_read_b32 v198, a10\n"
"v_mul_f32 v198, v198, v199\n"
"v_accvgpr_write_b32 a10, v198\n"
"v_accvgpr_read_b32 v198, a11\n"
"v_mul_f32 v198, v198, v199\n"
"v_accvgpr_write_b32 a11, v198\n"
"v_accvgpr_read_b32 v198, a32\n"
"v_mul_f32 v198, v198, v199\n"
"v_accvgpr_write_b32 a32, v198\n"
"v_accvgpr_read_b32 v198, a33\n"
"v_mul_f32 v198, v198, v199\n"
"v_accvgpr_write_b32 a33, v198\n"
"v_accvgpr_read_b32 v198, a34\n"
"v_mul_f32 v198, v198, v199\n"
"v_accvgpr_write_b32 a34, v198\n"
"v_accvgpr_read_b32 v198, a35\n"
"v_mul_f32 v198, v198, v199\n"
"v_accvgpr_write_b32 a35, v198\n"
"v_accvgpr_read_b32 v198, a56\n"
"v_mul_f32 v198, v198, v199\n"
"v_accvgpr_write_b32 a56, v198\n"
"v_accvgpr_read_b32 v198, a57\n"
"v_mul_f32 v198, v198, v199\n"
"v_accvgpr_write_b32 a57, v198\n"
"v_accvgpr_read_b32 v198, a58\n"
"v_mul_f32 v198, v198, v199\n"
"v_accvgpr_write_b32 a58, v198\n"
"v_accvgpr_read_b32 v198, a59\n"
"v_mul_f32 v198, v198, v199\n"
"v_accvgpr_write_b32 a59, v198\n"
"v_accvgpr_read_b32 v198, a80\n"
"v_mul_f32 v198, v198, v199\n"
"v_accvgpr_write_b32 a80, v198\n"
"v_accvgpr_read_b32 v198, a81\n"
"v_mul_f32 v198, v198, v199\n"
"v_accvgpr_write_b32 a81, v198\n"
"v_accvgpr_read_b32 v198, a82\n"
"v_mul_f32 v198, v198, v199\n"
"v_accvgpr_write_b32 a82, v198\n"
"v_accvgpr_read_b32 v198, a83\n"
"v_mul_f32 v198, v198, v199\n"
"v_accvgpr_write_b32 a83, v198\n"
"v_accvgpr_read_b32 v198, a104\n"
"v_mul_f32 v198, v198, v199\n"
"v_accvgpr_write_b32 a104, v198\n"
"v_accvgpr_read_b32 v198, a105\n"
"v_mul_f32 v198, v198, v199\n"
"v_accvgpr_write_b32 a105, v198\n"
"v_accvgpr_read_b32 v198, a106\n"
"v_mul_f32 v198, v198, v199\n"
"v_accvgpr_write_b32 a106, v198\n"
"v_accvgpr_read_b32 v198, a107\n"
"v_mul_f32 v198, v198, v199\n"
"v_accvgpr_write_b32 a107, v198\n"
"v_accvgpr_read_b32 v198, a128\n"
"v_mul_f32 v198, v198, v199\n"
"v_accvgpr_write_b32 a128, v198\n"
"v_accvgpr_read_b32 v198, a129\n"
"v_mul_f32 v198, v198, v199\n"
"v_accvgpr_write_b32 a129, v198\n"
"v_accvgpr_read_b32 v198, a130\n"
"v_mul_f32 v198, v198, v199\n"
"v_accvgpr_write_b32 a130, v198\n"
"v_accvgpr_read_b32 v198, a131\n"
"v_mul_f32 v198, v198, v199\n"
"v_accvgpr_write_b32 a131, v198\n"
"v_accvgpr_read_b32 v198, a152\n"
"v_mul_f32 v198, v198, v199\n"
"v_accvgpr_write_b32 a152, v198\n"
"v_accvgpr_read_b32 v198, a153\n"
"v_mul_f32 v198, v198, v199\n"
"v_accvgpr_write_b32 a153, v198\n"
"v_accvgpr_read_b32 v198, a154\n"
"v_mul_f32 v198, v198, v199\n"
"v_accvgpr_write_b32 a154, v198\n"
"v_accvgpr_read_b32 v198, a155\n"
"v_mul_f32 v198, v198, v199\n"
"v_accvgpr_write_b32 a155, v198\n"
"v_accvgpr_read_b32 v198, a176\n"
"v_mul_f32 v198, v198, v199\n"
"v_accvgpr_write_b32 a176, v198\n"
"v_accvgpr_read_b32 v198, a177\n"
"v_mul_f32 v198, v198, v199\n"
"v_accvgpr_write_b32 a177, v198\n"
"v_accvgpr_read_b32 v198, a178\n"
"v_mul_f32 v198, v198, v199\n"
"v_accvgpr_write_b32 a178, v198\n"
"v_accvgpr_read_b32 v198, a179\n"
"v_mul_f32 v198, v198, v199\n"
"v_accvgpr_write_b32 a179, v198\n"
"v_max3_f32 v202, v232, v233, v234\n"
"v_max3_f32 v202, v202, v235, v236\n"
"v_max3_f32 v202, v202, v237, v238\n"
"v_max_f32 v202, v202, v239\n"
"v_mov_b32 v199, v202\n"
"s_nop 1\n"
"v_permlane32_swap_b32 v202, v199\n"
"v_max_f32 v202, v202, v199\n"
"v_mov_b32 v199, v202\n"
"s_nop 1\n"
"v_permlane16_swap_b32 v202, v199\n"
"v_max_f32 v202, v202, v199\n"
"v_sub_f32 v202, 0xc1800000, v202\n"
"v_min_f32 v202, v202, v156\n"
"v_sub_f32 v200, v202, v156\n"
"v_exp_f32 v199, v200\n"
"v_mov_b32 v156, v202\n"
"v_mov_b32 v157, v202\n"
"v_mov_b32 v158, v202\n"
"v_mov_b32 v159, v202\n"
"v_add_f32 v232, v232, v202\n"
"v_add_f32 v233, v233, v202\n"
"v_add_f32 v234, v234, v202\n"
"v_add_f32 v235, v235, v202\n"
"v_add_f32 v236, v236, v202\n"
"v_add_f32 v237, v237, v202\n"
"v_add_f32 v238, v238, v202\n"
"v_add_f32 v239, v239, v202\n"
"v_add_f32 v120, v120, v200\n"
"v_add_f32 v121, v121, v200\n"
"v_add_f32 v122, v122, v200\n"
"v_add_f32 v123, v123, v200\n"
"v_add_f32 v124, v124, v200\n"
"v_add_f32 v125, v125, v200\n"
"v_add_f32 v126, v126, v200\n"
"v_add_f32 v127, v127, v200\n"
"v_mul_f32 v180, v180, v199\n"
"v_mul_f32 v181, v181, v199\n"
"v_mul_f32 v182, v182, v199\n"
"v_mul_f32 v183, v183, v199\n"
"v_accvgpr_read_b32 v198, a12\n"
"v_mul_f32 v198, v198, v199\n"
"v_accvgpr_write_b32 a12, v198\n"
"v_accvgpr_read_b32 v198, a13\n"
"v_mul_f32 v198, v198, v199\n"
"v_accvgpr_write_b32 a13, v198\n"
"v_accvgpr_read_b32 v198, a14\n"
"v_mul_f32 v198, v198, v199\n"
"v_accvgpr_write_b32 a14, v198\n"
"v_accvgpr_read_b32 v198, a15\n"
"v_mul_f32 v198, v198, v199\n"
"v_accvgpr_write_b32 a15, v198\n"
"v_accvgpr_read_b32 v198, a36\n"
"v_mul_f32 v198, v198, v199\n"
"v_accvgpr_write_b32 a36, v198\n"
"v_accvgpr_read_b32 v198, a37\n"
"v_mul_f32 v198, v198, v199\n"
"v_accvgpr_write_b32 a37, v198\n"
"v_accvgpr_read_b32 v198, a38\n"
"v_mul_f32 v198, v198, v199\n"
"v_accvgpr_write_b32 a38, v198\n"
"v_accvgpr_read_b32 v198, a39\n"
"v_mul_f32 v198, v198, v199\n"
"v_accvgpr_write_b32 a39, v198\n"
"v_accvgpr_read_b32 v198, a60\n"
"v_mul_f32 v198, v198, v199\n"
"v_accvgpr_write_b32 a60, v198\n"
"v_accvgpr_read_b32 v198, a61\n"
"v_mul_f32 v198, v198, v199\n"
"v_accvgpr_write_b32 a61, v198\n"
"v_accvgpr_read_b32 v198, a62\n"
"v_mul_f32 v198, v198, v199\n"
"v_accvgpr_write_b32 a62, v198\n"
"v_accvgpr_read_b32 v198, a63\n"
"v_mul_f32 v198, v198, v199\n"
"v_accvgpr_write_b32 a63, v198\n"
"v_accvgpr_read_b32 v198, a84\n"
"v_mul_f32 v198, v198, v199\n"
"v_accvgpr_write_b32 a84, v198\n"
"v_accvgpr_read_b32 v198, a85\n"
"v_mul_f32 v198, v198, v199\n"
"v_accvgpr_write_b32 a85, v198\n"
"v_accvgpr_read_b32 v198, a86\n"
"v_mul_f32 v198, v198, v199\n"
"v_accvgpr_write_b32 a86, v198\n"
"v_accvgpr_read_b32 v198, a87\n"
"v_mul_f32 v198, v198, v199\n"
"v_accvgpr_write_b32 a87, v198\n"
"v_accvgpr_read_b32 v198, a108\n"
"v_mul_f32 v198, v198, v199\n"
"v_accvgpr_write_b32 a108, v198\n"
"v_accvgpr_read_b32 v198, a109\n"
"v_mul_f32 v198, v198, v199\n"
"v_accvgpr_write_b32 a109, v198\n"
"v_accvgpr_read_b32 v198, a110\n"
"v_mul_f32 v198, v198, v199\n"
"v_accvgpr_write_b32 a110, v198\n"
"v_accvgpr_read_b32 v198, a111\n"
"v_mul_f32 v198, v198, v199\n"
"v_accvgpr_write_b32 a111, v198\n"
"v_accvgpr_read_b32 v198, a132\n"
"v_mul_f32 v198, v198, v199\n"
"v_accvgpr_write_b32 a132, v198\n"
"v_accvgpr_read_b32 v198, a133\n"
"v_mul_f32 v198, v198, v199\n"
"v_accvgpr_write_b32 a133, v198\n"
"v_accvgpr_read_b32 v198, a134\n"
"v_mul_f32 v198, v198, v199\n"
"v_accvgpr_write_b32 a134, v198\n"
"v_accvgpr_read_b32 v198, a135\n"
"v_mul_f32 v198, v198, v199\n"
"v_accvgpr_write_b32 a135, v198\n"
"v_accvgpr_read_b32 v198, a156\n"
"v_mul_f32 v198, v198, v199\n"
"v_accvgpr_write_b32 a156, v198\n"
"v_accvgpr_read_b32 v198, a157\n"
"v_mul_f32 v198, v198, v199\n"
"v_accvgpr_write_b32 a157, v198\n"
"v_accvgpr_read_b32 v198, a158\n"
"v_mul_f32 v198, v198, v199\n"
"v_accvgpr_write_b32 a158, v198\n"
"v_accvgpr_read_b32 v198, a159\n"
"v_mul_f32 v198, v198, v199\n"
"v_accvgpr_write_b32 a159, v198\n"
"v_accvgpr_read_b32 v198, a180\n"
"v_mul_f32 v198, v198, v199\n"
"v_accvgpr_write_b32 a180, v198\n"
"v_accvgpr_read_b32 v198, a181\n"
"v_mul_f32 v198, v198, v199\n"
"v_accvgpr_write_b32 a181, v198\n"
"v_accvgpr_read_b32 v198, a182\n"
"v_mul_f32 v198, v198, v199\n"
"v_accvgpr_write_b32 a182, v198\n"
"v_accvgpr_read_b32 v198, a183\n"
"v_mul_f32 v198, v198, v199\n"
"v_accvgpr_write_b32 a183, v198\n"
"v_max3_f32 v202, v240, v241, v242\n"
"v_max3_f32 v202, v202, v243, v244\n"
"v_max3_f32 v202, v202, v245, v246\n"
"v_max_f32 v202, v202, v247\n"
"v_mov_b32 v199, v202\n"
"s_nop 1\n"
"v_permlane32_swap_b32 v202, v199\n"
"v_max_f32 v202, v202, v199\n"
"v_mov_b32 v199, v202\n"
"s_nop 1\n"
"v_permlane16_swap_b32 v202, v199\n"
"v_max_f32 v202, v202, v199\n"
"v_sub_f32 v202, 0xc1800000, v202\n"
"v_min_f32 v202, v202, v160\n"
"v_sub_f32 v200, v202, v160\n"
"v_exp_f32 v199, v200\n"
"v_mov_b32 v160, v202\n"
"v_mov_b32 v161, v202\n"
"v_mov_b32 v162, v202\n"
"v_mov_b32 v163, v202\n"
"v_add_f32 v240, v240, v202\n"
"v_add_f32 v241, v241, v202\n"
"v_add_f32 v242, v242, v202\n"
"v_add_f32 v243, v243, v202\n"
"v_add_f32 v244, v244, v202\n"
"v_add_f32 v245, v245, v202\n"
"v_add_f32 v246, v246, v202\n"
"v_add_f32 v247, v247, v202\n"
"v_add_f32 v128, v128, v200\n"
"v_add_f32 v129, v129, v200\n"
"v_add_f32 v130, v130, v200\n"
"v_add_f32 v131, v131, v200\n"
"v_add_f32 v132, v132, v200\n"
"v_add_f32 v133, v133, v200\n"
"v_add_f32 v134, v134, v200\n"
"v_add_f32 v135, v135, v200\n"
"v_mul_f32 v184, v184, v199\n"
"v_mul_f32 v185, v185, v199\n"
"v_mul_f32 v186, v186, v199\n"
"v_mul_f32 v187, v187, v199\n"
"v_accvgpr_read_b32 v198, a16\n"
"v_mul_f32 v198, v198, v199\n"
"v_accvgpr_write_b32 a16, v198\n"
"v_accvgpr_read_b32 v198, a17\n"
"v_mul_f32 v198, v198, v199\n"
"v_accvgpr_write_b32 a17, v198\n"
"v_accvgpr_read_b32 v198, a18\n"
"v_mul_f32 v198, v198, v199\n"
"v_accvgpr_write_b32 a18, v198\n"
"v_accvgpr_read_b32 v198, a19\n"
"v_mul_f32 v198, v198, v199\n"
"v_accvgpr_write_b32 a19, v198\n"
"v_accvgpr_read_b32 v198, a40\n"
"v_mul_f32 v198, v198, v199\n"
"v_accvgpr_write_b32 a40, v198\n"
"v_accvgpr_read_b32 v198, a41\n"
"v_mul_f32 v198, v198, v199\n"
"v_accvgpr_write_b32 a41, v198\n"
"v_accvgpr_read_b32 v198, a42\n"
"v_mul_f32 v198, v198, v199\n"
"v_accvgpr_write_b32 a42, v198\n"
"v_accvgpr_read_b32 v198, a43\n"
"v_mul_f32 v198, v198, v199\n"
"v_accvgpr_write_b32 a43, v198\n"
"v_accvgpr_read_b32 v198, a64\n"
"v_mul_f32 v198, v198, v199\n"
"v_accvgpr_write_b32 a64, v198\n"
"v_accvgpr_read_b32 v198, a65\n"
"v_mul_f32 v198, v198, v199\n"
"v_accvgpr_write_b32 a65, v198\n"
"v_accvgpr_read_b32 v198, a66\n"
"v_mul_f32 v198, v198, v199\n"
"v_accvgpr_write_b32 a66, v198\n"
"v_accvgpr_read_b32 v198, a67\n"
"v_mul_f32 v198, v198, v199\n"
"v_accvgpr_write_b32 a67, v198\n"
"v_accvgpr_read_b32 v198, a88\n"
"v_mul_f32 v198, v198, v199\n"
"v_accvgpr_write_b32 a88, v198\n"
"v_accvgpr_read_b32 v198, a89\n"
"v_mul_f32 v198, v198, v199\n"
"v_accvgpr_write_b32 a89, v198\n"
"v_accvgpr_read_b32 v198, a90\n"
"v_mul_f32 v198, v198, v199\n"
"v_accvgpr_write_b32 a90, v198\n"
"v_accvgpr_read_b32 v198, a91\n"
"v_mul_f32 v198, v198, v199\n"
"v_accvgpr_write_b32 a91, v198\n"
"v_accvgpr_read_b32 v198, a112\n"
"v_mul_f32 v198, v198, v199\n"
"v_accvgpr_write_b32 a112, v198\n"
"v_accvgpr_read_b32 v198, a113\n"
"v_mul_f32 v198, v198, v199\n"
"v_accvgpr_write_b32 a113, v198\n"
"v_accvgpr_read_b32 v198, a114\n"
"v_mul_f32 v198, v198, v199\n"
"v_accvgpr_write_b32 a114, v198\n"
"v_accvgpr_read_b32 v198, a115\n"
"v_mul_f32 v198, v198, v199\n"
"v_accvgpr_write_b32 a115, v198\n"
"v_accvgpr_read_b32 v198, a136\n"
"v_mul_f32 v198, v198, v199\n"
"v_accvgpr_write_b32 a136, v198\n"
"v_accvgpr_read_b32 v198, a137\n"
"v_mul_f32 v198, v198, v199\n"
"v_accvgpr_write_b32 a137, v198\n"
"v_accvgpr_read_b32 v198, a138\n"
"v_mul_f32 v198, v198, v199\n"
"v_accvgpr_write_b32 a138, v198\n"
"v_accvgpr_read_b32 v198, a139\n"
"v_mul_f32 v198, v198, v199\n"
"v_accvgpr_write_b32 a139, v198\n"
"v_accvgpr_read_b32 v198, a160\n"
"v_mul_f32 v198, v198, v199\n"
"v_accvgpr_write_b32 a160, v198\n"
"v_accvgpr_read_b32 v198, a161\n"
"v_mul_f32 v198, v198, v199\n"
"v_accvgpr_write_b32 a161, v198\n"
"v_accvgpr_read_b32 v198, a162\n"
"v_mul_f32 v198, v198, v199\n"
"v_accvgpr_write_b32 a162, v198\n"
"v_accvgpr_read_b32 v198, a163\n"
"v_mul_f32 v198, v198, v199\n"
"v_accvgpr_write_b32 a163, v198\n"
"v_accvgpr_read_b32 v198, a184\n"
"v_mul_f32 v198, v198, v199\n"
"v_accvgpr_write_b32 a184, v198\n"
"v_accvgpr_read_b32 v198, a185\n"
"v_mul_f32 v198, v198, v199\n"
"v_accvgpr_write_b32 a185, v198\n"
"v_accvgpr_read_b32 v198, a186\n"
"v_mul_f32 v198, v198, v199\n"
"v_accvgpr_write_b32 a186, v198\n"
"v_accvgpr_read_b32 v198, a187\n"
"v_mul_f32 v198, v198, v199\n"
"v_accvgpr_write_b32 a187, v198\n"
"v_max3_f32 v202, v248, v249, v250\n"
"v_max3_f32 v202, v202, v251, v252\n"
"v_max3_f32 v202, v202, v253, v254\n"
"v_max_f32 v202, v202, v255\n"
"v_mov_b32 v199, v202\n"
"s_nop 1\n"
"v_permlane32_swap_b32 v202, v199\n"
"v_max_f32 v202, v202, v199\n"
"v_mov_b32 v199, v202\n"
"s_nop 1\n"
"v_permlane16_swap_b32 v202, v199\n"
"v_max_f32 v202, v202, v199\n"
"v_sub_f32 v202, 0xc1800000, v202\n"
"v_min_f32 v202, v202, v164\n"
"v_sub_f32 v200, v202, v164\n"
"v_exp_f32 v199, v200\n"
"v_mov_b32 v164, v202\n"
"v_mov_b32 v165, v202\n"
"v_mov_b32 v166, v202\n"
"v_mov_b32 v167, v202\n"
"v_add_f32 v248, v248, v202\n"
"v_add_f32 v249, v249, v202\n"
"v_add_f32 v250, v250, v202\n"
"v_add_f32 v251, v251, v202\n"
"v_add_f32 v252, v252, v202\n"
"v_add_f32 v253, v253, v202\n"
"v_add_f32 v254, v254, v202\n"
"v_add_f32 v255, v255, v202\n"
"v_add_f32 v136, v136, v200\n"
"v_add_f32 v137, v137, v200\n"
"v_add_f32 v138, v138, v200\n"
"v_add_f32 v139, v139, v200\n"
"v_add_f32 v140, v140, v200\n"
"v_add_f32 v141, v141, v200\n"
"v_add_f32 v142, v142, v200\n"
"v_add_f32 v143, v143, v200\n"
"v_mul_f32 v188, v188, v199\n"
"v_mul_f32 v189, v189, v199\n"
"v_mul_f32 v190, v190, v199\n"
"v_mul_f32 v191, v191, v199\n"
"v_accvgpr_read_b32 v198, a20\n"
"v_mul_f32 v198, v198, v199\n"
"v_accvgpr_write_b32 a20, v198\n"
"v_accvgpr_read_b32 v198, a21\n"
"v_mul_f32 v198, v198, v199\n"
"v_accvgpr_write_b32 a21, v198\n"
"v_accvgpr_read_b32 v198, a22\n"
"v_mul_f32 v198, v198, v199\n"
"v_accvgpr_write_b32 a22, v198\n"
"v_accvgpr_read_b32 v198, a23\n"
"v_mul_f32 v198, v198, v199\n"
"v_accvgpr_write_b32 a23, v198\n"
"v_accvgpr_read_b32 v198, a44\n"
"v_mul_f32 v198, v198, v199\n"
"v_accvgpr_write_b32 a44, v198\n"
"v_accvgpr_read_b32 v198, a45\n"
"v_mul_f32 v198, v198, v199\n"
"v_accvgpr_write_b32 a45, v198\n"
"v_accvgpr_read_b32 v198, a46\n"
"v_mul_f32 v198, v198, v199\n"
"v_accvgpr_write_b32 a46, v198\n"
"v_accvgpr_read_b32 v198, a47\n"
"v_mul_f32 v198, v198, v199\n"
"v_accvgpr_write_b32 a47, v198\n"
"v_accvgpr_read_b32 v198, a68\n"
"v_mul_f32 v198, v198, v199\n"
"v_accvgpr_write_b32 a68, v198\n"
"v_accvgpr_read_b32 v198, a69\n"
"v_mul_f32 v198, v198, v199\n"
"v_accvgpr_write_b32 a69, v198\n"
"v_accvgpr_read_b32 v198, a70\n"
"v_mul_f32 v198, v198, v199\n"
"v_accvgpr_write_b32 a70, v198\n"
"v_accvgpr_read_b32 v198, a71\n"
"v_mul_f32 v198, v198, v199\n"
"v_accvgpr_write_b32 a71, v198\n"
"v_accvgpr_read_b32 v198, a92\n"
"v_mul_f32 v198, v198, v199\n"
"v_accvgpr_write_b32 a92, v198\n"
"v_accvgpr_read_b32 v198, a93\n"
"v_mul_f32 v198, v198, v199\n"
"v_accvgpr_write_b32 a93, v198\n"
"v_accvgpr_read_b32 v198, a94\n"
"v_mul_f32 v198, v198, v199\n"
"v_accvgpr_write_b32 a94, v198\n"
"v_accvgpr_read_b32 v198, a95\n"
"v_mul_f32 v198, v198, v199\n"
"v_accvgpr_write_b32 a95, v198\n"
"v_accvgpr_read_b32 v198, a116\n"
"v_mul_f32 v198, v198, v199\n"
"v_accvgpr_write_b32 a116, v198\n"
"v_accvgpr_read_b32 v198, a117\n"
"v_mul_f32 v198, v198, v199\n"
"v_accvgpr_write_b32 a117, v198\n"
"v_accvgpr_read_b32 v198, a118\n"
"v_mul_f32 v198, v198, v199\n"
"v_accvgpr_write_b32 a118, v198\n"
"v_accvgpr_read_b32 v198, a119\n"
"v_mul_f32 v198, v198, v199\n"
"v_accvgpr_write_b32 a119, v198\n"
"v_accvgpr_read_b32 v198, a140\n"
"v_mul_f32 v198, v198, v199\n"
"v_accvgpr_write_b32 a140, v198\n"
"v_accvgpr_read_b32 v198, a141\n"
"v_mul_f32 v198, v198, v199\n"
"v_accvgpr_write_b32 a141, v198\n"
"v_accvgpr_read_b32 v198, a142\n"
"v_mul_f32 v198, v198, v199\n"
"v_accvgpr_write_b32 a142, v198\n"
"v_accvgpr_read_b32 v198, a143\n"
"v_mul_f32 v198, v198, v199\n"
"v_accvgpr_write_b32 a143, v198\n"
"v_accvgpr_read_b32 v198, a164\n"
"v_mul_f32 v198, v198, v199\n"
"v_accvgpr_write_b32 a164, v198\n"
"v_accvgpr_read_b32 v198, a165\n"
"v_mul_f32 v198, v198, v199\n"
"v_accvgpr_write_b32 a165, v198\n"
"v_accvgpr_read_b32 v198, a166\n"
"v_mul_f32 v198, v198, v199\n"
"v_accvgpr_write_b32 a166, v198\n"
"v_accvgpr_read_b32 v198, a167\n"
"v_mul_f32 v198, v198, v199\n"
"v_accvgpr_write_b32 a167, v198\n"
"v_accvgpr_read_b32 v198, a188\n"
"v_mul_f32 v198, v198, v199\n"
"v_accvgpr_write_b32 a188, v198\n"
"v_accvgpr_read_b32 v198, a189\n"
"v_mul_f32 v198, v198, v199\n"
"v_accvgpr_write_b32 a189, v198\n"
"v_accvgpr_read_b32 v198, a190\n"
"v_mul_f32 v198, v198, v199\n"
"v_accvgpr_write_b32 a190, v198\n"
"v_accvgpr_read_b32 v198, a191\n"
"v_mul_f32 v198, v198, v199\n"
"v_accvgpr_write_b32 a191, v198\n"
"v_exp_f32 v208, v208\n"
"v_exp_f32 v209, v209\n"
"v_exp_f32 v210, v210\n"
"v_exp_f32 v211, v211\n"
"v_cvt_pk_bf16_f32 v208, v208, v209\n"
"v_cvt_pk_bf16_f32 v209, v210, v211\n"
"v_or_b32 v198, v208, v209\n"
"v_exp_f32 v212, v212\n"
"v_exp_f32 v213, v213\n"
"v_exp_f32 v214, v214\n"
"v_exp_f32 v215, v215\n"
"v_cvt_pk_bf16_f32 v210, v212, v213\n"
"v_cvt_pk_bf16_f32 v211, v214, v215\n"
"v_or3_b32 v198, v198, v210, v211\n"
"v_exp_f32 v216, v216\n"
"v_exp_f32 v217, v217\n"
"v_exp_f32 v218, v218\n"
"v_exp_f32 v219, v219\n"
"v_cvt_pk_bf16_f32 v216, v216, v217\n"
"v_cvt_pk_bf16_f32 v217, v218, v219\n"
"v_or3_b32 v198, v198, v216, v217\n"
"v_exp_f32 v220, v220\n"
"v_exp_f32 v221, v221\n"
"v_exp_f32 v222, v222\n"
"v_exp_f32 v223, v223\n"
"v_cvt_pk_bf16_f32 v218, v220, v221\n"
"v_cvt_pk_bf16_f32 v219, v222, v223\n"
"v_or3_b32 v198, v198, v218, v219\n"
"v_exp_f32 v224, v224\n"
"v_exp_f32 v225, v225\n"
"v_exp_f32 v226, v226\n"
"v_exp_f32 v227, v227\n"
"v_cvt_pk_bf16_f32 v224, v224, v225\n"
"v_cvt_pk_bf16_f32 v225, v226, v227\n"
"v_or3_b32 v198, v198, v224, v225\n"
"v_exp_f32 v228, v228\n"
"v_exp_f32 v229, v229\n"
"v_exp_f32 v230, v230\n"
"v_exp_f32 v231, v231\n"
"v_cvt_pk_bf16_f32 v226, v228, v229\n"
"v_cvt_pk_bf16_f32 v227, v230, v231\n"
"v_or3_b32 v198, v198, v226, v227\n"
"v_exp_f32 v232, v232\n"
"v_exp_f32 v233, v233\n"
"v_exp_f32 v234, v234\n"
"v_exp_f32 v235, v235\n"
"v_cvt_pk_bf16_f32 v232, v232, v233\n"
"v_cvt_pk_bf16_f32 v233, v234, v235\n"
"v_or3_b32 v198, v198, v232, v233\n"
"v_exp_f32 v236, v236\n"
"v_exp_f32 v237, v237\n"
"v_exp_f32 v238, v238\n"
"v_exp_f32 v239, v239\n"
"v_cvt_pk_bf16_f32 v234, v236, v237\n"
"v_cvt_pk_bf16_f32 v235, v238, v239\n"
"v_or3_b32 v198, v198, v234, v235\n"
"v_exp_f32 v240, v240\n"
"v_exp_f32 v241, v241\n"
"v_exp_f32 v242, v242\n"
"v_exp_f32 v243, v243\n"
"v_cvt_pk_bf16_f32 v240, v240, v241\n"
"v_cvt_pk_bf16_f32 v241, v242, v243\n"
"v_or3_b32 v198, v198, v240, v241\n"
"v_exp_f32 v244, v244\n"
"v_exp_f32 v245, v245\n"
"v_exp_f32 v246, v246\n"
"v_exp_f32 v247, v247\n"
"v_cvt_pk_bf16_f32 v242, v244, v245\n"
"v_cvt_pk_bf16_f32 v243, v246, v247\n"
"v_or3_b32 v198, v198, v242, v243\n"
"v_exp_f32 v248, v248\n"
"v_exp_f32 v249, v249\n"
"v_exp_f32 v250, v250\n"
"v_exp_f32 v251, v251\n"
"v_cvt_pk_bf16_f32 v248, v248, v249\n"
"v_cvt_pk_bf16_f32 v249, v250, v251\n"
"v_or3_b32 v198, v198, v248, v249\n"
"v_exp_f32 v252, v252\n"
"v_exp_f32 v253, v253\n"
"v_exp_f32 v254, v254\n"
"v_exp_f32 v255, v255\n"
"v_cvt_pk_bf16_f32 v250, v252, v253\n"
"v_cvt_pk_bf16_f32 v251, v254, v255\n"
"v_or3_b32 v198, v198, v250, v251\n"
"s_nop 3\n"
"s_setpc_b64 s[80:81]\n"
".Lrr11:\n"
"s_waitcnt vmcnt(0) lgkmcnt(0)\n"
"s_nop 15\n"
"s_nop 7\n"
"v_mbcnt_lo_u32_b32 v200, -1, 0\n"
"v_mbcnt_hi_u32_b32 v200, -1, v200\n"
"v_and_b32 v199, 15, v200\n"
"v_lshlrev_b32 v199, 8, v199\n"
"v_lshrrev_b32 v200, 4, v200\n"
"v_lshl_add_u32 v199, v200, 4, v199\n"
"v_add_u32 v200, 4096, v199\n"
"buffer_load_dwordx4 a[192:195], v199, s[24:27], s64 offen\n"
"buffer_load_dwordx4 a[196:199], v200, s[24:27], s64 offen\n"
"buffer_load_dwordx4 a[200:203], v199, s[24:27], s64 offen offset:64\n"
"buffer_load_dwordx4 a[204:207], v200, s[24:27], s64 offen offset:64\n"
"buffer_load_dwordx4 a[208:211], v199, s[24:27], s64 offen offset:128\n"
"buffer_load_dwordx4 a[212:215], v200, s[24:27], s64 offen offset:128\n"
"buffer_load_dwordx4 a[216:219], v199, s[24:27], s64 offen offset:192\n"
"buffer_load_dwordx4 a[220:223], v200, s[24:27], s64 offen offset:192\n"
"s_waitcnt vmcnt(0)\n"
"v_mfma_f32_16x16x32_bf16 v[96:99], a[192:195], v[0:3], 0\n"
"v_mfma_f32_16x16x32_bf16 v[104:107], a[192:195], v[4:7], 0\n"
"v_mfma_f32_16x16x32_bf16 v[112:115], a[192:195], v[8:11], 0\n"
"v_mfma_f32_16x16x32_bf16 v[120:123], a[192:195], v[12:15], 0\n"
"v_mfma_f32_16x16x32_bf16 v[128:131], a[192:195], v[16:19], 0\n"
"v_mfma_f32_16x16x32_bf16 v[136:139], a[192:195], v[20:23], 0\n"
"v_mfma_f32_16x16x32_bf16 v[100:103], a[196:199], v[0:3], 0\n"
"v_mfma_f32_16x16x32_bf16 v[108:111], a[196:199], v[4:7], 0\n"
"v_mfma_f32_16x16x32_bf16 v[116:119], a[196:199], v[8:11], 0\n"
"v_mfma_f32_16x16x32_bf16 v[124:127], a[196:199], v[12:15], 0\n"
"v_mfma_f32_16x16x32_bf16 v[132:135], a[196:199], v[16:19], 0\n"
"v_mfma_f32_16x16x32_bf16 v[140:143], a[196:199], v[20:23], 0\n"
"v_mfma_f32_16x16x32_bf16 v[96:99], a[200:203], v[24:27], v[96:99]\n"
"v_mfma_f32_16x16x32_bf16 v[104:107], a[200:203], v[28:31], v[104:107]\n"
"v_mfma_f32_16x16x32_bf16 v[112:115], a[200:203], v[32:35], v[112:115]\n"
"v_mfma_f32_16x16x32_bf16 v[120:123], a[200:203], v[36:39], v[120:123]\n"
"v_mfma_f32_16x16x32_bf16 v[128:131], a[200:203], v[40:43], v[128:131]\n"
"v_mfma_f32_16x16x32_bf16 v[136:139], a[200:203], v[44:47], v[136:139]\n"
"v_mfma_f32_16x16x32_bf16 v[100:103], a[204:207], v[24:27], v[100:103]\n"
"v_mfma_f32_16x16x32_bf16 v[108:111], a[204:207], v[28:31], v[108:111]\n"
"v_mfma_f32_16x16x32_bf16 v[116:119], a[204:207], v[32:35], v[116:119]\n"
"v_mfma_f32_16x16x32_bf16 v[124:127], a[204:207], v[36:39], v[124:127]\n"
"v_mfma_f32_16x16x32_bf16 v[132:135], a[204:207], v[40:43], v[132:135]\n"
"v_mfma_f32_16x16x32_bf16 v[140:143], a[204:207], v[44:47], v[140:143]\n"
"v_mfma_f32_16x16x32_bf16 v[96:99], a[208:211], v[48:51], v[96:99]\n"
"v_mfma_f32_16x16x32_bf16 v[104:107], a[208:211], v[52:55], v[104:107]\n"
"v_mfma_f32_16x16x32_bf16 v[112:115], a[208:211], v[56:59], v[112:115]\n"
"v_mfma_f32_16x16x32_bf16 v[120:123], a[208:211], v[60:63], v[120:123]\n"
"v_mfma_f32_16x16x32_bf16 v[128:131], a[208:211], v[64:67], v[128:131]\n"
"v_mfma_f32_16x16x32_bf16 v[136:139], a[208:211], v[68:71], v[136:139]\n"
"v_mfma_f32_16x16x32_bf16 v[100:103], a[212:215], v[48:51], v[100:103]\n"
"v_mfma_f32_16x16x32_bf16 v[108:111], a[212:215], v[52:55], v[108:111]\n"
"v_mfma_f32_16x16x32_bf16 v[116:119], a[212:215], v[56:59], v[116:119]\n"
"v_mfma_f32_16x16x32_bf16 v[124:127], a[212:215], v[60:63], v[124:127]\n"
"v_mfma_f32_16x16x32_bf16 v[132:135], a[212:215], v[64:67], v[132:135]\n"
"v_mfma_f32_16x16x32_bf16 v[140:143], a[212:215], v[68:71], v[140:143]\n"
"v_mfma_f32_16x16x32_bf16 v[96:99], a[216:219], v[72:75], v[96:99]\n"
"v_mfma_f32_16x16x32_bf16 v[104:107], a[216:219], v[76:79], v[104:107]\n"
"v_mfma_f32_16x16x32_bf16 v[112:115], a[216:219], v[80:83], v[112:115]\n"
"v_mfma_f32_16x16x32_bf16 v[120:123], a[216:219], v[84:87], v[120:123]\n"
"v_mfma_f32_16x16x32_bf16 v[128:131], a[216:219], v[88:91], v[128:131]\n"
"v_mfma_f32_16x16x32_bf16 v[136:139], a[216:219], v[92:95], v[136:139]\n"
"v_mfma_f32_16x16x32_bf16 v[100:103], a[220:223], v[72:75], v[100:103]\n"
"v_mfma_f32_16x16x32_bf16 v[108:111], a[220:223], v[76:79], v[108:111]\n"
"v_mfma_f32_16x16x32_bf16 v[116:119], a[220:223], v[80:83], v[116:119]\n"
"v_mfma_f32_16x16x32_bf16 v[124:127], a[220:223], v[84:87], v[124:127]\n"
"v_mfma_f32_16x16x32_bf16 v[132:135], a[220:223], v[88:91], v[132:135]\n"
"v_mfma_f32_16x16x32_bf16 v[140:143], a[220:223], v[92:95], v[140:143]\n"
"v_max3_f32 v202, v96, v97, v98\n"
"s_nop 1\n"
"v_max3_f32 v202, v202, v99, v100\n"
"v_max3_f32 v202, v202, v101, v102\n"
"v_max_f32 v202, v202, v103\n"
"v_mov_b32 v199, v202\n"
"s_nop 1\n"
"v_permlane32_swap_b32 v202, v199\n"
"v_max_f32 v202, v202, v199\n"
"v_mov_b32 v199, v202\n"
"s_nop 1\n"
"v_permlane16_swap_b32 v202, v199\n"
"v_max_f32 v202, v202, v199\n"
"v_sub_f32 v202, 0xc1800000, v202\n"
"v_min_f32 v202, v202, v144\n"
"v_sub_f32 v200, v202, v144\n"
"v_exp_f32 v199, v200\n"
"v_mov_b32 v144, v202\n"
"v_mov_b32 v145, v202\n"
"v_mov_b32 v146, v202\n"
"v_mov_b32 v147, v202\n"
"v_add_f32 v96, v96, v202\n"
"v_add_f32 v97, v97, v202\n"
"v_add_f32 v98, v98, v202\n"
"v_add_f32 v99, v99, v202\n"
"v_add_f32 v100, v100, v202\n"
"v_add_f32 v101, v101, v202\n"
"v_add_f32 v102, v102, v202\n"
"v_add_f32 v103, v103, v202\n"
"v_add_f32 v208, v208, v200\n"
"v_add_f32 v209, v209, v200\n"
"v_add_f32 v210, v210, v200\n"
"v_add_f32 v211, v211, v200\n"
"v_add_f32 v212, v212, v200\n"
"v_add_f32 v213, v213, v200\n"
"v_add_f32 v214, v214, v200\n"
"v_add_f32 v215, v215, v200\n"
"v_mul_f32 v168, v168, v199\n"
"v_mul_f32 v169, v169, v199\n"
"v_mul_f32 v170, v170, v199\n"
"v_mul_f32 v171, v171, v199\n"
"v_accvgpr_read_b32 v198, a0\n"
"v_mul_f32 v198, v198, v199\n"
"v_accvgpr_write_b32 a0, v198\n"
"v_accvgpr_read_b32 v198, a1\n"
"v_mul_f32 v198, v198, v199\n"
"v_accvgpr_write_b32 a1, v198\n"
"v_accvgpr_read_b32 v198, a2\n"
"v_mul_f32 v198, v198, v199\n"
"v_accvgpr_write_b32 a2, v198\n"
"v_accvgpr_read_b32 v198, a3\n"
"v_mul_f32 v198, v198, v199\n"
"v_accvgpr_write_b32 a3, v198\n"
"v_accvgpr_read_b32 v198, a24\n"
"v_mul_f32 v198, v198, v199\n"
"v_accvgpr_write_b32 a24, v198\n"
"v_accvgpr_read_b32 v198, a25\n"
"v_mul_f32 v198, v198, v199\n"
"v_accvgpr_write_b32 a25, v198\n"
"v_accvgpr_read_b32 v198, a26\n"
"v_mul_f32 v198, v198, v199\n"
"v_accvgpr_write_b32 a26, v198\n"
"v_accvgpr_read_b32 v198, a27\n"
"v_mul_f32 v198, v198, v199\n"
"v_accvgpr_write_b32 a27, v198\n"
"v_accvgpr_read_b32 v198, a48\n"
"v_mul_f32 v198, v198, v199\n"
"v_accvgpr_write_b32 a48, v198\n"
"v_accvgpr_read_b32 v198, a49\n"
"v_mul_f32 v198, v198, v199\n"
"v_accvgpr_write_b32 a49, v198\n"
"v_accvgpr_read_b32 v198, a50\n"
"v_mul_f32 v198, v198, v199\n"
"v_accvgpr_write_b32 a50, v198\n"
"v_accvgpr_read_b32 v198, a51\n"
"v_mul_f32 v198, v198, v199\n"
"v_accvgpr_write_b32 a51, v198\n"
"v_accvgpr_read_b32 v198, a72\n"
"v_mul_f32 v198, v198, v199\n"
"v_accvgpr_write_b32 a72, v198\n"
"v_accvgpr_read_b32 v198, a73\n"
"v_mul_f32 v198, v198, v199\n"
"v_accvgpr_write_b32 a73, v198\n"
"v_accvgpr_read_b32 v198, a74\n"
"v_mul_f32 v198, v198, v199\n"
"v_accvgpr_write_b32 a74, v198\n"
"v_accvgpr_read_b32 v198, a75\n"
"v_mul_f32 v198, v198, v199\n"
"v_accvgpr_write_b32 a75, v198\n"
"v_accvgpr_read_b32 v198, a96\n"
"v_mul_f32 v198, v198, v199\n"
"v_accvgpr_write_b32 a96, v198\n"
"v_accvgpr_read_b32 v198, a97\n"
"v_mul_f32 v198, v198, v199\n"
"v_accvgpr_write_b32 a97, v198\n"
"v_accvgpr_read_b32 v198, a98\n"
"v_mul_f32 v198, v198, v199\n"
"v_accvgpr_write_b32 a98, v198\n"
"v_accvgpr_read_b32 v198, a99\n"
"v_mul_f32 v198, v198, v199\n"
"v_accvgpr_write_b32 a99, v198\n"
"v_accvgpr_read_b32 v198, a120\n"
"v_mul_f32 v198, v198, v199\n"
"v_accvgpr_write_b32 a120, v198\n"
"v_accvgpr_read_b32 v198, a121\n"
"v_mul_f32 v198, v198, v199\n"
"v_accvgpr_write_b32 a121, v198\n"
"v_accvgpr_read_b32 v198, a122\n"
"v_mul_f32 v198, v198, v199\n"
"v_accvgpr_write_b32 a122, v198\n"
"v_accvgpr_read_b32 v198, a123\n"
"v_mul_f32 v198, v198, v199\n"
"v_accvgpr_write_b32 a123, v198\n"
"v_accvgpr_read_b32 v198, a144\n"
"v_mul_f32 v198, v198, v199\n"
"v_accvgpr_write_b32 a144, v198\n"
"v_accvgpr_read_b32 v198, a145\n"
"v_mul_f32 v198, v198, v199\n"
"v_accvgpr_write_b32 a145, v198\n"
"v_accvgpr_read_b32 v198, a146\n"
"v_mul_f32 v198, v198, v199\n"
"v_accvgpr_write_b32 a146, v198\n"
"v_accvgpr_read_b32 v198, a147\n"
"v_mul_f32 v198, v198, v199\n"
"v_accvgpr_write_b32 a147, v198\n"
"v_accvgpr_read_b32 v198, a168\n"
"v_mul_f32 v198, v198, v199\n"
"v_accvgpr_write_b32 a168, v198\n"
"v_accvgpr_read_b32 v198, a169\n"
"v_mul_f32 v198, v198, v199\n"
"v_accvgpr_write_b32 a169, v198\n"
"v_accvgpr_read_b32 v198, a170\n"
"v_mul_f32 v198, v198, v199\n"
"v_accvgpr_write_b32 a170, v198\n"
"v_accvgpr_read_b32 v198, a171\n"
"v_mul_f32 v198, v198, v199\n"
"v_accvgpr_write_b32 a171, v198\n"
"v_max3_f32 v202, v104, v105, v106\n"
"v_max3_f32 v202, v202, v107, v108\n"
"v_max3_f32 v202, v202, v109, v110\n"
"v_max_f32 v202, v202, v111\n"
"v_mov_b32 v199, v202\n"
"s_nop 1\n"
"v_permlane32_swap_b32 v202, v199\n"
"v_max_f32 v202, v202, v199\n"
"v_mov_b32 v199, v202\n"
"s_nop 1\n"
"v_permlane16_swap_b32 v202, v199\n"
"v_max_f32 v202, v202, v199\n"
"v_sub_f32 v202, 0xc1800000, v202\n"
"v_min_f32 v202, v202, v148\n"
"v_sub_f32 v200, v202, v148\n"
"v_exp_f32 v199, v200\n"
"v_mov_b32 v148, v202\n"
"v_mov_b32 v149, v202\n"
"v_mov_b32 v150, v202\n"
"v_mov_b32 v151, v202\n"
"v_add_f32 v104, v104, v202\n"
"v_add_f32 v105, v105, v202\n"
"v_add_f32 v106, v106, v202\n"
"v_add_f32 v107, v107, v202\n"
"v_add_f32 v108, v108, v202\n"
"v_add_f32 v109, v109, v202\n"
"v_add_f32 v110, v110, v202\n"
"v_add_f32 v111, v111, v202\n"
"v_add_f32 v216, v216, v200\n"
"v_add_f32 v217, v217, v200\n"
"v_add_f32 v218, v218, v200\n"
"v_add_f32 v219, v219, v200\n"
"v_add_f32 v220, v220, v200\n"
"v_add_f32 v221, v221, v200\n"
"v_add_f32 v222, v222, v200\n"
"v_add_f32 v223, v223, v200\n"
"v_mul_f32 v172, v172, v199\n"
"v_mul_f32 v173, v173, v199\n"
"v_mul_f32 v174, v174, v199\n"
"v_mul_f32 v175, v175, v199\n"
"v_accvgpr_read_b32 v198, a4\n"
"v_mul_f32 v198, v198, v199\n"
"v_accvgpr_write_b32 a4, v198\n"
"v_accvgpr_read_b32 v198, a5\n"
"v_mul_f32 v198, v198, v199\n"
"v_accvgpr_write_b32 a5, v198\n"
"v_accvgpr_read_b32 v198, a6\n"
"v_mul_f32 v198, v198, v199\n"
"v_accvgpr_write_b32 a6, v198\n"
"v_accvgpr_read_b32 v198, a7\n"
"v_mul_f32 v198, v198, v199\n"
"v_accvgpr_write_b32 a7, v198\n"
"v_accvgpr_read_b32 v198, a28\n"
"v_mul_f32 v198, v198, v199\n"
"v_accvgpr_write_b32 a28, v198\n"
"v_accvgpr_read_b32 v198, a29\n"
"v_mul_f32 v198, v198, v199\n"
"v_accvgpr_write_b32 a29, v198\n"
"v_accvgpr_read_b32 v198, a30\n"
"v_mul_f32 v198, v198, v199\n"
"v_accvgpr_write_b32 a30, v198\n"
"v_accvgpr_read_b32 v198, a31\n"
"v_mul_f32 v198, v198, v199\n"
"v_accvgpr_write_b32 a31, v198\n"
"v_accvgpr_read_b32 v198, a52\n"
"v_mul_f32 v198, v198, v199\n"
"v_accvgpr_write_b32 a52, v198\n"
"v_accvgpr_read_b32 v198, a53\n"
"v_mul_f32 v198, v198, v199\n"
"v_accvgpr_write_b32 a53, v198\n"
"v_accvgpr_read_b32 v198, a54\n"
"v_mul_f32 v198, v198, v199\n"
"v_accvgpr_write_b32 a54, v198\n"
"v_accvgpr_read_b32 v198, a55\n"
"v_mul_f32 v198, v198, v199\n"
"v_accvgpr_write_b32 a55, v198\n"
"v_accvgpr_read_b32 v198, a76\n"
"v_mul_f32 v198, v198, v199\n"
"v_accvgpr_write_b32 a76, v198\n"
"v_accvgpr_read_b32 v198, a77\n"
"v_mul_f32 v198, v198, v199\n"
"v_accvgpr_write_b32 a77, v198\n"
"v_accvgpr_read_b32 v198, a78\n"
"v_mul_f32 v198, v198, v199\n"
"v_accvgpr_write_b32 a78, v198\n"
"v_accvgpr_read_b32 v198, a79\n"
"v_mul_f32 v198, v198, v199\n"
"v_accvgpr_write_b32 a79, v198\n"
"v_accvgpr_read_b32 v198, a100\n"
"v_mul_f32 v198, v198, v199\n"
"v_accvgpr_write_b32 a100, v198\n"
"v_accvgpr_read_b32 v198, a101\n"
"v_mul_f32 v198, v198, v199\n"
"v_accvgpr_write_b32 a101, v198\n"
"v_accvgpr_read_b32 v198, a102\n"
"v_mul_f32 v198, v198, v199\n"
"v_accvgpr_write_b32 a102, v198\n"
"v_accvgpr_read_b32 v198, a103\n"
"v_mul_f32 v198, v198, v199\n"
"v_accvgpr_write_b32 a103, v198\n"
"v_accvgpr_read_b32 v198, a124\n"
"v_mul_f32 v198, v198, v199\n"
"v_accvgpr_write_b32 a124, v198\n"
"v_accvgpr_read_b32 v198, a125\n"
"v_mul_f32 v198, v198, v199\n"
"v_accvgpr_write_b32 a125, v198\n"
"v_accvgpr_read_b32 v198, a126\n"
"v_mul_f32 v198, v198, v199\n"
"v_accvgpr_write_b32 a126, v198\n"
"v_accvgpr_read_b32 v198, a127\n"
"v_mul_f32 v198, v198, v199\n"
"v_accvgpr_write_b32 a127, v198\n"
"v_accvgpr_read_b32 v198, a148\n"
"v_mul_f32 v198, v198, v199\n"
"v_accvgpr_write_b32 a148, v198\n"
"v_accvgpr_read_b32 v198, a149\n"
"v_mul_f32 v198, v198, v199\n"
"v_accvgpr_write_b32 a149, v198\n"
"v_accvgpr_read_b32 v198, a150\n"
"v_mul_f32 v198, v198, v199\n"
"v_accvgpr_write_b32 a150, v198\n"
"v_accvgpr_read_b32 v198, a151\n"
"v_mul_f32 v198, v198, v199\n"
"v_accvgpr_write_b32 a151, v198\n"
"v_accvgpr_read_b32 v198, a172\n"
"v_mul_f32 v198, v198, v199\n"
"v_accvgpr_write_b32 a172, v198\n"
"v_accvgpr_read_b32 v198, a173\n"
"v_mul_f32 v198, v198, v199\n"
"v_accvgpr_write_b32 a173, v198\n"
"v_accvgpr_read_b32 v198, a174\n"
"v_mul_f32 v198, v198, v199\n"
"v_accvgpr_write_b32 a174, v198\n"
"v_accvgpr_read_b32 v198, a175\n"
"v_mul_f32 v198, v198, v199\n"
"v_accvgpr_write_b32 a175, v198\n"
"v_max3_f32 v202, v112, v113, v114\n"
"v_max3_f32 v202, v202, v115, v116\n"
"v_max3_f32 v202, v202, v117, v118\n"
"v_max_f32 v202, v202, v119\n"
"v_mov_b32 v199, v202\n"
"s_nop 1\n"
"v_permlane32_swap_b32 v202, v199\n"
"v_max_f32 v202, v202, v199\n"
"v_mov_b32 v199, v202\n"
"s_nop 1\n"
"v_permlane16_swap_b32 v202, v199\n"
"v_max_f32 v202, v202, v199\n"
"v_sub_f32 v202, 0xc1800000, v202\n"
"v_min_f32 v202, v202, v152\n"
"v_sub_f32 v200, v202, v152\n"
"v_exp_f32 v199, v200\n"
"v_mov_b32 v152, v202\n"
"v_mov_b32 v153, v202\n"
"v_mov_b32 v154, v202\n"
"v_mov_b32 v155, v202\n"
"v_add_f32 v112, v112, v202\n"
"v_add_f32 v113, v113, v202\n"
"v_add_f32 v114, v114, v202\n"
"v_add_f32 v115, v115, v202\n"
"v_add_f32 v116, v116, v202\n"
"v_add_f32 v117, v117, v202\n"
"v_add_f32 v118, v118, v202\n"
"v_add_f32 v119, v119, v202\n"
"v_add_f32 v224, v224, v200\n"
"v_add_f32 v225, v225, v200\n"
"v_add_f32 v226, v226, v200\n"
"v_add_f32 v227, v227, v200\n"
"v_add_f32 v228, v228, v200\n"
"v_add_f32 v229, v229, v200\n"
"v_add_f32 v230, v230, v200\n"
"v_add_f32 v231, v231, v200\n"
"v_mul_f32 v176, v176, v199\n"
"v_mul_f32 v177, v177, v199\n"
"v_mul_f32 v178, v178, v199\n"
"v_mul_f32 v179, v179, v199\n"
"v_accvgpr_read_b32 v198, a8\n"
"v_mul_f32 v198, v198, v199\n"
"v_accvgpr_write_b32 a8, v198\n"
"v_accvgpr_read_b32 v198, a9\n"
"v_mul_f32 v198, v198, v199\n"
"v_accvgpr_write_b32 a9, v198\n"
"v_accvgpr_read_b32 v198, a10\n"
"v_mul_f32 v198, v198, v199\n"
"v_accvgpr_write_b32 a10, v198\n"
"v_accvgpr_read_b32 v198, a11\n"
"v_mul_f32 v198, v198, v199\n"
"v_accvgpr_write_b32 a11, v198\n"
"v_accvgpr_read_b32 v198, a32\n"
"v_mul_f32 v198, v198, v199\n"
"v_accvgpr_write_b32 a32, v198\n"
"v_accvgpr_read_b32 v198, a33\n"
"v_mul_f32 v198, v198, v199\n"
"v_accvgpr_write_b32 a33, v198\n"
"v_accvgpr_read_b32 v198, a34\n"
"v_mul_f32 v198, v198, v199\n"
"v_accvgpr_write_b32 a34, v198\n"
"v_accvgpr_read_b32 v198, a35\n"
"v_mul_f32 v198, v198, v199\n"
"v_accvgpr_write_b32 a35, v198\n"
"v_accvgpr_read_b32 v198, a56\n"
"v_mul_f32 v198, v198, v199\n"
"v_accvgpr_write_b32 a56, v198\n"
"v_accvgpr_read_b32 v198, a57\n"
"v_mul_f32 v198, v198, v199\n"
"v_accvgpr_write_b32 a57, v198\n"
"v_accvgpr_read_b32 v198, a58\n"
"v_mul_f32 v198, v198, v199\n"
"v_accvgpr_write_b32 a58, v198\n"
"v_accvgpr_read_b32 v198, a59\n"
"v_mul_f32 v198, v198, v199\n"
"v_accvgpr_write_b32 a59, v198\n"
"v_accvgpr_read_b32 v198, a80\n"
"v_mul_f32 v198, v198, v199\n"
"v_accvgpr_write_b32 a80, v198\n"
"v_accvgpr_read_b32 v198, a81\n"
"v_mul_f32 v198, v198, v199\n"
"v_accvgpr_write_b32 a81, v198\n"
"v_accvgpr_read_b32 v198, a82\n"
"v_mul_f32 v198, v198, v199\n"
"v_accvgpr_write_b32 a82, v198\n"
"v_accvgpr_read_b32 v198, a83\n"
"v_mul_f32 v198, v198, v199\n"
"v_accvgpr_write_b32 a83, v198\n"
"v_accvgpr_read_b32 v198, a104\n"
"v_mul_f32 v198, v198, v199\n"
"v_accvgpr_write_b32 a104, v198\n"
"v_accvgpr_read_b32 v198, a105\n"
"v_mul_f32 v198, v198, v199\n"
"v_accvgpr_write_b32 a105, v198\n"
"v_accvgpr_read_b32 v198, a106\n"
"v_mul_f32 v198, v198, v199\n"
"v_accvgpr_write_b32 a106, v198\n"
"v_accvgpr_read_b32 v198, a107\n"
"v_mul_f32 v198, v198, v199\n"
"v_accvgpr_write_b32 a107, v198\n"
"v_accvgpr_read_b32 v198, a128\n"
"v_mul_f32 v198, v198, v199\n"
"v_accvgpr_write_b32 a128, v198\n"
"v_accvgpr_read_b32 v198, a129\n"
"v_mul_f32 v198, v198, v199\n"
"v_accvgpr_write_b32 a129, v198\n"
"v_accvgpr_read_b32 v198, a130\n"
"v_mul_f32 v198, v198, v199\n"
"v_accvgpr_write_b32 a130, v198\n"
"v_accvgpr_read_b32 v198, a131\n"
"v_mul_f32 v198, v198, v199\n"
"v_accvgpr_write_b32 a131, v198\n"
"v_accvgpr_read_b32 v198, a152\n"
"v_mul_f32 v198, v198, v199\n"
"v_accvgpr_write_b32 a152, v198\n"
"v_accvgpr_read_b32 v198, a153\n"
"v_mul_f32 v198, v198, v199\n"
"v_accvgpr_write_b32 a153, v198\n"
"v_accvgpr_read_b32 v198, a154\n"
"v_mul_f32 v198, v198, v199\n"
"v_accvgpr_write_b32 a154, v198\n"
"v_accvgpr_read_b32 v198, a155\n"
"v_mul_f32 v198, v198, v199\n"
"v_accvgpr_write_b32 a155, v198\n"
"v_accvgpr_read_b32 v198, a176\n"
"v_mul_f32 v198, v198, v199\n"
"v_accvgpr_write_b32 a176, v198\n"
"v_accvgpr_read_b32 v198, a177\n"
"v_mul_f32 v198, v198, v199\n"
"v_accvgpr_write_b32 a177, v198\n"
"v_accvgpr_read_b32 v198, a178\n"
"v_mul_f32 v198, v198, v199\n"
"v_accvgpr_write_b32 a178, v198\n"
"v_accvgpr_read_b32 v198, a179\n"
"v_mul_f32 v198, v198, v199\n"
"v_accvgpr_write_b32 a179, v198\n"
"v_max3_f32 v202, v120, v121, v122\n"
"v_max3_f32 v202, v202, v123, v124\n"
"v_max3_f32 v202, v202, v125, v126\n"
"v_max_f32 v202, v202, v127\n"
"v_mov_b32 v199, v202\n"
"s_nop 1\n"
"v_permlane32_swap_b32 v202, v199\n"
"v_max_f32 v202, v202, v199\n"
"v_mov_b32 v199, v202\n"
"s_nop 1\n"
"v_permlane16_swap_b32 v202, v199\n"
"v_max_f32 v202, v202, v199\n"
"v_sub_f32 v202, 0xc1800000, v202\n"
"v_min_f32 v202, v202, v156\n"
"v_sub_f32 v200, v202, v156\n"
"v_exp_f32 v199, v200\n"
"v_mov_b32 v156, v202\n"
"v_mov_b32 v157, v202\n"
"v_mov_b32 v158, v202\n"
"v_mov_b32 v159, v202\n"
"v_add_f32 v120, v120, v202\n"
"v_add_f32 v121, v121, v202\n"
"v_add_f32 v122, v122, v202\n"
"v_add_f32 v123, v123, v202\n"
"v_add_f32 v124, v124, v202\n"
"v_add_f32 v125, v125, v202\n"
"v_add_f32 v126, v126, v202\n"
"v_add_f32 v127, v127, v202\n"
"v_add_f32 v232, v232, v200\n"
"v_add_f32 v233, v233, v200\n"
"v_add_f32 v234, v234, v200\n"
"v_add_f32 v235, v235, v200\n"
"v_add_f32 v236, v236, v200\n"
"v_add_f32 v237, v237, v200\n"
"v_add_f32 v238, v238, v200\n"
"v_add_f32 v239, v239, v200\n"
"v_mul_f32 v180, v180, v199\n"
"v_mul_f32 v181, v181, v199\n"
"v_mul_f32 v182, v182, v199\n"
"v_mul_f32 v183, v183, v199\n"
"v_accvgpr_read_b32 v198, a12\n"
"v_mul_f32 v198, v198, v199\n"
"v_accvgpr_write_b32 a12, v198\n"
"v_accvgpr_read_b32 v198, a13\n"
"v_mul_f32 v198, v198, v199\n"
"v_accvgpr_write_b32 a13, v198\n"
"v_accvgpr_read_b32 v198, a14\n"
"v_mul_f32 v198, v198, v199\n"
"v_accvgpr_write_b32 a14, v198\n"
"v_accvgpr_read_b32 v198, a15\n"
"v_mul_f32 v198, v198, v199\n"
"v_accvgpr_write_b32 a15, v198\n"
"v_accvgpr_read_b32 v198, a36\n"
"v_mul_f32 v198, v198, v199\n"
"v_accvgpr_write_b32 a36, v198\n"
"v_accvgpr_read_b32 v198, a37\n"
"v_mul_f32 v198, v198, v199\n"
"v_accvgpr_write_b32 a37, v198\n"
"v_accvgpr_read_b32 v198, a38\n"
"v_mul_f32 v198, v198, v199\n"
"v_accvgpr_write_b32 a38, v198\n"
"v_accvgpr_read_b32 v198, a39\n"
"v_mul_f32 v198, v198, v199\n"
"v_accvgpr_write_b32 a39, v198\n"
"v_accvgpr_read_b32 v198, a60\n"
"v_mul_f32 v198, v198, v199\n"
"v_accvgpr_write_b32 a60, v198\n"
"v_accvgpr_read_b32 v198, a61\n"
"v_mul_f32 v198, v198, v199\n"
"v_accvgpr_write_b32 a61, v198\n"
"v_accvgpr_read_b32 v198, a62\n"
"v_mul_f32 v198, v198, v199\n"
"v_accvgpr_write_b32 a62, v198\n"
"v_accvgpr_read_b32 v198, a63\n"
"v_mul_f32 v198, v198, v199\n"
"v_accvgpr_write_b32 a63, v198\n"
"v_accvgpr_read_b32 v198, a84\n"
"v_mul_f32 v198, v198, v199\n"
"v_accvgpr_write_b32 a84, v198\n"
"v_accvgpr_read_b32 v198, a85\n"
"v_mul_f32 v198, v198, v199\n"
"v_accvgpr_write_b32 a85, v198\n"
"v_accvgpr_read_b32 v198, a86\n"
"v_mul_f32 v198, v198, v199\n"
"v_accvgpr_write_b32 a86, v198\n"
"v_accvgpr_read_b32 v198, a87\n"
"v_mul_f32 v198, v198, v199\n"
"v_accvgpr_write_b32 a87, v198\n"
"v_accvgpr_read_b32 v198, a108\n"
"v_mul_f32 v198, v198, v199\n"
"v_accvgpr_write_b32 a108, v198\n"
"v_accvgpr_read_b32 v198, a109\n"
"v_mul_f32 v198, v198, v199\n"
"v_accvgpr_write_b32 a109, v198\n"
"v_accvgpr_read_b32 v198, a110\n"
"v_mul_f32 v198, v198, v199\n"
"v_accvgpr_write_b32 a110, v198\n"
"v_accvgpr_read_b32 v198, a111\n"
"v_mul_f32 v198, v198, v199\n"
"v_accvgpr_write_b32 a111, v198\n"
"v_accvgpr_read_b32 v198, a132\n"
"v_mul_f32 v198, v198, v199\n"
"v_accvgpr_write_b32 a132, v198\n"
"v_accvgpr_read_b32 v198, a133\n"
"v_mul_f32 v198, v198, v199\n"
"v_accvgpr_write_b32 a133, v198\n"
"v_accvgpr_read_b32 v198, a134\n"
"v_mul_f32 v198, v198, v199\n"
"v_accvgpr_write_b32 a134, v198\n"
"v_accvgpr_read_b32 v198, a135\n"
"v_mul_f32 v198, v198, v199\n"
"v_accvgpr_write_b32 a135, v198\n"
"v_accvgpr_read_b32 v198, a156\n"
"v_mul_f32 v198, v198, v199\n"
"v_accvgpr_write_b32 a156, v198\n"
"v_accvgpr_read_b32 v198, a157\n"
"v_mul_f32 v198, v198, v199\n"
"v_accvgpr_write_b32 a157, v198\n"
"v_accvgpr_read_b32 v198, a158\n"
"v_mul_f32 v198, v198, v199\n"
"v_accvgpr_write_b32 a158, v198\n"
"v_accvgpr_read_b32 v198, a159\n"
"v_mul_f32 v198, v198, v199\n"
"v_accvgpr_write_b32 a159, v198\n"
"v_accvgpr_read_b32 v198, a180\n"
"v_mul_f32 v198, v198, v199\n"
"v_accvgpr_write_b32 a180, v198\n"
"v_accvgpr_read_b32 v198, a181\n"
"v_mul_f32 v198, v198, v199\n"
"v_accvgpr_write_b32 a181, v198\n"
"v_accvgpr_read_b32 v198, a182\n"
"v_mul_f32 v198, v198, v199\n"
"v_accvgpr_write_b32 a182, v198\n"
"v_accvgpr_read_b32 v198, a183\n"
"v_mul_f32 v198, v198, v199\n"
"v_accvgpr_write_b32 a183, v198\n"
"v_max3_f32 v202, v128, v129, v130\n"
"v_max3_f32 v202, v202, v131, v132\n"
"v_max3_f32 v202, v202, v133, v134\n"
"v_max_f32 v202, v202, v135\n"
"v_mov_b32 v199, v202\n"
"s_nop 1\n"
"v_permlane32_swap_b32 v202, v199\n"
"v_max_f32 v202, v202, v199\n"
"v_mov_b32 v199, v202\n"
"s_nop 1\n"
"v_permlane16_swap_b32 v202, v199\n"
"v_max_f32 v202, v202, v199\n"
"v_sub_f32 v202, 0xc1800000, v202\n"
"v_min_f32 v202, v202, v160\n"
"v_sub_f32 v200, v202, v160\n"
"v_exp_f32 v199, v200\n"
"v_mov_b32 v160, v202\n"
"v_mov_b32 v161, v202\n"
"v_mov_b32 v162, v202\n"
"v_mov_b32 v163, v202\n"
"v_add_f32 v128, v128, v202\n"
"v_add_f32 v129, v129, v202\n"
"v_add_f32 v130, v130, v202\n"
"v_add_f32 v131, v131, v202\n"
"v_add_f32 v132, v132, v202\n"
"v_add_f32 v133, v133, v202\n"
"v_add_f32 v134, v134, v202\n"
"v_add_f32 v135, v135, v202\n"
"v_add_f32 v240, v240, v200\n"
"v_add_f32 v241, v241, v200\n"
"v_add_f32 v242, v242, v200\n"
"v_add_f32 v243, v243, v200\n"
"v_add_f32 v244, v244, v200\n"
"v_add_f32 v245, v245, v200\n"
"v_add_f32 v246, v246, v200\n"
"v_add_f32 v247, v247, v200\n"
"v_mul_f32 v184, v184, v199\n"
"v_mul_f32 v185, v185, v199\n"
"v_mul_f32 v186, v186, v199\n"
"v_mul_f32 v187, v187, v199\n"
"v_accvgpr_read_b32 v198, a16\n"
"v_mul_f32 v198, v198, v199\n"
"v_accvgpr_write_b32 a16, v198\n"
"v_accvgpr_read_b32 v198, a17\n"
"v_mul_f32 v198, v198, v199\n"
"v_accvgpr_write_b32 a17, v198\n"
"v_accvgpr_read_b32 v198, a18\n"
"v_mul_f32 v198, v198, v199\n"
"v_accvgpr_write_b32 a18, v198\n"
"v_accvgpr_read_b32 v198, a19\n"
"v_mul_f32 v198, v198, v199\n"
"v_accvgpr_write_b32 a19, v198\n"
"v_accvgpr_read_b32 v198, a40\n"
"v_mul_f32 v198, v198, v199\n"
"v_accvgpr_write_b32 a40, v198\n"
"v_accvgpr_read_b32 v198, a41\n"
"v_mul_f32 v198, v198, v199\n"
"v_accvgpr_write_b32 a41, v198\n"
"v_accvgpr_read_b32 v198, a42\n"
"v_mul_f32 v198, v198, v199\n"
"v_accvgpr_write_b32 a42, v198\n"
"v_accvgpr_read_b32 v198, a43\n"
"v_mul_f32 v198, v198, v199\n"
"v_accvgpr_write_b32 a43, v198\n"
"v_accvgpr_read_b32 v198, a64\n"
"v_mul_f32 v198, v198, v199\n"
"v_accvgpr_write_b32 a64, v198\n"
"v_accvgpr_read_b32 v198, a65\n"
"v_mul_f32 v198, v198, v199\n"
"v_accvgpr_write_b32 a65, v198\n"
"v_accvgpr_read_b32 v198, a66\n"
"v_mul_f32 v198, v198, v199\n"
"v_accvgpr_write_b32 a66, v198\n"
"v_accvgpr_read_b32 v198, a67\n"
"v_mul_f32 v198, v198, v199\n"
"v_accvgpr_write_b32 a67, v198\n"
"v_accvgpr_read_b32 v198, a88\n"
"v_mul_f32 v198, v198, v199\n"
"v_accvgpr_write_b32 a88, v198\n"
"v_accvgpr_read_b32 v198, a89\n"
"v_mul_f32 v198, v198, v199\n"
"v_accvgpr_write_b32 a89, v198\n"
"v_accvgpr_read_b32 v198, a90\n"
"v_mul_f32 v198, v198, v199\n"
"v_accvgpr_write_b32 a90, v198\n"
"v_accvgpr_read_b32 v198, a91\n"
"v_mul_f32 v198, v198, v199\n"
"v_accvgpr_write_b32 a91, v198\n"
"v_accvgpr_read_b32 v198, a112\n"
"v_mul_f32 v198, v198, v199\n"
"v_accvgpr_write_b32 a112, v198\n"
"v_accvgpr_read_b32 v198, a113\n"
"v_mul_f32 v198, v198, v199\n"
"v_accvgpr_write_b32 a113, v198\n"
"v_accvgpr_read_b32 v198, a114\n"
"v_mul_f32 v198, v198, v199\n"
"v_accvgpr_write_b32 a114, v198\n"
"v_accvgpr_read_b32 v198, a115\n"
"v_mul_f32 v198, v198, v199\n"
"v_accvgpr_write_b32 a115, v198\n"
"v_accvgpr_read_b32 v198, a136\n"
"v_mul_f32 v198, v198, v199\n"
"v_accvgpr_write_b32 a136, v198\n"
"v_accvgpr_read_b32 v198, a137\n"
"v_mul_f32 v198, v198, v199\n"
"v_accvgpr_write_b32 a137, v198\n"
"v_accvgpr_read_b32 v198, a138\n"
"v_mul_f32 v198, v198, v199\n"
"v_accvgpr_write_b32 a138, v198\n"
"v_accvgpr_read_b32 v198, a139\n"
"v_mul_f32 v198, v198, v199\n"
"v_accvgpr_write_b32 a139, v198\n"
"v_accvgpr_read_b32 v198, a160\n"
"v_mul_f32 v198, v198, v199\n"
"v_accvgpr_write_b32 a160, v198\n"
"v_accvgpr_read_b32 v198, a161\n"
"v_mul_f32 v198, v198, v199\n"
"v_accvgpr_write_b32 a161, v198\n"
"v_accvgpr_read_b32 v198, a162\n"
"v_mul_f32 v198, v198, v199\n"
"v_accvgpr_write_b32 a162, v198\n"
"v_accvgpr_read_b32 v198, a163\n"
"v_mul_f32 v198, v198, v199\n"
"v_accvgpr_write_b32 a163, v198\n"
"v_accvgpr_read_b32 v198, a184\n"
"v_mul_f32 v198, v198, v199\n"
"v_accvgpr_write_b32 a184, v198\n"
"v_accvgpr_read_b32 v198, a185\n"
"v_mul_f32 v198, v198, v199\n"
"v_accvgpr_write_b32 a185, v198\n"
"v_accvgpr_read_b32 v198, a186\n"
"v_mul_f32 v198, v198, v199\n"
"v_accvgpr_write_b32 a186, v198\n"
"v_accvgpr_read_b32 v198, a187\n"
"v_mul_f32 v198, v198, v199\n"
"v_accvgpr_write_b32 a187, v198\n"
"v_max3_f32 v202, v136, v137, v138\n"
"v_max3_f32 v202, v202, v139, v140\n"
"v_max3_f32 v202, v202, v141, v142\n"
"v_max_f32 v202, v202, v143\n"
"v_mov_b32 v199, v202\n"
"s_nop 1\n"
"v_permlane32_swap_b32 v202, v199\n"
"v_max_f32 v202, v202, v199\n"
"v_mov_b32 v199, v202\n"
"s_nop 1\n"
"v_permlane16_swap_b32 v202, v199\n"
"v_max_f32 v202, v202, v199\n"
"v_sub_f32 v202, 0xc1800000, v202\n"
"v_min_f32 v202, v202, v164\n"
"v_sub_f32 v200, v202, v164\n"
"v_exp_f32 v199, v200\n"
"v_mov_b32 v164, v202\n"
"v_mov_b32 v165, v202\n"
"v_mov_b32 v166, v202\n"
"v_mov_b32 v167, v202\n"
"v_add_f32 v136, v136, v202\n"
"v_add_f32 v137, v137, v202\n"
"v_add_f32 v138, v138, v202\n"
"v_add_f32 v139, v139, v202\n"
"v_add_f32 v140, v140, v202\n"
"v_add_f32 v141, v141, v202\n"
"v_add_f32 v142, v142, v202\n"
"v_add_f32 v143, v143, v202\n"
"v_add_f32 v248, v248, v200\n"
"v_add_f32 v249, v249, v200\n"
"v_add_f32 v250, v250, v200\n"
"v_add_f32 v251, v251, v200\n"
"v_add_f32 v252, v252, v200\n"
"v_add_f32 v253, v253, v200\n"
"v_add_f32 v254, v254, v200\n"
"v_add_f32 v255, v255, v200\n"
"v_mul_f32 v188, v188, v199\n"
"v_mul_f32 v189, v189, v199\n"
"v_mul_f32 v190, v190, v199\n"
"v_mul_f32 v191, v191, v199\n"
"v_accvgpr_read_b32 v198, a20\n"
"v_mul_f32 v198, v198, v199\n"
"v_accvgpr_write_b32 a20, v198\n"
"v_accvgpr_read_b32 v198, a21\n"
"v_mul_f32 v198, v198, v199\n"
"v_accvgpr_write_b32 a21, v198\n"
"v_accvgpr_read_b32 v198, a22\n"
"v_mul_f32 v198, v198, v199\n"
"v_accvgpr_write_b32 a22, v198\n"
"v_accvgpr_read_b32 v198, a23\n"
"v_mul_f32 v198, v198, v199\n"
"v_accvgpr_write_b32 a23, v198\n"
"v_accvgpr_read_b32 v198, a44\n"
"v_mul_f32 v198, v198, v199\n"
"v_accvgpr_write_b32 a44, v198\n"
"v_accvgpr_read_b32 v198, a45\n"
"v_mul_f32 v198, v198, v199\n"
"v_accvgpr_write_b32 a45, v198\n"
"v_accvgpr_read_b32 v198, a46\n"
"v_mul_f32 v198, v198, v199\n"
"v_accvgpr_write_b32 a46, v198\n"
"v_accvgpr_read_b32 v198, a47\n"
"v_mul_f32 v198, v198, v199\n"
"v_accvgpr_write_b32 a47, v198\n"
"v_accvgpr_read_b32 v198, a68\n"
"v_mul_f32 v198, v198, v199\n"
"v_accvgpr_write_b32 a68, v198\n"
"v_accvgpr_read_b32 v198, a69\n"
"v_mul_f32 v198, v198, v199\n"
"v_accvgpr_write_b32 a69, v198\n"
"v_accvgpr_read_b32 v198, a70\n"
"v_mul_f32 v198, v198, v199\n"
"v_accvgpr_write_b32 a70, v198\n"
"v_accvgpr_read_b32 v198, a71\n"
"v_mul_f32 v198, v198, v199\n"
"v_accvgpr_write_b32 a71, v198\n"
"v_accvgpr_read_b32 v198, a92\n"
"v_mul_f32 v198, v198, v199\n"
"v_accvgpr_write_b32 a92, v198\n"
"v_accvgpr_read_b32 v198, a93\n"
"v_mul_f32 v198, v198, v199\n"
"v_accvgpr_write_b32 a93, v198\n"
"v_accvgpr_read_b32 v198, a94\n"
"v_mul_f32 v198, v198, v199\n"
"v_accvgpr_write_b32 a94, v198\n"
"v_accvgpr_read_b32 v198, a95\n"
"v_mul_f32 v198, v198, v199\n"
"v_accvgpr_write_b32 a95, v198\n"
"v_accvgpr_read_b32 v198, a116\n"
"v_mul_f32 v198, v198, v199\n"
"v_accvgpr_write_b32 a116, v198\n"
"v_accvgpr_read_b32 v198, a117\n"
"v_mul_f32 v198, v198, v199\n"
"v_accvgpr_write_b32 a117, v198\n"
"v_accvgpr_read_b32 v198, a118\n"
"v_mul_f32 v198, v198, v199\n"
"v_accvgpr_write_b32 a118, v198\n"
"v_accvgpr_read_b32 v198, a119\n"
"v_mul_f32 v198, v198, v199\n"
"v_accvgpr_write_b32 a119, v198\n"
"v_accvgpr_read_b32 v198, a140\n"
"v_mul_f32 v198, v198, v199\n"
"v_accvgpr_write_b32 a140, v198\n"
"v_accvgpr_read_b32 v198, a141\n"
"v_mul_f32 v198, v198, v199\n"
"v_accvgpr_write_b32 a141, v198\n"
"v_accvgpr_read_b32 v198, a142\n"
"v_mul_f32 v198, v198, v199\n"
"v_accvgpr_write_b32 a142, v198\n"
"v_accvgpr_read_b32 v198, a143\n"
"v_mul_f32 v198, v198, v199\n"
"v_accvgpr_write_b32 a143, v198\n"
"v_accvgpr_read_b32 v198, a164\n"
"v_mul_f32 v198, v198, v199\n"
"v_accvgpr_write_b32 a164, v198\n"
"v_accvgpr_read_b32 v198, a165\n"
"v_mul_f32 v198, v198, v199\n"
"v_accvgpr_write_b32 a165, v198\n"
"v_accvgpr_read_b32 v198, a166\n"
"v_mul_f32 v198, v198, v199\n"
"v_accvgpr_write_b32 a166, v198\n"
"v_accvgpr_read_b32 v198, a167\n"
"v_mul_f32 v198, v198, v199\n"
"v_accvgpr_write_b32 a167, v198\n"
"v_accvgpr_read_b32 v198, a188\n"
"v_mul_f32 v198, v198, v199\n"
"v_accvgpr_write_b32 a188, v198\n"
"v_accvgpr_read_b32 v198, a189\n"
"v_mul_f32 v198, v198, v199\n"
"v_accvgpr_write_b32 a189, v198\n"
"v_accvgpr_read_b32 v198, a190\n"
"v_mul_f32 v198, v198, v199\n"
"v_accvgpr_write_b32 a190, v198\n"
"v_accvgpr_read_b32 v198, a191\n"
"v_mul_f32 v198, v198, v199\n"
"v_accvgpr_write_b32 a191, v198\n"
"v_exp_f32 v96, v96\n"
"v_exp_f32 v97, v97\n"
"v_exp_f32 v98, v98\n"
"v_exp_f32 v99, v99\n"
"v_cvt_pk_bf16_f32 v96, v96, v97\n"
"v_cvt_pk_bf16_f32 v97, v98, v99\n"
"v_or_b32 v198, v96, v97\n"
"v_exp_f32 v100, v100\n"
"v_exp_f32 v101, v101\n"
"v_exp_f32 v102, v102\n"
"v_exp_f32 v103, v103\n"
"v_cvt_pk_bf16_f32 v98, v100, v101\n"
"v_cvt_pk_bf16_f32 v99, v102, v103\n"
"v_or3_b32 v198, v198, v98, v99\n"
"v_exp_f32 v104, v104\n"
"v_exp_f32 v105, v105\n"
"v_exp_f32 v106, v106\n"
"v_exp_f32 v107, v107\n"
"v_cvt_pk_bf16_f32 v104, v104, v105\n"
"v_cvt_pk_bf16_f32 v105, v106, v107\n"
"v_or3_b32 v198, v198, v104, v105\n"
"v_exp_f32 v108, v108\n"
"v_exp_f32 v109, v109\n"
"v_exp_f32 v110, v110\n"
"v_exp_f32 v111, v111\n"
"v_cvt_pk_bf16_f32 v106, v108, v109\n"
"v_cvt_pk_bf16_f32 v107, v110, v111\n"
"v_or3_b32 v198, v198, v106, v107\n"
"v_exp_f32 v112, v112\n"
"v_exp_f32 v113, v113\n"
"v_exp_f32 v114, v114\n"
"v_exp_f32 v115, v115\n"
"v_cvt_pk_bf16_f32 v112, v112, v113\n"
"v_cvt_pk_bf16_f32 v113, v114, v115\n"
"v_or3_b32 v198, v198, v112, v113\n"
"v_exp_f32 v116, v116\n"
"v_exp_f32 v117, v117\n"
"v_exp_f32 v118, v118\n"
"v_exp_f32 v119, v119\n"
"v_cvt_pk_bf16_f32 v114, v116, v117\n"
"v_cvt_pk_bf16_f32 v115, v118, v119\n"
"v_or3_b32 v198, v198, v114, v115\n"
"v_exp_f32 v120, v120\n"
"v_exp_f32 v121, v121\n"
"v_exp_f32 v122, v122\n"
"v_exp_f32 v123, v123\n"
"v_cvt_pk_bf16_f32 v120, v120, v121\n"
"v_cvt_pk_bf16_f32 v121, v122, v123\n"
"v_or3_b32 v198, v198, v120, v121\n"
"v_exp_f32 v124, v124\n"
"v_exp_f32 v125, v125\n"
"v_exp_f32 v126, v126\n"
"v_exp_f32 v127, v127\n"
"v_cvt_pk_bf16_f32 v122, v124, v125\n"
"v_cvt_pk_bf16_f32 v123, v126, v127\n"
"v_or3_b32 v198, v198, v122, v123\n"
"v_exp_f32 v128, v128\n"
"v_exp_f32 v129, v129\n"
"v_exp_f32 v130, v130\n"
"v_exp_f32 v131, v131\n"
"v_cvt_pk_bf16_f32 v128, v128, v129\n"
"v_cvt_pk_bf16_f32 v129, v130, v131\n"
"v_or3_b32 v198, v198, v128, v129\n"
"v_exp_f32 v132, v132\n"
"v_exp_f32 v133, v133\n"
"v_exp_f32 v134, v134\n"
"v_exp_f32 v135, v135\n"
"v_cvt_pk_bf16_f32 v130, v132, v133\n"
"v_cvt_pk_bf16_f32 v131, v134, v135\n"
"v_or3_b32 v198, v198, v130, v131\n"
"v_exp_f32 v136, v136\n"
"v_exp_f32 v137, v137\n"
"v_exp_f32 v138, v138\n"
"v_exp_f32 v139, v139\n"
"v_cvt_pk_bf16_f32 v136, v136, v137\n"
"v_cvt_pk_bf16_f32 v137, v138, v139\n"
"v_or3_b32 v198, v198, v136, v137\n"
"v_exp_f32 v140, v140\n"
"v_exp_f32 v141, v141\n"
"v_exp_f32 v142, v142\n"
"v_exp_f32 v143, v143\n"
"v_cvt_pk_bf16_f32 v138, v140, v141\n"
"v_cvt_pk_bf16_f32 v139, v142, v143\n"
"v_or3_b32 v198, v198, v138, v139\n"
"s_nop 3\n"
"s_setpc_b64 s[80:81]\n"
".Lrr10:\n"
"s_waitcnt vmcnt(0) lgkmcnt(0)\n"
"s_nop 15\n"
"s_nop 7\n"
"v_mbcnt_lo_u32_b32 v200, -1, 0\n"
"v_mbcnt_hi_u32_b32 v200, -1, v200\n"
"v_and_b32 v199, 15, v200\n"
"v_lshlrev_b32 v199, 8, v199\n"
"v_lshrrev_b32 v200, 4, v200\n"
"v_lshl_add_u32 v199, v200, 4, v199\n"
"v_add_u32 v200, 4096, v199\n"
"buffer_load_dwordx4 a[192:195], v199, s[24:27], s64 offen\n"
"buffer_load_dwordx4 a[196:199], v200, s[24:27], s64 offen\n"
"buffer_load_dwordx4 a[200:203], v199, s[24:27], s64 offen offset:64\n"
"buffer_load_dwordx4 a[204:207], v200, s[24:27], s64 offen offset:64\n"
"buffer_load_dwordx4 a[208:211], v199, s[24:27], s64 offen offset:128\n"
"buffer_load_dwordx4 a[212:215], v200, s[24:27], s64 offen offset:128\n"
"buffer_load_dwordx4 a[216:219], v199, s[24:27], s64 offen offset:192\n"
"buffer_load_dwordx4 a[220:223], v200, s[24:27], s64 offen offset:192\n"
"s_waitcnt vmcnt(0)\n"
"v_mfma_f32_16x16x32_bf16 v[96:99], a[192:195], v[0:3], 0\n"
"v_mfma_f32_16x16x32_bf16 v[104:107], a[192:195], v[4:7], 0\n"
"v_mfma_f32_16x16x32_bf16 v[112:115], a[192:195], v[8:11], 0\n"
"v_mfma_f32_16x16x32_bf16 v[120:123], a[192:195], v[12:15], 0\n"
"v_mfma_f32_16x16x32_bf16 v[128:131], a[192:195], v[16:19], 0\n"
"v_mfma_f32_16x16x32_bf16 v[136:139], a[192:195], v[20:23], 0\n"
"v_mfma_f32_16x16x32_bf16 v[100:103], a[196:199], v[0:3], 0\n"
"v_mfma_f32_16x16x32_bf16 v[108:111], a[196:199], v[4:7], 0\n"
"v_mfma_f32_16x16x32_bf16 v[116:119], a[196:199], v[8:11], 0\n"
"v_mfma_f32_16x16x32_bf16 v[124:127], a[196:199], v[12:15], 0\n"
"v_mfma_f32_16x16x32_bf16 v[132:135], a[196:199], v[16:19], 0\n"
"v_mfma_f32_16x16x32_bf16 v[140:143], a[196:199], v[20:23], 0\n"
"v_mfma_f32_16x16x32_bf16 v[96:99], a[200:203], v[24:27], v[96:99]\n"
"v_mfma_f32_16x16x32_bf16 v[104:107], a[200:203], v[28:31], v[104:107]\n"
"v_mfma_f32_16x16x32_bf16 v[112:115], a[200:203], v[32:35], v[112:115]\n"
"v_mfma_f32_16x16x32_bf16 v[120:123], a[200:203], v[36:39], v[120:123]\n"
"v_mfma_f32_16x16x32_bf16 v[128:131], a[200:203], v[40:43], v[128:131]\n"
"v_mfma_f32_16x16x32_bf16 v[136:139], a[200:203], v[44:47], v[136:139]\n"
"v_mfma_f32_16x16x32_bf16 v[100:103], a[204:207], v[24:27], v[100:103]\n"
"v_mfma_f32_16x16x32_bf16 v[108:111], a[204:207], v[28:31], v[108:111]\n"
"v_mfma_f32_16x16x32_bf16 v[116:119], a[204:207], v[32:35], v[116:119]\n"
"v_mfma_f32_16x16x32_bf16 v[124:127], a[204:207], v[36:39], v[124:127]\n"
"v_mfma_f32_16x16x32_bf16 v[132:135], a[204:207], v[40:43], v[132:135]\n"
"v_mfma_f32_16x16x32_bf16 v[140:143], a[204:207], v[44:47], v[140:143]\n"
"v_mfma_f32_16x16x32_bf16 v[96:99], a[208:211], v[48:51], v[96:99]\n"
"v_mfma_f32_16x16x32_bf16 v[104:107], a[208:211], v[52:55], v[104:107]\n"
"v_mfma_f32_16x16x32_bf16 v[112:115], a[208:211], v[56:59], v[112:115]\n"
"v_mfma_f32_16x16x32_bf16 v[120:123], a[208:211], v[60:63], v[120:123]\n"
"v_mfma_f32_16x16x32_bf16 v[128:131], a[208:211], v[64:67], v[128:131]\n"
"v_mfma_f32_16x16x32_bf16 v[136:139], a[208:211], v[68:71], v[136:139]\n"
"v_mfma_f32_16x16x32_bf16 v[100:103], a[212:215], v[48:51], v[100:103]\n"
"v_mfma_f32_16x16x32_bf16 v[108:111], a[212:215], v[52:55], v[108:111]\n"
"v_mfma_f32_16x16x32_bf16 v[116:119], a[212:215], v[56:59], v[116:119]\n"
"v_mfma_f32_16x16x32_bf16 v[124:127], a[212:215], v[60:63], v[124:127]\n"
"v_mfma_f32_16x16x32_bf16 v[132:135], a[212:215], v[64:67], v[132:135]\n"
"v_mfma_f32_16x16x32_bf16 v[140:143], a[212:215], v[68:71], v[140:143]\n"
"v_mfma_f32_16x16x32_bf16 v[96:99], a[216:219], v[72:75], v[96:99]\n"
"v_mfma_f32_16x16x32_bf16 v[104:107], a[216:219], v[76:79], v[104:107]\n"
"v_mfma_f32_16x16x32_bf16 v[112:115], a[216:219], v[80:83], v[112:115]\n"
"v_mfma_f32_16x16x32_bf16 v[120:123], a[216:219], v[84:87], v[120:123]\n"
"v_mfma_f32_16x16x32_bf16 v[128:131], a[216:219], v[88:91], v[128:131]\n"
"v_mfma_f32_16x16x32_bf16 v[136:139], a[216:219], v[92:95], v[136:139]\n"
"v_mfma_f32_16x16x32_bf16 v[100:103], a[220:223], v[72:75], v[100:103]\n"
"v_mfma_f32_16x16x32_bf16 v[108:111], a[220:223], v[76:79], v[108:111]\n"
"v_mfma_f32_16x16x32_bf16 v[116:119], a[220:223], v[80:83], v[116:119]\n"
"v_mfma_f32_16x16x32_bf16 v[124:127], a[220:223], v[84:87], v[124:127]\n"
"v_mfma_f32_16x16x32_bf16 v[132:135], a[220:223], v[88:91], v[132:135]\n"
"v_mfma_f32_16x16x32_bf16 v[140:143], a[220:223], v[92:95], v[140:143]\n"
"v_max3_f32 v202, v96, v97, v98\n"
"s_nop 1\n"
"v_max3_f32 v202, v202, v99, v100\n"
"v_max3_f32 v202, v202, v101, v102\n"
"v_max_f32 v202, v202, v103\n"
"v_mov_b32 v199, v202\n"
"s_nop 1\n"
"v_permlane32_swap_b32 v202, v199\n"
"v_max_f32 v202, v202, v199\n"
"v_mov_b32 v199, v202\n"
"s_nop 1\n"
"v_permlane16_swap_b32 v202, v199\n"
"v_max_f32 v202, v202, v199\n"
"v_sub_f32 v202, 0xc1800000, v202\n"
"v_min_f32 v202, v202, v144\n"
"v_sub_f32 v200, v202, v144\n"
"v_exp_f32 v199, v200\n"
"v_mov_b32 v144, v202\n"
"v_mov_b32 v145, v202\n"
"v_mov_b32 v146, v202\n"
"v_mov_b32 v147, v202\n"
"v_add_f32 v96, v96, v202\n"
"v_add_f32 v97, v97, v202\n"
"v_add_f32 v98, v98, v202\n"
"v_add_f32 v99, v99, v202\n"
"v_add_f32 v100, v100, v202\n"
"v_add_f32 v101, v101, v202\n"
"v_add_f32 v102, v102, v202\n"
"v_add_f32 v103, v103, v202\n"
"v_mul_f32 v168, v168, v199\n"
"v_mul_f32 v169, v169, v199\n"
"v_mul_f32 v170, v170, v199\n"
"v_mul_f32 v171, v171, v199\n"
"v_accvgpr_read_b32 v198, a0\n"
"v_mul_f32 v198, v198, v199\n"
"v_accvgpr_write_b32 a0, v198\n"
"v_accvgpr_read_b32 v198, a1\n"
"v_mul_f32 v198, v198, v199\n"
"v_accvgpr_write_b32 a1, v198\n"
"v_accvgpr_read_b32 v198, a2\n"
"v_mul_f32 v198, v198, v199\n"
"v_accvgpr_write_b32 a2, v198\n"
"v_accvgpr_read_b32 v198, a3\n"
"v_mul_f32 v198, v198, v199\n"
"v_accvgpr_write_b32 a3, v198\n"
"v_accvgpr_read_b32 v198, a24\n"
"v_mul_f32 v198, v198, v199\n"
"v_accvgpr_write_b32 a24, v198\n"
"v_accvgpr_read_b32 v198, a25\n"
"v_mul_f32 v198, v198, v199\n"
"v_accvgpr_write_b32 a25, v198\n"
"v_accvgpr_read_b32 v198, a26\n"
"v_mul_f32 v198, v198, v199\n"
"v_accvgpr_write_b32 a26, v198\n"
"v_accvgpr_read_b32 v198, a27\n"
"v_mul_f32 v198, v198, v199\n"
"v_accvgpr_write_b32 a27, v198\n"
"v_accvgpr_read_b32 v198, a48\n"
"v_mul_f32 v198, v198, v199\n"
"v_accvgpr_write_b32 a48, v198\n"
"v_accvgpr_read_b32 v198, a49\n"
"v_mul_f32 v198, v198, v199\n"
"v_accvgpr_write_b32 a49, v198\n"
"v_accvgpr_read_b32 v198, a50\n"
"v_mul_f32 v198, v198, v199\n"
"v_accvgpr_write_b32 a50, v198\n"
"v_accvgpr_read_b32 v198, a51\n"
"v_mul_f32 v198, v198, v199\n"
"v_accvgpr_write_b32 a51, v198\n"
"v_accvgpr_read_b32 v198, a72\n"
"v_mul_f32 v198, v198, v199\n"
"v_accvgpr_write_b32 a72, v198\n"
"v_accvgpr_read_b32 v198, a73\n"
"v_mul_f32 v198, v198, v199\n"
"v_accvgpr_write_b32 a73, v198\n"
"v_accvgpr_read_b32 v198, a74\n"
"v_mul_f32 v198, v198, v199\n"
"v_accvgpr_write_b32 a74, v198\n"
"v_accvgpr_read_b32 v198, a75\n"
"v_mul_f32 v198, v198, v199\n"
"v_accvgpr_write_b32 a75, v198\n"
"v_accvgpr_read_b32 v198, a96\n"
"v_mul_f32 v198, v198, v199\n"
"v_accvgpr_write_b32 a96, v198\n"
"v_accvgpr_read_b32 v198, a97\n"
"v_mul_f32 v198, v198, v199\n"
"v_accvgpr_write_b32 a97, v198\n"
"v_accvgpr_read_b32 v198, a98\n"
"v_mul_f32 v198, v198, v199\n"
"v_accvgpr_write_b32 a98, v198\n"
"v_accvgpr_read_b32 v198, a99\n"
"v_mul_f32 v198, v198, v199\n"
"v_accvgpr_write_b32 a99, v198\n"
"v_accvgpr_read_b32 v198, a120\n"
"v_mul_f32 v198, v198, v199\n"
"v_accvgpr_write_b32 a120, v198\n"
"v_accvgpr_read_b32 v198, a121\n"
"v_mul_f32 v198, v198, v199\n"
"v_accvgpr_write_b32 a121, v198\n"
"v_accvgpr_read_b32 v198, a122\n"
"v_mul_f32 v198, v198, v199\n"
"v_accvgpr_write_b32 a122, v198\n"
"v_accvgpr_read_b32 v198, a123\n"
"v_mul_f32 v198, v198, v199\n"
"v_accvgpr_write_b32 a123, v198\n"
"v_accvgpr_read_b32 v198, a144\n"
"v_mul_f32 v198, v198, v199\n"
"v_accvgpr_write_b32 a144, v198\n"
"v_accvgpr_read_b32 v198, a145\n"
"v_mul_f32 v198, v198, v199\n"
"v_accvgpr_write_b32 a145, v198\n"
"v_accvgpr_read_b32 v198, a146\n"
"v_mul_f32 v198, v198, v199\n"
"v_accvgpr_write_b32 a146, v198\n"
"v_accvgpr_read_b32 v198, a147\n"
"v_mul_f32 v198, v198, v199\n"
"v_accvgpr_write_b32 a147, v198\n"
"v_accvgpr_read_b32 v198, a168\n"
"v_mul_f32 v198, v198, v199\n"
"v_accvgpr_write_b32 a168, v198\n"
"v_accvgpr_read_b32 v198, a169\n"
"v_mul_f32 v198, v198, v199\n"
"v_accvgpr_write_b32 a169, v198\n"
"v_accvgpr_read_b32 v198, a170\n"
"v_mul_f32 v198, v198, v199\n"
"v_accvgpr_write_b32 a170, v198\n"
"v_accvgpr_read_b32 v198, a171\n"
"v_mul_f32 v198, v198, v199\n"
"v_accvgpr_write_b32 a171, v198\n"
"v_max3_f32 v202, v104, v105, v106\n"
"v_max3_f32 v202, v202, v107, v108\n"
"v_max3_f32 v202, v202, v109, v110\n"
"v_max_f32 v202, v202, v111\n"
"v_mov_b32 v199, v202\n"
"s_nop 1\n"
"v_permlane32_swap_b32 v202, v199\n"
"v_max_f32 v202, v202, v199\n"
"v_mov_b32 v199, v202\n"
"s_nop 1\n"
"v_permlane16_swap_b32 v202, v199\n"
"v_max_f32 v202, v202, v199\n"
"v_sub_f32 v202, 0xc1800000, v202\n"
"v_min_f32 v202, v202, v148\n"
"v_sub_f32 v200, v202, v148\n"
"v_exp_f32 v199, v200\n"
"v_mov_b32 v148, v202\n"
"v_mov_b32 v149, v202\n"
"v_mov_b32 v150, v202\n"
"v_mov_b32 v151, v202\n"
"v_add_f32 v104, v104, v202\n"
"v_add_f32 v105, v105, v202\n"
"v_add_f32 v106, v106, v202\n"
"v_add_f32 v107, v107, v202\n"
"v_add_f32 v108, v108, v202\n"
"v_add_f32 v109, v109, v202\n"
"v_add_f32 v110, v110, v202\n"
"v_add_f32 v111, v111, v202\n"
"v_mul_f32 v172, v172, v199\n"
"v_mul_f32 v173, v173, v199\n"
"v_mul_f32 v174, v174, v199\n"
"v_mul_f32 v175, v175, v199\n"
"v_accvgpr_read_b32 v198, a4\n"
"v_mul_f32 v198, v198, v199\n"
"v_accvgpr_write_b32 a4, v198\n"
"v_accvgpr_read_b32 v198, a5\n"
"v_mul_f32 v198, v198, v199\n"
"v_accvgpr_write_b32 a5, v198\n"
"v_accvgpr_read_b32 v198, a6\n"
"v_mul_f32 v198, v198, v199\n"
"v_accvgpr_write_b32 a6, v198\n"
"v_accvgpr_read_b32 v198, a7\n"
"v_mul_f32 v198, v198, v199\n"
"v_accvgpr_write_b32 a7, v198\n"
"v_accvgpr_read_b32 v198, a28\n"
"v_mul_f32 v198, v198, v199\n"
"v_accvgpr_write_b32 a28, v198\n"
"v_accvgpr_read_b32 v198, a29\n"
"v_mul_f32 v198, v198, v199\n"
"v_accvgpr_write_b32 a29, v198\n"
"v_accvgpr_read_b32 v198, a30\n"
"v_mul_f32 v198, v198, v199\n"
"v_accvgpr_write_b32 a30, v198\n"
"v_accvgpr_read_b32 v198, a31\n"
"v_mul_f32 v198, v198, v199\n"
"v_accvgpr_write_b32 a31, v198\n"
"v_accvgpr_read_b32 v198, a52\n"
"v_mul_f32 v198, v198, v199\n"
"v_accvgpr_write_b32 a52, v198\n"
"v_accvgpr_read_b32 v198, a53\n"
"v_mul_f32 v198, v198, v199\n"
"v_accvgpr_write_b32 a53, v198\n"
"v_accvgpr_read_b32 v198, a54\n"
"v_mul_f32 v198, v198, v199\n"
"v_accvgpr_write_b32 a54, v198\n"
"v_accvgpr_read_b32 v198, a55\n"
"v_mul_f32 v198, v198, v199\n"
"v_accvgpr_write_b32 a55, v198\n"
"v_accvgpr_read_b32 v198, a76\n"
"v_mul_f32 v198, v198, v199\n"
"v_accvgpr_write_b32 a76, v198\n"
"v_accvgpr_read_b32 v198, a77\n"
"v_mul_f32 v198, v198, v199\n"
"v_accvgpr_write_b32 a77, v198\n"
"v_accvgpr_read_b32 v198, a78\n"
"v_mul_f32 v198, v198, v199\n"
"v_accvgpr_write_b32 a78, v198\n"
"v_accvgpr_read_b32 v198, a79\n"
"v_mul_f32 v198, v198, v199\n"
"v_accvgpr_write_b32 a79, v198\n"
"v_accvgpr_read_b32 v198, a100\n"
"v_mul_f32 v198, v198, v199\n"
"v_accvgpr_write_b32 a100, v198\n"
"v_accvgpr_read_b32 v198, a101\n"
"v_mul_f32 v198, v198, v199\n"
"v_accvgpr_write_b32 a101, v198\n"
"v_accvgpr_read_b32 v198, a102\n"
"v_mul_f32 v198, v198, v199\n"
"v_accvgpr_write_b32 a102, v198\n"
"v_accvgpr_read_b32 v198, a103\n"
"v_mul_f32 v198, v198, v199\n"
"v_accvgpr_write_b32 a103, v198\n"
"v_accvgpr_read_b32 v198, a124\n"
"v_mul_f32 v198, v198, v199\n"
"v_accvgpr_write_b32 a124, v198\n"
"v_accvgpr_read_b32 v198, a125\n"
"v_mul_f32 v198, v198, v199\n"
"v_accvgpr_write_b32 a125, v198\n"
"v_accvgpr_read_b32 v198, a126\n"
"v_mul_f32 v198, v198, v199\n"
"v_accvgpr_write_b32 a126, v198\n"
"v_accvgpr_read_b32 v198, a127\n"
"v_mul_f32 v198, v198, v199\n"
"v_accvgpr_write_b32 a127, v198\n"
"v_accvgpr_read_b32 v198, a148\n"
"v_mul_f32 v198, v198, v199\n"
"v_accvgpr_write_b32 a148, v198\n"
"v_accvgpr_read_b32 v198, a149\n"
"v_mul_f32 v198, v198, v199\n"
"v_accvgpr_write_b32 a149, v198\n"
"v_accvgpr_read_b32 v198, a150\n"
"v_mul_f32 v198, v198, v199\n"
"v_accvgpr_write_b32 a150, v198\n"
"v_accvgpr_read_b32 v198, a151\n"
"v_mul_f32 v198, v198, v199\n"
"v_accvgpr_write_b32 a151, v198\n"
"v_accvgpr_read_b32 v198, a172\n"
"v_mul_f32 v198, v198, v199\n"
"v_accvgpr_write_b32 a172, v198\n"
"v_accvgpr_read_b32 v198, a173\n"
"v_mul_f32 v198, v198, v199\n"
"v_accvgpr_write_b32 a173, v198\n"
"v_accvgpr_read_b32 v198, a174\n"
"v_mul_f32 v198, v198, v199\n"
"v_accvgpr_write_b32 a174, v198\n"
"v_accvgpr_read_b32 v198, a175\n"
"v_mul_f32 v198, v198, v199\n"
"v_accvgpr_write_b32 a175, v198\n"
"v_max3_f32 v202, v112, v113, v114\n"
"v_max3_f32 v202, v202, v115, v116\n"
"v_max3_f32 v202, v202, v117, v118\n"
"v_max_f32 v202, v202, v119\n"
"v_mov_b32 v199, v202\n"
"s_nop 1\n"
"v_permlane32_swap_b32 v202, v199\n"
"v_max_f32 v202, v202, v199\n"
"v_mov_b32 v199, v202\n"
"s_nop 1\n"
"v_permlane16_swap_b32 v202, v199\n"
"v_max_f32 v202, v202, v199\n"
"v_sub_f32 v202, 0xc1800000, v202\n"
"v_min_f32 v202, v202, v152\n"
"v_sub_f32 v200, v202, v152\n"
"v_exp_f32 v199, v200\n"
"v_mov_b32 v152, v202\n"
"v_mov_b32 v153, v202\n"
"v_mov_b32 v154, v202\n"
"v_mov_b32 v155, v202\n"
"v_add_f32 v112, v112, v202\n"
"v_add_f32 v113, v113, v202\n"
"v_add_f32 v114, v114, v202\n"
"v_add_f32 v115, v115, v202\n"
"v_add_f32 v116, v116, v202\n"
"v_add_f32 v117, v117, v202\n"
"v_add_f32 v118, v118, v202\n"
"v_add_f32 v119, v119, v202\n"
"v_mul_f32 v176, v176, v199\n"
"v_mul_f32 v177, v177, v199\n"
"v_mul_f32 v178, v178, v199\n"
"v_mul_f32 v179, v179, v199\n"
"v_accvgpr_read_b32 v198, a8\n"
"v_mul_f32 v198, v198, v199\n"
"v_accvgpr_write_b32 a8, v198\n"
"v_accvgpr_read_b32 v198, a9\n"
"v_mul_f32 v198, v198, v199\n"
"v_accvgpr_write_b32 a9, v198\n"
"v_accvgpr_read_b32 v198, a10\n"
"v_mul_f32 v198, v198, v199\n"
"v_accvgpr_write_b32 a10, v198\n"
"v_accvgpr_read_b32 v198, a11\n"
"v_mul_f32 v198, v198, v199\n"
"v_accvgpr_write_b32 a11, v198\n"
"v_accvgpr_read_b32 v198, a32\n"
"v_mul_f32 v198, v198, v199\n"
"v_accvgpr_write_b32 a32, v198\n"
"v_accvgpr_read_b32 v198, a33\n"
"v_mul_f32 v198, v198, v199\n"
"v_accvgpr_write_b32 a33, v198\n"
"v_accvgpr_read_b32 v198, a34\n"
"v_mul_f32 v198, v198, v199\n"
"v_accvgpr_write_b32 a34, v198\n"
"v_accvgpr_read_b32 v198, a35\n"
"v_mul_f32 v198, v198, v199\n"
"v_accvgpr_write_b32 a35, v198\n"
"v_accvgpr_read_b32 v198, a56\n"
"v_mul_f32 v198, v198, v199\n"
"v_accvgpr_write_b32 a56, v198\n"
"v_accvgpr_read_b32 v198, a57\n"
"v_mul_f32 v198, v198, v199\n"
"v_accvgpr_write_b32 a57, v198\n"
"v_accvgpr_read_b32 v198, a58\n"
"v_mul_f32 v198, v198, v199\n"
"v_accvgpr_write_b32 a58, v198\n"
"v_accvgpr_read_b32 v198, a59\n"
"v_mul_f32 v198, v198, v199\n"
"v_accvgpr_write_b32 a59, v198\n"
"v_accvgpr_read_b32 v198, a80\n"
"v_mul_f32 v198, v198, v199\n"
"v_accvgpr_write_b32 a80, v198\n"
"v_accvgpr_read_b32 v198, a81\n"
"v_mul_f32 v198, v198, v199\n"
"v_accvgpr_write_b32 a81, v198\n"
"v_accvgpr_read_b32 v198, a82\n"
"v_mul_f32 v198, v198, v199\n"
"v_accvgpr_write_b32 a82, v198\n"
"v_accvgpr_read_b32 v198, a83\n"
"v_mul_f32 v198, v198, v199\n"
"v_accvgpr_write_b32 a83, v198\n"
"v_accvgpr_read_b32 v198, a104\n"
"v_mul_f32 v198, v198, v199\n"
"v_accvgpr_write_b32 a104, v198\n"
"v_accvgpr_read_b32 v198, a105\n"
"v_mul_f32 v198, v198, v199\n"
"v_accvgpr_write_b32 a105, v198\n"
"v_accvgpr_read_b32 v198, a106\n"
"v_mul_f32 v198, v198, v199\n"
"v_accvgpr_write_b32 a106, v198\n"
"v_accvgpr_read_b32 v198, a107\n"
"v_mul_f32 v198, v198, v199\n"
"v_accvgpr_write_b32 a107, v198\n"
"v_accvgpr_read_b32 v198, a128\n"
"v_mul_f32 v198, v198, v199\n"
"v_accvgpr_write_b32 a128, v198\n"
"v_accvgpr_read_b32 v198, a129\n"
"v_mul_f32 v198, v198, v199\n"
"v_accvgpr_write_b32 a129, v198\n"
"v_accvgpr_read_b32 v198, a130\n"
"v_mul_f32 v198, v198, v199\n"
"v_accvgpr_write_b32 a130, v198\n"
"v_accvgpr_read_b32 v198, a131\n"
"v_mul_f32 v198, v198, v199\n"
"v_accvgpr_write_b32 a131, v198\n"
"v_accvgpr_read_b32 v198, a152\n"
"v_mul_f32 v198, v198, v199\n"
"v_accvgpr_write_b32 a152, v198\n"
"v_accvgpr_read_b32 v198, a153\n"
"v_mul_f32 v198, v198, v199\n"
"v_accvgpr_write_b32 a153, v198\n"
"v_accvgpr_read_b32 v198, a154\n"
"v_mul_f32 v198, v198, v199\n"
"v_accvgpr_write_b32 a154, v198\n"
"v_accvgpr_read_b32 v198, a155\n"
"v_mul_f32 v198, v198, v199\n"
"v_accvgpr_write_b32 a155, v198\n"
"v_accvgpr_read_b32 v198, a176\n"
"v_mul_f32 v198, v198, v199\n"
"v_accvgpr_write_b32 a176, v198\n"
"v_accvgpr_read_b32 v198, a177\n"
"v_mul_f32 v198, v198, v199\n"
"v_accvgpr_write_b32 a177, v198\n"
"v_accvgpr_read_b32 v198, a178\n"
"v_mul_f32 v198, v198, v199\n"
"v_accvgpr_write_b32 a178, v198\n"
"v_accvgpr_read_b32 v198, a179\n"
"v_mul_f32 v198, v198, v199\n"
"v_accvgpr_write_b32 a179, v198\n"
"v_max3_f32 v202, v120, v121, v122\n"
"v_max3_f32 v202, v202, v123, v124\n"
"v_max3_f32 v202, v202, v125, v126\n"
"v_max_f32 v202, v202, v127\n"
"v_mov_b32 v199, v202\n"
"s_nop 1\n"
"v_permlane32_swap_b32 v202, v199\n"
"v_max_f32 v202, v202, v199\n"
"v_mov_b32 v199, v202\n"
"s_nop 1\n"
"v_permlane16_swap_b32 v202, v199\n"
"v_max_f32 v202, v202, v199\n"
"v_sub_f32 v202, 0xc1800000, v202\n"
"v_min_f32 v202, v202, v156\n"
"v_sub_f32 v200, v202, v156\n"
"v_exp_f32 v199, v200\n"
"v_mov_b32 v156, v202\n"
"v_mov_b32 v157, v202\n"
"v_mov_b32 v158, v202\n"
"v_mov_b32 v159, v202\n"
"v_add_f32 v120, v120, v202\n"
"v_add_f32 v121, v121, v202\n"
"v_add_f32 v122, v122, v202\n"
"v_add_f32 v123, v123, v202\n"
"v_add_f32 v124, v124, v202\n"
"v_add_f32 v125, v125, v202\n"
"v_add_f32 v126, v126, v202\n"
"v_add_f32 v127, v127, v202\n"
"v_mul_f32 v180, v180, v199\n"
"v_mul_f32 v181, v181, v199\n"
"v_mul_f32 v182, v182, v199\n"
"v_mul_f32 v183, v183, v199\n"
"v_accvgpr_read_b32 v198, a12\n"
"v_mul_f32 v198, v198, v199\n"
"v_accvgpr_write_b32 a12, v198\n"
"v_accvgpr_read_b32 v198, a13\n"
"v_mul_f32 v198, v198, v199\n"
"v_accvgpr_write_b32 a13, v198\n"
"v_accvgpr_read_b32 v198, a14\n"
"v_mul_f32 v198, v198, v199\n"
"v_accvgpr_write_b32 a14, v198\n"
"v_accvgpr_read_b32 v198, a15\n"
"v_mul_f32 v198, v198, v199\n"
"v_accvgpr_write_b32 a15, v198\n"
"v_accvgpr_read_b32 v198, a36\n"
"v_mul_f32 v198, v198, v199\n"
"v_accvgpr_write_b32 a36, v198\n"
"v_accvgpr_read_b32 v198, a37\n"
"v_mul_f32 v198, v198, v199\n"
"v_accvgpr_write_b32 a37, v198\n"
"v_accvgpr_read_b32 v198, a38\n"
"v_mul_f32 v198, v198, v199\n"
"v_accvgpr_write_b32 a38, v198\n"
"v_accvgpr_read_b32 v198, a39\n"
"v_mul_f32 v198, v198, v199\n"
"v_accvgpr_write_b32 a39, v198\n"
"v_accvgpr_read_b32 v198, a60\n"
"v_mul_f32 v198, v198, v199\n"
"v_accvgpr_write_b32 a60, v198\n"
"v_accvgpr_read_b32 v198, a61\n"
"v_mul_f32 v198, v198, v199\n"
"v_accvgpr_write_b32 a61, v198\n"
"v_accvgpr_read_b32 v198, a62\n"
"v_mul_f32 v198, v198, v199\n"
"v_accvgpr_write_b32 a62, v198\n"
"v_accvgpr_read_b32 v198, a63\n"
"v_mul_f32 v198, v198, v199\n"
"v_accvgpr_write_b32 a63, v198\n"
"v_accvgpr_read_b32 v198, a84\n"
"v_mul_f32 v198, v198, v199\n"
"v_accvgpr_write_b32 a84, v198\n"
"v_accvgpr_read_b32 v198, a85\n"
"v_mul_f32 v198, v198, v199\n"
"v_accvgpr_write_b32 a85, v198\n"
"v_accvgpr_read_b32 v198, a86\n"
"v_mul_f32 v198, v198, v199\n"
"v_accvgpr_write_b32 a86, v198\n"
"v_accvgpr_read_b32 v198, a87\n"
"v_mul_f32 v198, v198, v199\n"
"v_accvgpr_write_b32 a87, v198\n"
"v_accvgpr_read_b32 v198, a108\n"
"v_mul_f32 v198, v198, v199\n"
"v_accvgpr_write_b32 a108, v198\n"
"v_accvgpr_read_b32 v198, a109\n"
"v_mul_f32 v198, v198, v199\n"
"v_accvgpr_write_b32 a109, v198\n"
"v_accvgpr_read_b32 v198, a110\n"
"v_mul_f32 v198, v198, v199\n"
"v_accvgpr_write_b32 a110, v198\n"
"v_accvgpr_read_b32 v198, a111\n"
"v_mul_f32 v198, v198, v199\n"
"v_accvgpr_write_b32 a111, v198\n"
"v_accvgpr_read_b32 v198, a132\n"
"v_mul_f32 v198, v198, v199\n"
"v_accvgpr_write_b32 a132, v198\n"
"v_accvgpr_read_b32 v198, a133\n"
"v_mul_f32 v198, v198, v199\n"
"v_accvgpr_write_b32 a133, v198\n"
"v_accvgpr_read_b32 v198, a134\n"
"v_mul_f32 v198, v198, v199\n"
"v_accvgpr_write_b32 a134, v198\n"
"v_accvgpr_read_b32 v198, a135\n"
"v_mul_f32 v198, v198, v199\n"
"v_accvgpr_write_b32 a135, v198\n"
"v_accvgpr_read_b32 v198, a156\n"
"v_mul_f32 v198, v198, v199\n"
"v_accvgpr_write_b32 a156, v198\n"
"v_accvgpr_read_b32 v198, a157\n"
"v_mul_f32 v198, v198, v199\n"
"v_accvgpr_write_b32 a157, v198\n"
"v_accvgpr_read_b32 v198, a158\n"
"v_mul_f32 v198, v198, v199\n"
"v_accvgpr_write_b32 a158, v198\n"
"v_accvgpr_read_b32 v198, a159\n"
"v_mul_f32 v198, v198, v199\n"
"v_accvgpr_write_b32 a159, v198\n"
"v_accvgpr_read_b32 v198, a180\n"
"v_mul_f32 v198, v198, v199\n"
"v_accvgpr_write_b32 a180, v198\n"
"v_accvgpr_read_b32 v198, a181\n"
"v_mul_f32 v198, v198, v199\n"
"v_accvgpr_write_b32 a181, v198\n"
"v_accvgpr_read_b32 v198, a182\n"
"v_mul_f32 v198, v198, v199\n"
"v_accvgpr_write_b32 a182, v198\n"
"v_accvgpr_read_b32 v198, a183\n"
"v_mul_f32 v198, v198, v199\n"
"v_accvgpr_write_b32 a183, v198\n"
"v_max3_f32 v202, v128, v129, v130\n"
"v_max3_f32 v202, v202, v131, v132\n"
"v_max3_f32 v202, v202, v133, v134\n"
"v_max_f32 v202, v202, v135\n"
"v_mov_b32 v199, v202\n"
"s_nop 1\n"
"v_permlane32_swap_b32 v202, v199\n"
"v_max_f32 v202, v202, v199\n"
"v_mov_b32 v199, v202\n"
"s_nop 1\n"
"v_permlane16_swap_b32 v202, v199\n"
"v_max_f32 v202, v202, v199\n"
"v_sub_f32 v202, 0xc1800000, v202\n"
"v_min_f32 v202, v202, v160\n"
"v_sub_f32 v200, v202, v160\n"
"v_exp_f32 v199, v200\n"
"v_mov_b32 v160, v202\n"
"v_mov_b32 v161, v202\n"
"v_mov_b32 v162, v202\n"
"v_mov_b32 v163, v202\n"
"v_add_f32 v128, v128, v202\n"
"v_add_f32 v129, v129, v202\n"
"v_add_f32 v130, v130, v202\n"
"v_add_f32 v131, v131, v202\n"
"v_add_f32 v132, v132, v202\n"
"v_add_f32 v133, v133, v202\n"
"v_add_f32 v134, v134, v202\n"
"v_add_f32 v135, v135, v202\n"
"v_mul_f32 v184, v184, v199\n"
"v_mul_f32 v185, v185, v199\n"
"v_mul_f32 v186, v186, v199\n"
"v_mul_f32 v187, v187, v199\n"
"v_accvgpr_read_b32 v198, a16\n"
"v_mul_f32 v198, v198, v199\n"
"v_accvgpr_write_b32 a16, v198\n"
"v_accvgpr_read_b32 v198, a17\n"
"v_mul_f32 v198, v198, v199\n"
"v_accvgpr_write_b32 a17, v198\n"
"v_accvgpr_read_b32 v198, a18\n"
"v_mul_f32 v198, v198, v199\n"
"v_accvgpr_write_b32 a18, v198\n"
"v_accvgpr_read_b32 v198, a19\n"
"v_mul_f32 v198, v198, v199\n"
"v_accvgpr_write_b32 a19, v198\n"
"v_accvgpr_read_b32 v198, a40\n"
"v_mul_f32 v198, v198, v199\n"
"v_accvgpr_write_b32 a40, v198\n"
"v_accvgpr_read_b32 v198, a41\n"
"v_mul_f32 v198, v198, v199\n"
"v_accvgpr_write_b32 a41, v198\n"
"v_accvgpr_read_b32 v198, a42\n"
"v_mul_f32 v198, v198, v199\n"
"v_accvgpr_write_b32 a42, v198\n"
"v_accvgpr_read_b32 v198, a43\n"
"v_mul_f32 v198, v198, v199\n"
"v_accvgpr_write_b32 a43, v198\n"
"v_accvgpr_read_b32 v198, a64\n"
"v_mul_f32 v198, v198, v199\n"
"v_accvgpr_write_b32 a64, v198\n"
"v_accvgpr_read_b32 v198, a65\n"
"v_mul_f32 v198, v198, v199\n"
"v_accvgpr_write_b32 a65, v198\n"
"v_accvgpr_read_b32 v198, a66\n"
"v_mul_f32 v198, v198, v199\n"
"v_accvgpr_write_b32 a66, v198\n"
"v_accvgpr_read_b32 v198, a67\n"
"v_mul_f32 v198, v198, v199\n"
"v_accvgpr_write_b32 a67, v198\n"
"v_accvgpr_read_b32 v198, a88\n"
"v_mul_f32 v198, v198, v199\n"
"v_accvgpr_write_b32 a88, v198\n"
"v_accvgpr_read_b32 v198, a89\n"
"v_mul_f32 v198, v198, v199\n"
"v_accvgpr_write_b32 a89, v198\n"
"v_accvgpr_read_b32 v198, a90\n"
"v_mul_f32 v198, v198, v199\n"
"v_accvgpr_write_b32 a90, v198\n"
"v_accvgpr_read_b32 v198, a91\n"
"v_mul_f32 v198, v198, v199\n"
"v_accvgpr_write_b32 a91, v198\n"
"v_accvgpr_read_b32 v198, a112\n"
"v_mul_f32 v198, v198, v199\n"
"v_accvgpr_write_b32 a112, v198\n"
"v_accvgpr_read_b32 v198, a113\n"
"v_mul_f32 v198, v198, v199\n"
"v_accvgpr_write_b32 a113, v198\n"
"v_accvgpr_read_b32 v198, a114\n"
"v_mul_f32 v198, v198, v199\n"
"v_accvgpr_write_b32 a114, v198\n"
"v_accvgpr_read_b32 v198, a115\n"
"v_mul_f32 v198, v198, v199\n"
"v_accvgpr_write_b32 a115, v198\n"
"v_accvgpr_read_b32 v198, a136\n"
"v_mul_f32 v198, v198, v199\n"
"v_accvgpr_write_b32 a136, v198\n"
"v_accvgpr_read_b32 v198, a137\n"
"v_mul_f32 v198, v198, v199\n"
"v_accvgpr_write_b32 a137, v198\n"
"v_accvgpr_read_b32 v198, a138\n"
"v_mul_f32 v198, v198, v199\n"
"v_accvgpr_write_b32 a138, v198\n"
"v_accvgpr_read_b32 v198, a139\n"
"v_mul_f32 v198, v198, v199\n"
"v_accvgpr_write_b32 a139, v198\n"
"v_accvgpr_read_b32 v198, a160\n"
"v_mul_f32 v198, v198, v199\n"
"v_accvgpr_write_b32 a160, v198\n"
"v_accvgpr_read_b32 v198, a161\n"
"v_mul_f32 v198, v198, v199\n"
"v_accvgpr_write_b32 a161, v198\n"
"v_accvgpr_read_b32 v198, a162\n"
"v_mul_f32 v198, v198, v199\n"
"v_accvgpr_write_b32 a162, v198\n"
"v_accvgpr_read_b32 v198, a163\n"
"v_mul_f32 v198, v198, v199\n"
"v_accvgpr_write_b32 a163, v198\n"
"v_accvgpr_read_b32 v198, a184\n"
"v_mul_f32 v198, v198, v199\n"
"v_accvgpr_write_b32 a184, v198\n"
"v_accvgpr_read_b32 v198, a185\n"
"v_mul_f32 v198, v198, v199\n"
"v_accvgpr_write_b32 a185, v198\n"
"v_accvgpr_read_b32 v198, a186\n"
"v_mul_f32 v198, v198, v199\n"
"v_accvgpr_write_b32 a186, v198\n"
"v_accvgpr_read_b32 v198, a187\n"
"v_mul_f32 v198, v198, v199\n"
"v_accvgpr_write_b32 a187, v198\n"
"v_max3_f32 v202, v136, v137, v138\n"
"v_max3_f32 v202, v202, v139, v140\n"
"v_max3_f32 v202, v202, v141, v142\n"
"v_max_f32 v202, v202, v143\n"
"v_mov_b32 v199, v202\n"
"s_nop 1\n"
"v_permlane32_swap_b32 v202, v199\n"
"v_max_f32 v202, v202, v199\n"
"v_mov_b32 v199, v202\n"
"s_nop 1\n"
"v_permlane16_swap_b32 v202, v199\n"
"v_max_f32 v202, v202, v199\n"
"v_sub_f32 v202, 0xc1800000, v202\n"
"v_min_f32 v202, v202, v164\n"
"v_sub_f32 v200, v202, v164\n"
"v_exp_f32 v199, v200\n"
"v_mov_b32 v164, v202\n"
"v_mov_b32 v165, v202\n"
"v_mov_b32 v166, v202\n"
"v_mov_b32 v167, v202\n"
"v_add_f32 v136, v136, v202\n"
"v_add_f32 v137, v137, v202\n"
"v_add_f32 v138, v138, v202\n"
"v_add_f32 v139, v139, v202\n"
"v_add_f32 v140, v140, v202\n"
"v_add_f32 v141, v141, v202\n"
"v_add_f32 v142, v142, v202\n"
"v_add_f32 v143, v143, v202\n"
"v_mul_f32 v188, v188, v199\n"
"v_mul_f32 v189, v189, v199\n"
"v_mul_f32 v190, v190, v199\n"
"v_mul_f32 v191, v191, v199\n"
"v_accvgpr_read_b32 v198, a20\n"
"v_mul_f32 v198, v198, v199\n"
"v_accvgpr_write_b32 a20, v198\n"
"v_accvgpr_read_b32 v198, a21\n"
"v_mul_f32 v198, v198, v199\n"
"v_accvgpr_write_b32 a21, v198\n"
"v_accvgpr_read_b32 v198, a22\n"
"v_mul_f32 v198, v198, v199\n"
"v_accvgpr_write_b32 a22, v198\n"
"v_accvgpr_read_b32 v198, a23\n"
"v_mul_f32 v198, v198, v199\n"
"v_accvgpr_write_b32 a23, v198\n"
"v_accvgpr_read_b32 v198, a44\n"
"v_mul_f32 v198, v198, v199\n"
"v_accvgpr_write_b32 a44, v198\n"
"v_accvgpr_read_b32 v198, a45\n"
"v_mul_f32 v198, v198, v199\n"
"v_accvgpr_write_b32 a45, v198\n"
"v_accvgpr_read_b32 v198, a46\n"
"v_mul_f32 v198, v198, v199\n"
"v_accvgpr_write_b32 a46, v198\n"
"v_accvgpr_read_b32 v198, a47\n"
"v_mul_f32 v198, v198, v199\n"
"v_accvgpr_write_b32 a47, v198\n"
"v_accvgpr_read_b32 v198, a68\n"
"v_mul_f32 v198, v198, v199\n"
"v_accvgpr_write_b32 a68, v198\n"
"v_accvgpr_read_b32 v198, a69\n"
"v_mul_f32 v198, v198, v199\n"
"v_accvgpr_write_b32 a69, v198\n"
"v_accvgpr_read_b32 v198, a70\n"
"v_mul_f32 v198, v198, v199\n"
"v_accvgpr_write_b32 a70, v198\n"
"v_accvgpr_read_b32 v198, a71\n"
"v_mul_f32 v198, v198, v199\n"
"v_accvgpr_write_b32 a71, v198\n"
"v_accvgpr_read_b32 v198, a92\n"
"v_mul_f32 v198, v198, v199\n"
"v_accvgpr_write_b32 a92, v198\n"
"v_accvgpr_read_b32 v198, a93\n"
"v_mul_f32 v198, v198, v199\n"
"v_accvgpr_write_b32 a93, v198\n"
"v_accvgpr_read_b32 v198, a94\n"
"v_mul_f32 v198, v198, v199\n"
"v_accvgpr_write_b32 a94, v198\n"
"v_accvgpr_read_b32 v198, a95\n"
"v_mul_f32 v198, v198, v199\n"
"v_accvgpr_write_b32 a95, v198\n"
"v_accvgpr_read_b32 v198, a116\n"
"v_mul_f32 v198, v198, v199\n"
"v_accvgpr_write_b32 a116, v198\n"
"v_accvgpr_read_b32 v198, a117\n"
"v_mul_f32 v198, v198, v199\n"
"v_accvgpr_write_b32 a117, v198\n"
"v_accvgpr_read_b32 v198, a118\n"
"v_mul_f32 v198, v198, v199\n"
"v_accvgpr_write_b32 a118, v198\n"
"v_accvgpr_read_b32 v198, a119\n"
"v_mul_f32 v198, v198, v199\n"
"v_accvgpr_write_b32 a119, v198\n"
"v_accvgpr_read_b32 v198, a140\n"
"v_mul_f32 v198, v198, v199\n"
"v_accvgpr_write_b32 a140, v198\n"
"v_accvgpr_read_b32 v198, a141\n"
"v_mul_f32 v198, v198, v199\n"
"v_accvgpr_write_b32 a141, v198\n"
"v_accvgpr_read_b32 v198, a142\n"
"v_mul_f32 v198, v198, v199\n"
"v_accvgpr_write_b32 a142, v198\n"
"v_accvgpr_read_b32 v198, a143\n"
"v_mul_f32 v198, v198, v199\n"
"v_accvgpr_write_b32 a143, v198\n"
"v_accvgpr_read_b32 v198, a164\n"
"v_mul_f32 v198, v198, v199\n"
"v_accvgpr_write_b32 a164, v198\n"
"v_accvgpr_read_b32 v198, a165\n"
"v_mul_f32 v198, v198, v199\n"
"v_accvgpr_write_b32 a165, v198\n"
"v_accvgpr_read_b32 v198, a166\n"
"v_mul_f32 v198, v198, v199\n"
"v_accvgpr_write_b32 a166, v198\n"
"v_accvgpr_read_b32 v198, a167\n"
"v_mul_f32 v198, v198, v199\n"
"v_accvgpr_write_b32 a167, v198\n"
"v_accvgpr_read_b32 v198, a188\n"
"v_mul_f32 v198, v198, v199\n"
"v_accvgpr_write_b32 a188, v198\n"
"v_accvgpr_read_b32 v198, a189\n"
"v_mul_f32 v198, v198, v199\n"
"v_accvgpr_write_b32 a189, v198\n"
"v_accvgpr_read_b32 v198, a190\n"
"v_mul_f32 v198, v198, v199\n"
"v_accvgpr_write_b32 a190, v198\n"
"v_accvgpr_read_b32 v198, a191\n"
"v_mul_f32 v198, v198, v199\n"
"v_accvgpr_write_b32 a191, v198\n"
"v_exp_f32 v96, v96\n"
"v_exp_f32 v97, v97\n"
"v_exp_f32 v98, v98\n"
"v_exp_f32 v99, v99\n"
"v_cvt_pk_bf16_f32 v96, v96, v97\n"
"v_cvt_pk_bf16_f32 v97, v98, v99\n"
"v_or_b32 v198, v96, v97\n"
"v_exp_f32 v100, v100\n"
"v_exp_f32 v101, v101\n"
"v_exp_f32 v102, v102\n"
"v_exp_f32 v103, v103\n"
"v_cvt_pk_bf16_f32 v98, v100, v101\n"
"v_cvt_pk_bf16_f32 v99, v102, v103\n"
"v_or3_b32 v198, v198, v98, v99\n"
"v_exp_f32 v104, v104\n"
"v_exp_f32 v105, v105\n"
"v_exp_f32 v106, v106\n"
"v_exp_f32 v107, v107\n"
"v_cvt_pk_bf16_f32 v104, v104, v105\n"
"v_cvt_pk_bf16_f32 v105, v106, v107\n"
"v_or3_b32 v198, v198, v104, v105\n"
"v_exp_f32 v108, v108\n"
"v_exp_f32 v109, v109\n"
"v_exp_f32 v110, v110\n"
"v_exp_f32 v111, v111\n"
"v_cvt_pk_bf16_f32 v106, v108, v109\n"
"v_cvt_pk_bf16_f32 v107, v110, v111\n"
"v_or3_b32 v198, v198, v106, v107\n"
"v_exp_f32 v112, v112\n"
"v_exp_f32 v113, v113\n"
"v_exp_f32 v114, v114\n"
"v_exp_f32 v115, v115\n"
"v_cvt_pk_bf16_f32 v112, v112, v113\n"
"v_cvt_pk_bf16_f32 v113, v114, v115\n"
"v_or3_b32 v198, v198, v112, v113\n"
"v_exp_f32 v116, v116\n"
"v_exp_f32 v117, v117\n"
"v_exp_f32 v118, v118\n"
"v_exp_f32 v119, v119\n"
"v_cvt_pk_bf16_f32 v114, v116, v117\n"
"v_cvt_pk_bf16_f32 v115, v118, v119\n"
"v_or3_b32 v198, v198, v114, v115\n"
"v_exp_f32 v120, v120\n"
"v_exp_f32 v121, v121\n"
"v_exp_f32 v122, v122\n"
"v_exp_f32 v123, v123\n"
"v_cvt_pk_bf16_f32 v120, v120, v121\n"
"v_cvt_pk_bf16_f32 v121, v122, v123\n"
"v_or3_b32 v198, v198, v120, v121\n"
"v_exp_f32 v124, v124\n"
"v_exp_f32 v125, v125\n"
"v_exp_f32 v126, v126\n"
"v_exp_f32 v127, v127\n"
"v_cvt_pk_bf16_f32 v122, v124, v125\n"
"v_cvt_pk_bf16_f32 v123, v126, v127\n"
"v_or3_b32 v198, v198, v122, v123\n"
"v_exp_f32 v128, v128\n"
"v_exp_f32 v129, v129\n"
"v_exp_f32 v130, v130\n"
"v_exp_f32 v131, v131\n"
"v_cvt_pk_bf16_f32 v128, v128, v129\n"
"v_cvt_pk_bf16_f32 v129, v130, v131\n"
"v_or3_b32 v198, v198, v128, v129\n"
"v_exp_f32 v132, v132\n"
"v_exp_f32 v133, v133\n"
"v_exp_f32 v134, v134\n"
"v_exp_f32 v135, v135\n"
"v_cvt_pk_bf16_f32 v130, v132, v133\n"
"v_cvt_pk_bf16_f32 v131, v134, v135\n"
"v_or3_b32 v198, v198, v130, v131\n"
"v_exp_f32 v136, v136\n"
"v_exp_f32 v137, v137\n"
"v_exp_f32 v138, v138\n"
"v_exp_f32 v139, v139\n"
"v_cvt_pk_bf16_f32 v136, v136, v137\n"
"v_cvt_pk_bf16_f32 v137, v138, v139\n"
"v_or3_b32 v198, v198, v136, v137\n"
"v_exp_f32 v140, v140\n"
"v_exp_f32 v141, v141\n"
"v_exp_f32 v142, v142\n"
"v_exp_f32 v143, v143\n"
"v_cvt_pk_bf16_f32 v138, v140, v141\n"
"v_cvt_pk_bf16_f32 v139, v142, v143\n"
"v_or3_b32 v198, v198, v138, v139\n"
"s_nop 3\n"
"s_setpc_b64 s[80:81]\n"
".Lend:\n"
  :: "s"(xb), "s"(kb), "s"(vb), "s"(wb), "s"(ws), "s"(wob), "s"(ob), "s"(meta)
  : "memory", "vcc", "scc", "s24", "s25", "s26", "s27", "s28", "s29", "s30", "s31", "s32", "s33", "s34", "s35", "s36", "s37", "s38", "s39", "s40", "s41", "s42", "s43", "s44", "s45", "s46", "s47", "s48", "s49", "s50", "s51", "s52", "s53", "s54", "s55", "s56", "s57", "s58", "s59", "s60", "s61", "s62", "s63", "s64", "s65", "s66", "s67", "s68", "s69", "s70", "s71", "s72", "s73", "s74", "s75", "s76", "s77", "s78", "s79", "s80", "s81", "s82", "s83", "s84", "s85", "s86", "s87", "s88", "s89", "s90", "s91", "s92", "s93", "s94", "s95", "s96", "s97", "s98", "s99", "v0", "v1", "v2", "v3", "v4", "v5", "v6", "v7", "v8", "v9", "v10", "v11", "v12", "v13", "v14", "v15", "v16", "v17", "v18", "v19", "v20", "v21", "v22", "v23", "v24", "v25", "v26", "v27", "v28", "v29", "v30", "v31", "v32", "v33", "v34", "v35", "v36", "v37", "v38", "v39", "v40", "v41", "v42", "v43", "v44", "v45", "v46", "v47", "v48", "v49", "v50", "v51", "v52", "v53", "v54", "v55", "v56", "v57", "v58", "v59", "v60", "v61", "v62", "v63", "v64", "v65", "v66", "v67", "v68", "v69", "v70", "v71", "v72", "v73", "v74", "v75", "v76", "v77", "v78", "v79", "v80", "v81", "v82", "v83", "v84", "v85", "v86", "v87", "v88", "v89", "v90", "v91", "v92", "v93", "v94", "v95", "v96", "v97", "v98", "v99", "v100", "v101", "v102", "v103", "v104", "v105", "v106", "v107", "v108", "v109", "v110", "v111", "v112", "v113", "v114", "v115", "v116", "v117", "v118", "v119", "v120", "v121", "v122", "v123", "v124", "v125", "v126", "v127", "v128", "v129", "v130", "v131", "v132", "v133", "v134", "v135", "v136", "v137", "v138", "v139", "v140", "v141", "v142", "v143", "v144", "v145", "v146", "v147", "v148", "v149", "v150", "v151", "v152", "v153", "v154", "v155", "v156", "v157", "v158", "v159", "v160", "v161", "v162", "v163", "v164", "v165", "v166", "v167", "v168", "v169", "v170", "v171", "v172", "v173", "v174", "v175", "v176", "v177", "v178", "v179", "v180", "v181", "v182", "v183", "v184", "v185", "v186", "v187", "v188", "v189", "v190", "v191", "v192", "v193", "v194", "v195", "v196", "v197", "v198", "v199", "v200", "v201", "v202", "v203", "v204", "v205", "v206", "v207", "v208", "v209", "v210", "v211", "v212", "v213", "v214", "v215", "v216", "v217", "v218", "v219", "v220", "v221", "v222", "v223", "v224", "v225", "v226", "v227", "v228", "v229", "v230", "v231", "v232", "v233", "v234", "v235", "v236", "v237", "v238", "v239", "v240", "v241", "v242", "v243", "v244", "v245", "v246", "v247", "v248", "v249", "v250", "v251", "v252", "v253", "v254", "v255", "a0", "a1", "a2", "a3", "a4", "a5", "a6", "a7", "a8", "a9", "a10", "a11", "a12", "a13", "a14", "a15", "a16", "a17", "a18", "a19", "a20", "a21", "a22", "a23", "a24", "a25", "a26", "a27", "a28", "a29", "a30", "a31", "a32", "a33", "a34", "a35", "a36", "a37", "a38", "a39", "a40", "a41", "a42", "a43", "a44", "a45", "a46", "a47", "a48", "a49", "a50", "a51", "a52", "a53", "a54", "a55", "a56", "a57", "a58", "a59", "a60", "a61", "a62", "a63", "a64", "a65", "a66", "a67", "a68", "a69", "a70", "a71", "a72", "a73", "a74", "a75", "a76", "a77", "a78", "a79", "a80", "a81", "a82", "a83", "a84", "a85", "a86", "a87", "a88", "a89", "a90", "a91", "a92", "a93", "a94", "a95", "a96", "a97", "a98", "a99", "a100", "a101", "a102", "a103", "a104", "a105", "a106", "a107", "a108", "a109", "a110", "a111", "a112", "a113", "a114", "a115", "a116", "a117", "a118", "a119", "a120", "a121", "a122", "a123", "a124", "a125", "a126", "a127", "a128", "a129", "a130", "a131", "a132", "a133", "a134", "a135", "a136", "a137", "a138", "a139", "a140", "a141", "a142", "a143", "a144", "a145", "a146", "a147", "a148", "a149", "a150", "a151", "a152", "a153", "a154", "a155", "a156", "a157", "a158", "a159", "a160", "a161", "a162", "a163", "a164", "a165", "a166", "a167", "a168", "a169", "a170", "a171", "a172", "a173", "a174", "a175", "a176", "a177", "a178", "a179", "a180", "a181", "a182", "a183", "a184", "a185", "a186", "a187", "a188", "a189", "a190", "a191", "a192", "a193", "a194", "a195", "a196", "a197", "a198", "a199", "a200", "a201", "a202", "a203", "a204", "a205", "a206", "a207", "a208", "a209", "a210", "a211", "a212", "a213", "a214", "a215", "a216", "a217", "a218", "a219", "a220", "a221", "a222", "a223", "a224", "a225", "a226", "a227", "a228", "a229", "a230", "a231", "a232", "a233", "a234", "a235", "a236", "a237", "a238", "a239", "a240", "a241", "a242", "a243", "a244", "a245", "a246", "a247", "a248", "a249", "a250", "a251", "a252", "a253", "a254", "a255");
}

extern "C" void kernel_launch(void* const* d_in, const int* in_sizes, int n_in, void* d_out, int out_size, void* d_ws, size_t ws_size, hipStream_t stream) {
  if (n_in != 5 || ws_size < 0x2800000u) return;
  mha_fused<<<256, 256, 0, stream>>>((const u16*)d_in[0], (const u16*)d_in[1], (const u16*)d_in[2], (const u16*)d_in[3], (const u16*)d_in[4], (u16*)d_out, (char*)d_ws);
}
```
